```python
import torch

def _softmax(x, axis=-1):
    return torch.softmax(x, dim=axis)
def _gelu(x):
    return torch.nn.functional.gelu(x, approximate="tanh")
def _silu(x):
    return torch.nn.functional.silu(x)
def _sigmoid(x):
    return torch.sigmoid(x)
def _layernorm(x, axis=-1, eps=1e-5):
    m = x.mean(dim=axis, keepdim=True)
    v = x.var(dim=axis, keepdim=True, unbiased=False)
    return (x - m) / torch.sqrt(v + eps)
def _scatter_add(tgt, idx, src, axis=0):
    return tgt.clone().index_add_(axis, idx, src)
def _segment_sum(src, seg, n):
    out = torch.zeros((n,) + src.shape[1:], dtype=src.dtype)
    return out.index_add_(0, seg, src)
def _pad(x, pw):
    flat = tuple(v for pair in reversed(pw) for v in pair)
    return torch.nn.functional.pad(x, flat)

kernel_name = 'interleave_layers__sliding_local_dilated__ssm_conv_s4__prompt'

def setup_inputs():

    _w = (lambda a: (lambda t: t.to(torch.bfloat16) if t.is_floating_point() else t)(torch.as_tensor(a)))
    def _A_setup():
        B, S, D = 1, 2048, 512
        return {
            "x": _w((lambda *s: torch.randn(*s, dtype=torch.bfloat16) * 0.05)(B, S, D)),
            "wqkv": _w((lambda *s: torch.randn(*s, dtype=torch.bfloat16) * 0.05)(D, 3 * D)),
            "wo": _w((lambda *s: torch.randn(*s, dtype=torch.bfloat16) * 0.05)(D, D)),
        }
    _A_in = _A_setup()

    def _B_setup():
        B, L, D, N = 1, 2048, 512, 128
        A_re = -(lambda _x: torch.exp(torch.as_tensor(_x)))((lambda *s: torch.randn(*s, dtype=torch.bfloat16) * 3.2)(D, N))
        A_im = torch.pi * (lambda *s: torch.randn(*s, dtype=torch.bfloat16) * 0.1)(D, N)
        B_re = (lambda *s: torch.randn(*s, dtype=torch.bfloat16) * 0.1)(D, N)
        B_im = (lambda *s: torch.randn(*s, dtype=torch.bfloat16) * 0.1)(D, N)
        C_re = (lambda *s: torch.randn(*s, dtype=torch.bfloat16) * 0.1)(D, N)
        C_im = (lambda *s: torch.randn(*s, dtype=torch.bfloat16) * 0.1)(D, N)
        log_dt = (lambda *s: torch.randn(*s, dtype=torch.bfloat16) * 0.1)(D) * 0.5 - 4.0
        Dskip = (lambda *s: torch.randn(*s, dtype=torch.bfloat16) * 0.1)(D)
        return {
            "u": _w((lambda *s: torch.randn(*s, dtype=torch.bfloat16) * 0.05)(B, L, D)),
            "A_re": _w(A_re),
            "A_im": _w(A_im),
            "B_re": _w(B_re),
            "B_im": _w(B_im),
            "C_re": _w(C_re),
            "C_im": _w(C_im),
            "log_dt": _w(log_dt),
            "Dskip": _w(Dskip),
        }
    _B_in = _B_setup()

    return {
        "A_x": _A_in["x"],
        "A_wqkv": _A_in["wqkv"],
        "A_wo": _A_in["wo"],
        "B_u": _B_in["u"],
        "B_A_re": _B_in["A_re"],
        "B_A_im": _B_in["A_im"],
        "B_B_re": _B_in["B_re"],
        "B_B_im": _B_in["B_im"],
        "B_C_re": _B_in["C_re"],
        "B_C_im": _B_in["C_im"],
        "B_log_dt": _B_in["log_dt"],
        "B_Dskip": _B_in["Dskip"],
    }

def reference(A_x, A_wqkv, A_wo, B_u, B_A_re, B_A_im, B_B_re, B_B_im, B_C_re, B_C_im, B_log_dt, B_Dskip):
    def _A_ref(x, wqkv, wo):
        B, S, D = x.shape
        H = 32; Dh = D // H
        W, dil = 8, 2
        qkv = (lambda x, *d: x.permute(*d))((x @ wqkv).reshape(B, S, 3, H, Dh), 2, 0, 3, 1, 4)
        q, k, v = qkv[0], qkv[1], qkv[2]
        scale = Dh ** -0.5
        pos = torch.arange(S)
        off = torch.arange(W) * dil
        idx = pos[:, None] - off[None, :]
        mask = idx >= 0
        idx_c = torch.where(mask, idx, 0).reshape(-1)
        kg = k[:, :, idx_c, :].reshape(B, H, S, W, Dh)
        vg = v[:, :, idx_c, :].reshape(B, H, S, W, Dh)
        logits = torch.einsum("bhsd,bhswd->bhsw", q, kg) * scale
        logits = torch.where(mask[None, None, :, :], logits, -1e9)
        attn = _softmax(logits, axis=-1)
        o = torch.einsum("bhsw,bhswd->bhsd", attn, vg)
        return (lambda x, *d: x.permute(*d))(o, 0, 2, 1, 3).reshape(B, S, D) @ wo

    def _B_ref(u, A_re, A_im, B_re, B_im, C_re, C_im, log_dt, Dskip):
        Bsz, L, D = u.shape
        dt = torch.exp(log_dt)[:, None]
        A = A_re + 1j * A_im
        Bc = B_re + 1j * B_im
        Cc = C_re + 1j * C_im
        logdA = dt * A
        dB = dt * Bc
        l = (lambda x, dt: x.to(dt))(torch.arange(L), A_re.dtype)
        P = torch.exp(logdA[:, :, None] * l[None, None, :])
        K = torch.einsum("dn,dnl,dn->dl", Cc, P, dB).real
        L2 = 2 * L
        Kp = _pad(K, ((0, 0), (0, L)))
        up = (lambda x, *d: x.permute(*d))(u, 0, 2, 1)
        up = _pad(up, ((0, 0), (0, 0), (0, L)))
        Kf = (lambda x, axes=(-2,-1): torch.fft.rfft2(x.float(), dim=axes))(Kp[:, None, :], axes=(-2, -1))
        Uf = (lambda x, axes=(-2,-1): torch.fft.rfft2(x.float(), dim=axes))(up[:, :, None, :], axes=(-2, -1))
        Yf = Uf * Kf[None, :, :, :]
        y = (lambda x, s, axes=(-2,-1): torch.fft.irfft2(x, s=s, dim=axes).to(torch.bfloat16))(Yf, (1, L2), axes=(-2, -1))[:, :, 0, :L]
        y = (lambda x, *d: x.permute(*d))(y, 0, 2, 1) + u * Dskip[None, None, :]
        return y

    x = A_x
    x = x + _A_ref(_layernorm(x, axis=-1), A_wqkv, A_wo)
    x = x + _B_ref(_layernorm(x, axis=-1), B_A_re, B_A_im, B_B_re, B_B_im, B_C_re, B_C_im, B_log_dt, B_Dskip)
    x = x + _A_ref(_layernorm(x, axis=-1), A_wqkv, A_wo)
    x = x + _B_ref(_layernorm(x, axis=-1), B_A_re, B_A_im, B_B_re, B_B_im, B_C_re, B_C_im, B_log_dt, B_Dskip)
    return x
```

```cpp
#include <hip/hip_runtime.h>
#include <hip/hip_bf16.h>
#include <stdint.h>

typedef unsigned short u16;
typedef float f2 __attribute__((ext_vector_type(2)));
typedef float f4 __attribute__((ext_vector_type(4)));
typedef short bf16x8 __attribute__((ext_vector_type(8)));
typedef _Float16 h2 __attribute__((ext_vector_type(2)));
typedef _Float16 h8 __attribute__((ext_vector_type(8)));
typedef __attribute__((ext_vector_type(2))) __bf16 bfx2;
typedef unsigned int u32x4 __attribute__((ext_vector_type(4)));
typedef unsigned int u32x2 __attribute__((ext_vector_type(2)));
typedef unsigned int u32x8 __attribute__((ext_vector_type(8)));
typedef unsigned int u32x16 __attribute__((ext_vector_type(16)));
typedef unsigned int u32x32 __attribute__((ext_vector_type(32)));
typedef float f32x4 __attribute__((ext_vector_type(4)));
typedef float f32x16 __attribute__((ext_vector_type(16)));
#define LDSP(p) ((__attribute__((address_space(3))) uint32_t*)(p))
#define LAS(T, a) (*(__attribute__((address_space(3))) T*)(uintptr_t)(a))
#define SB0 __builtin_amdgcn_sched_barrier(0);
#define OPQ(x) asm("" : "+v"(x))
#define BAR asm volatile("s_waitcnt lgkmcnt(0)\n\ts_barrier" ::: "memory");
#define VM0 asm volatile("s_waitcnt vmcnt(0)" ::: "memory");

#define DM 512
#define SENT 0xFFFFFFFFu
#define MB (1u << 20)
#define W_OT 0u
#define W_X1 (2u * MB)
#define W_ST (4u * MB)
#define W_X2 (6u * MB)
#define W_X3 (18u * MB)
#define W_PS (13u * MB)
#define W_WOP (14u * MB)
#define W_SYN (16u * MB)
#define SC 98304
#define QP 208
#define L_QT SC
#define L_HST (SC + 48128)
#define L_OT (SC + 30208)
#define L_SP (SC + 62976)
#define L_CS (SC + 64512)
#define L_HS (SC + 64896)
#define L_FL (SC + 65408)
#define L_PR SC
#define L_CP SC
#define CPP 2112
#define L_KE (SC + 16896)
#define L_TP (SC + 20480)
#define L_PF SC
#define L_U (SC + 20480)
#define UCH 6480
#define L_S (SC + 33536)
#define S8A 4032
#define S16A 2304
#define SCH 12672
#define FW 0
#define FH 16
#define FC 32
#define FX 48
#define FT 64
#define FS 80
#define FR 96
#define FK 112

__device__ __forceinline__ float bf2f(u16 v) { return __uint_as_float(((uint32_t)v) << 16); }
__device__ __forceinline__ u16 f2bf(float f) { return __bfloat16_as_ushort(__float2bfloat16(f)); }
__device__ __forceinline__ float rbf(float f) { return bf2f(f2bf(f)); }
__device__ __forceinline__ float lo2f(uint32_t v) { return __uint_as_float(v << 16); }
__device__ __forceinline__ float hi2f(uint32_t v) { return __uint_as_float(v & 0xffff0000u); }
__device__ __forceinline__ uint32_t pack2(float lo, float hi) { f2 v = {lo, hi}; bfx2 r = __builtin_convertvector(v, bfx2); return *(uint32_t*)&r; }
__device__ __forceinline__ float rb1(float x) { return __uint_as_float(pack2(0.f, x)); }
__device__ __forceinline__ f2 rb2(f2 x) { return (f2){rb1(x.x), rb1(x.y)}; }
__device__ __forceinline__ uint32_t packh(float lo, float hi) { f2 v = {lo, hi}; h2 r = __builtin_convertvector(v, h2); return *(uint32_t*)&r; }
__device__ __forceinline__ float dot2(uint32_t a, uint32_t b, float c) { return __builtin_amdgcn_fdot2_f32_bf16(*(bfx2*)&a, *(bfx2*)&b, c, false); }
__device__ __forceinline__ float xsum16(float x) { auto r = __builtin_amdgcn_permlane16_swap(__float_as_uint(x), __float_as_uint(x), false, false); return __uint_as_float(r[0]) + __uint_as_float(r[1]); }
__device__ __forceinline__ float xsum32(float x) { auto r = __builtin_amdgcn_permlane32_swap(__float_as_uint(x), __float_as_uint(x), false, false); return __uint_as_float(r[0]) + __uint_as_float(r[1]); }
__device__ __forceinline__ uint32_t mx4(const u32x4& q) { return max(max(q[0], q[1]), max(q[2], q[3])); }
#define MFB(a, b, c) __builtin_amdgcn_mfma_f32_32x32x16_bf16(*(const bf16x8*)&(a), *(const bf16x8*)&(b), c, 0, 0, 0)
#define MFS(a, b, c) __builtin_amdgcn_mfma_f32_16x16x32_bf16(*(const bf16x8*)&(a), *(const bf16x8*)&(b), c, 0, 0, 0)
#define MFK(a, b, c) __builtin_amdgcn_mfma_f32_16x16x32_f16(*(const h8*)&(a), *(const h8*)&(b), c, 0, 0, 0)
#define MFH(a, b, c) __builtin_amdgcn_mfma_f32_32x32x16_f16(*(const h8*)&(a), *(const h8*)&(b), c, 0, 0, 0)
#define ZERO16(z) _Pragma("unroll") for (int j_ = 0; j_ < 16; ++j_) z[j_] = 0.f;
#define AB(x, y) __builtin_amdgcn_alignbit(y, x, 16)

#define ST16(off, v, base) asm volatile("global_store_dwordx4 %0, %1, %2 sc1\n\ts_nop 1" :: "v"(off), "v"(v), "s"(base) : "memory");
#define FSET(a, v) asm volatile("s_mov_b64 exec, 1\n\tds_write_b32 %0, %1\n\ts_mov_b64 exec, -1" :: "v"(a), "v"(v) : "memory");
__device__ __forceinline__ void flag_set(uint32_t a, int v) {
  asm volatile("s_waitcnt lgkmcnt(0)\n\ts_mov_b64 exec, 1\n\tds_write_b32 %0, %1\n\ts_mov_b64 exec, -1" :: "v"(a), "v"(v) : "memory");
}
__device__ __forceinline__ void flag_wait(uint32_t a, int v) {
#pragma unroll 1
  for (int it = 0; it < (1 << 20); ++it) {
    int q;
    asm volatile("ds_read_b32 %0, %1\n\ts_waitcnt lgkmcnt(0)" : "=v"(q) : "v"(a) : "memory");
    if (__all(q >= v)) break;
    __builtin_amdgcn_s_sleep(1);
  }
}

template <int CTRL> __device__ __forceinline__ float dppf(float v) { return __uint_as_float(__builtin_amdgcn_mov_dpp(__float_as_uint(v), CTRL, 0xf, 0xf, true)); }
__device__ __forceinline__ void cexp2(float e, float r, float& x, float& y) {
  const float m = __builtin_amdgcn_exp2f(e);
  r = __builtin_amdgcn_fractf(r);
  x = m * __builtin_amdgcn_cosf(r); OPQ(x);
  y = m * __builtin_amdgcn_sinf(r); OPQ(y);
}
__device__ __forceinline__ void split2(float x, float y, uint32_t& h, uint32_t& l) {
  h = pack2(x, y);
  float xl = x - lo2f(h); OPQ(xl);
  float yl = y - hi2f(h); OPQ(yl);
  l = pack2(xl, yl);
}
template <int J> __device__ __forceinline__ void kgen_mode(const float (&pm)[7], float tA0, float tA1, float tB0, u32x4 (&o)[4]) {
  const float a2 = dppf<0x150 + J>(pm[0]), b = dppf<0x150 + J>(pm[1]), lc = dppf<0x150 + J>(pm[2]), ph = dppf<0x150 + J>(pm[3]);
  const float gx = dppf<0x150 + J>(pm[4]), gy = dppf<0x150 + J>(pm[5]);
  float x, y, e, r, u, v;
  e = fmaf(a2, tA0, lc); OPQ(e); r = fmaf(-b, tA0, ph); OPQ(r); cexp2(e, r, x, y); o[0][J] = packh(x, y);
  e = fmaf(a2, tA1, lc); OPQ(e); r = fmaf(-b, tA1, ph); OPQ(r); cexp2(e, r, x, y); o[1][J] = packh(x, y);
  e = a2 * tB0; OPQ(e); r = b * tB0; OPQ(r); cexp2(e, r, x, y); o[2][J] = packh(x, y);
  e = gy * y; OPQ(e); r = gy * x; OPQ(r); u = fmaf(gx, x, -e); OPQ(u); v = fmaf(gx, y, r); OPQ(v);
  o[3][J] = packh(u, v);
}
constexpr int rnd_l(int v) { int e = 0; while ((v >> e) >= 256) ++e; if (!e) return v; const int h = 1 << (e - 1), q = v >> e, r = v & ((1 << e) - 1); return (q + ((r > h || (r == h && (q & 1))) ? 1 : 0)) << e; }
constexpr int slot_l(int v) { v = rnd_l(v); int e = 0; while ((v >> e) >= 256) ++e; return e * 128 + (v >> e); }
struct Tabs { u16 img[1032]; u16 prd[1024]; };
constexpr Tabs mk_tabs() {
  Tabs t{};
  for (int i = 0; i < 1032; ++i) {
    const int f = 1023 - i, o = f >> 3, a = (o - 74) / 12, m = f - 8 * (74 + 12 * a);
    int v = -1;
    if (f >= 32 && f - 32 <= 525) v = f - 32;
    if (o >= 74 && o < 122 && m < 64) v = a >= 2 ? 16 * (65 + m) - 8 * (a - 2) : (m < 62 ? 8 * (66 + m) + 4 * a : (m == 62 ? 1024 : -1));
    t.img[i] = (u16)(2 * (v < 0 ? 700 : slot_l(v)));
  }
  for (int w = 0; w < 1024; ++w) {
    const int tile = (w >> 9) * 2 + ((w >> 2) & 1), lane = (w >> 3) & 63;
    const int R = (tile >> 1) * 16 + (lane >> 4) * 4 + (w & 3), C = (tile & 1) * 16 + (lane & 15), r = R & 15;
    const int lr = 8 * C + (R < 16 ? (r < 8 ? r : 256 * ((r >> 2) - 1) + 2 * (r & 3)) : (r < 8 ? 768 + 256 * (r >> 1) + 4 * (r & 1) : 1792 + 256 * (r & 1)));
    const bool ok = R < 26 && lr <= 2048 && rnd_l(lr) == lr;
    t.prd[w] = (u16)(2 * (ok ? slot_l(lr) : 704 + (lane & 63)));
  }
  return t;
}
__device__ __attribute__((aligned(16))) const Tabs g_tabs = mk_tabs();

struct Args { const u16 *x, *w, *wo, *A_re, *A_im, *B_re, *B_im, *C_re, *C_im, *log_dt, *Dskip; u16* out; char* ws; };
#define DL1A 5
#define DL2A 5
#define WPR 2
#define DL4 12
#define OAUX "sc1"
#define DL1 18
#define DL2 5
#define DL3 4
#define RB_ ((xcd >> 1) * 4 + (bi & 3))
#define CG_ ((xcd & 1) * 8 + (bi >> 2))
#define ROT (((bi >> 2) + (bi & 3) * 2) & 7)
#define KGW 4
#define EQ3 7
#define APR 1

#define DSV(v, s1, s2) s1 = dot2(v[0], 0x3F803F80u, s1); s2 = dot2(v[0], v[0], s2); s1 = dot2(v[1], 0x3F803F80u, s1); s2 = dot2(v[1], v[1], s2); \
                       s1 = dot2(v[2], 0x3F803F80u, s1); s2 = dot2(v[2], v[2], s2); s1 = dot2(v[3], 0x3F803F80u, s1); s2 = dot2(v[3], v[3], s2);
#define LNSTAT(so, s1, s2) { const float mean_ = (s1) * (1.f / 512.f); const float var_ = fmaxf((s2) * (1.f / 512.f) - mean_ * mean_, 0.f); \
      so.x = rbf(mean_); so.y = __builtin_amdgcn_rcpf(rbf(__builtin_amdgcn_sqrtf(rbf(rbf(var_) + 1e-5f)))); }
#define L16(o0, o1) \
    "global_load_dword %0, %18, %19 offset:-2*" #o0 "+0*" #o1 " sc1\n\t" \
    "global_load_dword %1, %18, %19 offset:-2*" #o0 "+1*" #o1 " sc1\n\t" \
    "global_load_dword %2, %18, %19 offset:-2*" #o0 "+2*" #o1 " sc1\n\t" \
    "global_load_dword %3, %18, %19 offset:-2*" #o0 "+3*" #o1 " sc1\n\t" \
    "global_load_dword %4, %18, %19 offset:-1*" #o0 "+0*" #o1 " sc1\n\t" \
    "global_load_dword %5, %18, %19 offset:-1*" #o0 "+1*" #o1 " sc1\n\t" \
    "global_load_dword %6, %18, %19 offset:-1*" #o0 "+2*" #o1 " sc1\n\t" \
    "global_load_dword %7, %18, %19 offset:-1*" #o0 "+3*" #o1 " sc1\n\t" \
    "global_load_dword %8, %18, %19 offset:0*" #o0 "+0*" #o1 " sc1\n\t" \
    "global_load_dword %9, %18, %19 offset:0*" #o0 "+1*" #o1 " sc1\n\t" \
    "global_load_dword %10, %18, %19 offset:0*" #o0 "+2*" #o1 " sc1\n\t" \
    "global_load_dword %11, %18, %19 offset:0*" #o0 "+3*" #o1 " sc1\n\t" \
    "global_load_dword %12, %18, %19 offset:1*" #o0 "+0*" #o1 " sc1\n\t" \
    "global_load_dword %13, %18, %19 offset:1*" #o0 "+1*" #o1 " sc1\n\t" \
    "global_load_dword %14, %18, %19 offset:1*" #o0 "+2*" #o1 " sc1\n\t" \
    "global_load_dword %15, %18, %19 offset:1*" #o0 "+3*" #o1 " sc1\n\t"
#define CHK16(r, n, voff, base, o0, o1) { uint32_t t_, c_; \
    asm volatile("s_waitcnt vmcnt(%20)\n\ts_mov_b32 %17, 0x10000\n" \
      "1:\n\tv_max3_u32 %16, %0, %1, %2\n\tv_max3_u32 %16, %16, %3, %4\n\tv_max3_u32 %16, %16, %5, %6\n\tv_max3_u32 %16, %16, %7, %8\n\t" \
      "v_max3_u32 %16, %16, %9, %10\n\tv_max3_u32 %16, %16, %11, %12\n\tv_max3_u32 %16, %16, %13, %14\n\tv_max_u32 %16, %16, %15\n\t" \
      "v_cmp_eq_u32 vcc, -1, %16\n\ts_cbranch_vccz 2f\n\ts_sub_u32 %17, %17, 1\n\ts_cbranch_scc1 2f\n\ts_sleep 2\n\t" L16(o0, o1) \
      "s_waitcnt vmcnt(0)\n\ts_branch 1b\n2:" \
      : "+v"(r[0]), "+v"(r[1]), "+v"(r[2]), "+v"(r[3]), "+v"(r[4]), "+v"(r[5]), "+v"(r[6]), "+v"(r[7]), \
        "+v"(r[8]), "+v"(r[9]), "+v"(r[10]), "+v"(r[11]), "+v"(r[12]), "+v"(r[13]), "+v"(r[14]), "+v"(r[15]), "=&v"(t_), "=&s"(c_) \
      : "v"(voff), "s"(base), "i"(n) : "vcc", "scc", "memory"); }

#define IDS int tid = threadIdx.x; uint32_t bx = blockIdx.x, wj = wu & 3; char* ws = ar.ws; asm volatile("" : "+v"(tid)); asm volatile("" : "+s"(bx), "+s"(wj), "+s"(ws)); \
  const int lane = tid & 63, g = lane >> 4, r16 = lane & 15, n = lane & 31, hh = lane >> 5; \
  const int xcd = bx & 7, bi = bx >> 3; \
  const int rb = RB_, cg = CG_;     \
  const int rt = xcd * 8 + (bi & 7), cb = bi >> 3;                              \
  const int p = xcd * 32 + bi;                                                  \
  const int rot = ROT;                             \
  const uint32_t fl4 = sb + L_FL + (lane & 3) * 4, flw = sb + L_FL + wj * 4; \
  const uint32_t pw32 = sb + (8 * hh + (r16 >> 2)) * 64 + (((g & 1) ^ hh) * 32) + (lane & 3) * 8;     \
  (void)g; (void)r16; (void)n; (void)hh; (void)rb; (void)cg; (void)rt; (void)cb; (void)p; (void)rot; (void)fl4; (void)flw; (void)pw32;
#define LSTA \
    "s_load_dwordx16 s[64:79], %4, 0\n\ts_load_dwordx4 s[80:83], %4, 0x40\n\ts_load_dwordx2 s[48:49], %4, 0x60\n\ts_cmp_eq_u32 %6, 4\n\ts_cbranch_scc1 .Lls_%=\n\t" \
    "s_and_b32 s50, %5, 7\n\ts_lshr_b32 s51, %5, 3\n\ts_lshl_b32 s50, s50, 5\n\tv_lshlrev_b32 v67, 1, %7\n\ts_add_u32 s50, s50, s51\n\tv_and_b32 v64, 0x1fc, v67\n\t" \
    "s_lshl_b32 s54, s50, 9\n\ts_lshl_b32 s52, s50, 2\n\tv_or_b32 v64, s54, v64\n\tv_mov_b32 v66, s52\n\ts_waitcnt lgkmcnt(0)\n\tglobal_load_dword v160, v64, s[70:71]\n\t" \
    "global_load_dword v161, v64, s[72:73]\n\tglobal_load_dword v162, v64, s[78:79]\n\tglobal_load_dword v163, v64, s[80:81]\n\tglobal_load_dword v164, v64, s[74:75]\n\t" \
    "global_load_dword v165, v64, s[76:77]\n\tglobal_load_dword v167, v66, s[82:83]\n\ts_branch .Lle_%=\n\t.Lls_%=:\ns_lshr_b32 s51, %5, 5\n\ts_bfe_u32 s60, %5, 0x20003\n\t" \
    "v_bfe_u32 v64, %7, 2, 4\n\ts_lshl_b32 s50, s60, 1\n\tv_lshrrev_b32 v65, 4, %7\n\ts_add_u32 s50, s50, s51\n\tv_mul_u32_u24 v64, 0xc00, v64\n\ts_and_b32 s50, s50, 7\n\t" \
    "v_and_b32 v66, 2, v65\n\ts_and_b32 s61, %5, 1\n\tv_xor_b32 v66, v66, %7\n\ts_lshl_b32 s61, s61, 3\n\tv_and_b32 v66, 3, v66\n\ts_add_u32 s51, s51, s61\n\t" \
    "v_lshl_add_u32 v64, v66, 4, v64\n\ts_lshl_b32 s51, s51, 6\n\ts_mul_i32 s59, s50, 196608\n\ts_mul_i32 s56, s50, 12288\n\tv_add_u32 v64, s51, v64\n\ts_waitcnt lgkmcnt(0)\n\t" \
    "s_add_u32 s52, s66, s59\n\ts_addc_u32 s53, s67, 0\n\ts_mov_b32 m0, s56\n\ts_nop 0\n\tglobal_load_lds_dwordx4 v64, s[52:53] offset:0\n\t" \
    "global_load_lds_dwordx4 v64, s[52:53] offset:1024\n\tglobal_load_lds_dwordx4 v64, s[52:53] offset:2048\n\ts_add_u32 s56, s56, 3072\n\ts_mov_b32 m0, s56\n\t" \
    "s_add_u32 s52, s52, 49152\n\ts_addc_u32 s53, s53, 0\n\tglobal_load_lds_dwordx4 v64, s[52:53] offset:0\n\tglobal_load_lds_dwordx4 v64, s[52:53] offset:1024\n\t" \
    "global_load_lds_dwordx4 v64, s[52:53] offset:2048\n\ts_add_u32 s56, s56, 3072\n\ts_mov_b32 m0, s56\n\ts_add_u32 s52, s52, 49152\n\ts_addc_u32 s53, s53, 0\n\t" \
    "global_load_lds_dwordx4 v64, s[52:53] offset:0\n\tglobal_load_lds_dwordx4 v64, s[52:53] offset:1024\n\tglobal_load_lds_dwordx4 v64, s[52:53] offset:2048\n\t" \
    "s_add_u32 s56, s56, 3072\n\ts_mov_b32 m0, s56\n\ts_add_u32 s52, s52, 49152\n\ts_addc_u32 s53, s53, 0\n\tglobal_load_lds_dwordx4 v64, s[52:53] offset:0\n\t" \
    "global_load_lds_dwordx4 v64, s[52:53] offset:1024\n\tglobal_load_lds_dwordx4 v64, s[52:53] offset:2048\n\ts_add_u32 s56, s56, 3072\n\ts_bfe_u32 s61, %5, 0x20001\n\t" \
    "v_and_b32 v65, 3, v65\n\ts_lshl_b32 s61, s61, 2\n\tv_xor_b32 v65, v65, %7\n\ts_add_u32 s61, s61, s60\n\tv_and_b32 v65, 7, v65\n\ts_lshl_b32 s61, s61, 17\n\t" \
    "v_bfe_u32 v66, %7, 3, 3\n\ts_sub_u32 s62, s61, 16384\n\tv_lshlrev_b32 v66, 10, v66\n\ts_max_i32 s62, s62, 0\n\tv_lshl_add_u32 v65, v65, 4, v66\n\ts_add_u32 s62, s64, s62\n\t" \
    "s_addc_u32 s63, s65, 0\n\ts_add_u32 s64, s64, s61\n\ts_addc_u32 s65, s65, 0\n\tv_xor_b32 v66, 64, v65\n\tv_add_u32 v67, 14336, v65\n\tv_add_u32 v68, 21504, v66\n\t" \
    "v_add_u32 v66, 7168, v66\n\ts_mov_b32 s57, 98304\n\ts_lshl_b32 s59, s50, 7\n\ts_add_u32 s54, s64, s59\n\ts_addc_u32 s55, s65, 0\n\ts_add_u32 m0, s57, 0\n\ts_nop 0\n\t" \
    "global_load_lds_dwordx4 v65, s[54:55] offset:0\n\tglobal_load_lds_dwordx4 v66, s[54:55] offset:1024\n\tglobal_load_lds_dwordx4 v67, s[54:55] offset:2048\n\t" \
    "global_load_lds_dwordx4 v68, s[54:55] offset:3072\n\ts_add_u32 m0, s57, 12288\n\ts_add_u32 s54, s54, 32768\n\ts_addc_u32 s55, s55, 0\n\t" \
    "global_load_lds_dwordx4 v65, s[54:55] offset:0\n\tglobal_load_lds_dwordx4 v66, s[54:55] offset:1024\n\tglobal_load_lds_dwordx4 v67, s[54:55] offset:2048\n\t" \
    "global_load_lds_dwordx4 v68, s[54:55] offset:3072\n\ts_add_u32 m0, s57, 24576\n\ts_add_u32 s54, s54, 32768\n\ts_addc_u32 s55, s55, 0\n\t" \
    "global_load_lds_dwordx4 v65, s[54:55] offset:0\n\tglobal_load_lds_dwordx4 v66, s[54:55] offset:1024\n\tglobal_load_lds_dwordx4 v67, s[54:55] offset:2048\n\t" \
    "global_load_lds_dwordx4 v68, s[54:55] offset:3072\n\ts_add_u32 m0, s57, 36864\n\ts_add_u32 s54, s54, 32768\n\ts_addc_u32 s55, s55, 0\n\t" \
    "global_load_lds_dwordx4 v65, s[54:55] offset:0\n\tglobal_load_lds_dwordx4 v66, s[54:55] offset:1024\n\tglobal_load_lds_dwordx4 v67, s[54:55] offset:2048\n\t" \
    "global_load_lds_dwordx4 v68, s[54:55] offset:3072\n\ts_add_u32 s50, s50, 1\n\ts_and_b32 s50, s50, 7\n\tv_and_b32 v71, 63, %7\n\tv_lshlrev_b32 v71, 4, v71\n\t.Lle_%=:\n"
#define LSTA_CLOB "v160", "v161", "v162", "v163", "v164", "v165", "v166", "v167", "s68", "s69", "s70", "s71", "s72", "s73", "s74", "s75", "s76", "s77", "s78", "s79", "s80", "s81", "s82", "s83", "s86", "s87", "scc", "memory"
#define LSTB \
    "s_mov_b32 %5, 2\n\ts_cmp_lg_u32 %6, 0\n\ts_cbranch_scc1 .Llx_%=\n\tv_mov_b32 v69, 163712\n\tv_mov_b32 v72, 1\n\tds_write_b32 v69, v72 offset:32\n\t" \
    "ds_write_b32 v69, v72 offset:112\n\ts_mov_b32 s58, 2\n\t.Lll_%=:\nds_read_b128 v[74:77], v69 offset:96\n\ts_mul_i32 s59, s50, 196608\n\ts_mul_i32 s56, s50, 12288\n\t" \
    "s_add_u32 s52, s66, s59\n\ts_addc_u32 s53, s67, 0\n\ts_mov_b32 m0, s56\n\ts_nop 0\n\tglobal_load_lds_dwordx4 v64, s[52:53] offset:0\n\t" \
    "global_load_lds_dwordx4 v64, s[52:53] offset:1024\n\tglobal_load_lds_dwordx4 v64, s[52:53] offset:2048\n\ts_add_u32 s56, s56, 3072\n\ts_mov_b32 m0, s56\n\t" \
    "s_add_u32 s52, s52, 49152\n\ts_addc_u32 s53, s53, 0\n\tglobal_load_lds_dwordx4 v64, s[52:53] offset:0\n\tglobal_load_lds_dwordx4 v64, s[52:53] offset:1024\n\t" \
    "global_load_lds_dwordx4 v64, s[52:53] offset:2048\n\ts_add_u32 s56, s56, 3072\n\ts_mov_b32 m0, s56\n\ts_add_u32 s52, s52, 49152\n\ts_addc_u32 s53, s53, 0\n\t" \
    "global_load_lds_dwordx4 v64, s[52:53] offset:0\n\tglobal_load_lds_dwordx4 v64, s[52:53] offset:1024\n\tglobal_load_lds_dwordx4 v64, s[52:53] offset:2048\n\t" \
    "s_add_u32 s56, s56, 3072\n\ts_add_u32 s59, s58, -1\n\ts_cmp_lg_u32 s58, 4\n\ts_cbranch_scc1 4f\n\ts_mov_b32 s59, s58\n\ts_waitcnt vmcnt(23)\n\t4:\n" \
    "v_mov_b32 v70, s59\n\ts_mov_b64 exec, 1\n\ts_waitcnt vmcnt(25)\n\tds_max_i32 v69, v70\n\ts_mov_b64 exec, -1\n\ts_mov_b32 m0, s56\n\ts_add_u32 s52, s52, 49152\n\t" \
    "s_addc_u32 s53, s53, 0\n\tglobal_load_lds_dwordx4 v64, s[52:53] offset:0\n\tglobal_load_lds_dwordx4 v64, s[52:53] offset:1024\n\t" \
    "global_load_lds_dwordx4 v64, s[52:53] offset:2048\n\ts_add_u32 s56, s56, 3072\n\ts_lshl_b32 s59, s50, 7\n\ts_add_u32 s54, s64, s59\n\ts_addc_u32 s55, s65, 0\n\t" \
    "s_add_u32 s57, s57, 4096\n\ts_cmp_eq_u32 s57, 110592\n\ts_cselect_b32 s57, 98304, s57\n\ts_sub_u32 s59, s58, 4\n\ts_mov_b32 s60, 0x100000\n\ts_waitcnt lgkmcnt(0)\n\t1:\n" \
    "v_min3_i32 v74, v74, v75, v76\n\tv_min_i32 v74, v74, v77\n\tv_cmp_gt_i32 vcc, s59, v74\n\ts_cbranch_vccz 2f\n\ts_sub_u32 s60, s60, 1\n\ts_cbranch_scc1 2f\n\t" \
    "v_mov_b32 v70, s58\n\ts_mov_b64 exec, 1\n\ts_waitcnt vmcnt(12)\n\tds_max_i32 v69, v70\n\ts_mov_b64 exec, -1\n\ts_sleep 1\n\tds_read_b128 v[74:77], v69 offset:96\n\t" \
    "s_waitcnt lgkmcnt(0)\n\ts_branch 1b\n\t2:\ns_add_u32 m0, s57, 0\n\ts_nop 0\n\tglobal_load_lds_dwordx4 v65, s[54:55] offset:0\n\t" \
    "global_load_lds_dwordx4 v66, s[54:55] offset:1024\n\tglobal_load_lds_dwordx4 v67, s[54:55] offset:2048\n\tglobal_load_lds_dwordx4 v68, s[54:55] offset:3072\n\t" \
    "s_add_u32 m0, s57, 12288\n\ts_add_u32 s54, s54, 32768\n\ts_addc_u32 s55, s55, 0\n\tglobal_load_lds_dwordx4 v65, s[54:55] offset:0\n\t" \
    "global_load_lds_dwordx4 v66, s[54:55] offset:1024\n\tglobal_load_lds_dwordx4 v67, s[54:55] offset:2048\n\tglobal_load_lds_dwordx4 v68, s[54:55] offset:3072\n\t" \
    "s_add_u32 m0, s57, 24576\n\ts_add_u32 s54, s54, 32768\n\ts_addc_u32 s55, s55, 0\n\tglobal_load_lds_dwordx4 v65, s[54:55] offset:0\n\t" \
    "global_load_lds_dwordx4 v66, s[54:55] offset:1024\n\tglobal_load_lds_dwordx4 v67, s[54:55] offset:2048\n\tglobal_load_lds_dwordx4 v68, s[54:55] offset:3072\n\t" \
    "s_add_u32 s59, s58, 0\n\tv_mov_b32 v70, s59\n\ts_mov_b64 exec, 1\n\ts_waitcnt vmcnt(24)\n\tds_max_i32 v69, v70\n\ts_mov_b64 exec, -1\n\ts_add_u32 m0, s57, 36864\n\t" \
    "s_add_u32 s54, s54, 32768\n\ts_addc_u32 s55, s55, 0\n\tglobal_load_lds_dwordx4 v65, s[54:55] offset:0\n\tglobal_load_lds_dwordx4 v66, s[54:55] offset:1024\n\t" \
    "global_load_lds_dwordx4 v67, s[54:55] offset:2048\n\tglobal_load_lds_dwordx4 v68, s[54:55] offset:3072\n\ts_cmp_lg_u32 s58, 2\n\ts_cbranch_scc1 3f\n\ts_mov_b32 m0, 146432\n\t" \
    "v_xor_b32 v72, 32, v71\n\tglobal_load_lds_dwordx4 v72, s[62:63] offset:2048\n\tv_xor_b32 v73, 48, v71\n\tglobal_load_lds_dwordx4 v73, s[62:63] offset:3072\n\t" \
    "s_mov_b32 m0, 150528\n\tv_xor_b32 v72, 4160, v71\n\tglobal_load_lds_dwordx4 v72, s[62:63] offset:0\n\tv_xor_b32 v73, 4176, v71\n\t" \
    "global_load_lds_dwordx4 v73, s[62:63] offset:1024\n\tv_xor_b32 v72, 4192, v71\n\tglobal_load_lds_dwordx4 v72, s[62:63] offset:2048\n\t" \
    "v_xor_b32 v73, 4208, v71\n\tglobal_load_lds_dwordx4 v73, s[62:63] offset:3072\n\ts_mov_b32 m0, 154624\n\tv_xor_b32 v72, 8320, v71\n\t" \
    "global_load_lds_dwordx4 v72, s[62:63] offset:0\n\tv_xor_b32 v73, 8336, v71\n\tglobal_load_lds_dwordx4 v73, s[62:63] offset:1024\n\ts_add_u32 s59, s58, 1\n\t" \
    "v_mov_b32 v70, s59\n\ts_mov_b64 exec, 1\n\ts_waitcnt vmcnt(24)\n\tds_max_i32 v69, v70\n\ts_mov_b64 exec, -1\n\tv_xor_b32 v72, 8352, v71\n\t" \
    "global_load_lds_dwordx4 v72, s[62:63] offset:2048\n\tv_xor_b32 v73, 8368, v71\n\tglobal_load_lds_dwordx4 v73, s[62:63] offset:3072\n\ts_mov_b32 m0, 158720\n\t" \
    "v_xor_b32 v72, 12480, v71\n\tglobal_load_lds_dwordx4 v72, s[62:63] offset:0\n\tv_xor_b32 v73, 12496, v71\n\tglobal_load_lds_dwordx4 v73, s[62:63] offset:1024\n\t" \
    "v_xor_b32 v72, 12512, v71\n\tglobal_load_lds_dwordx4 v72, s[62:63] offset:2048\n\tv_xor_b32 v73, 12528, v71\n\t" \
    "global_load_lds_dwordx4 v73, s[62:63] offset:3072\n\t3:\ns_add_u32 s50, s50, 1\n\ts_and_b32 s50, s50, 7\n\ts_add_u32 s58, s58, 2\n\ts_cmp_lt_u32 s58, 16\n\t" \
    "s_cbranch_scc1 .Lll_%=\n\tv_mov_b32 v72, 16777216\n\tv_lshlrev_b32 v73, 1, v71\n\tv_and_b32 v73, 0x780, v73\n\tv_and_b32 v74, 48, v71\n\tv_add3_u32 v73, v73, v74, v72\n\t" \
    "global_load_dword v78, v72, s[48:49] sc1\n\tv_add_u32 v73, 0x2000, v73\n\tglobal_load_dwordx4 v[252:255], v73, s[48:49] sc1\n\ts_lshl_b32 s51, s51, 5\n\t" \
    "s_add_u32 s61, s61, s51\n\tv_mov_b32 v74, -1\n\tv_mov_b32 v75, -1\n\tv_mov_b32 v76, -1\n\tv_mov_b32 v77, -1\n\tv_add_u32 v72, s61, v71\n\ts_add_u32 s59, s58, -1\n\t" \
    "v_mov_b32 v70, s59\n\ts_mov_b64 exec, 1\n\ts_waitcnt vmcnt(18)\n\tds_max_i32 v69, v70\n\ts_mov_b64 exec, -1\n\tglobal_store_dwordx4 v72, v[74:77], s[48:49] offset:0 sc1\n\t" \
    "global_store_dwordx4 v72, v[74:77], s[48:49] offset:1024 sc1\n\tv_add_u32 v72, 0x8000, v72\n\tglobal_store_dwordx4 v72, v[74:77], s[48:49] offset:0 sc1\n\t" \
    "global_store_dwordx4 v72, v[74:77], s[48:49] offset:1024 sc1\n\tv_add_u32 v72, 0x8000, v72\n\tglobal_store_dwordx4 v72, v[74:77], s[48:49] offset:0 sc1\n\t" \
    "global_store_dwordx4 v72, v[74:77], s[48:49] offset:1024 sc1\n\tv_add_u32 v72, 0x8000, v72\n\tglobal_store_dwordx4 v72, v[74:77], s[48:49] offset:0 sc1\n\t" \
    "global_store_dwordx4 v72, v[74:77], s[48:49] offset:1024 sc1\n\tv_add_u32 v72, 0x8000, v72\n\ts_add_u32 s59, s58, 0\n\tv_mov_b32 v70, s59\n\ts_mov_b64 exec, 1\n\t" \
    "s_waitcnt vmcnt(10)\n\tds_max_i32 v69, v70\n\ts_mov_b64 exec, -1\n\ts_waitcnt vmcnt(8)\n\tv_readfirstlane_b32 %4, v78\n\tv_mov_b32 v70, 17\n\ts_mul_i32 %4, %4, 0x9E3779B1\n\t" \
    "s_add_u32 %4, %4, 0x7F4A7C15\n\tv_xor_b32 v252, %4, v252\n\tv_xor_b32 v253, %4, v253\n\tv_xor_b32 v254, %4, v254\n\tv_xor_b32 v255, %4, v255\n\t" \
    "v_or3_b32 v78, v252, v253, v254\n\tv_or_b32 v78, v78, v255\n\tv_cmp_ne_u32 vcc, 0, v78\n\ts_cmp_eq_u64 vcc, 0\n\ts_cselect_b32 %5, 1, 0\n\tv_mov_b32 v80, %5\n\t" \
    "v_mov_b32 v81, %5\n\ts_mov_b64 exec, 1\n\tds_write2_b64 v69, v[80:81], v[80:81] offset0:6 offset1:7\n\tds_write_b32 v69, v70\n\ts_mov_b64 exec, -1\n\ts_lshl_b32 %5, %5, 1\n\t" \
    ".Llx_%=:\n"
#define LSTB_CLOB "v80", "v81", "s86", "s87", "vcc", "scc", "memory"
#define GEMM1 \
    "s_setprio 0\n\ts_mov_b32 s81, 0x3b000000\n\ts_mov_b32 s84, -1\n\ts_mov_b32 s85, 0\n\ts_mov_b32 s46, 0\n\ts_mov_b32 s45, 2\n\ts_mov_b32 s47, %4\n\ts_mul_i32 s44, %4, 12288\n\t" \
    "v_mov_b32 v245, 0x3f803f80\n\tv_mov_b32 v243, 0\n\tv_mov_b32 v246, 0\n\tv_add_u32 v232, s46, %7\n\tv_xor_b32 v233, 32, v232\n\tv_xor_b32 v234, 64, v232\n\t" \
    "v_xor_b32 v235, 0x60, v232\n\tv_add_u32 v242, s44, %1\n\tv_mov_b64 v[64:65], 0\n\tv_mov_b64 v[66:67], 0\n\tv_mov_b64 v[68:69], 0\n\tv_mov_b64 v[70:71], 0\n\t" \
    "v_mov_b64 v[72:73], 0\n\tv_mov_b64 v[74:75], 0\n\tv_mov_b64 v[76:77], 0\n\tv_mov_b64 v[78:79], 0\n\tv_mov_b64 v[80:81], 0\n\tv_mov_b64 v[82:83], 0\n\tv_mov_b64 v[84:85], 0\n\t" \
    "v_mov_b64 v[86:87], 0\n\tv_mov_b64 v[88:89], 0\n\tv_mov_b64 v[90:91], 0\n\tv_mov_b64 v[92:93], 0\n\tv_mov_b64 v[94:95], 0\n\tv_mov_b64 v[96:97], 0\n\tv_mov_b64 v[98:99], 0\n\t" \
    "v_mov_b64 v[100:101], 0\n\tv_mov_b64 v[102:103], 0\n\tv_mov_b64 v[104:105], 0\n\tv_mov_b64 v[106:107], 0\n\tv_mov_b64 v[108:109], 0\n\tv_mov_b64 v[110:111], 0\n\t" \
    "v_mov_b64 v[112:113], 0\n\tv_mov_b64 v[114:115], 0\n\tv_mov_b64 v[116:117], 0\n\tv_mov_b64 v[118:119], 0\n\tv_mov_b64 v[120:121], 0\n\tv_mov_b64 v[122:123], 0\n\t" \
    "v_mov_b64 v[124:125], 0\n\tv_mov_b64 v[126:127], 0\n\tv_mov_b64 v[128:129], 0\n\tv_mov_b64 v[130:131], 0\n\tv_mov_b64 v[132:133], 0\n\tv_mov_b64 v[134:135], 0\n\t" \
    "v_mov_b32 v244, 1\n\ts_mov_b64 exec, 1\n\ts_waitcnt vmcnt(0)\n\tds_write_b32 %3, v244\n\ts_mov_b64 exec, -1\n\ts_cmp_lg_u32 %14, 3\n\ts_cbranch_scc1 .Lgf_%=\n\t" \
    "v_cmp_gt_i32 vcc, 1, v243\n\ts_cbranch_vccz 2f\n\ts_mov_b32 s86, 0x100000\n\t1:\nds_read_b32 v243, %13\n\ts_waitcnt lgkmcnt(0)\n\tv_cmp_gt_i32 vcc, 1, v243\n\t" \
    "s_sub_u32 s86, s86, 1\n\ts_cbranch_scc1 2f\n\ts_cbranch_vccnz 1b\n\t2:\ns_mov_b64 exec, 1\n\tglobal_store_dword %10, %12, %11 sc1\n\ts_mov_b64 exec, -1\n\t" \
    "v_mov_b32 v243, 0\n\t.Lgf_%=:\nv_mov_b32 v244, 2\n\ts_mov_b32 s45, 3\n\tv_cmp_gt_i32 vcc, 1, v243\n\ts_cbranch_vccz 2f\n\ts_mov_b32 s86, 0x100000\n\t1:\ns_sleep 2\n\t" \
    "ds_read_b32 v243, %2\n\ts_waitcnt lgkmcnt(0)\n\tv_cmp_gt_i32 vcc, 1, v243\n\ts_sub_u32 s86, s86, 1\n\ts_cbranch_scc1 2f\n\ts_cbranch_vccnz 1b\n\t2:\n" \
    "ds_read_b64_tr_b16 v[208:209], v242 offset:0\n\tds_read_b64_tr_b16 v[210:211], v242 offset:256\n\tds_read_b64_tr_b16 v[212:213], v242 offset:3072\n\t" \
    "ds_read_b64_tr_b16 v[214:215], v242 offset:3328\n\tv_cmp_gt_i32 vcc, 2, v243\n\ts_cbranch_vccz 2f\n\ts_mov_b32 s86, 0x100000\n\t1:\ns_sleep 1\n\tds_read_b32 v243, %2\n\t" \
    "s_waitcnt lgkmcnt(0)\n\tv_cmp_gt_i32 vcc, 2, v243\n\ts_sub_u32 s86, s86, 1\n\ts_cbranch_scc1 2f\n\ts_cbranch_vccnz 1b\n\t2:\nds_read_b128 v[144:147], v232\n\t" \
    "ds_read_b64_tr_b16 v[216:217], v242 offset:6144\n\tds_read_b64_tr_b16 v[218:219], v242 offset:6400\n\tds_read_b128 v[148:151], v233\n\t" \
    "ds_read_b64_tr_b16 v[220:221], v242 offset:9216\n\tds_read_b64_tr_b16 v[222:223], v242 offset:9472\n\tds_read_b128 v[152:155], v234\n\t" \
    "ds_read_b64_tr_b16 v[224:225], v242 offset:1024\n\tds_read_b64_tr_b16 v[226:227], v242 offset:1280\n\tds_read_b128 v[156:159], v235\n\t" \
    "ds_read_b64_tr_b16 v[228:229], v242 offset:4096\n\tds_read_b64_tr_b16 v[230:231], v242 offset:4352\n\t.Lgl_%=:\ns_waitcnt lgkmcnt(11)\n\tv_dot2c_f32_bf16 v112, v245, v144\n\t" \
    "v_dot2c_f32_bf16 v113, v144, v144\n\tv_dot2c_f32_bf16 v114, v245, v145\n\tv_dot2c_f32_bf16 v115, v145, v145\n\tv_dot2c_f32_bf16 v116, v245, v146\n\t" \
    "v_dot2c_f32_bf16 v117, v146, v146\n\tv_dot2c_f32_bf16 v118, v245, v147\n\tv_dot2c_f32_bf16 v119, v147, v147\n\ts_waitcnt lgkmcnt(8)\n\tv_dot2c_f32_bf16 v112, v245, v148\n\t" \
    "v_dot2c_f32_bf16 v113, v148, v148\n\tv_dot2c_f32_bf16 v114, v245, v149\n\tv_dot2c_f32_bf16 v115, v149, v149\n\tv_dot2c_f32_bf16 v116, v245, v150\n\t" \
    "v_dot2c_f32_bf16 v117, v150, v150\n\tv_dot2c_f32_bf16 v118, v245, v151\n\tv_dot2c_f32_bf16 v119, v151, v151\n\ts_waitcnt lgkmcnt(5)\n\tv_dot2c_f32_bf16 v112, v245, v152\n\t" \
    "v_dot2c_f32_bf16 v113, v152, v152\n\tv_dot2c_f32_bf16 v114, v245, v153\n\tv_dot2c_f32_bf16 v115, v153, v153\n\tv_dot2c_f32_bf16 v116, v245, v154\n\t" \
    "v_dot2c_f32_bf16 v117, v154, v154\n\tv_dot2c_f32_bf16 v118, v245, v155\n\tv_dot2c_f32_bf16 v119, v155, v155\n\ts_waitcnt lgkmcnt(2)\n\tv_dot2c_f32_bf16 v112, v245, v156\n\t" \
    "v_dot2c_f32_bf16 v113, v156, v156\n\tv_dot2c_f32_bf16 v114, v245, v157\n\tv_dot2c_f32_bf16 v115, v157, v157\n\tv_dot2c_f32_bf16 v116, v245, v158\n\t" \
    "v_dot2c_f32_bf16 v117, v158, v158\n\tv_dot2c_f32_bf16 v118, v245, v159\n\tv_dot2c_f32_bf16 v119, v159, v159\n\t" \
    "v_mfma_f32_32x32x16_bf16 v[64:79], v[208:211], v[144:147], v[64:79]\n\tds_read_b64_tr_b16 v[208:209], v242 offset:7168\n\tds_read_b64_tr_b16 v[210:211], v242 offset:7424\n\t" \
    "s_mov_b64 exec, 1\n\tds_write_b32 %3, v244\n\ts_mov_b64 exec, -1\n\tv_mfma_f32_32x32x16_bf16 v[64:79], v[212:215], v[148:151], v[64:79]\n\t" \
    "ds_read_b64_tr_b16 v[212:213], v242 offset:10240\n\tds_read_b64_tr_b16 v[214:215], v242 offset:10496\n\tds_read_b32 v243, %2\n\ts_add_u32 s47, s47, 1\n\t" \
    "s_add_u32 s46, s46, 4096\n\tv_mfma_f32_32x32x16_bf16 v[64:79], v[216:219], v[152:155], v[64:79]\n\tds_read_b64_tr_b16 v[216:217], v242 offset:2048\n\t" \
    "ds_read_b64_tr_b16 v[218:219], v242 offset:2304\n\ts_and_b32 s47, s47, 7\n\ts_cmp_eq_u32 s46, 12288\n\ts_cselect_b32 s46, 0, s46\n\t" \
    "v_mfma_f32_32x32x16_bf16 v[64:79], v[220:223], v[156:159], v[64:79]\n\tds_read_b64_tr_b16 v[220:221], v242 offset:5120\n\tds_read_b64_tr_b16 v[222:223], v242 offset:5376\n\t" \
    "s_mul_i32 s44, s47, 12288\n\tv_add_u32 v232, s46, %7\n\tv_xor_b32 v233, 32, v232\n\tv_mfma_f32_32x32x16_bf16 v[80:95], v[224:227], v[144:147], v[80:95]\n\t" \
    "ds_read_b64_tr_b16 v[224:225], v242 offset:8192\n\tds_read_b64_tr_b16 v[226:227], v242 offset:8448\n\ts_waitcnt lgkmcnt(6)\n\tv_readfirstlane_b32 s49, v243\n\t" \
    "ds_read_b32 v243, %2\n\tv_mfma_f32_32x32x16_bf16 v[80:95], v[228:231], v[148:151], v[80:95]\n\tds_read_b64_tr_b16 v[228:229], v242 offset:11264\n\t" \
    "ds_read_b64_tr_b16 v[230:231], v242 offset:11520\n\tv_add_u32 v242, s44, %1\n\ts_cmp_lt_i32 s49, s45\n\ts_cbranch_scc1 .Lgs0_%=\n\t.Lgr0_%=:\n" \
    "v_mfma_f32_32x32x16_bf16 v[80:95], v[208:211], v[152:155], v[80:95]\n\tds_read_b64_tr_b16 v[208:209], v242 offset:0\n\tds_read_b64_tr_b16 v[210:211], v242 offset:256\n\t" \
    "s_add_u32 s45, s45, 1\n\tv_xor_b32 v234, 64, v232\n\tv_xor_b32 v235, 0x60, v232\n\tv_mfma_f32_32x32x16_bf16 v[80:95], v[212:215], v[156:159], v[80:95]\n\t" \
    "ds_read_b64_tr_b16 v[212:213], v242 offset:3072\n\tds_read_b64_tr_b16 v[214:215], v242 offset:3328\n\ts_waitcnt lgkmcnt(6)\n\tv_readfirstlane_b32 s49, v243\n\t" \
    "v_add_u32 v244, 2, v244\n\tv_mfma_f32_32x32x16_bf16 v[96:111], v[216:219], v[144:147], v[96:111]\n\ts_cmp_lt_i32 s49, s45\n\ts_cbranch_scc1 .Lgs1_%=\n\t.Lgr1_%=:\n" \
    "ds_read_b128 v[144:147], v232\n\tds_read_b64_tr_b16 v[216:217], v242 offset:6144\n\tds_read_b64_tr_b16 v[218:219], v242 offset:6400\n\t" \
    "v_mfma_f32_32x32x16_bf16 v[96:111], v[220:223], v[148:151], v[96:111]\n\tds_read_b128 v[148:151], v233\n\tds_read_b64_tr_b16 v[220:221], v242 offset:9216\n\t" \
    "ds_read_b64_tr_b16 v[222:223], v242 offset:9472\n\ts_add_u32 s45, s45, 1\n\tv_mfma_f32_32x32x16_bf16 v[96:111], v[224:227], v[152:155], v[96:111]\n\t" \
    "ds_read_b128 v[152:155], v234\n\tds_read_b64_tr_b16 v[224:225], v242 offset:1024\n\tds_read_b64_tr_b16 v[226:227], v242 offset:1280\n\ts_cmp_lt_u32 s45, 17\n\t" \
    "s_waitcnt lgkmcnt(13)\n\tv_mfma_f32_32x32x16_bf16 v[96:111], v[228:231], v[156:159], v[96:111]\n\tds_read_b128 v[156:159], v235\n\t" \
    "ds_read_b64_tr_b16 v[228:229], v242 offset:4096\n\tds_read_b64_tr_b16 v[230:231], v242 offset:4352\n\ts_cbranch_scc1 .Lgl_%=\n\ts_waitcnt lgkmcnt(11)\n\t" \
    "v_dot2c_f32_bf16 v112, v245, v144\n\tv_dot2c_f32_bf16 v113, v144, v144\n\tv_dot2c_f32_bf16 v114, v245, v145\n\tv_dot2c_f32_bf16 v115, v145, v145\n\t" \
    "v_dot2c_f32_bf16 v116, v245, v146\n\tv_dot2c_f32_bf16 v117, v146, v146\n\tv_dot2c_f32_bf16 v118, v245, v147\n\tv_dot2c_f32_bf16 v119, v147, v147\n\ts_waitcnt lgkmcnt(8)\n\t" \
    "v_dot2c_f32_bf16 v112, v245, v148\n\tv_dot2c_f32_bf16 v113, v148, v148\n\tv_dot2c_f32_bf16 v114, v245, v149\n\tv_dot2c_f32_bf16 v115, v149, v149\n\t" \
    "v_dot2c_f32_bf16 v116, v245, v150\n\tv_dot2c_f32_bf16 v117, v150, v150\n\tv_dot2c_f32_bf16 v118, v245, v151\n\tv_dot2c_f32_bf16 v119, v151, v151\n\ts_waitcnt lgkmcnt(5)\n\t" \
    "v_dot2c_f32_bf16 v112, v245, v152\n\tv_dot2c_f32_bf16 v113, v152, v152\n\tv_dot2c_f32_bf16 v114, v245, v153\n\tv_dot2c_f32_bf16 v115, v153, v153\n\t" \
    "v_dot2c_f32_bf16 v116, v245, v154\n\tv_dot2c_f32_bf16 v117, v154, v154\n\tv_dot2c_f32_bf16 v118, v245, v155\n\tv_dot2c_f32_bf16 v119, v155, v155\n\ts_waitcnt lgkmcnt(2)\n\t" \
    "v_dot2c_f32_bf16 v112, v245, v156\n\tv_dot2c_f32_bf16 v113, v156, v156\n\tv_dot2c_f32_bf16 v114, v245, v157\n\tv_dot2c_f32_bf16 v115, v157, v157\n\t" \
    "v_dot2c_f32_bf16 v116, v245, v158\n\tv_dot2c_f32_bf16 v117, v158, v158\n\tv_dot2c_f32_bf16 v118, v245, v159\n\tv_dot2c_f32_bf16 v119, v159, v159\n\ts_mov_b64 exec, 1\n\t" \
    "ds_write_b32 %3, v244\n\ts_mov_b64 exec, -1\n\tv_mfma_f32_32x32x16_bf16 v[64:79], v[208:211], v[144:147], v[64:79]\n\tds_read_b64_tr_b16 v[208:209], v242 offset:7168\n\t" \
    "ds_read_b64_tr_b16 v[210:211], v242 offset:7424\n\tv_add_f32 v112, v112, v114\n\tv_add_f32 v113, v113, v115\n\tv_add_f32 v116, v116, v118\n\tv_add_f32 v117, v117, v119\n\t" \
    "v_mfma_f32_32x32x16_bf16 v[64:79], v[212:215], v[148:151], v[64:79]\n\tds_read_b64_tr_b16 v[212:213], v242 offset:10240\n\tds_read_b64_tr_b16 v[214:215], v242 offset:10496\n\t" \
    "v_add_f32 v112, v112, v116\n\tv_add_f32 v113, v113, v117\n\tv_mfma_f32_32x32x16_bf16 v[64:79], v[216:219], v[152:155], v[64:79]\n\t" \
    "ds_read_b64_tr_b16 v[216:217], v242 offset:2048\n\tds_read_b64_tr_b16 v[218:219], v242 offset:2304\n\tv_permlane32_swap_b32 v112, v113\n\tv_add_f32 v138, v112, v113\n\t" \
    "v_add_f32 v139, v112, v113\n\tv_mfma_f32_32x32x16_bf16 v[64:79], v[220:223], v[156:159], v[64:79]\n\tds_read_b64_tr_b16 v[220:221], v242 offset:5120\n\t" \
    "ds_read_b64_tr_b16 v[222:223], v242 offset:5376\n\tv_permlane32_swap_b32 v138, v139\n\tv_mul_f32 v138, s81, v138\n\tv_mul_f32 v112, v138, v138\n\tds_read_b32 v243, %8\n\t" \
    "v_mfma_f32_32x32x16_bf16 v[80:95], v[224:227], v[144:147], v[80:95]\n\tds_read_b64_tr_b16 v[224:225], v242 offset:8192\n\tds_read_b64_tr_b16 v[226:227], v242 offset:8448\n\t" \
    "v_fma_f32 v139, v139, s81, -v112\n\tv_max_f32 v139, 0, v139\n\tv_cvt_pk_bf16_f32 v139, 0, v139\n\ts_waitcnt lgkmcnt(2)\n\t" \
    "v_mfma_f32_32x32x16_bf16 v[80:95], v[228:231], v[148:151], v[80:95]\n\tds_read_b64_tr_b16 v[228:229], v242 offset:11264\n\tds_read_b64_tr_b16 v[230:231], v242 offset:11520\n\t" \
    "v_add_f32 v139, 0x3727c5ac, v139\n\tv_cvt_pk_bf16_f32 v139, 0, v139\n\tv_cmp_gt_i32 vcc, %9, v243\n\ts_cbranch_vccz 2f\n\ts_mov_b32 s86, 0x100000\n\t1:\ns_sleep 1\n\t" \
    "ds_read_b32 v243, %8\n\ts_waitcnt lgkmcnt(0)\n\tv_cmp_gt_i32 vcc, %9, v243\n\ts_sub_u32 s86, s86, 1\n\ts_cbranch_scc1 2f\n\ts_cbranch_vccnz 1b\n\t2:\n" \
    "ds_read_b32 v124, %6 offset:0\n\tds_read_b32 v128, %6 offset:128\n\tds_read_b32 v132, %6 offset:256\n\tv_mfma_f32_32x32x16_bf16 v[80:95], v[208:211], v[152:155], v[80:95]\n\t" \
    "v_sqrt_f32 v139, v139\n\tv_cvt_pk_bf16_f32 v138, -v138, -v138\n\ts_nop 0\n\tv_mfma_f32_32x32x16_bf16 v[80:95], v[212:215], v[156:159], v[80:95]\n\t" \
    "v_cndmask_b32 v120, 0, v138, s[84:85]\n\tv_cvt_pk_bf16_f32 v139, 0, v139\n\tv_rcp_f32 v136, v139\n\ts_waitcnt lgkmcnt(0)\n\tv_mov_b32 v137, v136\n\ts_nop 0\n\t" \
    "v_mfma_f32_32x32x16_bf16 v[64:79], v[124:127], v[120:123], v[64:79]\n\tv_mfma_f32_32x32x16_bf16 v[80:95], v[128:131], v[120:123], v[80:95]\n\t" \
    "v_mfma_f32_32x32x16_bf16 v[96:111], v[216:219], v[144:147], v[96:111]\n\ts_nop 9\n\tv_mul_f32 v64, v64, v136\n\tv_mul_f32 v65, v65, v136\n\tv_mul_f32 v66, v66, v136\n\t" \
    "v_mul_f32 v67, v67, v136\n\tv_cvt_pk_bf16_f32 v64, v64, v65\n\tv_cvt_pk_bf16_f32 v65, v66, v67\n\tds_write_b64 %5, v[64:65] offset:0\n\t" \
    "v_mfma_f32_32x32x16_bf16 v[96:111], v[220:223], v[148:151], v[96:111]\n\tv_mul_f32 v68, v68, v136\n\tv_mul_f32 v69, v69, v136\n\tv_mul_f32 v70, v70, v136\n\t" \
    "v_mul_f32 v71, v71, v136\n\tv_cvt_pk_bf16_f32 v68, v68, v69\n\tv_cvt_pk_bf16_f32 v69, v70, v71\n\tds_write_b64 %5, v[68:69] offset:16\n\t" \
    "v_mfma_f32_32x32x16_bf16 v[96:111], v[224:227], v[152:155], v[96:111]\n\tv_mul_f32 v72, v72, v136\n\tv_mul_f32 v73, v73, v136\n\tv_mul_f32 v74, v74, v136\n\t" \
    "v_mul_f32 v75, v75, v136\n\tv_cvt_pk_bf16_f32 v72, v72, v73\n\tv_cvt_pk_bf16_f32 v73, v74, v75\n\tds_write_b64 %5, v[72:73] offset:32\n\t" \
    "v_mfma_f32_32x32x16_bf16 v[96:111], v[228:231], v[156:159], v[96:111]\n\tv_mul_f32 v76, v76, v136\n\tv_mul_f32 v77, v77, v136\n\tv_mul_f32 v78, v78, v136\n\t" \
    "v_mul_f32 v79, v79, v136\n\tv_cvt_pk_bf16_f32 v76, v76, v77\n\tv_cvt_pk_bf16_f32 v77, v78, v79\n\tds_write_b64 %5, v[76:77] offset:48\n\t" \
    "v_mfma_f32_32x32x16_bf16 v[96:111], v[132:135], v[120:123], v[96:111]\n\tv_mul_f32 v80, v80, v136\n\tv_mul_f32 v81, v81, v136\n\tv_mul_f32 v82, v82, v136\n\t" \
    "v_mul_f32 v83, v83, v136\n\tv_cvt_pk_bf16_f32 v80, v80, v81\n\tv_cvt_pk_bf16_f32 v81, v82, v83\n\tds_write_b64 %5, v[80:81] offset:64\n\tv_mul_f32 v84, v84, v136\n\t" \
    "v_mul_f32 v85, v85, v136\n\tv_mul_f32 v86, v86, v136\n\tv_mul_f32 v87, v87, v136\n\tv_cvt_pk_bf16_f32 v84, v84, v85\n\tv_cvt_pk_bf16_f32 v85, v86, v87\n\t" \
    "ds_write_b64 %5, v[84:85] offset:80\n\tv_mul_f32 v88, v88, v136\n\tv_mul_f32 v89, v89, v136\n\tv_mul_f32 v90, v90, v136\n\tv_mul_f32 v91, v91, v136\n\t" \
    "v_cvt_pk_bf16_f32 v88, v88, v89\n\tv_cvt_pk_bf16_f32 v89, v90, v91\n\tds_write_b64 %5, v[88:89] offset:96\n\tv_mul_f32 v92, v92, v136\n\tv_mul_f32 v93, v93, v136\n\t" \
    "v_mul_f32 v94, v94, v136\n\tv_mul_f32 v95, v95, v136\n\tv_cvt_pk_bf16_f32 v92, v92, v93\n\tv_cvt_pk_bf16_f32 v93, v94, v95\n\tds_write_b64 %5, v[92:93] offset:112\n\t" \
    "v_pk_mul_f32 v[96:97], v[96:97], v[136:137]\n\tv_pk_mul_f32 v[98:99], v[98:99], v[136:137]\n\tv_cvt_pk_bf16_f32 v96, v96, v97\n\tv_cvt_pk_bf16_f32 v97, v98, v99\n\t" \
    "ds_write_b64 %5, v[96:97] offset:128\n\tv_pk_mul_f32 v[100:101], v[100:101], v[136:137]\n\tv_pk_mul_f32 v[102:103], v[102:103], v[136:137]\n\t" \
    "v_cvt_pk_bf16_f32 v100, v100, v101\n\tv_cvt_pk_bf16_f32 v101, v102, v103\n\tds_write_b64 %5, v[100:101] offset:144\n\tv_pk_mul_f32 v[104:105], v[104:105], v[136:137]\n\t" \
    "v_pk_mul_f32 v[106:107], v[106:107], v[136:137]\n\tv_cvt_pk_bf16_f32 v104, v104, v105\n\tv_cvt_pk_bf16_f32 v105, v106, v107\n\tds_write_b64 %5, v[104:105] offset:160\n\t" \
    "v_pk_mul_f32 v[108:109], v[108:109], v[136:137]\n\tv_pk_mul_f32 v[110:111], v[110:111], v[136:137]\n\tv_cvt_pk_bf16_f32 v108, v108, v109\n\t" \
    "v_cvt_pk_bf16_f32 v109, v110, v111\n\tds_write_b64 %5, v[108:109] offset:176\n\ts_setprio 0\n\ts_branch .Lgx_%=\n\t.Lgs0_%=:\ns_mov_b32 s86, 0x100000\n\t1:\n" \
    "ds_read_b32 v243, %2\n\ts_waitcnt lgkmcnt(0)\n\tv_readfirstlane_b32 s49, v243\n\ts_sub_u32 s86, s86, 1\n\ts_cbranch_scc1 .Lgr0_%=\n\ts_cmp_lt_i32 s49, s45\n\t" \
    "s_cbranch_scc1 1b\n\ts_branch .Lgr0_%=\n\t.Lgs1_%=:\ns_mov_b32 s86, 0x100000\n\t1:\nds_read_b32 v243, %2\n\ts_waitcnt lgkmcnt(0)\n\t" \
    "v_readfirstlane_b32 s49, v243\n\ts_sub_u32 s86, s86, 1\n\ts_cbranch_scc1 .Lgr1_%=\n\ts_cmp_lt_i32 s49, s45\n\ts_cbranch_scc1 1b\n\ts_branch .Lgr1_%=\n\t.Lgx_%=:\n"
#define GEMM1_CLOB "v64", "v65", "v66", "v67", "v68", "v69", "v70", "v71", "v72", "v73", "v74", "v75", "v76", "v77", "v78", "v79", "v80", "v81", "v82", "v83", "v84", "v85", "v86", "v87", "v88", "v89", "v90", "v91", "v92", "v93", "v94", "v95", "v96", "v97", "v98", "v99", "v100", "v101", "v102", "v103", "v104", "v105", "v106", "v107", "v108", "v109", "v110", "v111", "v112", "v113", "v114", "v115", "v116", "v117", "v118", "v119", "v120", "v121", "v122", "v123", "v124", "v125", "v126", "v127", "v128", "v129", "v130", "v131", "v132", "v133", "v134", "v135", "v136", "v137", "v138", "v139", "v144", "v145", "v146", "v147", "v148", "v149", "v150", "v151", "v152", "v153", "v154", "v155", "v156", "v157", "v158", "v159", "v208", "v209", "v210", "v211", "v212", "v213", "v214", "v215", "v216", "v217", "v218", "v219", "v220", "v221", "v222", "v223", "v224", "v225", "v226", "v227", "v228", "v229", "v230", "v231", "v232", "v233", "v234", "v235", "v242", "v243", "v244", "v245", "v246", "s44", "s45", "s46", "s47", "s49", "s50", "s51", "s52", "s53", "s54", "s55", "s56", "s57", "s58", "s59", "s60", "s61", "s62", "s63", "s64", "s65", "s81", "s84", "s85", "s86", "s87", "vcc", "scc", "memory"
#define HADR \
    "s_add_u32 s44, %4, 0\n\ts_and_b32 s44, s44, 7\n\ts_mul_i32 s45, s44, 12288\n\ts_lshl_b32 s44, s44, 7\n\tv_add_u32 v192, s45, %2\n\tv_xor_b32 v208, s44, %3\n\t" \
    "v_xor_b32 v193, 32, v192\n\tv_xor_b32 v209, 64, v208\n\ts_add_u32 s44, %4, 1\n\ts_and_b32 s44, s44, 7\n\ts_mul_i32 s45, s44, 12288\n\ts_lshl_b32 s44, s44, 7\n\t" \
    "v_add_u32 v194, s45, %2\n\tv_xor_b32 v210, s44, %3\n\tv_xor_b32 v195, 32, v194\n\tv_xor_b32 v211, 64, v210\n\ts_add_u32 s44, %4, 2\n\ts_and_b32 s44, s44, 7\n\t" \
    "s_mul_i32 s45, s44, 12288\n\ts_lshl_b32 s44, s44, 7\n\tv_add_u32 v196, s45, %2\n\tv_xor_b32 v212, s44, %3\n\tv_xor_b32 v197, 32, v196\n\tv_xor_b32 v213, 64, v212\n\t" \
    "s_add_u32 s44, %4, 3\n\ts_and_b32 s44, s44, 7\n\ts_mul_i32 s45, s44, 12288\n\ts_lshl_b32 s44, s44, 7\n\tv_add_u32 v198, s45, %2\n\tv_xor_b32 v214, s44, %3\n\t" \
    "v_xor_b32 v199, 32, v198\n\tv_xor_b32 v215, 64, v214\n\ts_add_u32 s44, %4, 4\n\ts_and_b32 s44, s44, 7\n\ts_mul_i32 s45, s44, 12288\n\ts_lshl_b32 s44, s44, 7\n\t" \
    "v_add_u32 v200, s45, %2\n\tv_xor_b32 v216, s44, %3\n\tv_xor_b32 v201, 32, v200\n\tv_xor_b32 v217, 64, v216\n\ts_add_u32 s44, %4, 5\n\ts_and_b32 s44, s44, 7\n\t" \
    "s_mul_i32 s45, s44, 12288\n\ts_lshl_b32 s44, s44, 7\n\tv_add_u32 v202, s45, %2\n\tv_xor_b32 v218, s44, %3\n\tv_xor_b32 v203, 32, v202\n\tv_xor_b32 v219, 64, v218\n\t" \
    "s_add_u32 s44, %4, 6\n\ts_and_b32 s44, s44, 7\n\ts_mul_i32 s45, s44, 12288\n\ts_lshl_b32 s44, s44, 7\n\tv_add_u32 v204, s45, %2\n\tv_xor_b32 v220, s44, %3\n\t" \
    "v_xor_b32 v205, 32, v204\n\tv_xor_b32 v221, 64, v220\n\ts_add_u32 s44, %4, 7\n\ts_and_b32 s44, s44, 7\n\ts_mul_i32 s45, s44, 12288\n\ts_lshl_b32 s44, s44, 7\n\t" \
    "v_add_u32 v206, s45, %2\n\tv_xor_b32 v222, s44, %3\n\tv_xor_b32 v207, 32, v206\n\tv_xor_b32 v223, 64, v222\n\t"
#define HALO \
    "s_mov_b32 s48, 0\n\ts_setprio 0\n\ts_cmp_lt_i32 s48, 5\n\ts_cbranch_scc1 .Lhs0_%=\n\t.Lhr0_%=:\nds_read_b128 v[64:67], v208\n\tds_read_b128 v[68:71], v209\n\t" \
    "ds_read_b128 v[72:75], v210\n\tds_read_b128 v[76:79], v211\n\tds_read_b64_tr_b16 v[128:129], v192 offset:0\n\tds_read_b64_tr_b16 v[130:131], v192 offset:256\n\t" \
    "ds_read_b64_tr_b16 v[132:133], v193 offset:0\n\tds_read_b64_tr_b16 v[134:135], v193 offset:256\n\tds_read_b64_tr_b16 v[136:137], v192 offset:6144\n\t" \
    "ds_read_b64_tr_b16 v[138:139], v192 offset:6400\n\tds_read_b64_tr_b16 v[140:141], v193 offset:6144\n\tds_read_b64_tr_b16 v[142:143], v193 offset:6400\n\ts_setprio 0\n\t" \
    "s_waitcnt lgkmcnt(4)\n\tv_mfma_f32_16x16x32_bf16 v[144:147], v[128:131], v[64:67], 0\n\tv_mfma_f32_16x16x32_bf16 v[148:151], v[132:135], v[64:67], 0\n\t" \
    "s_waitcnt lgkmcnt(0)\n\tv_mfma_f32_16x16x32_bf16 v[144:147], v[136:139], v[68:71], v[144:147]\n\tv_mfma_f32_16x16x32_bf16 v[148:151], v[140:143], v[68:71], v[148:151]\n\t" \
    "s_setprio 0\n\ts_cmp_lt_i32 s48, 3\n\ts_cbranch_scc1 .Lhs1_%=\n\t.Lhr1_%=:\nds_read_b128 v[80:83], v212\n\tds_read_b128 v[84:87], v213\n\tds_read_b128 v[88:91], v214\n\t" \
    "ds_read_b128 v[92:95], v215\n\tds_read_b64_tr_b16 v[128:129], v194 offset:0\n\tds_read_b64_tr_b16 v[130:131], v194 offset:256\n\t" \
    "ds_read_b64_tr_b16 v[132:133], v195 offset:0\n\tds_read_b64_tr_b16 v[134:135], v195 offset:256\n\tds_read_b64_tr_b16 v[136:137], v194 offset:6144\n\t" \
    "ds_read_b64_tr_b16 v[138:139], v194 offset:6400\n\tds_read_b64_tr_b16 v[140:141], v195 offset:6144\n\tds_read_b64_tr_b16 v[142:143], v195 offset:6400\n\ts_setprio 0\n\t" \
    "s_waitcnt lgkmcnt(4)\n\tv_mfma_f32_16x16x32_bf16 v[144:147], v[128:131], v[72:75], v[144:147]\n\tv_mfma_f32_16x16x32_bf16 v[148:151], v[132:135], v[72:75], v[148:151]\n\t" \
    "s_waitcnt lgkmcnt(0)\n\tv_mfma_f32_16x16x32_bf16 v[144:147], v[136:139], v[76:79], v[144:147]\n\tv_mfma_f32_16x16x32_bf16 v[148:151], v[140:143], v[76:79], v[148:151]\n\t" \
    "s_setprio 0\n\ts_cmp_lt_i32 s48, 5\n\ts_cbranch_scc1 .Lhs2_%=\n\t.Lhr2_%=:\nds_read_b128 v[96:99], v216\n\tds_read_b128 v[100:103], v217\n\tds_read_b128 v[104:107], v218\n\t" \
    "ds_read_b128 v[108:111], v219\n\tds_read_b64_tr_b16 v[128:129], v196 offset:0\n\tds_read_b64_tr_b16 v[130:131], v196 offset:256\n\t" \
    "ds_read_b64_tr_b16 v[132:133], v197 offset:0\n\tds_read_b64_tr_b16 v[134:135], v197 offset:256\n\tds_read_b64_tr_b16 v[136:137], v196 offset:6144\n\t" \
    "ds_read_b64_tr_b16 v[138:139], v196 offset:6400\n\tds_read_b64_tr_b16 v[140:141], v197 offset:6144\n\tds_read_b64_tr_b16 v[142:143], v197 offset:6400\n\ts_setprio 0\n\t" \
    "v_mfma_f32_16x16x32_bf16 v[152:155], v[64:67], v[64:67], 0\n\tv_mfma_f32_16x16x32_bf16 v[152:155], v[68:71], v[68:71], v[152:155]\n\t" \
    "v_mfma_f32_16x16x32_bf16 v[152:155], v[72:75], v[72:75], v[152:155]\n\tv_mfma_f32_16x16x32_bf16 v[152:155], v[76:79], v[76:79], v[152:155]\n\ts_waitcnt lgkmcnt(4)\n\t" \
    "v_mfma_f32_16x16x32_bf16 v[144:147], v[128:131], v[80:83], v[144:147]\n\tv_mfma_f32_16x16x32_bf16 v[148:151], v[132:135], v[80:83], v[148:151]\n\ts_waitcnt lgkmcnt(0)\n\t" \
    "v_mfma_f32_16x16x32_bf16 v[144:147], v[136:139], v[84:87], v[144:147]\n\tv_mfma_f32_16x16x32_bf16 v[148:151], v[140:143], v[84:87], v[148:151]\n\ts_setprio 0\n\t" \
    "s_cmp_lt_i32 s48, 7\n\ts_cbranch_scc1 .Lhs3_%=\n\t.Lhr3_%=:\nds_read_b128 v[112:115], v220\n\tds_read_b128 v[116:119], v221\n\tds_read_b128 v[120:123], v222\n\t" \
    "ds_read_b128 v[124:127], v223\n\tds_read_b64_tr_b16 v[128:129], v198 offset:0\n\tds_read_b64_tr_b16 v[130:131], v198 offset:256\n\t" \
    "ds_read_b64_tr_b16 v[132:133], v199 offset:0\n\tds_read_b64_tr_b16 v[134:135], v199 offset:256\n\tds_read_b64_tr_b16 v[136:137], v198 offset:6144\n\t" \
    "ds_read_b64_tr_b16 v[138:139], v198 offset:6400\n\tds_read_b64_tr_b16 v[140:141], v199 offset:6144\n\tds_read_b64_tr_b16 v[142:143], v199 offset:6400\n\ts_setprio 0\n\t" \
    "v_mfma_f32_16x16x32_bf16 v[152:155], v[80:83], v[80:83], v[152:155]\n\tv_mfma_f32_16x16x32_bf16 v[152:155], v[84:87], v[84:87], v[152:155]\n\t" \
    "v_mfma_f32_16x16x32_bf16 v[152:155], v[88:91], v[88:91], v[152:155]\n\tv_mfma_f32_16x16x32_bf16 v[152:155], v[92:95], v[92:95], v[152:155]\n\ts_waitcnt lgkmcnt(4)\n\t" \
    "v_mfma_f32_16x16x32_bf16 v[144:147], v[128:131], v[88:91], v[144:147]\n\tv_mfma_f32_16x16x32_bf16 v[148:151], v[132:135], v[88:91], v[148:151]\n\ts_waitcnt lgkmcnt(0)\n\t" \
    "v_mfma_f32_16x16x32_bf16 v[144:147], v[136:139], v[92:95], v[144:147]\n\tv_mfma_f32_16x16x32_bf16 v[148:151], v[140:143], v[92:95], v[148:151]\n\ts_setprio 0\n\t" \
    "s_cmp_lt_i32 s48, 9\n\ts_cbranch_scc1 .Lhs4_%=\n\t.Lhr4_%=:\nds_read_b64_tr_b16 v[128:129], v200 offset:0\n\tds_read_b64_tr_b16 v[130:131], v200 offset:256\n\t" \
    "ds_read_b64_tr_b16 v[132:133], v201 offset:0\n\tds_read_b64_tr_b16 v[134:135], v201 offset:256\n\tds_read_b64_tr_b16 v[136:137], v200 offset:6144\n\t" \
    "ds_read_b64_tr_b16 v[138:139], v200 offset:6400\n\tds_read_b64_tr_b16 v[140:141], v201 offset:6144\n\tds_read_b64_tr_b16 v[142:143], v201 offset:6400\n\ts_setprio 0\n\t" \
    "v_mfma_f32_16x16x32_bf16 v[152:155], v[96:99], v[96:99], v[152:155]\n\tv_mfma_f32_16x16x32_bf16 v[152:155], v[100:103], v[100:103], v[152:155]\n\t" \
    "v_mfma_f32_16x16x32_bf16 v[152:155], v[104:107], v[104:107], v[152:155]\n\tv_mfma_f32_16x16x32_bf16 v[152:155], v[108:111], v[108:111], v[152:155]\n\ts_waitcnt lgkmcnt(4)\n\t" \
    "v_mfma_f32_16x16x32_bf16 v[144:147], v[128:131], v[96:99], v[144:147]\n\tv_mfma_f32_16x16x32_bf16 v[148:151], v[132:135], v[96:99], v[148:151]\n\ts_waitcnt lgkmcnt(0)\n\t" \
    "v_mfma_f32_16x16x32_bf16 v[144:147], v[136:139], v[100:103], v[144:147]\n\tv_mfma_f32_16x16x32_bf16 v[148:151], v[140:143], v[100:103], v[148:151]\n\ts_setprio 0\n\t" \
    "s_cmp_lt_i32 s48, 11\n\ts_cbranch_scc1 .Lhs5_%=\n\t.Lhr5_%=:\nds_read_b64_tr_b16 v[128:129], v202 offset:0\n\tds_read_b64_tr_b16 v[130:131], v202 offset:256\n\t" \
    "ds_read_b64_tr_b16 v[132:133], v203 offset:0\n\tds_read_b64_tr_b16 v[134:135], v203 offset:256\n\tds_read_b64_tr_b16 v[136:137], v202 offset:6144\n\t" \
    "ds_read_b64_tr_b16 v[138:139], v202 offset:6400\n\tds_read_b64_tr_b16 v[140:141], v203 offset:6144\n\tds_read_b64_tr_b16 v[142:143], v203 offset:6400\n\ts_setprio 0\n\t" \
    "v_mfma_f32_16x16x32_bf16 v[152:155], v[112:115], v[112:115], v[152:155]\n\tv_mfma_f32_16x16x32_bf16 v[152:155], v[116:119], v[116:119], v[152:155]\n\t" \
    "v_mfma_f32_16x16x32_bf16 v[152:155], v[120:123], v[120:123], v[152:155]\n\tv_mfma_f32_16x16x32_bf16 v[152:155], v[124:127], v[124:127], v[152:155]\n\ts_waitcnt lgkmcnt(4)\n\t" \
    "v_mfma_f32_16x16x32_bf16 v[144:147], v[128:131], v[104:107], v[144:147]\n\tv_mfma_f32_16x16x32_bf16 v[148:151], v[132:135], v[104:107], v[148:151]\n\ts_waitcnt lgkmcnt(0)\n\t" \
    "v_mfma_f32_16x16x32_bf16 v[144:147], v[136:139], v[108:111], v[144:147]\n\tv_mfma_f32_16x16x32_bf16 v[148:151], v[140:143], v[108:111], v[148:151]\n\ts_setprio 0\n\t" \
    "s_cmp_lt_i32 s48, 13\n\ts_cbranch_scc1 .Lhs6_%=\n\t.Lhr6_%=:\nds_read_b64_tr_b16 v[128:129], v204 offset:0\n\tds_read_b64_tr_b16 v[130:131], v204 offset:256\n\t" \
    "ds_read_b64_tr_b16 v[132:133], v205 offset:0\n\tds_read_b64_tr_b16 v[134:135], v205 offset:256\n\tds_read_b64_tr_b16 v[136:137], v204 offset:6144\n\t" \
    "ds_read_b64_tr_b16 v[138:139], v204 offset:6400\n\tds_read_b64_tr_b16 v[140:141], v205 offset:6144\n\tds_read_b64_tr_b16 v[142:143], v205 offset:6400\n\ts_setprio 0\n\t" \
    "s_waitcnt lgkmcnt(4)\n\tv_mfma_f32_16x16x32_bf16 v[144:147], v[128:131], v[112:115], v[144:147]\n\tv_mfma_f32_16x16x32_bf16 v[148:151], v[132:135], v[112:115], v[148:151]\n\t" \
    "s_waitcnt lgkmcnt(0)\n\tv_mfma_f32_16x16x32_bf16 v[144:147], v[136:139], v[116:119], v[144:147]\n\tv_mfma_f32_16x16x32_bf16 v[148:151], v[140:143], v[116:119], v[148:151]\n\t" \
    "s_setprio 0\n\ts_mov_b32 s44, 0xaaaaaaaa\n\ts_mov_b32 s45, s44\n\ts_mov_b32 s46, 0xcccccccc\n\ts_mov_b32 s47, s46\n\tv_cndmask_b32 v156, v152, v153, s[44:45]\n\t" \
    "v_cndmask_b32 v157, v154, v155, s[44:45]\n\ts_mov_b32 s44, 0x00f0000f\n\ts_mov_b32 s45, 0xf0000f00\n\tv_cndmask_b32 v156, v156, v157, s[46:47]\n\t" \
    "s_mov_b64 s[46:47], 0xffff\n\tv_cndmask_b32 v156, 0, v156, s[44:45]\n\tv_cndmask_b32 v157, 0, v153, s[46:47]\n\ts_mov_b32 s44, 0x3b000000\n\ts_nop 0\n\t" \
    "v_permlane32_swap_b32 v156, v157\n\tv_add_f32 v158, v156, v157\n\tv_add_f32 v159, v156, v157\n\ts_nop 1\n\tv_permlane16_swap_b32 v158, v159\n\tv_add_f32 v156, v158, v159\n\t" \
    "v_add_f32 v157, v158, v159\n\ts_nop 1\n\tv_permlane32_swap_b32 v156, v157\n\tv_mul_f32 v157, s44, v157\n\tv_mul_f32 v158, v157, v157\n\tv_fma_f32 v158, v156, s44, -v158\n\t" \
    "v_max_f32 v158, 0, v158\n\tv_cvt_pk_bf16_f32 v158, 0, v158\n\tv_add_f32 v158, 0x3727c5ac, v158\n\tv_cvt_pk_bf16_f32 v158, 0, v158\n\tv_sqrt_f32 v158, v158\n\t" \
    "v_cvt_pk_bf16_f32 v157, 0, -v157\n\tv_cvt_pk_bf16_f32 v158, 0, v158\n\tv_rcp_f32 v158, v158\n\tv_mov_b32 v159, 1\n\ts_mov_b32 s46, 0xffff0000\n\ts_mov_b32 s44, 0x00020002\n\t" \
    "s_mov_b32 s45, s44\n\ts_cmp_lt_i32 s48, 15\n\ts_cbranch_scc1 .Lhs7_%=\n\t.Lhr7_%=:\ns_setprio 3\n\tds_read_b64_tr_b16 v[128:129], v206 offset:0\n\t" \
    "ds_read_b64_tr_b16 v[130:131], v206 offset:256\n\tds_read_b64_tr_b16 v[132:133], v207 offset:0\n\tds_read_b64_tr_b16 v[134:135], v207 offset:256\n\t" \
    "ds_read_b64_tr_b16 v[136:137], v206 offset:6144\n\tds_read_b64_tr_b16 v[138:139], v206 offset:6400\n\tds_read_b64_tr_b16 v[140:141], v207 offset:6144\n\t" \
    "ds_read_b64_tr_b16 v[142:143], v207 offset:6400\n\ts_waitcnt lgkmcnt(4)\n\tv_mfma_f32_16x16x32_bf16 v[144:147], v[128:131], v[120:123], v[144:147]\n\t" \
    "v_mfma_f32_16x16x32_bf16 v[148:151], v[132:135], v[120:123], v[148:151]\n\ts_waitcnt lgkmcnt(0)\n\tv_mfma_f32_16x16x32_bf16 v[144:147], v[136:139], v[124:127], v[144:147]\n\t" \
    "v_mfma_f32_16x16x32_bf16 v[148:151], v[140:143], v[124:127], v[148:151]\n\ts_nop 6\n\tv_and_b32 v160, s46, v144\n\tv_and_b32 v161, s46, v145\n\tv_and_b32 v162, s46, v146\n\t" \
    "v_and_b32 v163, s46, v147\n\tv_and_b32 v164, s46, v148\n\tv_and_b32 v165, s46, v149\n\tv_and_b32 v166, s46, v150\n\tv_and_b32 v167, s46, v151\n\tv_sub_f32 v168, v144, v160\n\t" \
    "v_sub_f32 v169, v145, v161\n\tv_sub_f32 v170, v146, v162\n\tv_sub_f32 v171, v147, v163\n\tv_sub_f32 v172, v148, v164\n\tv_sub_f32 v173, v149, v165\n\t" \
    "v_sub_f32 v174, v150, v166\n\tv_sub_f32 v175, v151, v167\n\tv_cvt_pk_bf16_f32 v160, v160, v168\n\tv_cvt_pk_bf16_f32 v161, v161, v169\n\tv_cvt_pk_bf16_f32 v162, v162, v170\n\t" \
    "v_cvt_pk_bf16_f32 v163, v163, v171\n\tv_cvt_pk_bf16_f32 v164, v164, v172\n\tv_cvt_pk_bf16_f32 v165, v165, v173\n\tv_cvt_pk_bf16_f32 v166, v166, v174\n\t" \
    "v_cvt_pk_bf16_f32 v167, v167, v175\n\ts_mov_b64 exec, s[44:45]\n\tds_write_b128 %5, v[160:163]\n\tds_write_b128 %5, v[164:167] offset:64\n\tds_write_b32 %6, v159\n\t" \
    "s_mov_b64 exec, -1\n\tds_read_b32 v156, %4\n\tv_fmac_f32_dpp v144, v144, v157 row_newbcast:1 row_mask:0xf bank_mask:0xf\n\t" \
    "v_fmac_f32_dpp v145, v145, v157 row_newbcast:1 row_mask:0xf bank_mask:0xf\n\tv_fmac_f32_dpp v146, v146, v157 row_newbcast:1 row_mask:0xf bank_mask:0xf\n\t" \
    "v_fmac_f32_dpp v147, v147, v157 row_newbcast:1 row_mask:0xf bank_mask:0xf\n\tv_fmac_f32_dpp v148, v148, v157 row_newbcast:1 row_mask:0xf bank_mask:0xf\n\t" \
    "v_fmac_f32_dpp v149, v149, v157 row_newbcast:1 row_mask:0xf bank_mask:0xf\n\tv_fmac_f32_dpp v150, v150, v157 row_newbcast:1 row_mask:0xf bank_mask:0xf\n\t" \
    "v_fmac_f32_dpp v151, v151, v157 row_newbcast:1 row_mask:0xf bank_mask:0xf\n\tv_mul_f32 v144, v144, v158\n\tv_mul_f32 v145, v145, v158\n\tv_mul_f32 v146, v146, v158\n\t" \
    "v_mul_f32 v147, v147, v158\n\tv_mul_f32 v148, v148, v158\n\tv_mul_f32 v149, v149, v158\n\ts_waitcnt lgkmcnt(0)\n\tv_readfirstlane_b32 s48, v156\n\t" \
    "v_mul_f32 v150, v150, v158\n\tv_mul_f32 v151, v151, v158\n\tv_cvt_pk_bf16_f32 v144, v144, v145\n\tv_cvt_pk_bf16_f32 v145, v146, v147\n\tv_cvt_pk_bf16_f32 v146, v148, v149\n\t" \
    "v_cvt_pk_bf16_f32 v147, v150, v151\n\ts_setprio 0\n\ts_cmp_lt_i32 s48, 17\n\ts_cbranch_scc1 .Lhs8_%=\n\t.Lhr8_%=:\ns_branch .Lhx_%=\n\t.Lhs0_%=:\ns_mov_b32 s86, 0x100000\n\t" \
    "1:\nds_read_b32 v156, %4\n\ts_waitcnt lgkmcnt(0)\n\tv_readfirstlane_b32 s48, v156\n\ts_sub_u32 s86, s86, 1\n\ts_cbranch_scc1 .Lhr0_%=\n\ts_cmp_ge_i32 s48, 5\n\t" \
    "s_cbranch_scc1 .Lhr0_%=\n\ts_sleep 1\n\ts_branch 1b\n\t.Lhs1_%=:\ns_mov_b32 s86, 0x100000\n\t1:\nds_read_b32 v156, %4\n\ts_waitcnt lgkmcnt(0)\n\t" \
    "v_readfirstlane_b32 s48, v156\n\ts_sub_u32 s86, s86, 1\n\ts_cbranch_scc1 .Lhr1_%=\n\ts_cmp_ge_i32 s48, 3\n\ts_cbranch_scc1 .Lhr1_%=\n\ts_sleep 1\n\ts_branch 1b\n\t.Lhs2_%=:\n" \
    "s_mov_b32 s86, 0x100000\n\t1:\nds_read_b32 v156, %4\n\ts_waitcnt lgkmcnt(0)\n\tv_readfirstlane_b32 s48, v156\n\ts_sub_u32 s86, s86, 1\n\t" \
    "s_cbranch_scc1 .Lhr2_%=\n\ts_cmp_ge_i32 s48, 5\n\ts_cbranch_scc1 .Lhr2_%=\n\ts_sleep 1\n\ts_branch 1b\n\t.Lhs3_%=:\ns_mov_b32 s86, 0x100000\n\t1:\n" \
    "ds_read_b32 v156, %4\n\ts_waitcnt lgkmcnt(0)\n\tv_readfirstlane_b32 s48, v156\n\ts_sub_u32 s86, s86, 1\n\ts_cbranch_scc1 .Lhr3_%=\n\ts_cmp_ge_i32 s48, 7\n\t" \
    "s_cbranch_scc1 .Lhr3_%=\n\ts_sleep 1\n\ts_branch 1b\n\t.Lhs4_%=:\ns_mov_b32 s86, 0x100000\n\t1:\nds_read_b32 v156, %4\n\ts_waitcnt lgkmcnt(0)\n\t" \
    "v_readfirstlane_b32 s48, v156\n\ts_sub_u32 s86, s86, 1\n\ts_cbranch_scc1 .Lhr4_%=\n\ts_cmp_ge_i32 s48, 9\n\ts_cbranch_scc1 .Lhr4_%=\n\ts_sleep 1\n\ts_branch 1b\n\t.Lhs5_%=:\n" \
    "s_mov_b32 s86, 0x100000\n\t1:\nds_read_b32 v156, %4\n\ts_waitcnt lgkmcnt(0)\n\tv_readfirstlane_b32 s48, v156\n\ts_sub_u32 s86, s86, 1\n\t" \
    "s_cbranch_scc1 .Lhr5_%=\n\ts_cmp_ge_i32 s48, 11\n\ts_cbranch_scc1 .Lhr5_%=\n\ts_sleep 1\n\ts_branch 1b\n\t.Lhs6_%=:\ns_mov_b32 s86, 0x100000\n\t1:\n" \
    "ds_read_b32 v156, %4\n\ts_waitcnt lgkmcnt(0)\n\tv_readfirstlane_b32 s48, v156\n\ts_sub_u32 s86, s86, 1\n\ts_cbranch_scc1 .Lhr6_%=\n\ts_cmp_ge_i32 s48, 13\n\t" \
    "s_cbranch_scc1 .Lhr6_%=\n\ts_sleep 1\n\ts_branch 1b\n\t.Lhs7_%=:\ns_mov_b32 s86, 0x100000\n\t1:\nds_read_b32 v156, %4\n\ts_waitcnt lgkmcnt(0)\n\t" \
    "v_readfirstlane_b32 s48, v156\n\ts_sub_u32 s86, s86, 1\n\ts_cbranch_scc1 .Lhr7_%=\n\ts_cmp_ge_i32 s48, 15\n\ts_cbranch_scc1 .Lhr7_%=\n\ts_branch 1b\n\t.Lhs8_%=:\n" \
    "s_mov_b32 s86, 0x100000\n\t1:\nds_read_b32 v156, %4\n\ts_waitcnt lgkmcnt(0)\n\tv_readfirstlane_b32 s48, v156\n\ts_sub_u32 s86, s86, 1\n\t" \
    "s_cbranch_scc1 .Lhr8_%=\n\ts_cmp_ge_i32 s48, 17\n\ts_cbranch_scc1 .Lhr8_%=\n\ts_sleep 1\n\ts_branch 1b\n\t.Lhx_%=:\n"
#define HALO_CLOB "v64", "v65", "v66", "v67", "v68", "v69", "v70", "v71", "v72", "v73", "v74", "v75", "v76", "v77", "v78", "v79", "v80", "v81", "v82", "v83", "v84", "v85", "v86", "v87", "v88", "v89", "v90", "v91", "v92", "v93", "v94", "v95", "v96", "v97", "v98", "v99", "v100", "v101", "v102", "v103", "v104", "v105", "v106", "v107", "v108", "v109", "v110", "v111", "v112", "v113", "v114", "v115", "v116", "v117", "v118", "v119", "v120", "v121", "v122", "v123", "v124", "v125", "v126", "v127", "v128", "v129", "v130", "v131", "v132", "v133", "v134", "v135", "v136", "v137", "v138", "v139", "v140", "v141", "v142", "v143", "v148", "v149", "v150", "v151", "v152", "v153", "v154", "v155", "v156", "v157", "v158", "v159", "v160", "v161", "v162", "v163", "v164", "v165", "v166", "v167", "v168", "v169", "v170", "v171", "v172", "v173", "v174", "v175", "s44", "s45", "s46", "s47", "s48", "s86", "s87", "vcc", "scc", "memory"

#define GEMM2 \
    "v_mov_b32 v245, 0x3f803f80\n\ts_mov_b64 s[82:83], %9\n\ts_add_u32 s86, %5, 0\n\ts_and_b32 s86, s86, 7\n\ts_lshl_b32 s44, s86, 12\n\ts_add_u32 s86, %5, 1\n\t" \
    "s_and_b32 s86, s86, 7\n\ts_lshl_b32 s45, s86, 12\n\ts_add_u32 s86, %5, 2\n\ts_and_b32 s86, s86, 7\n\ts_lshl_b32 s46, s86, 12\n\ts_add_u32 s86, %5, 3\n\t" \
    "s_and_b32 s86, s86, 7\n\ts_lshl_b32 s47, s86, 12\n\ts_add_u32 s86, %5, 4\n\ts_and_b32 s86, s86, 7\n\ts_lshl_b32 s48, s86, 12\n\ts_add_u32 s86, %5, 5\n\t" \
    "s_and_b32 s86, s86, 7\n\ts_lshl_b32 s49, s86, 12\n\ts_add_u32 s86, %5, 6\n\ts_and_b32 s86, s86, 7\n\ts_lshl_b32 s50, s86, 12\n\ts_add_u32 s86, %5, 7\n\t" \
    "s_and_b32 s86, s86, 7\n\ts_lshl_b32 s51, s86, 12\n\ts_add_u32 s52, s82, s44\n\ts_addc_u32 s53, s83, 0\n\ts_add_u32 s54, s82, s45\n\ts_addc_u32 s55, s83, 0\n\t" \
    "s_add_u32 s56, s82, s46\n\ts_addc_u32 s57, s83, 0\n\ts_add_u32 s58, s82, s47\n\ts_addc_u32 s59, s83, 0\n\ts_add_u32 s60, s82, s48\n\ts_addc_u32 s61, s83, 0\n\t" \
    "s_add_u32 s62, s82, s49\n\ts_addc_u32 s63, s83, 0\n\ts_add_u32 s64, s82, s50\n\ts_addc_u32 s65, s83, 0\n\ts_add_u32 s66, s82, s51\n\ts_addc_u32 s67, s83, 0\n\t" \
    "s_mov_b32 s81, 0x3b000000\n\ts_mov_b32 s84, -1\n\ts_mov_b32 s85, 0\n\tv_mov_b32 v112, 0\n\tv_mov_b32 v113, 0\n\tv_mov_b32 v114, 0\n\tv_mov_b32 v115, 0\n\tv_mov_b32 v121, 0\n\t" \
    "v_mov_b32 v122, 0\n\tv_mov_b32 v123, 0\n\tv_mov_b32 v125, 0\n\tv_mov_b32 v126, 0\n\tv_mov_b32 v127, 0\n\tv_mov_b32 v129, 0\n\tv_mov_b32 v130, 0\n\tv_mov_b32 v131, 0\n\t" \
    "v_mov_b32 v133, 0\n\tv_mov_b32 v134, 0\n\tv_mov_b32 v135, 0\n\tv_mad_u32_u24 v242, s44, 3, %2\n\ts_mov_b32 s80, 0x10000\n\ts_waitcnt vmcnt(0) lgkmcnt(0)\n\ts_barrier\n\t" \
    "global_load_dword v144, %8, s[52:53] offset:-2048 sc1\n\tglobal_load_dword v145, %8, s[52:53] offset:-1920 sc1\n\tglobal_load_dword v146, %8, s[52:53] offset:-1792 sc1\n\t" \
    "global_load_dword v147, %8, s[52:53] offset:-1664 sc1\n\tglobal_load_dword v148, %8, s[52:53] offset:-1024 sc1\n\tglobal_load_dword v149, %8, s[52:53] offset:-896 sc1\n\t" \
    "global_load_dword v150, %8, s[52:53] offset:-768 sc1\n\tglobal_load_dword v151, %8, s[52:53] offset:-640 sc1\n\tglobal_load_dword v152, %8, s[52:53] offset:0 sc1\n\t" \
    "global_load_dword v153, %8, s[52:53] offset:128 sc1\n\tglobal_load_dword v154, %8, s[52:53] offset:256 sc1\n\tglobal_load_dword v155, %8, s[52:53] offset:384 sc1\n\t" \
    "global_load_dword v156, %8, s[52:53] offset:1024 sc1\n\tglobal_load_dword v157, %8, s[52:53] offset:1152 sc1\n\tglobal_load_dword v158, %8, s[52:53] offset:1280 sc1\n\t" \
    "global_load_dword v159, %8, s[52:53] offset:1408 sc1\n\tglobal_load_dword v160, %8, s[54:55] offset:-2048 sc1\n\tglobal_load_dword v161, %8, s[54:55] offset:-1920 sc1\n\t" \
    "global_load_dword v162, %8, s[54:55] offset:-1792 sc1\n\tglobal_load_dword v163, %8, s[54:55] offset:-1664 sc1\n\tglobal_load_dword v164, %8, s[54:55] offset:-1024 sc1\n\t" \
    "global_load_dword v165, %8, s[54:55] offset:-896 sc1\n\tglobal_load_dword v166, %8, s[54:55] offset:-768 sc1\n\tglobal_load_dword v167, %8, s[54:55] offset:-640 sc1\n\t" \
    "global_load_dword v168, %8, s[54:55] offset:0 sc1\n\tglobal_load_dword v169, %8, s[54:55] offset:128 sc1\n\tglobal_load_dword v170, %8, s[54:55] offset:256 sc1\n\t" \
    "global_load_dword v171, %8, s[54:55] offset:384 sc1\n\tglobal_load_dword v172, %8, s[54:55] offset:1024 sc1\n\tglobal_load_dword v173, %8, s[54:55] offset:1152 sc1\n\t" \
    "global_load_dword v174, %8, s[54:55] offset:1280 sc1\n\tglobal_load_dword v175, %8, s[54:55] offset:1408 sc1\n\tglobal_load_dword v176, %8, s[56:57] offset:-2048 sc1\n\t" \
    "global_load_dword v177, %8, s[56:57] offset:-1920 sc1\n\tglobal_load_dword v178, %8, s[56:57] offset:-1792 sc1\n\tglobal_load_dword v179, %8, s[56:57] offset:-1664 sc1\n\t" \
    "global_load_dword v180, %8, s[56:57] offset:-1024 sc1\n\tglobal_load_dword v181, %8, s[56:57] offset:-896 sc1\n\tglobal_load_dword v182, %8, s[56:57] offset:-768 sc1\n\t" \
    "global_load_dword v183, %8, s[56:57] offset:-640 sc1\n\tglobal_load_dword v184, %8, s[56:57] offset:0 sc1\n\tglobal_load_dword v185, %8, s[56:57] offset:128 sc1\n\t" \
    "global_load_dword v186, %8, s[56:57] offset:256 sc1\n\tglobal_load_dword v187, %8, s[56:57] offset:384 sc1\n\tglobal_load_dword v188, %8, s[56:57] offset:1024 sc1\n\t" \
    "global_load_dword v189, %8, s[56:57] offset:1152 sc1\n\tglobal_load_dword v190, %8, s[56:57] offset:1280 sc1\n\tglobal_load_dword v191, %8, s[56:57] offset:1408 sc1\n\t" \
    "ds_read_b64_tr_b16 v[208:209], v242 offset:0\n\tds_read_b64_tr_b16 v[210:211], v242 offset:256\n\tds_read_b64_tr_b16 v[212:213], v242 offset:3072\n\t" \
    "ds_read_b64_tr_b16 v[214:215], v242 offset:3328\n\tds_read_b64_tr_b16 v[216:217], v242 offset:6144\n\tds_read_b64_tr_b16 v[218:219], v242 offset:6400\n\t" \
    "ds_read_b64_tr_b16 v[220:221], v242 offset:9216\n\tds_read_b64_tr_b16 v[222:223], v242 offset:9472\n\tds_read_b64_tr_b16 v[224:225], v242 offset:1024\n\t" \
    "ds_read_b64_tr_b16 v[226:227], v242 offset:1280\n\tds_read_b64_tr_b16 v[228:229], v242 offset:4096\n\tds_read_b64_tr_b16 v[230:231], v242 offset:4352\n\t" \
    "ds_read_b32 v124, %7 offset:0\n\tds_read_b32 v128, %7 offset:128\n\tds_read_b32 v132, %7 offset:256\n\ts_waitcnt vmcnt(32)\n\t.Lredo0_%=:\n" \
    "v_dot2_f32_bf16 v117, v144, v144, 0\n\tv_dot2_f32_bf16 v119, v145, v145, 0\n\tv_dot2_f32_bf16 v116, v144, v245, 0\n\tv_dot2_f32_bf16 v118, v145, v245, 0\n\t" \
    "v_dot2c_f32_bf16 v117, v146, v146\n\tv_dot2c_f32_bf16 v119, v147, v147\n\tv_dot2c_f32_bf16 v117, v148, v148\n\tv_dot2c_f32_bf16 v116, v245, v146\n\t" \
    "v_dot2c_f32_bf16 v119, v149, v149\n\tv_dot2c_f32_bf16 v118, v245, v147\n\tv_dot2c_f32_bf16 v117, v150, v150\n\tv_dot2c_f32_bf16 v116, v245, v148\n\t" \
    "v_dot2c_f32_bf16 v119, v151, v151\n\tv_dot2c_f32_bf16 v118, v245, v149\n\tv_dot2c_f32_bf16 v117, v152, v152\n\tv_dot2c_f32_bf16 v116, v245, v150\n\t" \
    "v_dot2c_f32_bf16 v119, v153, v153\n\tv_dot2c_f32_bf16 v118, v245, v151\n\tv_dot2c_f32_bf16 v117, v154, v154\n\tv_dot2c_f32_bf16 v116, v245, v152\n\t" \
    "v_dot2c_f32_bf16 v119, v155, v155\n\tv_dot2c_f32_bf16 v118, v245, v153\n\tv_dot2c_f32_bf16 v117, v156, v156\n\tv_dot2c_f32_bf16 v116, v245, v154\n\t" \
    "v_dot2c_f32_bf16 v119, v157, v157\n\tv_dot2c_f32_bf16 v118, v245, v155\n\tv_dot2c_f32_bf16 v117, v158, v158\n\tv_dot2c_f32_bf16 v116, v245, v156\n\t" \
    "v_dot2c_f32_bf16 v119, v159, v159\n\tv_dot2c_f32_bf16 v118, v245, v157\n\tv_dot2c_f32_bf16 v116, v245, v158\n\tv_dot2c_f32_bf16 v118, v245, v159\n\t" \
    "v_cmp_u_f32 vcc, v117, v119\n\ts_cbranch_vccnz .Lslow0_%=\n\t.Lok0_%=:\ns_nop 0\n\tv_add_f32 v116, v116, v118\n\tv_add_f32 v117, v117, v119\n\ts_lshr_b32 s86, s44, 4\n\t" \
    "v_add_u32 v244, s86, %10\n\tv_permlane32_swap_b32 v116, v117\n\tv_add_f32 v116, v116, v117\n\tv_min_u32 v116, -2, v116\n\tglobal_store_dword v244, v116, %11 sc1\n\t" \
    "s_waitcnt lgkmcnt(0)\n\tv_mfma_f32_32x32x16_bf16 v[64:79], v[208:211], v[144:147], 0\n\tds_read_b64_tr_b16 v[208:209], v242 offset:7168\n\t" \
    "ds_read_b64_tr_b16 v[210:211], v242 offset:7424\n\tglobal_load_dword v192, %8, s[58:59] offset:-2048 sc1\n\tglobal_load_dword v193, %8, s[58:59] offset:-1920 sc1\n\t" \
    "v_mfma_f32_32x32x16_bf16 v[64:79], v[212:215], v[148:151], v[64:79]\n\tds_read_b64_tr_b16 v[212:213], v242 offset:10240\n\tds_read_b64_tr_b16 v[214:215], v242 offset:10496\n\t" \
    "global_load_dword v194, %8, s[58:59] offset:-1792 sc1\n\tglobal_load_dword v195, %8, s[58:59] offset:-1664 sc1\n\t" \
    "v_mfma_f32_32x32x16_bf16 v[64:79], v[216:219], v[152:155], v[64:79]\n\tds_read_b64_tr_b16 v[216:217], v242 offset:2048\n\tds_read_b64_tr_b16 v[218:219], v242 offset:2304\n\t" \
    "global_load_dword v196, %8, s[58:59] offset:-1024 sc1\n\tglobal_load_dword v197, %8, s[58:59] offset:-896 sc1\n\ts_waitcnt lgkmcnt(6)\n\t" \
    "v_mfma_f32_32x32x16_bf16 v[64:79], v[220:223], v[156:159], v[64:79]\n\tds_read_b64_tr_b16 v[220:221], v242 offset:5120\n\tds_read_b64_tr_b16 v[222:223], v242 offset:5376\n\t" \
    "global_load_dword v198, %8, s[58:59] offset:-768 sc1\n\tglobal_load_dword v199, %8, s[58:59] offset:-640 sc1\n\t" \
    "v_mfma_f32_32x32x16_bf16 v[80:95], v[224:227], v[144:147], 0\n\tds_read_b64_tr_b16 v[224:225], v242 offset:8192\n\tds_read_b64_tr_b16 v[226:227], v242 offset:8448\n\t" \
    "global_load_dword v200, %8, s[58:59] offset:0 sc1\n\tglobal_load_dword v201, %8, s[58:59] offset:128 sc1\n\tv_mad_u32_u24 v240, s45, 3, %2\n\t" \
    "v_mfma_f32_32x32x16_bf16 v[80:95], v[228:231], v[148:151], v[80:95]\n\tds_read_b64_tr_b16 v[228:229], v242 offset:11264\n\tds_read_b64_tr_b16 v[230:231], v242 offset:11520\n\t" \
    "global_load_dword v202, %8, s[58:59] offset:256 sc1\n\tglobal_load_dword v203, %8, s[58:59] offset:384 sc1\n\ts_waitcnt lgkmcnt(6)\n\t" \
    "v_mfma_f32_32x32x16_bf16 v[80:95], v[208:211], v[152:155], v[80:95]\n\tds_read_b64_tr_b16 v[208:209], v240 offset:0\n\tds_read_b64_tr_b16 v[210:211], v240 offset:256\n\t" \
    "global_load_dword v204, %8, s[58:59] offset:1024 sc1\n\tglobal_load_dword v205, %8, s[58:59] offset:1152 sc1\n\t" \
    "v_mfma_f32_32x32x16_bf16 v[80:95], v[212:215], v[156:159], v[80:95]\n\tds_read_b64_tr_b16 v[212:213], v240 offset:3072\n\tds_read_b64_tr_b16 v[214:215], v240 offset:3328\n\t" \
    "global_load_dword v206, %8, s[58:59] offset:1280 sc1\n\tglobal_load_dword v207, %8, s[58:59] offset:1408 sc1\n\t" \
    "v_mfma_f32_32x32x16_bf16 v[96:111], v[216:219], v[144:147], 0\n\tds_read_b64_tr_b16 v[216:217], v240 offset:6144\n\tds_read_b64_tr_b16 v[218:219], v240 offset:6400\n\t" \
    "s_waitcnt lgkmcnt(6)\n\tv_mfma_f32_32x32x16_bf16 v[96:111], v[220:223], v[148:151], v[96:111]\n\tds_read_b64_tr_b16 v[220:221], v240 offset:9216\n\t" \
    "ds_read_b64_tr_b16 v[222:223], v240 offset:9472\n\tv_mfma_f32_32x32x16_bf16 v[96:111], v[224:227], v[152:155], v[96:111]\n\tds_read_b64_tr_b16 v[224:225], v240 offset:1024\n\t" \
    "ds_read_b64_tr_b16 v[226:227], v240 offset:1280\n\tv_mfma_f32_32x32x16_bf16 v[96:111], v[228:231], v[156:159], v[96:111]\n\tds_read_b64_tr_b16 v[228:229], v240 offset:4096\n\t" \
    "ds_read_b64_tr_b16 v[230:231], v240 offset:4352\n\ts_waitcnt vmcnt(33)\n\t.Lredo1_%=:\nv_max3_u32 v244, v160, v161, v162\n\tv_max3_u32 v243, v163, v164, v165\n\t" \
    "v_max3_u32 v244, v244, v166, v167\n\tv_max3_u32 v243, v243, v168, v169\n\tv_max3_u32 v244, v244, v170, v171\n\tv_max3_u32 v243, v243, v172, v173\n\t" \
    "v_max3_u32 v244, v244, v174, v175\n\tv_max_u32 v243, v243, v244\n\tv_cmp_eq_u32 vcc, -1, v243\n\ts_cbranch_vccnz .Lslow1_%=\n\t.Lok1_%=:\ns_waitcnt lgkmcnt(6)\n\t" \
    "v_mfma_f32_32x32x16_bf16 v[64:79], v[208:211], v[160:163], v[64:79]\n\tds_read_b64_tr_b16 v[208:209], v240 offset:7168\n\tds_read_b64_tr_b16 v[210:211], v240 offset:7424\n\t" \
    "global_load_dword v144, %8, s[60:61] offset:-2048 sc1\n\tglobal_load_dword v145, %8, s[60:61] offset:-1920 sc1\n\t" \
    "v_mfma_f32_32x32x16_bf16 v[64:79], v[212:215], v[164:167], v[64:79]\n\tds_read_b64_tr_b16 v[212:213], v240 offset:10240\n\tds_read_b64_tr_b16 v[214:215], v240 offset:10496\n\t" \
    "global_load_dword v146, %8, s[60:61] offset:-1792 sc1\n\tglobal_load_dword v147, %8, s[60:61] offset:-1664 sc1\n\t" \
    "v_mfma_f32_32x32x16_bf16 v[64:79], v[216:219], v[168:171], v[64:79]\n\tds_read_b64_tr_b16 v[216:217], v240 offset:2048\n\tds_read_b64_tr_b16 v[218:219], v240 offset:2304\n\t" \
    "global_load_dword v148, %8, s[60:61] offset:-1024 sc1\n\tglobal_load_dword v149, %8, s[60:61] offset:-896 sc1\n\ts_waitcnt lgkmcnt(6)\n\t" \
    "v_mfma_f32_32x32x16_bf16 v[64:79], v[220:223], v[172:175], v[64:79]\n\tds_read_b64_tr_b16 v[220:221], v240 offset:5120\n\tds_read_b64_tr_b16 v[222:223], v240 offset:5376\n\t" \
    "global_load_dword v150, %8, s[60:61] offset:-768 sc1\n\tglobal_load_dword v151, %8, s[60:61] offset:-640 sc1\n\t" \
    "v_mfma_f32_32x32x16_bf16 v[80:95], v[224:227], v[160:163], v[80:95]\n\tds_read_b64_tr_b16 v[224:225], v240 offset:8192\n\tds_read_b64_tr_b16 v[226:227], v240 offset:8448\n\t" \
    "global_load_dword v152, %8, s[60:61] offset:0 sc1\n\tglobal_load_dword v153, %8, s[60:61] offset:128 sc1\n\tv_mad_u32_u24 v242, s46, 3, %2\n\t" \
    "v_mfma_f32_32x32x16_bf16 v[80:95], v[228:231], v[164:167], v[80:95]\n\tds_read_b64_tr_b16 v[228:229], v240 offset:11264\n\tds_read_b64_tr_b16 v[230:231], v240 offset:11520\n\t" \
    "global_load_dword v154, %8, s[60:61] offset:256 sc1\n\tglobal_load_dword v155, %8, s[60:61] offset:384 sc1\n\ts_waitcnt lgkmcnt(6)\n\t" \
    "v_mfma_f32_32x32x16_bf16 v[80:95], v[208:211], v[168:171], v[80:95]\n\tds_read_b64_tr_b16 v[208:209], v242 offset:0\n\tds_read_b64_tr_b16 v[210:211], v242 offset:256\n\t" \
    "global_load_dword v156, %8, s[60:61] offset:1024 sc1\n\tglobal_load_dword v157, %8, s[60:61] offset:1152 sc1\n\t" \
    "v_mfma_f32_32x32x16_bf16 v[80:95], v[212:215], v[172:175], v[80:95]\n\tds_read_b64_tr_b16 v[212:213], v242 offset:3072\n\tds_read_b64_tr_b16 v[214:215], v242 offset:3328\n\t" \
    "global_load_dword v158, %8, s[60:61] offset:1280 sc1\n\tglobal_load_dword v159, %8, s[60:61] offset:1408 sc1\n\t" \
    "v_mfma_f32_32x32x16_bf16 v[96:111], v[216:219], v[160:163], v[96:111]\n\tds_read_b64_tr_b16 v[216:217], v242 offset:6144\n\tds_read_b64_tr_b16 v[218:219], v242 offset:6400\n\t" \
    "s_waitcnt lgkmcnt(6)\n\tv_mfma_f32_32x32x16_bf16 v[96:111], v[220:223], v[164:167], v[96:111]\n\tds_read_b64_tr_b16 v[220:221], v242 offset:9216\n\t" \
    "ds_read_b64_tr_b16 v[222:223], v242 offset:9472\n\tv_mfma_f32_32x32x16_bf16 v[96:111], v[224:227], v[168:171], v[96:111]\n\tds_read_b64_tr_b16 v[224:225], v242 offset:1024\n\t" \
    "ds_read_b64_tr_b16 v[226:227], v242 offset:1280\n\tv_mfma_f32_32x32x16_bf16 v[96:111], v[228:231], v[172:175], v[96:111]\n\tds_read_b64_tr_b16 v[228:229], v242 offset:4096\n\t" \
    "ds_read_b64_tr_b16 v[230:231], v242 offset:4352\n\ts_waitcnt vmcnt(33)\n\t.Lredo2_%=:\nv_max3_u32 v244, v176, v177, v178\n\tv_max3_u32 v243, v179, v180, v181\n\t" \
    "v_max3_u32 v244, v244, v182, v183\n\tv_max3_u32 v243, v243, v184, v185\n\tv_max3_u32 v244, v244, v186, v187\n\tv_max3_u32 v243, v243, v188, v189\n\t" \
    "v_max3_u32 v244, v244, v190, v191\n\tv_max_u32 v243, v243, v244\n\tv_cmp_eq_u32 vcc, -1, v243\n\ts_cbranch_vccnz .Lslow2_%=\n\t.Lok2_%=:\ns_waitcnt lgkmcnt(6)\n\t" \
    "v_mfma_f32_32x32x16_bf16 v[64:79], v[208:211], v[176:179], v[64:79]\n\tds_read_b64_tr_b16 v[208:209], v242 offset:7168\n\tds_read_b64_tr_b16 v[210:211], v242 offset:7424\n\t" \
    "global_load_dword v160, %8, s[62:63] offset:-2048 sc1\n\tglobal_load_dword v161, %8, s[62:63] offset:-1920 sc1\n\t" \
    "v_mfma_f32_32x32x16_bf16 v[64:79], v[212:215], v[180:183], v[64:79]\n\tds_read_b64_tr_b16 v[212:213], v242 offset:10240\n\tds_read_b64_tr_b16 v[214:215], v242 offset:10496\n\t" \
    "global_load_dword v162, %8, s[62:63] offset:-1792 sc1\n\tglobal_load_dword v163, %8, s[62:63] offset:-1664 sc1\n\t" \
    "v_mfma_f32_32x32x16_bf16 v[64:79], v[216:219], v[184:187], v[64:79]\n\tds_read_b64_tr_b16 v[216:217], v242 offset:2048\n\tds_read_b64_tr_b16 v[218:219], v242 offset:2304\n\t" \
    "global_load_dword v164, %8, s[62:63] offset:-1024 sc1\n\tglobal_load_dword v165, %8, s[62:63] offset:-896 sc1\n\ts_waitcnt lgkmcnt(6)\n\t" \
    "v_mfma_f32_32x32x16_bf16 v[64:79], v[220:223], v[188:191], v[64:79]\n\tds_read_b64_tr_b16 v[220:221], v242 offset:5120\n\tds_read_b64_tr_b16 v[222:223], v242 offset:5376\n\t" \
    "global_load_dword v166, %8, s[62:63] offset:-768 sc1\n\tglobal_load_dword v167, %8, s[62:63] offset:-640 sc1\n\t" \
    "v_mfma_f32_32x32x16_bf16 v[80:95], v[224:227], v[176:179], v[80:95]\n\tds_read_b64_tr_b16 v[224:225], v242 offset:8192\n\tds_read_b64_tr_b16 v[226:227], v242 offset:8448\n\t" \
    "global_load_dword v168, %8, s[62:63] offset:0 sc1\n\tglobal_load_dword v169, %8, s[62:63] offset:128 sc1\n\tv_mad_u32_u24 v240, s47, 3, %2\n\t" \
    "v_mfma_f32_32x32x16_bf16 v[80:95], v[228:231], v[180:183], v[80:95]\n\tds_read_b64_tr_b16 v[228:229], v242 offset:11264\n\tds_read_b64_tr_b16 v[230:231], v242 offset:11520\n\t" \
    "global_load_dword v170, %8, s[62:63] offset:256 sc1\n\tglobal_load_dword v171, %8, s[62:63] offset:384 sc1\n\ts_waitcnt lgkmcnt(6)\n\t" \
    "v_mfma_f32_32x32x16_bf16 v[80:95], v[208:211], v[184:187], v[80:95]\n\tds_read_b64_tr_b16 v[208:209], v240 offset:0\n\tds_read_b64_tr_b16 v[210:211], v240 offset:256\n\t" \
    "global_load_dword v172, %8, s[62:63] offset:1024 sc1\n\tglobal_load_dword v173, %8, s[62:63] offset:1152 sc1\n\t" \
    "v_mfma_f32_32x32x16_bf16 v[80:95], v[212:215], v[188:191], v[80:95]\n\tds_read_b64_tr_b16 v[212:213], v240 offset:3072\n\tds_read_b64_tr_b16 v[214:215], v240 offset:3328\n\t" \
    "global_load_dword v174, %8, s[62:63] offset:1280 sc1\n\tglobal_load_dword v175, %8, s[62:63] offset:1408 sc1\n\t" \
    "v_mfma_f32_32x32x16_bf16 v[96:111], v[216:219], v[176:179], v[96:111]\n\tds_read_b64_tr_b16 v[216:217], v240 offset:6144\n\tds_read_b64_tr_b16 v[218:219], v240 offset:6400\n\t" \
    "s_waitcnt lgkmcnt(6)\n\tv_mfma_f32_32x32x16_bf16 v[96:111], v[220:223], v[180:183], v[96:111]\n\tds_read_b64_tr_b16 v[220:221], v240 offset:9216\n\t" \
    "ds_read_b64_tr_b16 v[222:223], v240 offset:9472\n\tv_mfma_f32_32x32x16_bf16 v[96:111], v[224:227], v[184:187], v[96:111]\n\tds_read_b64_tr_b16 v[224:225], v240 offset:1024\n\t" \
    "ds_read_b64_tr_b16 v[226:227], v240 offset:1280\n\tv_mfma_f32_32x32x16_bf16 v[96:111], v[228:231], v[188:191], v[96:111]\n\tds_read_b64_tr_b16 v[228:229], v240 offset:4096\n\t" \
    "ds_read_b64_tr_b16 v[230:231], v240 offset:4352\n\ts_waitcnt vmcnt(32)\n\t.Lredo3_%=:\nv_max3_u32 v244, v192, v193, v194\n\tv_max3_u32 v243, v195, v196, v197\n\t" \
    "v_max3_u32 v244, v244, v198, v199\n\tv_max3_u32 v243, v243, v200, v201\n\tv_max3_u32 v244, v244, v202, v203\n\tv_max3_u32 v243, v243, v204, v205\n\t" \
    "v_max3_u32 v244, v244, v206, v207\n\tv_max_u32 v243, v243, v244\n\tv_cmp_eq_u32 vcc, -1, v243\n\ts_cbranch_vccnz .Lslow3_%=\n\t.Lok3_%=:\ns_waitcnt lgkmcnt(6)\n\t" \
    "v_mfma_f32_32x32x16_bf16 v[64:79], v[208:211], v[192:195], v[64:79]\n\tds_read_b64_tr_b16 v[208:209], v240 offset:7168\n\tds_read_b64_tr_b16 v[210:211], v240 offset:7424\n\t" \
    "global_load_dword v176, %8, s[64:65] offset:-2048 sc1\n\tglobal_load_dword v177, %8, s[64:65] offset:-1920 sc1\n\t" \
    "v_mfma_f32_32x32x16_bf16 v[64:79], v[212:215], v[196:199], v[64:79]\n\tds_read_b64_tr_b16 v[212:213], v240 offset:10240\n\tds_read_b64_tr_b16 v[214:215], v240 offset:10496\n\t" \
    "global_load_dword v178, %8, s[64:65] offset:-1792 sc1\n\tglobal_load_dword v179, %8, s[64:65] offset:-1664 sc1\n\t" \
    "v_mfma_f32_32x32x16_bf16 v[64:79], v[216:219], v[200:203], v[64:79]\n\tds_read_b64_tr_b16 v[216:217], v240 offset:2048\n\tds_read_b64_tr_b16 v[218:219], v240 offset:2304\n\t" \
    "global_load_dword v180, %8, s[64:65] offset:-1024 sc1\n\tglobal_load_dword v181, %8, s[64:65] offset:-896 sc1\n\ts_waitcnt lgkmcnt(6)\n\t" \
    "v_mfma_f32_32x32x16_bf16 v[64:79], v[220:223], v[204:207], v[64:79]\n\tds_read_b64_tr_b16 v[220:221], v240 offset:5120\n\tds_read_b64_tr_b16 v[222:223], v240 offset:5376\n\t" \
    "global_load_dword v182, %8, s[64:65] offset:-768 sc1\n\tglobal_load_dword v183, %8, s[64:65] offset:-640 sc1\n\t" \
    "v_mfma_f32_32x32x16_bf16 v[80:95], v[224:227], v[192:195], v[80:95]\n\tds_read_b64_tr_b16 v[224:225], v240 offset:8192\n\tds_read_b64_tr_b16 v[226:227], v240 offset:8448\n\t" \
    "global_load_dword v184, %8, s[64:65] offset:0 sc1\n\tglobal_load_dword v185, %8, s[64:65] offset:128 sc1\n\tv_mad_u32_u24 v242, s48, 3, %2\n\t" \
    "v_mfma_f32_32x32x16_bf16 v[80:95], v[228:231], v[196:199], v[80:95]\n\tds_read_b64_tr_b16 v[228:229], v240 offset:11264\n\tds_read_b64_tr_b16 v[230:231], v240 offset:11520\n\t" \
    "global_load_dword v186, %8, s[64:65] offset:256 sc1\n\tglobal_load_dword v187, %8, s[64:65] offset:384 sc1\n\ts_waitcnt lgkmcnt(6)\n\t" \
    "v_mfma_f32_32x32x16_bf16 v[80:95], v[208:211], v[200:203], v[80:95]\n\tds_read_b64_tr_b16 v[208:209], v242 offset:0\n\tds_read_b64_tr_b16 v[210:211], v242 offset:256\n\t" \
    "global_load_dword v188, %8, s[64:65] offset:1024 sc1\n\tglobal_load_dword v189, %8, s[64:65] offset:1152 sc1\n\t" \
    "v_mfma_f32_32x32x16_bf16 v[80:95], v[212:215], v[204:207], v[80:95]\n\tds_read_b64_tr_b16 v[212:213], v242 offset:3072\n\tds_read_b64_tr_b16 v[214:215], v242 offset:3328\n\t" \
    "global_load_dword v190, %8, s[64:65] offset:1280 sc1\n\tglobal_load_dword v191, %8, s[64:65] offset:1408 sc1\n\t" \
    "v_mfma_f32_32x32x16_bf16 v[96:111], v[216:219], v[192:195], v[96:111]\n\tds_read_b64_tr_b16 v[216:217], v242 offset:6144\n\tds_read_b64_tr_b16 v[218:219], v242 offset:6400\n\t" \
    "s_waitcnt lgkmcnt(6)\n\tv_mfma_f32_32x32x16_bf16 v[96:111], v[220:223], v[196:199], v[96:111]\n\tds_read_b64_tr_b16 v[220:221], v242 offset:9216\n\t" \
    "ds_read_b64_tr_b16 v[222:223], v242 offset:9472\n\tv_mfma_f32_32x32x16_bf16 v[96:111], v[224:227], v[200:203], v[96:111]\n\tds_read_b64_tr_b16 v[224:225], v242 offset:1024\n\t" \
    "ds_read_b64_tr_b16 v[226:227], v242 offset:1280\n\tv_mfma_f32_32x32x16_bf16 v[96:111], v[228:231], v[204:207], v[96:111]\n\tds_read_b64_tr_b16 v[228:229], v242 offset:4096\n\t" \
    "ds_read_b64_tr_b16 v[230:231], v242 offset:4352\n\ts_waitcnt vmcnt(32)\n\t.Lredo4_%=:\nv_max3_u32 v244, v144, v145, v146\n\tv_max3_u32 v243, v147, v148, v149\n\t" \
    "v_max3_u32 v244, v244, v150, v151\n\tv_max3_u32 v243, v243, v152, v153\n\tv_max3_u32 v244, v244, v154, v155\n\tv_max3_u32 v243, v243, v156, v157\n\t" \
    "v_max3_u32 v244, v244, v158, v159\n\tv_max_u32 v243, v243, v244\n\tv_cmp_eq_u32 vcc, -1, v243\n\ts_cbranch_vccnz .Lslow4_%=\n\t.Lok4_%=:\ns_waitcnt lgkmcnt(6)\n\t" \
    "v_mfma_f32_32x32x16_bf16 v[64:79], v[208:211], v[144:147], v[64:79]\n\tds_read_b64_tr_b16 v[208:209], v242 offset:7168\n\tds_read_b64_tr_b16 v[210:211], v242 offset:7424\n\t" \
    "global_load_dword v192, %8, s[66:67] offset:-2048 sc1\n\tglobal_load_dword v193, %8, s[66:67] offset:-1920 sc1\n\t" \
    "v_mfma_f32_32x32x16_bf16 v[64:79], v[212:215], v[148:151], v[64:79]\n\tds_read_b64_tr_b16 v[212:213], v242 offset:10240\n\tds_read_b64_tr_b16 v[214:215], v242 offset:10496\n\t" \
    "global_load_dword v194, %8, s[66:67] offset:-1792 sc1\n\tglobal_load_dword v195, %8, s[66:67] offset:-1664 sc1\n\t" \
    "v_mfma_f32_32x32x16_bf16 v[64:79], v[216:219], v[152:155], v[64:79]\n\tds_read_b64_tr_b16 v[216:217], v242 offset:2048\n\tds_read_b64_tr_b16 v[218:219], v242 offset:2304\n\t" \
    "global_load_dword v196, %8, s[66:67] offset:-1024 sc1\n\tglobal_load_dword v197, %8, s[66:67] offset:-896 sc1\n\ts_waitcnt lgkmcnt(6)\n\t" \
    "v_mfma_f32_32x32x16_bf16 v[64:79], v[220:223], v[156:159], v[64:79]\n\tds_read_b64_tr_b16 v[220:221], v242 offset:5120\n\tds_read_b64_tr_b16 v[222:223], v242 offset:5376\n\t" \
    "global_load_dword v198, %8, s[66:67] offset:-768 sc1\n\tglobal_load_dword v199, %8, s[66:67] offset:-640 sc1\n\t" \
    "v_mfma_f32_32x32x16_bf16 v[80:95], v[224:227], v[144:147], v[80:95]\n\tds_read_b64_tr_b16 v[224:225], v242 offset:8192\n\tds_read_b64_tr_b16 v[226:227], v242 offset:8448\n\t" \
    "global_load_dword v200, %8, s[66:67] offset:0 sc1\n\tglobal_load_dword v201, %8, s[66:67] offset:128 sc1\n\tv_mad_u32_u24 v240, s49, 3, %2\n\t" \
    "v_mfma_f32_32x32x16_bf16 v[80:95], v[228:231], v[148:151], v[80:95]\n\tds_read_b64_tr_b16 v[228:229], v242 offset:11264\n\tds_read_b64_tr_b16 v[230:231], v242 offset:11520\n\t" \
    "global_load_dword v202, %8, s[66:67] offset:256 sc1\n\tglobal_load_dword v203, %8, s[66:67] offset:384 sc1\n\ts_waitcnt lgkmcnt(6)\n\t" \
    "v_mfma_f32_32x32x16_bf16 v[80:95], v[208:211], v[152:155], v[80:95]\n\tds_read_b64_tr_b16 v[208:209], v240 offset:0\n\tds_read_b64_tr_b16 v[210:211], v240 offset:256\n\t" \
    "global_load_dword v204, %8, s[66:67] offset:1024 sc1\n\tglobal_load_dword v205, %8, s[66:67] offset:1152 sc1\n\t" \
    "v_mfma_f32_32x32x16_bf16 v[80:95], v[212:215], v[156:159], v[80:95]\n\tds_read_b64_tr_b16 v[212:213], v240 offset:3072\n\tds_read_b64_tr_b16 v[214:215], v240 offset:3328\n\t" \
    "global_load_dword v206, %8, s[66:67] offset:1280 sc1\n\tglobal_load_dword v207, %8, s[66:67] offset:1408 sc1\n\t" \
    "v_mfma_f32_32x32x16_bf16 v[96:111], v[216:219], v[144:147], v[96:111]\n\tds_read_b64_tr_b16 v[216:217], v240 offset:6144\n\tds_read_b64_tr_b16 v[218:219], v240 offset:6400\n\t" \
    "s_waitcnt lgkmcnt(6)\n\tv_mfma_f32_32x32x16_bf16 v[96:111], v[220:223], v[148:151], v[96:111]\n\tds_read_b64_tr_b16 v[220:221], v240 offset:9216\n\t" \
    "ds_read_b64_tr_b16 v[222:223], v240 offset:9472\n\tv_mfma_f32_32x32x16_bf16 v[96:111], v[224:227], v[152:155], v[96:111]\n\tds_read_b64_tr_b16 v[224:225], v240 offset:1024\n\t" \
    "ds_read_b64_tr_b16 v[226:227], v240 offset:1280\n\tv_mfma_f32_32x32x16_bf16 v[96:111], v[228:231], v[156:159], v[96:111]\n\tds_read_b64_tr_b16 v[228:229], v240 offset:4096\n\t" \
    "ds_read_b64_tr_b16 v[230:231], v240 offset:4352\n\ts_waitcnt vmcnt(32)\n\t.Lredo5_%=:\nv_max3_u32 v244, v160, v161, v162\n\tv_max3_u32 v243, v163, v164, v165\n\t" \
    "v_max3_u32 v244, v244, v166, v167\n\tv_max3_u32 v243, v243, v168, v169\n\tv_max3_u32 v244, v244, v170, v171\n\tv_max3_u32 v243, v243, v172, v173\n\t" \
    "v_max3_u32 v244, v244, v174, v175\n\tv_max_u32 v243, v243, v244\n\tv_cmp_eq_u32 vcc, -1, v243\n\ts_cbranch_vccnz .Lslow5_%=\n\t.Lok5_%=:\ns_waitcnt lgkmcnt(6)\n\t" \
    "v_mfma_f32_32x32x16_bf16 v[64:79], v[208:211], v[160:163], v[64:79]\n\tds_read_b64_tr_b16 v[208:209], v240 offset:7168\n\tds_read_b64_tr_b16 v[210:211], v240 offset:7424\n\t" \
    "v_mfma_f32_32x32x16_bf16 v[64:79], v[212:215], v[164:167], v[64:79]\n\tds_read_b64_tr_b16 v[212:213], v240 offset:10240\n\tds_read_b64_tr_b16 v[214:215], v240 offset:10496\n\t" \
    "v_mfma_f32_32x32x16_bf16 v[64:79], v[216:219], v[168:171], v[64:79]\n\tds_read_b64_tr_b16 v[216:217], v240 offset:2048\n\tds_read_b64_tr_b16 v[218:219], v240 offset:2304\n\t" \
    "s_waitcnt lgkmcnt(6)\n\tv_mfma_f32_32x32x16_bf16 v[64:79], v[220:223], v[172:175], v[64:79]\n\tds_read_b64_tr_b16 v[220:221], v240 offset:5120\n\t" \
    "ds_read_b64_tr_b16 v[222:223], v240 offset:5376\n\tv_mfma_f32_32x32x16_bf16 v[80:95], v[224:227], v[160:163], v[80:95]\n\tds_read_b64_tr_b16 v[224:225], v240 offset:8192\n\t" \
    "ds_read_b64_tr_b16 v[226:227], v240 offset:8448\n\tv_mad_u32_u24 v242, s50, 3, %2\n\tv_mfma_f32_32x32x16_bf16 v[80:95], v[228:231], v[164:167], v[80:95]\n\t" \
    "ds_read_b64_tr_b16 v[228:229], v240 offset:11264\n\tds_read_b64_tr_b16 v[230:231], v240 offset:11520\n\ts_waitcnt lgkmcnt(6)\n\t" \
    "v_mfma_f32_32x32x16_bf16 v[80:95], v[208:211], v[168:171], v[80:95]\n\tds_read_b64_tr_b16 v[208:209], v242 offset:0\n\tds_read_b64_tr_b16 v[210:211], v242 offset:256\n\t" \
    "v_mfma_f32_32x32x16_bf16 v[80:95], v[212:215], v[172:175], v[80:95]\n\tds_read_b64_tr_b16 v[212:213], v242 offset:3072\n\tds_read_b64_tr_b16 v[214:215], v242 offset:3328\n\t" \
    "v_mfma_f32_32x32x16_bf16 v[96:111], v[216:219], v[160:163], v[96:111]\n\tds_read_b64_tr_b16 v[216:217], v242 offset:6144\n\tds_read_b64_tr_b16 v[218:219], v242 offset:6400\n\t" \
    "s_waitcnt lgkmcnt(6)\n\tv_mfma_f32_32x32x16_bf16 v[96:111], v[220:223], v[164:167], v[96:111]\n\tds_read_b64_tr_b16 v[220:221], v242 offset:9216\n\t" \
    "ds_read_b64_tr_b16 v[222:223], v242 offset:9472\n\tv_mfma_f32_32x32x16_bf16 v[96:111], v[224:227], v[168:171], v[96:111]\n\tds_read_b64_tr_b16 v[224:225], v242 offset:1024\n\t" \
    "ds_read_b64_tr_b16 v[226:227], v242 offset:1280\n\tv_mfma_f32_32x32x16_bf16 v[96:111], v[228:231], v[172:175], v[96:111]\n\tds_read_b64_tr_b16 v[228:229], v242 offset:4096\n\t" \
    "ds_read_b64_tr_b16 v[230:231], v242 offset:4352\n\ts_waitcnt vmcnt(16)\n\t.Lredo6_%=:\nv_max3_u32 v244, v176, v177, v178\n\tv_max3_u32 v243, v179, v180, v181\n\t" \
    "v_max3_u32 v244, v244, v182, v183\n\tv_max3_u32 v243, v243, v184, v185\n\tv_max3_u32 v244, v244, v186, v187\n\tv_max3_u32 v243, v243, v188, v189\n\t" \
    "v_max3_u32 v244, v244, v190, v191\n\tv_max_u32 v243, v243, v244\n\tv_cmp_eq_u32 vcc, -1, v243\n\ts_cbranch_vccnz .Lslow6_%=\n\t.Lok6_%=:\n" \
    "global_load_dword v112, %10, %11 offset:0 sc1\n\tglobal_load_dword v113, %10, %11 offset:256 sc1\n\tglobal_load_dword v114, %10, %11 offset:512 sc1\n\t" \
    "global_load_dword v115, %10, %11 offset:768 sc1\n\tglobal_load_dword v116, %10, %11 offset:1024 sc1\n\tglobal_load_dword v117, %10, %11 offset:1280 sc1\n\t" \
    "global_load_dword v118, %10, %11 offset:1536 sc1\n\tglobal_load_dword v119, %10, %11 offset:1792 sc1\n\ts_waitcnt lgkmcnt(6)\n\t" \
    "v_mfma_f32_32x32x16_bf16 v[64:79], v[208:211], v[176:179], v[64:79]\n\tds_read_b64_tr_b16 v[208:209], v242 offset:7168\n\tds_read_b64_tr_b16 v[210:211], v242 offset:7424\n\t" \
    "v_mfma_f32_32x32x16_bf16 v[64:79], v[212:215], v[180:183], v[64:79]\n\tds_read_b64_tr_b16 v[212:213], v242 offset:10240\n\tds_read_b64_tr_b16 v[214:215], v242 offset:10496\n\t" \
    "v_mov_b32 v243, 3\n\ts_mov_b64 exec, 1\n\tds_write_b32 %4, v243 offset:48\n\ts_mov_b64 exec, -1\n\tv_mfma_f32_32x32x16_bf16 v[64:79], v[216:219], v[184:187], v[64:79]\n\t" \
    "ds_read_b64_tr_b16 v[216:217], v242 offset:2048\n\tds_read_b64_tr_b16 v[218:219], v242 offset:2304\n\ts_waitcnt lgkmcnt(6)\n\t" \
    "v_mfma_f32_32x32x16_bf16 v[64:79], v[220:223], v[188:191], v[64:79]\n\tds_read_b64_tr_b16 v[220:221], v242 offset:5120\n\tds_read_b64_tr_b16 v[222:223], v242 offset:5376\n\t" \
    "v_mfma_f32_32x32x16_bf16 v[80:95], v[224:227], v[176:179], v[80:95]\n\tds_read_b64_tr_b16 v[224:225], v242 offset:8192\n\tds_read_b64_tr_b16 v[226:227], v242 offset:8448\n\t" \
    "v_mad_u32_u24 v240, s51, 3, %2\n\tv_mfma_f32_32x32x16_bf16 v[80:95], v[228:231], v[180:183], v[80:95]\n\tds_read_b64_tr_b16 v[228:229], v242 offset:11264\n\t" \
    "ds_read_b64_tr_b16 v[230:231], v242 offset:11520\n\ts_waitcnt lgkmcnt(6)\n\tv_mfma_f32_32x32x16_bf16 v[80:95], v[208:211], v[184:187], v[80:95]\n\t" \
    "ds_read_b64_tr_b16 v[208:209], v240 offset:0\n\tds_read_b64_tr_b16 v[210:211], v240 offset:256\n\tv_mfma_f32_32x32x16_bf16 v[80:95], v[212:215], v[188:191], v[80:95]\n\t" \
    "ds_read_b64_tr_b16 v[212:213], v240 offset:3072\n\tds_read_b64_tr_b16 v[214:215], v240 offset:3328\n\tv_mfma_f32_32x32x16_bf16 v[96:111], v[216:219], v[176:179], v[96:111]\n\t" \
    "ds_read_b64_tr_b16 v[216:217], v240 offset:6144\n\tds_read_b64_tr_b16 v[218:219], v240 offset:6400\n\ts_waitcnt lgkmcnt(6)\n\t" \
    "v_mfma_f32_32x32x16_bf16 v[96:111], v[220:223], v[180:183], v[96:111]\n\tds_read_b64_tr_b16 v[220:221], v240 offset:9216\n\tds_read_b64_tr_b16 v[222:223], v240 offset:9472\n\t" \
    "v_mfma_f32_32x32x16_bf16 v[96:111], v[224:227], v[184:187], v[96:111]\n\tds_read_b64_tr_b16 v[224:225], v240 offset:1024\n\tds_read_b64_tr_b16 v[226:227], v240 offset:1280\n\t" \
    "v_mfma_f32_32x32x16_bf16 v[96:111], v[228:231], v[188:191], v[96:111]\n\tds_read_b64_tr_b16 v[228:229], v240 offset:4096\n\tds_read_b64_tr_b16 v[230:231], v240 offset:4352\n\t" \
    "s_waitcnt vmcnt(8)\n\t.Lredo7_%=:\nv_max3_u32 v244, v192, v193, v194\n\tv_max3_u32 v243, v195, v196, v197\n\tv_max3_u32 v244, v244, v198, v199\n\t" \
    "v_max3_u32 v243, v243, v200, v201\n\tv_max3_u32 v244, v244, v202, v203\n\tv_max3_u32 v243, v243, v204, v205\n\tv_max3_u32 v244, v244, v206, v207\n\t" \
    "v_max_u32 v243, v243, v244\n\tv_cmp_eq_u32 vcc, -1, v243\n\ts_cbranch_vccnz .Lslow7_%=\n\t.Lok7_%=:\ns_waitcnt lgkmcnt(6)\n\t" \
    "v_mfma_f32_32x32x16_bf16 v[64:79], v[208:211], v[192:195], v[64:79]\n\tds_read_b64_tr_b16 v[208:209], v240 offset:7168\n\tds_read_b64_tr_b16 v[210:211], v240 offset:7424\n\t" \
    "v_mfma_f32_32x32x16_bf16 v[64:79], v[212:215], v[196:199], v[64:79]\n\tds_read_b64_tr_b16 v[212:213], v240 offset:10240\n\tds_read_b64_tr_b16 v[214:215], v240 offset:10496\n\t" \
    "s_waitcnt vmcnt(0)\n\t.Lredop_%=:\nv_add_f32 v138, v112, v113\n\tv_add_f32 v139, v114, v115\n\tv_add_f32 v244, v116, v117\n\tv_add_f32 v243, v118, v119\n\t" \
    "v_add_f32 v138, v138, v139\n\tv_add_f32 v244, v244, v243\n\tv_add_f32 v139, v138, v244\n\tv_add_f32 v138, v138, v244\n\tv_cmp_u_f32 vcc, v138, v138\n\t" \
    "s_cbranch_vccnz .Lslowp_%=\n\t.Lokp_%=:\nv_mfma_f32_32x32x16_bf16 v[64:79], v[216:219], v[200:203], v[64:79]\n\tds_read_b64_tr_b16 v[216:217], v240 offset:2048\n\t" \
    "ds_read_b64_tr_b16 v[218:219], v240 offset:2304\n\ts_waitcnt lgkmcnt(6)\n\tv_mfma_f32_32x32x16_bf16 v[64:79], v[220:223], v[204:207], v[64:79]\n\t" \
    "ds_read_b64_tr_b16 v[220:221], v240 offset:5120\n\tds_read_b64_tr_b16 v[222:223], v240 offset:5376\n\tv_permlane32_swap_b32 v138, v139\n\tv_mul_f32 v138, s81, v138\n\t" \
    "v_mul_f32 v112, v138, v138\n\tv_mfma_f32_32x32x16_bf16 v[80:95], v[224:227], v[192:195], v[80:95]\n\tds_read_b64_tr_b16 v[224:225], v240 offset:8192\n\t" \
    "ds_read_b64_tr_b16 v[226:227], v240 offset:8448\n\tv_fma_f32 v139, v139, s81, -v112\n\tv_max_f32 v139, 0, v139\n\tv_cvt_pk_bf16_f32 v139, 0, v139\n\t" \
    "v_mfma_f32_32x32x16_bf16 v[80:95], v[228:231], v[196:199], v[80:95]\n\tds_read_b64_tr_b16 v[228:229], v240 offset:11264\n\tds_read_b64_tr_b16 v[230:231], v240 offset:11520\n\t" \
    "v_add_f32 v139, 0x3727c5ac, v139\n\tv_cvt_pk_bf16_f32 v139, 0, v139\n\ts_waitcnt lgkmcnt(8)\n\tv_mfma_f32_32x32x16_bf16 v[80:95], v[208:211], v[200:203], v[80:95]\n\t" \
    "v_sqrt_f32 v139, v139\n\tv_cvt_pk_bf16_f32 v138, -v138, -v138\n\tv_mfma_f32_32x32x16_bf16 v[80:95], v[212:215], v[204:207], v[80:95]\n\t" \
    "v_cndmask_b32 v120, 0, v138, s[84:85]\n\tv_cvt_pk_bf16_f32 v139, 0, v139\n\tv_rcp_f32 v136, v139\n\ts_waitcnt lgkmcnt(0)\n\tv_mov_b32 v137, v136\n\t" \
    "v_mfma_f32_32x32x16_bf16 v[64:79], v[124:127], v[120:123], v[64:79]\n\tv_mfma_f32_32x32x16_bf16 v[80:95], v[128:131], v[120:123], v[80:95]\n\t" \
    "v_mfma_f32_32x32x16_bf16 v[96:111], v[216:219], v[192:195], v[96:111]\n\ts_nop 9\n\tv_mul_f32 v64, v64, v136\n\tv_mul_f32 v65, v65, v136\n\tv_mul_f32 v66, v66, v136\n\t" \
    "v_mul_f32 v67, v67, v136\n\tv_cvt_pk_bf16_f32 v64, v64, v65\n\tv_cvt_pk_bf16_f32 v65, v66, v67\n\tds_write_b64 %6, v[64:65] offset:0\n\t" \
    "v_mfma_f32_32x32x16_bf16 v[96:111], v[220:223], v[196:199], v[96:111]\n\tv_mul_f32 v68, v68, v136\n\tv_mul_f32 v69, v69, v136\n\tv_mul_f32 v70, v70, v136\n\t" \
    "v_mul_f32 v71, v71, v136\n\tv_cvt_pk_bf16_f32 v68, v68, v69\n\tv_cvt_pk_bf16_f32 v69, v70, v71\n\tds_write_b64 %6, v[68:69] offset:16\n\t" \
    "v_mfma_f32_32x32x16_bf16 v[96:111], v[224:227], v[200:203], v[96:111]\n\tv_mul_f32 v72, v72, v136\n\tv_mul_f32 v73, v73, v136\n\tv_mul_f32 v74, v74, v136\n\t" \
    "v_mul_f32 v75, v75, v136\n\tv_cvt_pk_bf16_f32 v72, v72, v73\n\tv_cvt_pk_bf16_f32 v73, v74, v75\n\tds_write_b64 %6, v[72:73] offset:32\n\t" \
    "v_mfma_f32_32x32x16_bf16 v[96:111], v[228:231], v[204:207], v[96:111]\n\tv_mul_f32 v76, v76, v136\n\tv_mul_f32 v77, v77, v136\n\tv_mul_f32 v78, v78, v136\n\t" \
    "v_mul_f32 v79, v79, v136\n\tv_cvt_pk_bf16_f32 v76, v76, v77\n\tv_cvt_pk_bf16_f32 v77, v78, v79\n\tds_write_b64 %6, v[76:77] offset:48\n\t" \
    "v_mfma_f32_32x32x16_bf16 v[96:111], v[132:135], v[120:123], v[96:111]\n\tv_mul_f32 v80, v80, v136\n\tv_mul_f32 v81, v81, v136\n\tv_mul_f32 v82, v82, v136\n\t" \
    "v_mul_f32 v83, v83, v136\n\tv_cvt_pk_bf16_f32 v80, v80, v81\n\tv_cvt_pk_bf16_f32 v81, v82, v83\n\tds_write_b64 %6, v[80:81] offset:64\n\tv_mul_f32 v84, v84, v136\n\t" \
    "v_mul_f32 v85, v85, v136\n\tv_mul_f32 v86, v86, v136\n\tv_mul_f32 v87, v87, v136\n\tv_cvt_pk_bf16_f32 v84, v84, v85\n\tv_cvt_pk_bf16_f32 v85, v86, v87\n\t" \
    "ds_write_b64 %6, v[84:85] offset:80\n\tv_mul_f32 v88, v88, v136\n\tv_mul_f32 v89, v89, v136\n\tv_mul_f32 v90, v90, v136\n\tv_mul_f32 v91, v91, v136\n\t" \
    "v_cvt_pk_bf16_f32 v88, v88, v89\n\tv_cvt_pk_bf16_f32 v89, v90, v91\n\tds_write_b64 %6, v[88:89] offset:96\n\tv_mul_f32 v92, v92, v136\n\tv_mul_f32 v93, v93, v136\n\t" \
    "v_mul_f32 v94, v94, v136\n\tv_mul_f32 v95, v95, v136\n\tv_cvt_pk_bf16_f32 v92, v92, v93\n\tv_cvt_pk_bf16_f32 v93, v94, v95\n\tds_write_b64 %6, v[92:93] offset:112\n\t" \
    "v_pk_mul_f32 v[96:97], v[96:97], v[136:137]\n\tv_pk_mul_f32 v[98:99], v[98:99], v[136:137]\n\tv_cvt_pk_bf16_f32 v96, v96, v97\n\tv_cvt_pk_bf16_f32 v97, v98, v99\n\t" \
    "ds_write_b64 %6, v[96:97] offset:128\n\tv_pk_mul_f32 v[100:101], v[100:101], v[136:137]\n\tv_pk_mul_f32 v[102:103], v[102:103], v[136:137]\n\t" \
    "v_cvt_pk_bf16_f32 v100, v100, v101\n\tv_cvt_pk_bf16_f32 v101, v102, v103\n\tds_write_b64 %6, v[100:101] offset:144\n\tv_pk_mul_f32 v[104:105], v[104:105], v[136:137]\n\t" \
    "v_pk_mul_f32 v[106:107], v[106:107], v[136:137]\n\tv_cvt_pk_bf16_f32 v104, v104, v105\n\tv_cvt_pk_bf16_f32 v105, v106, v107\n\tds_write_b64 %6, v[104:105] offset:160\n\t" \
    "v_pk_mul_f32 v[108:109], v[108:109], v[136:137]\n\tv_pk_mul_f32 v[110:111], v[110:111], v[136:137]\n\tv_cvt_pk_bf16_f32 v108, v108, v109\n\t" \
    "v_cvt_pk_bf16_f32 v109, v110, v111\n\tds_write_b64 %6, v[108:109] offset:176\n\ts_mov_b32 %1, s80\n\ts_branch .Lend_%=\n\t.Lslow0_%=:\nv_max3_u32 v243, v144, v145, v146\n\t" \
    "v_max3_u32 v243, v243, v147, v148\n\tv_max3_u32 v243, v243, v149, v150\n\tv_max3_u32 v243, v243, v151, v152\n\tv_max3_u32 v243, v243, v153, v154\n\t" \
    "v_max3_u32 v243, v243, v155, v156\n\tv_max3_u32 v243, v243, v157, v158\n\tv_max_u32 v243, v243, v159\n\tv_cmp_eq_u32 vcc, -1, v243\n\ts_cbranch_vccz .Lok0_%=\n\t" \
    "s_sub_u32 s80, s80, 1\n\ts_cbranch_scc1 .Lok0_%=\n\ts_sleep 2\n\tglobal_load_dword v144, %8, s[52:53] offset:-2048 sc1\n\t" \
    "global_load_dword v145, %8, s[52:53] offset:-1920 sc1\n\tglobal_load_dword v146, %8, s[52:53] offset:-1792 sc1\n\t" \
    "global_load_dword v147, %8, s[52:53] offset:-1664 sc1\n\tglobal_load_dword v148, %8, s[52:53] offset:-1024 sc1\n\t" \
    "global_load_dword v149, %8, s[52:53] offset:-896 sc1\n\tglobal_load_dword v150, %8, s[52:53] offset:-768 sc1\n\t" \
    "global_load_dword v151, %8, s[52:53] offset:-640 sc1\n\tglobal_load_dword v152, %8, s[52:53] offset:0 sc1\n\t" \
    "global_load_dword v153, %8, s[52:53] offset:128 sc1\n\tglobal_load_dword v154, %8, s[52:53] offset:256 sc1\n\t" \
    "global_load_dword v155, %8, s[52:53] offset:384 sc1\n\tglobal_load_dword v156, %8, s[52:53] offset:1024 sc1\n\t" \
    "global_load_dword v157, %8, s[52:53] offset:1152 sc1\n\tglobal_load_dword v158, %8, s[52:53] offset:1280 sc1\n\t" \
    "global_load_dword v159, %8, s[52:53] offset:1408 sc1\n\ts_waitcnt vmcnt(0)\n\ts_branch .Lredo0_%=\n\t.Lslow1_%=:\ns_sub_u32 s80, s80, 1\n\t" \
    "s_cbranch_scc1 .Lok1_%=\n\ts_sleep 2\n\tglobal_load_dword v160, %8, s[54:55] offset:-2048 sc1\n\tglobal_load_dword v161, %8, s[54:55] offset:-1920 sc1\n\t" \
    "global_load_dword v162, %8, s[54:55] offset:-1792 sc1\n\tglobal_load_dword v163, %8, s[54:55] offset:-1664 sc1\n\t" \
    "global_load_dword v164, %8, s[54:55] offset:-1024 sc1\n\tglobal_load_dword v165, %8, s[54:55] offset:-896 sc1\n\t" \
    "global_load_dword v166, %8, s[54:55] offset:-768 sc1\n\tglobal_load_dword v167, %8, s[54:55] offset:-640 sc1\n\t" \
    "global_load_dword v168, %8, s[54:55] offset:0 sc1\n\tglobal_load_dword v169, %8, s[54:55] offset:128 sc1\n\t" \
    "global_load_dword v170, %8, s[54:55] offset:256 sc1\n\tglobal_load_dword v171, %8, s[54:55] offset:384 sc1\n\t" \
    "global_load_dword v172, %8, s[54:55] offset:1024 sc1\n\tglobal_load_dword v173, %8, s[54:55] offset:1152 sc1\n\t" \
    "global_load_dword v174, %8, s[54:55] offset:1280 sc1\n\tglobal_load_dword v175, %8, s[54:55] offset:1408 sc1\n\ts_waitcnt vmcnt(0)\n\t" \
    "s_branch .Lredo1_%=\n\t.Lslow2_%=:\ns_sub_u32 s80, s80, 1\n\ts_cbranch_scc1 .Lok2_%=\n\ts_sleep 2\n\tglobal_load_dword v176, %8, s[56:57] offset:-2048 sc1\n\t" \
    "global_load_dword v177, %8, s[56:57] offset:-1920 sc1\n\tglobal_load_dword v178, %8, s[56:57] offset:-1792 sc1\n\t" \
    "global_load_dword v179, %8, s[56:57] offset:-1664 sc1\n\tglobal_load_dword v180, %8, s[56:57] offset:-1024 sc1\n\t" \
    "global_load_dword v181, %8, s[56:57] offset:-896 sc1\n\tglobal_load_dword v182, %8, s[56:57] offset:-768 sc1\n\t" \
    "global_load_dword v183, %8, s[56:57] offset:-640 sc1\n\tglobal_load_dword v184, %8, s[56:57] offset:0 sc1\n\t" \
    "global_load_dword v185, %8, s[56:57] offset:128 sc1\n\tglobal_load_dword v186, %8, s[56:57] offset:256 sc1\n\t" \
    "global_load_dword v187, %8, s[56:57] offset:384 sc1\n\tglobal_load_dword v188, %8, s[56:57] offset:1024 sc1\n\t" \
    "global_load_dword v189, %8, s[56:57] offset:1152 sc1\n\tglobal_load_dword v190, %8, s[56:57] offset:1280 sc1\n\t" \
    "global_load_dword v191, %8, s[56:57] offset:1408 sc1\n\ts_waitcnt vmcnt(0)\n\ts_branch .Lredo2_%=\n\t.Lslow3_%=:\ns_sub_u32 s80, s80, 1\n\t" \
    "s_cbranch_scc1 .Lok3_%=\n\ts_sleep 2\n\tglobal_load_dword v192, %8, s[58:59] offset:-2048 sc1\n\tglobal_load_dword v193, %8, s[58:59] offset:-1920 sc1\n\t" \
    "global_load_dword v194, %8, s[58:59] offset:-1792 sc1\n\tglobal_load_dword v195, %8, s[58:59] offset:-1664 sc1\n\t" \
    "global_load_dword v196, %8, s[58:59] offset:-1024 sc1\n\tglobal_load_dword v197, %8, s[58:59] offset:-896 sc1\n\t" \
    "global_load_dword v198, %8, s[58:59] offset:-768 sc1\n\tglobal_load_dword v199, %8, s[58:59] offset:-640 sc1\n\t" \
    "global_load_dword v200, %8, s[58:59] offset:0 sc1\n\tglobal_load_dword v201, %8, s[58:59] offset:128 sc1\n\t" \
    "global_load_dword v202, %8, s[58:59] offset:256 sc1\n\tglobal_load_dword v203, %8, s[58:59] offset:384 sc1\n\t" \
    "global_load_dword v204, %8, s[58:59] offset:1024 sc1\n\tglobal_load_dword v205, %8, s[58:59] offset:1152 sc1\n\t" \
    "global_load_dword v206, %8, s[58:59] offset:1280 sc1\n\tglobal_load_dword v207, %8, s[58:59] offset:1408 sc1\n\ts_waitcnt vmcnt(0)\n\t" \
    "s_branch .Lredo3_%=\n\t.Lslow4_%=:\ns_sub_u32 s80, s80, 1\n\ts_cbranch_scc1 .Lok4_%=\n\ts_sleep 2\n\tglobal_load_dword v144, %8, s[60:61] offset:-2048 sc1\n\t" \
    "global_load_dword v145, %8, s[60:61] offset:-1920 sc1\n\tglobal_load_dword v146, %8, s[60:61] offset:-1792 sc1\n\t" \
    "global_load_dword v147, %8, s[60:61] offset:-1664 sc1\n\tglobal_load_dword v148, %8, s[60:61] offset:-1024 sc1\n\t" \
    "global_load_dword v149, %8, s[60:61] offset:-896 sc1\n\tglobal_load_dword v150, %8, s[60:61] offset:-768 sc1\n\t" \
    "global_load_dword v151, %8, s[60:61] offset:-640 sc1\n\tglobal_load_dword v152, %8, s[60:61] offset:0 sc1\n\t" \
    "global_load_dword v153, %8, s[60:61] offset:128 sc1\n\tglobal_load_dword v154, %8, s[60:61] offset:256 sc1\n\t" \
    "global_load_dword v155, %8, s[60:61] offset:384 sc1\n\tglobal_load_dword v156, %8, s[60:61] offset:1024 sc1\n\t" \
    "global_load_dword v157, %8, s[60:61] offset:1152 sc1\n\tglobal_load_dword v158, %8, s[60:61] offset:1280 sc1\n\t" \
    "global_load_dword v159, %8, s[60:61] offset:1408 sc1\n\ts_waitcnt vmcnt(0)\n\ts_branch .Lredo4_%=\n\t.Lslow5_%=:\ns_sub_u32 s80, s80, 1\n\t" \
    "s_cbranch_scc1 .Lok5_%=\n\ts_sleep 2\n\tglobal_load_dword v160, %8, s[62:63] offset:-2048 sc1\n\tglobal_load_dword v161, %8, s[62:63] offset:-1920 sc1\n\t" \
    "global_load_dword v162, %8, s[62:63] offset:-1792 sc1\n\tglobal_load_dword v163, %8, s[62:63] offset:-1664 sc1\n\t" \
    "global_load_dword v164, %8, s[62:63] offset:-1024 sc1\n\tglobal_load_dword v165, %8, s[62:63] offset:-896 sc1\n\t" \
    "global_load_dword v166, %8, s[62:63] offset:-768 sc1\n\tglobal_load_dword v167, %8, s[62:63] offset:-640 sc1\n\t" \
    "global_load_dword v168, %8, s[62:63] offset:0 sc1\n\tglobal_load_dword v169, %8, s[62:63] offset:128 sc1\n\t" \
    "global_load_dword v170, %8, s[62:63] offset:256 sc1\n\tglobal_load_dword v171, %8, s[62:63] offset:384 sc1\n\t" \
    "global_load_dword v172, %8, s[62:63] offset:1024 sc1\n\tglobal_load_dword v173, %8, s[62:63] offset:1152 sc1\n\t" \
    "global_load_dword v174, %8, s[62:63] offset:1280 sc1\n\tglobal_load_dword v175, %8, s[62:63] offset:1408 sc1\n\ts_waitcnt vmcnt(0)\n\t" \
    "s_branch .Lredo5_%=\n\t.Lslow6_%=:\ns_sub_u32 s80, s80, 1\n\ts_cbranch_scc1 .Lok6_%=\n\ts_sleep 2\n\tglobal_load_dword v176, %8, s[64:65] offset:-2048 sc1\n\t" \
    "global_load_dword v177, %8, s[64:65] offset:-1920 sc1\n\tglobal_load_dword v178, %8, s[64:65] offset:-1792 sc1\n\t" \
    "global_load_dword v179, %8, s[64:65] offset:-1664 sc1\n\tglobal_load_dword v180, %8, s[64:65] offset:-1024 sc1\n\t" \
    "global_load_dword v181, %8, s[64:65] offset:-896 sc1\n\tglobal_load_dword v182, %8, s[64:65] offset:-768 sc1\n\t" \
    "global_load_dword v183, %8, s[64:65] offset:-640 sc1\n\tglobal_load_dword v184, %8, s[64:65] offset:0 sc1\n\t" \
    "global_load_dword v185, %8, s[64:65] offset:128 sc1\n\tglobal_load_dword v186, %8, s[64:65] offset:256 sc1\n\t" \
    "global_load_dword v187, %8, s[64:65] offset:384 sc1\n\tglobal_load_dword v188, %8, s[64:65] offset:1024 sc1\n\t" \
    "global_load_dword v189, %8, s[64:65] offset:1152 sc1\n\tglobal_load_dword v190, %8, s[64:65] offset:1280 sc1\n\t" \
    "global_load_dword v191, %8, s[64:65] offset:1408 sc1\n\ts_waitcnt vmcnt(0)\n\ts_branch .Lredo6_%=\n\t.Lslow7_%=:\ns_sub_u32 s80, s80, 1\n\t" \
    "s_cbranch_scc1 .Lok7_%=\n\ts_sleep 2\n\tglobal_load_dword v192, %8, s[66:67] offset:-2048 sc1\n\tglobal_load_dword v193, %8, s[66:67] offset:-1920 sc1\n\t" \
    "global_load_dword v194, %8, s[66:67] offset:-1792 sc1\n\tglobal_load_dword v195, %8, s[66:67] offset:-1664 sc1\n\t" \
    "global_load_dword v196, %8, s[66:67] offset:-1024 sc1\n\tglobal_load_dword v197, %8, s[66:67] offset:-896 sc1\n\t" \
    "global_load_dword v198, %8, s[66:67] offset:-768 sc1\n\tglobal_load_dword v199, %8, s[66:67] offset:-640 sc1\n\t" \
    "global_load_dword v200, %8, s[66:67] offset:0 sc1\n\tglobal_load_dword v201, %8, s[66:67] offset:128 sc1\n\t" \
    "global_load_dword v202, %8, s[66:67] offset:256 sc1\n\tglobal_load_dword v203, %8, s[66:67] offset:384 sc1\n\t" \
    "global_load_dword v204, %8, s[66:67] offset:1024 sc1\n\tglobal_load_dword v205, %8, s[66:67] offset:1152 sc1\n\t" \
    "global_load_dword v206, %8, s[66:67] offset:1280 sc1\n\tglobal_load_dword v207, %8, s[66:67] offset:1408 sc1\n\ts_waitcnt vmcnt(0)\n\t" \
    "s_branch .Lredo7_%=\n\t.Lslowp_%=:\nv_max3_u32 v243, v112, v113, v114\n\tv_max3_u32 v243, v243, v115, v116\n\tv_max3_u32 v243, v243, v117, v118\n\t" \
    "v_max_u32 v243, v243, v119\n\tv_cmp_eq_u32 vcc, -1, v243\n\ts_cbranch_vccz .Lokp_%=\n\ts_sub_u32 s80, s80, 1\n\ts_cbranch_scc1 .Lokp_%=\n\t" \
    "global_load_dword v112, %10, %11 offset:0 sc1\n\tglobal_load_dword v113, %10, %11 offset:256 sc1\n\tglobal_load_dword v114, %10, %11 offset:512 sc1\n\t" \
    "global_load_dword v115, %10, %11 offset:768 sc1\n\tglobal_load_dword v116, %10, %11 offset:1024 sc1\n\t" \
    "global_load_dword v117, %10, %11 offset:1280 sc1\n\tglobal_load_dword v118, %10, %11 offset:1536 sc1\n\t" \
    "global_load_dword v119, %10, %11 offset:1792 sc1\n\ts_waitcnt vmcnt(0)\n\ts_branch .Lredop_%=\n\t.Lend_%=:\n"
#define GEMM2_CLOB "v64", "v65", "v66", "v67", "v68", "v69", "v70", "v71", "v72", "v73", "v74", "v75", "v76", "v77", "v78", "v79", "v80", "v81", "v82", "v83", "v84", "v85", "v86", "v87", "v88", "v89", "v90", "v91", "v92", "v93", "v94", "v95", "v96", "v97", "v98", "v99", "v100", "v101", "v102", "v103", "v104", "v105", "v106", "v107", "v108", "v109", "v110", "v111", "v112", "v113", "v114", "v115", "v116", "v117", "v118", "v119", "v120", "v121", "v122", "v123", "v124", "v125", "v126", "v127", "v128", "v129", "v130", "v131", "v132", "v133", "v134", "v135", "v136", "v137", "v138", "v139", "v144", "v145", "v146", "v147", "v148", "v149", "v150", "v151", "v152", "v153", "v154", "v155", "v156", "v157", "v158", "v159", "v160", "v161", "v162", "v163", "v164", "v165", "v166", "v167", "v168", "v169", "v170", "v171", "v172", "v173", "v174", "v175", "v176", "v177", "v178", "v179", "v180", "v181", "v182", "v183", "v184", "v185", "v186", "v187", "v188", "v189", "v190", "v191", "v192", "v193", "v194", "v195", "v196", "v197", "v198", "v199", "v200", "v201", "v202", "v203", "v204", "v205", "v206", "v207", "v208", "v209", "v210", "v211", "v212", "v213", "v214", "v215", "v216", "v217", "v218", "v219", "v220", "v221", "v222", "v223", "v224", "v225", "v226", "v227", "v228", "v229", "v230", "v231", "v240", "v242", "v243", "v244", "v245", "s44", "s45", "s46", "s47", "s48", "s49", "s50", "s51", "s52", "s53", "s54", "s55", "s56", "s57", "s58", "s59", "s60", "s61", "s62", "s63", "s64", "s65", "s66", "s67", "s68", "s69", "s70", "s71", "s72", "s73", "s74", "s75", "s76", "s77", "s78", "s79", "s80", "s81", "s82", "s83", "s84", "s85", "s86", "s87", "vcc", "scc", "memory"

#define CONVG \
    "s_cmp_eq_u32 %23, 0\n\ts_cbranch_scc0 5f\n\tds_read_b128 v[220:223], %9 offset:1392\n\tds_read_b64 v[48:49], %12 offset:0\n\tds_read_b128 v[50:53], %11 offset:0\n\t" \
    "ds_read_b64 v[54:55], %13 offset:0\n\tds_read_b128 v[224:227], %9 offset:1360\n\tds_read_b128 v[228:231], %9 offset:1312\n\tds_read_b128 v[232:235], %9 offset:1280\n\t" \
    "ds_read_b64 v[56:57], %12 offset:1280\n\tds_read_b128 v[58:61], %11 offset:1280\n\tds_read_b64 v[62:63], %13 offset:1280\n\ts_waitcnt lgkmcnt(9)\n\t" \
    "v_mfma_f32_32x32x16_f16 v[204:219], v[64:67], v[220:223], 0\n\tds_read_b128 v[220:223], %9 offset:1232\n\ts_waitcnt lgkmcnt(7)\n\tv_alignbit_b32 v236, v50, v49, 16\n\t" \
    "v_alignbit_b32 v237, v51, v50, 16\n\tv_alignbit_b32 v238, v52, v51, 16\n\tv_alignbit_b32 v239, v53, v52, 16\n\tv_alignbit_b32 v240, v54, v53, 16\n\ts_waitcnt lgkmcnt(6)\n\t" \
    "v_mfma_f32_32x32x16_f16 v[204:219], v[68:71], v[224:227], v[204:219]\n\tds_read_b128 v[224:227], %9 offset:1200\n\tv_pk_add_f16 v64, v49, v236\n\t" \
    "v_pk_add_f16 v65, v50, v237\n\tv_pk_add_f16 v66, v51, v238\n\tv_pk_add_f16 v67, v52, v239\n\tv_pk_add_f16 v68, v53, v240\n\ts_waitcnt lgkmcnt(6)\n\t" \
    "v_mfma_f32_32x32x16_f16 v[204:219], v[72:75], v[228:231], v[204:219]\n\tds_read_b128 v[228:231], %9 offset:1152\n\tv_pk_add_f16 v72, v236, v65\n\t" \
    "v_pk_add_f16 v73, v237, v66\n\tv_pk_add_f16 v74, v238, v67\n\tv_pk_add_f16 v75, v239, v68\n\tds_write_b64 %14, v[72:73] offset:4040\n\ts_waitcnt lgkmcnt(7)\n\t" \
    "v_mfma_f32_32x32x16_f16 v[204:219], v[76:79], v[232:235], v[204:219]\n\tds_read_b128 v[232:235], %9 offset:1120\n\tv_pk_add_f16 v76, v64, v51\n\tv_pk_add_f16 v77, v65, v52\n\t" \
    "v_pk_add_f16 v78, v66, v53\n\tv_pk_add_f16 v79, v67, v54\n\tds_write_b64 %15, v[74:75] offset:4032\n\ts_waitcnt lgkmcnt(5)\n\t" \
    "v_mfma_f32_32x32x16_f16 v[204:219], v[80:83], v[220:223], v[204:219]\n\tds_read_b128 v[220:223], %9 offset:1072\n\tds_read_b64 v[48:49], %12 offset:2560\n\t" \
    "ds_read_b128 v[50:53], %11 offset:2560\n\tds_read_b64 v[54:55], %13 offset:2560\n\tv_pk_add_f16 v76, v76, v65\n\tv_pk_add_f16 v77, v77, v66\n\ts_waitcnt lgkmcnt(8)\n\t" \
    "v_mfma_f32_32x32x16_f16 v[204:219], v[84:87], v[224:227], v[204:219]\n\tds_read_b128 v[224:227], %9 offset:1040\n\tv_pk_add_f16 v78, v78, v67\n\tv_pk_add_f16 v79, v79, v68\n\t" \
    "ds_write_b128 %14, v[76:79] offset:0\n\tv_alignbit_b32 v236, v58, v57, 16\n\tv_alignbit_b32 v237, v59, v58, 16\n\ts_waitcnt lgkmcnt(9)\n\t" \
    "v_mfma_f32_32x32x16_f16 v[204:219], v[88:91], v[228:231], v[204:219]\n\tds_read_b128 v[228:231], %9 offset:992\n\tv_alignbit_b32 v238, v60, v59, 16\n\t" \
    "v_alignbit_b32 v239, v61, v60, 16\n\tv_alignbit_b32 v240, v62, v61, 16\n\tv_pk_add_f16 v64, v57, v236\n\tv_pk_add_f16 v65, v58, v237\n\ts_waitcnt lgkmcnt(8)\n\t" \
    "v_mfma_f32_32x32x16_f16 v[204:219], v[92:95], v[232:235], v[204:219]\n\tds_read_b128 v[232:235], %9 offset:960\n\tv_pk_add_f16 v66, v59, v238\n\t" \
    "v_pk_add_f16 v67, v60, v239\n\tv_pk_add_f16 v68, v61, v240\n\tv_pk_add_f16 v72, v236, v65\n\tv_pk_add_f16 v73, v237, v66\n\ts_waitcnt lgkmcnt(7)\n\t" \
    "v_mfma_f32_32x32x16_f16 v[204:219], v[96:99], v[220:223], v[204:219]\n\tds_read_b128 v[220:223], %9 offset:912\n\tv_pk_add_f16 v74, v238, v67\n\t" \
    "v_pk_add_f16 v75, v239, v68\n\tds_write_b64 %14, v[72:73] offset:5192\n\tv_pk_add_f16 v76, v64, v59\n\tv_pk_add_f16 v77, v65, v60\n\ts_waitcnt lgkmcnt(5)\n\t" \
    "v_mfma_f32_32x32x16_f16 v[204:219], v[100:103], v[224:227], v[204:219]\n\tds_read_b128 v[224:227], %9 offset:880\n\tv_pk_add_f16 v78, v66, v61\n\t" \
    "v_pk_add_f16 v79, v67, v62\n\tds_write_b64 %15, v[74:75] offset:5184\n\tv_pk_add_f16 v76, v76, v65\n\tv_pk_add_f16 v77, v77, v66\n\ts_waitcnt lgkmcnt(5)\n\t" \
    "v_mfma_f32_32x32x16_f16 v[204:219], v[104:107], v[228:231], v[204:219]\n\tds_read_b128 v[228:231], %9 offset:832\n\tv_pk_add_f16 v78, v78, v67\n\t" \
    "v_pk_add_f16 v79, v79, v68\n\tds_write_b128 %14, v[76:79] offset:1152\n\tv_alignbit_b32 v236, v50, v49, 16\n\tv_alignbit_b32 v237, v51, v50, 16\n\ts_waitcnt lgkmcnt(6)\n\t" \
    "v_mfma_f32_32x32x16_f16 v[204:219], v[108:111], v[232:235], v[204:219]\n\tds_read_b128 v[232:235], %9 offset:800\n\tv_alignbit_b32 v238, v52, v51, 16\n\t" \
    "v_alignbit_b32 v239, v53, v52, 16\n\tv_alignbit_b32 v240, v54, v53, 16\n\tv_pk_add_f16 v64, v49, v236\n\tv_pk_add_f16 v65, v50, v237\n\ts_waitcnt lgkmcnt(6)\n\t" \
    "v_mfma_f32_32x32x16_f16 v[204:219], v[112:115], v[220:223], v[204:219]\n\tds_read_b128 v[220:223], %9 offset:752\n\tv_pk_add_f16 v66, v51, v238\n\t" \
    "v_pk_add_f16 v67, v52, v239\n\tv_pk_add_f16 v68, v53, v240\n\tv_pk_add_f16 v72, v236, v65\n\tv_pk_add_f16 v73, v237, v66\n\ts_waitcnt lgkmcnt(5)\n\t" \
    "v_mfma_f32_32x32x16_f16 v[204:219], v[116:119], v[224:227], v[204:219]\n\tds_read_b128 v[224:227], %9 offset:720\n\tv_pk_add_f16 v74, v238, v67\n\t" \
    "v_pk_add_f16 v75, v239, v68\n\tds_write_b64 %16, v[72:73] offset:6344\n\tv_pk_add_f16 v76, v64, v51\n\tv_pk_add_f16 v77, v65, v52\n\ts_waitcnt lgkmcnt(5)\n\t" \
    "v_mfma_f32_32x32x16_f16 v[204:219], v[120:123], v[228:231], v[204:219]\n\tds_read_b128 v[228:231], %9 offset:672\n\tv_pk_add_f16 v78, v66, v53\n\t" \
    "v_pk_add_f16 v79, v67, v54\n\tds_write_b64 %17, v[74:75] offset:6336\n\tv_pk_add_f16 v76, v76, v65\n\tv_pk_add_f16 v77, v77, v66\n\ts_waitcnt lgkmcnt(5)\n\t" \
    "v_mfma_f32_32x32x16_f16 v[204:219], v[124:127], v[232:235], v[204:219]\n\tds_read_b128 v[232:235], %9 offset:640\n\tv_pk_add_f16 v78, v78, v67\n\t" \
    "v_pk_add_f16 v79, v79, v68\n\tds_write_b128 %16, v[76:79] offset:2304\n\ts_mov_b64 exec, 1\n\tds_write_b32 %19, %20 offset:80\n\ts_mov_b64 exec, -1\n\ts_waitcnt lgkmcnt(7)\n\t" \
    "v_mfma_f32_32x32x16_f16 v[204:219], v[128:131], v[220:223], v[204:219]\n\tds_read_b128 v[220:223], %9 offset:592\n\tv_mul_f32 v244, %21, v244\n\tv_mul_f32 v245, %22, v245\n\t" \
    "v_mul_f32 v246, %21, v246\n\tv_mul_f32 v247, %22, v247\n\tv_mul_f32 v248, %21, v248\n\ts_waitcnt lgkmcnt(7)\n\t" \
    "v_mfma_f32_32x32x16_f16 v[204:219], v[132:135], v[224:227], v[204:219]\n\tds_read_b128 v[224:227], %9 offset:560\n\tv_mul_f32 v249, %22, v249\n\tv_mul_f32 v250, %21, v250\n\t" \
    "v_mul_f32 v251, %22, v251\n\tv_cvt_pk_bf16_f32 v244, 0, v244\n\tv_cvt_pk_bf16_f32 v245, 0, v245\n\ts_waitcnt lgkmcnt(6)\n\t" \
    "v_mfma_f32_32x32x16_f16 v[204:219], v[136:139], v[228:231], v[204:219]\n\tds_read_b128 v[228:231], %9 offset:512\n\tv_cvt_pk_bf16_f32 v246, 0, v246\n\t" \
    "v_cvt_pk_bf16_f32 v247, 0, v247\n\tv_cvt_pk_bf16_f32 v248, 0, v248\n\tv_cvt_pk_bf16_f32 v249, 0, v249\n\tv_cvt_pk_bf16_f32 v250, 0, v250\n\ts_waitcnt lgkmcnt(5)\n\t" \
    "v_mfma_f32_32x32x16_f16 v[204:219], v[140:143], v[232:235], v[204:219]\n\tds_read_b128 v[232:235], %9 offset:480\n\tv_cvt_pk_bf16_f32 v251, 0, v251\n\t" \
    "v_xor_b32 v238, 32, %10\n\tv_xor_b32 v239, 64, %10\n\tv_xor_b32 v240, 0x60, %10\n\ts_waitcnt lgkmcnt(3)\n\t" \
    "v_mfma_f32_32x32x16_f16 v[204:219], v[144:147], v[220:223], v[204:219]\n\tds_read_b128 v[220:223], %9 offset:432\n\ts_waitcnt lgkmcnt(3)\n\t" \
    "v_mfma_f32_32x32x16_f16 v[204:219], v[148:151], v[224:227], v[204:219]\n\tds_read_b128 v[224:227], %9 offset:400\n\ts_waitcnt lgkmcnt(3)\n\t" \
    "v_mfma_f32_32x32x16_f16 v[204:219], v[152:155], v[228:231], v[204:219]\n\tds_read_b128 v[228:231], %9 offset:352\n\ts_waitcnt lgkmcnt(3)\n\t" \
    "v_mfma_f32_32x32x16_f16 v[204:219], v[156:159], v[232:235], v[204:219]\n\tds_read_b128 v[232:235], %9 offset:320\n\ts_waitcnt lgkmcnt(3)\n\t" \
    "v_mfma_f32_32x32x16_f16 v[204:219], v[160:163], v[220:223], v[204:219]\n\tds_read_b128 v[220:223], %9 offset:272\n\ts_waitcnt lgkmcnt(3)\n\t" \
    "v_mfma_f32_32x32x16_f16 v[204:219], v[164:167], v[224:227], v[204:219]\n\tds_read_b128 v[224:227], %9 offset:240\n\ts_waitcnt lgkmcnt(3)\n\t" \
    "v_mfma_f32_32x32x16_f16 v[204:219], v[168:171], v[228:231], v[204:219]\n\tds_read_b128 v[228:231], %9 offset:192\n\ts_waitcnt lgkmcnt(3)\n\t" \
    "v_mfma_f32_32x32x16_f16 v[204:219], v[172:175], v[232:235], v[204:219]\n\tds_read_b128 v[232:235], %9 offset:160\n\ts_waitcnt lgkmcnt(3)\n\t" \
    "v_mfma_f32_32x32x16_f16 v[204:219], v[176:179], v[220:223], v[204:219]\n\tds_read_b128 v[220:223], %9 offset:112\n\ts_waitcnt lgkmcnt(3)\n\t" \
    "v_mfma_f32_32x32x16_f16 v[204:219], v[180:183], v[224:227], v[204:219]\n\tds_read_b128 v[224:227], %9 offset:80\n\ts_waitcnt lgkmcnt(3)\n\t" \
    "v_mfma_f32_32x32x16_f16 v[204:219], v[184:187], v[228:231], v[204:219]\n\tds_read_b128 v[228:231], %9 offset:32\n\ts_waitcnt lgkmcnt(3)\n\t" \
    "v_mfma_f32_32x32x16_f16 v[204:219], v[188:191], v[232:235], v[204:219]\n\ts_waitcnt lgkmcnt(2)\n\tv_mfma_f32_32x32x16_f16 v[204:219], v[192:195], v[220:223], v[204:219]\n\t" \
    "ds_read_b32 v241, %18\n\ts_waitcnt lgkmcnt(2)\n\tv_mfma_f32_32x32x16_f16 v[204:219], v[196:199], v[224:227], v[204:219]\n\ts_waitcnt lgkmcnt(1)\n\t" \
    "v_mfma_f32_32x32x16_f16 v[204:219], v[200:203], v[228:231], v[204:219]\n\ts_waitcnt lgkmcnt(0)\n\tv_cmp_gt_i32 vcc, %20, v241\n\ts_cbranch_vccz 2f\n\t" \
    "s_mov_b32 s86, 0x100000\n\t1:\ns_sleep 1\n\tds_read_b32 v241, %18\n\ts_waitcnt lgkmcnt(0)\n\tv_cmp_gt_i32 vcc, %20, v241\n\ts_sub_u32 s86, s86, 1\n\ts_cbranch_scc1 2f\n\t" \
    "s_cbranch_vccnz 1b\n\t2:\ns_nop 8\n\tds_write_b128 %10, v[204:207]\n\tds_write_b128 v238, v[208:211]\n\tds_write_b128 v239, v[212:215]\n\tds_write_b128 v240, v[216:219]\n\t" \
    "s_branch 6f\n\t5:\nds_read_b128 v[220:223], %9 offset:1392\n\tds_read_b64 v[48:49], %12 offset:0\n\tds_read_b128 v[50:53], %11 offset:0\n\t" \
    "ds_read_b64 v[54:55], %13 offset:0\n\tds_read_b128 v[224:227], %9 offset:1360\n\tds_read_b128 v[228:231], %9 offset:1312\n\tds_read_b128 v[232:235], %9 offset:1280\n\t" \
    "ds_read_b64 v[56:57], %12 offset:1280\n\tds_read_b128 v[58:61], %11 offset:1280\n\tds_read_b64 v[62:63], %13 offset:1280\n\ts_waitcnt lgkmcnt(9)\n\t" \
    "v_mfma_f32_32x32x16_f16 v[204:219], v[64:67], v[220:223], 0\n\tds_read_b128 v[220:223], %9 offset:1232\n\ts_waitcnt lgkmcnt(7)\n\tv_alignbit_b32 v236, v49, v48, 16\n\t" \
    "v_alignbit_b32 v237, v50, v49, 16\n\tv_alignbit_b32 v238, v51, v50, 16\n\tv_alignbit_b32 v239, v52, v51, 16\n\tv_alignbit_b32 v240, v53, v52, 16\n\ts_waitcnt lgkmcnt(6)\n\t" \
    "v_mfma_f32_32x32x16_f16 v[204:219], v[68:71], v[224:227], v[204:219]\n\tds_read_b128 v[224:227], %9 offset:1200\n\tv_alignbit_b32 v241, v54, v53, 16\n\t" \
    "v_alignbit_b32 v242, v55, v54, 16\n\tv_pk_add_f16 v64, v48, v236\n\tv_pk_add_f16 v65, v49, v237\n\tv_pk_add_f16 v66, v50, v238\n\ts_waitcnt lgkmcnt(6)\n\t" \
    "v_mfma_f32_32x32x16_f16 v[204:219], v[72:75], v[228:231], v[204:219]\n\tds_read_b128 v[228:231], %9 offset:1152\n\tv_pk_add_f16 v67, v51, v239\n\t" \
    "v_pk_add_f16 v68, v52, v240\n\tv_pk_add_f16 v69, v53, v241\n\tv_pk_add_f16 v70, v54, v242\n\tv_pk_add_f16 v71, v66, v67\n\ts_waitcnt lgkmcnt(6)\n\t" \
    "v_mfma_f32_32x32x16_f16 v[204:219], v[76:79], v[232:235], v[204:219]\n\tds_read_b128 v[232:235], %9 offset:1120\n\tv_pk_add_f16 v243, v68, v69\n\t" \
    "v_pk_add_f16 v76, v64, v52\n\tv_pk_add_f16 v77, v65, v53\n\tv_pk_add_f16 v78, v66, v54\n\tv_pk_add_f16 v79, v67, v55\n\ts_waitcnt lgkmcnt(3)\n\t" \
    "v_mfma_f32_32x32x16_f16 v[204:219], v[80:83], v[220:223], v[204:219]\n\tds_read_b128 v[220:223], %9 offset:1072\n\tv_pk_add_f16 v80, v65, v71\n\tv_pk_add_f16 v81, v71, v68\n\t" \
    "v_pk_add_f16 v82, v67, v243\n\tv_pk_add_f16 v83, v243, v70\n\tv_pk_add_f16 v72, v80, v236\n\ts_waitcnt lgkmcnt(3)\n\t" \
    "v_mfma_f32_32x32x16_f16 v[204:219], v[84:87], v[224:227], v[204:219]\n\tds_read_b128 v[224:227], %9 offset:1040\n\tv_pk_add_f16 v73, v81, v237\n\t" \
    "v_pk_add_f16 v74, v82, v238\n\tv_pk_add_f16 v75, v83, v239\n\tds_write_b128 %15, v[72:75] offset:0\n\tv_pk_add_f16 v76, v76, v80\n\ts_waitcnt lgkmcnt(4)\n\t" \
    "v_mfma_f32_32x32x16_f16 v[204:219], v[88:91], v[228:231], v[204:219]\n\tds_read_b128 v[228:231], %9 offset:992\n\tv_pk_add_f16 v77, v77, v81\n\tv_pk_add_f16 v78, v78, v82\n\t" \
    "v_pk_add_f16 v79, v79, v83\n\tds_write_b128 %14, v[76:79] offset:0\n\tv_alignbit_b32 v236, v57, v56, 16\n\ts_waitcnt lgkmcnt(5)\n\t" \
    "v_mfma_f32_32x32x16_f16 v[204:219], v[92:95], v[232:235], v[204:219]\n\tds_read_b128 v[232:235], %9 offset:960\n\tv_alignbit_b32 v237, v58, v57, 16\n\t" \
    "v_alignbit_b32 v238, v59, v58, 16\n\tv_alignbit_b32 v239, v60, v59, 16\n\tv_alignbit_b32 v240, v61, v60, 16\n\tv_alignbit_b32 v241, v62, v61, 16\n\ts_waitcnt lgkmcnt(5)\n\t" \
    "v_mfma_f32_32x32x16_f16 v[204:219], v[96:99], v[220:223], v[204:219]\n\tds_read_b128 v[220:223], %9 offset:912\n\tv_alignbit_b32 v242, v63, v62, 16\n\t" \
    "v_pk_add_f16 v64, v56, v236\n\tv_pk_add_f16 v65, v57, v237\n\tv_pk_add_f16 v66, v58, v238\n\tv_pk_add_f16 v67, v59, v239\n\ts_waitcnt lgkmcnt(5)\n\t" \
    "v_mfma_f32_32x32x16_f16 v[204:219], v[100:103], v[224:227], v[204:219]\n\tds_read_b128 v[224:227], %9 offset:880\n\tv_pk_add_f16 v68, v60, v240\n\t" \
    "v_pk_add_f16 v69, v61, v241\n\tv_pk_add_f16 v70, v62, v242\n\tv_pk_add_f16 v71, v66, v67\n\tv_pk_add_f16 v243, v68, v69\n\ts_waitcnt lgkmcnt(4)\n\t" \
    "v_mfma_f32_32x32x16_f16 v[204:219], v[104:107], v[228:231], v[204:219]\n\tds_read_b128 v[228:231], %9 offset:832\n\tv_pk_add_f16 v76, v64, v60\n\t" \
    "v_pk_add_f16 v77, v65, v61\n\tv_pk_add_f16 v78, v66, v62\n\tv_pk_add_f16 v79, v67, v63\n\tv_pk_add_f16 v80, v65, v71\n\ts_waitcnt lgkmcnt(3)\n\t" \
    "v_mfma_f32_32x32x16_f16 v[204:219], v[108:111], v[232:235], v[204:219]\n\tds_read_b128 v[232:235], %9 offset:800\n\tv_pk_add_f16 v81, v71, v68\n\t" \
    "v_pk_add_f16 v82, v67, v243\n\tv_pk_add_f16 v83, v243, v70\n\tv_pk_add_f16 v72, v80, v236\n\tv_pk_add_f16 v73, v81, v237\n\ts_waitcnt lgkmcnt(3)\n\t" \
    "v_mfma_f32_32x32x16_f16 v[204:219], v[112:115], v[220:223], v[204:219]\n\tds_read_b128 v[220:223], %9 offset:752\n\tv_pk_add_f16 v74, v82, v238\n\t" \
    "v_pk_add_f16 v75, v83, v239\n\tds_write_b128 %15, v[72:75] offset:1152\n\tv_pk_add_f16 v76, v76, v80\n\tv_pk_add_f16 v77, v77, v81\n\ts_waitcnt lgkmcnt(4)\n\t" \
    "v_mfma_f32_32x32x16_f16 v[204:219], v[116:119], v[224:227], v[204:219]\n\tds_read_b128 v[224:227], %9 offset:720\n\tv_pk_add_f16 v78, v78, v82\n\t" \
    "v_pk_add_f16 v79, v79, v83\n\tds_write_b128 %16, v[76:79] offset:1152\n\ts_mov_b64 exec, 1\n\tds_write_b32 %19, %20 offset:80\n\ts_mov_b64 exec, -1\n\ts_waitcnt lgkmcnt(6)\n\t" \
    "v_mfma_f32_32x32x16_f16 v[204:219], v[120:123], v[228:231], v[204:219]\n\tds_read_b128 v[228:231], %9 offset:672\n\tv_mul_f32 v244, %21, v244\n\tv_mul_f32 v245, %22, v245\n\t" \
    "v_mul_f32 v246, %21, v246\n\tv_mul_f32 v247, %22, v247\n\tv_mul_f32 v248, %21, v248\n\ts_waitcnt lgkmcnt(6)\n\t" \
    "v_mfma_f32_32x32x16_f16 v[204:219], v[124:127], v[232:235], v[204:219]\n\tds_read_b128 v[232:235], %9 offset:640\n\tv_mul_f32 v249, %22, v249\n\tv_mul_f32 v250, %21, v250\n\t" \
    "v_mul_f32 v251, %22, v251\n\tv_cvt_pk_bf16_f32 v244, 0, v244\n\tv_cvt_pk_bf16_f32 v245, 0, v245\n\ts_waitcnt lgkmcnt(6)\n\t" \
    "v_mfma_f32_32x32x16_f16 v[204:219], v[128:131], v[220:223], v[204:219]\n\tds_read_b128 v[220:223], %9 offset:592\n\tv_cvt_pk_bf16_f32 v246, 0, v246\n\t" \
    "v_cvt_pk_bf16_f32 v247, 0, v247\n\tv_cvt_pk_bf16_f32 v248, 0, v248\n\tv_cvt_pk_bf16_f32 v249, 0, v249\n\tv_cvt_pk_bf16_f32 v250, 0, v250\n\ts_waitcnt lgkmcnt(5)\n\t" \
    "v_mfma_f32_32x32x16_f16 v[204:219], v[132:135], v[224:227], v[204:219]\n\tds_read_b128 v[224:227], %9 offset:560\n\tv_cvt_pk_bf16_f32 v251, 0, v251\n\t" \
    "v_xor_b32 v238, 32, %10\n\tv_xor_b32 v239, 64, %10\n\tv_xor_b32 v240, 0x60, %10\n\ts_waitcnt lgkmcnt(3)\n\t" \
    "v_mfma_f32_32x32x16_f16 v[204:219], v[136:139], v[228:231], v[204:219]\n\tds_read_b128 v[228:231], %9 offset:512\n\ts_waitcnt lgkmcnt(3)\n\t" \
    "v_mfma_f32_32x32x16_f16 v[204:219], v[140:143], v[232:235], v[204:219]\n\tds_read_b128 v[232:235], %9 offset:480\n\ts_waitcnt lgkmcnt(3)\n\t" \
    "v_mfma_f32_32x32x16_f16 v[204:219], v[144:147], v[220:223], v[204:219]\n\tds_read_b128 v[220:223], %9 offset:432\n\ts_waitcnt lgkmcnt(3)\n\t" \
    "v_mfma_f32_32x32x16_f16 v[204:219], v[148:151], v[224:227], v[204:219]\n\tds_read_b128 v[224:227], %9 offset:400\n\ts_waitcnt lgkmcnt(3)\n\t" \
    "v_mfma_f32_32x32x16_f16 v[204:219], v[152:155], v[228:231], v[204:219]\n\tds_read_b128 v[228:231], %9 offset:352\n\ts_waitcnt lgkmcnt(3)\n\t" \
    "v_mfma_f32_32x32x16_f16 v[204:219], v[156:159], v[232:235], v[204:219]\n\tds_read_b128 v[232:235], %9 offset:320\n\ts_waitcnt lgkmcnt(3)\n\t" \
    "v_mfma_f32_32x32x16_f16 v[204:219], v[160:163], v[220:223], v[204:219]\n\tds_read_b128 v[220:223], %9 offset:272\n\ts_waitcnt lgkmcnt(3)\n\t" \
    "v_mfma_f32_32x32x16_f16 v[204:219], v[164:167], v[224:227], v[204:219]\n\tds_read_b128 v[224:227], %9 offset:240\n\ts_waitcnt lgkmcnt(3)\n\t" \
    "v_mfma_f32_32x32x16_f16 v[204:219], v[168:171], v[228:231], v[204:219]\n\tds_read_b128 v[228:231], %9 offset:192\n\ts_waitcnt lgkmcnt(3)\n\t" \
    "v_mfma_f32_32x32x16_f16 v[204:219], v[172:175], v[232:235], v[204:219]\n\tds_read_b128 v[232:235], %9 offset:160\n\ts_waitcnt lgkmcnt(3)\n\t" \
    "v_mfma_f32_32x32x16_f16 v[204:219], v[176:179], v[220:223], v[204:219]\n\tds_read_b128 v[220:223], %9 offset:112\n\ts_waitcnt lgkmcnt(3)\n\t" \
    "v_mfma_f32_32x32x16_f16 v[204:219], v[180:183], v[224:227], v[204:219]\n\tds_read_b128 v[224:227], %9 offset:80\n\ts_waitcnt lgkmcnt(3)\n\t" \
    "v_mfma_f32_32x32x16_f16 v[204:219], v[184:187], v[228:231], v[204:219]\n\tds_read_b128 v[228:231], %9 offset:32\n\ts_waitcnt lgkmcnt(3)\n\t" \
    "v_mfma_f32_32x32x16_f16 v[204:219], v[188:191], v[232:235], v[204:219]\n\ts_waitcnt lgkmcnt(2)\n\tv_mfma_f32_32x32x16_f16 v[204:219], v[192:195], v[220:223], v[204:219]\n\t" \
    "ds_read_b32 v241, %18\n\ts_waitcnt lgkmcnt(2)\n\tv_mfma_f32_32x32x16_f16 v[204:219], v[196:199], v[224:227], v[204:219]\n\ts_waitcnt lgkmcnt(1)\n\t" \
    "v_mfma_f32_32x32x16_f16 v[204:219], v[200:203], v[228:231], v[204:219]\n\ts_waitcnt lgkmcnt(0)\n\tv_cmp_gt_i32 vcc, %20, v241\n\ts_cbranch_vccz 2f\n\t" \
    "s_mov_b32 s86, 0x100000\n\t1:\ns_sleep 1\n\tds_read_b32 v241, %18\n\ts_waitcnt lgkmcnt(0)\n\tv_cmp_gt_i32 vcc, %20, v241\n\ts_sub_u32 s86, s86, 1\n\ts_cbranch_scc1 2f\n\t" \
    "s_cbranch_vccnz 1b\n\t2:\ns_nop 8\n\tds_write_b128 %10, v[204:207]\n\tds_write_b128 v238, v[208:211]\n\tds_write_b128 v239, v[212:215]\n\tds_write_b128 v240, v[216:219]\n\t" \
    "6:\n"
#define TAPSG \
    "ds_read_b64 v[64:65], %5 offset:2016\n\tds_read_b64 v[66:67], %5 offset:2024\n\tds_read_b64 v[68:69], %5 offset:1984\n\tds_read_b64 v[70:71], %5 offset:1992\n\t" \
    "ds_read_b64 v[72:73], %5 offset:1952\n\tds_read_b64 v[74:75], %5 offset:1960\n\tds_read_b64 v[76:77], %5 offset:1920\n\tds_read_b64 v[78:79], %5 offset:1928\n\t" \
    "ds_read_b64 v[80:81], %5 offset:1888\n\tds_read_b64 v[82:83], %5 offset:1896\n\tds_read_b64 v[84:85], %5 offset:1856\n\tds_read_b64 v[86:87], %5 offset:1864\n\t" \
    "ds_read_b64 v[88:89], %5 offset:1824\n\tds_read_b64 v[90:91], %5 offset:1832\n\tds_read_b64 v[92:93], %5 offset:1792\n\tds_read_b64 v[94:95], %5 offset:1800\n\t" \
    "ds_read_b64 v[96:97], %5 offset:1760\n\tds_read_b64 v[98:99], %5 offset:1768\n\tds_read_b64 v[100:101], %5 offset:1728\n\tds_read_b64 v[102:103], %5 offset:1736\n\t" \
    "ds_read_b64 v[104:105], %5 offset:1696\n\tds_read_b64 v[106:107], %5 offset:1704\n\tds_read_b64 v[108:109], %5 offset:1664\n\tds_read_b64 v[110:111], %5 offset:1672\n\t" \
    "ds_read_b64 v[112:113], %5 offset:1632\n\tds_read_b64 v[114:115], %5 offset:1640\n\tds_read_b64 v[116:117], %5 offset:1600\n\tds_read_b64 v[118:119], %5 offset:1608\n\t" \
    "ds_read_b64 v[120:121], %5 offset:1568\n\tds_read_b64 v[122:123], %5 offset:1576\n\tds_read_b64 v[124:125], %5 offset:1536\n\tds_read_b64 v[126:127], %5 offset:1544\n\t" \
    "ds_read_b64 v[128:129], %5 offset:1504\n\tds_read_b64 v[130:131], %5 offset:1512\n\tds_read_b64 v[132:133], %5 offset:1472\n\tds_read_b64 v[134:135], %5 offset:1480\n\t" \
    "ds_read_b64 v[136:137], %5 offset:1440\n\tds_read_b64 v[138:139], %5 offset:1448\n\tds_read_b64 v[140:141], %5 offset:1408\n\tds_read_b64 v[142:143], %5 offset:1416\n\t" \
    "ds_read_b64 v[144:145], %5 offset:1376\n\tds_read_b64 v[146:147], %5 offset:1384\n\tds_read_b64 v[148:149], %5 offset:1344\n\tds_read_b64 v[150:151], %5 offset:1352\n\t" \
    "ds_read_b64 v[152:153], %5 offset:1312\n\tds_read_b64 v[154:155], %5 offset:1320\n\tds_read_b64 v[156:157], %5 offset:1280\n\tds_read_b64 v[158:159], %5 offset:1288\n\t" \
    "ds_read_b64 v[160:161], %5 offset:1248\n\tds_read_b64 v[162:163], %5 offset:1256\n\tds_read_b64 v[164:165], %5 offset:1216\n\tds_read_b64 v[166:167], %5 offset:1224\n\t" \
    "ds_read_b64 v[168:169], %5 offset:1184\n\tds_read_b64 v[170:171], %5 offset:1192\n\tds_read_b64 v[172:173], %5 offset:1152\n\tds_read_b64 v[174:175], %5 offset:1160\n\t" \
    "ds_read_b64 v[176:177], %5 offset:1120\n\tds_read_b64 v[178:179], %5 offset:1128\n\tds_read_b64 v[180:181], %5 offset:1088\n\tds_read_b64 v[182:183], %5 offset:1096\n\t" \
    "ds_read_b64 v[184:185], %5 offset:1056\n\tds_read_b64 v[186:187], %5 offset:1064\n\tds_read_b64 v[188:189], %5 offset:1024\n\tds_read_b64 v[190:191], %5 offset:1032\n\t" \
    "ds_read_b64 v[192:193], %5 offset:992\n\tds_read_b64 v[194:195], %5 offset:1000\n\tds_read_b64 v[196:197], %5 offset:960\n\tds_read_b64 v[198:199], %5 offset:968\n\t" \
    "ds_read_b64 v[200:201], %5 offset:928\n\tds_read_b64 v[202:203], %5 offset:936\n\ts_waitcnt lgkmcnt(0)\n\t"
#define CONVG_CLOB "v48", "v49", "v50", "v51", "v52", "v53", "v54", "v55", "v56", "v57", "v58", "v59", "v60", "v61", "v62", "v63", "v208", "v209", "v210", "v211", "v212", "v213", "v214", "v215", "v216", "v217", "v218", "v219", "v220", "v221", "v222", "v223", "v224", "v225", "v226", "v227", "v228", "v229", "v230", "v231", "v232", "v233", "v234", "v235", "v236", "v237", "v238", "v239", "v240", "v241", "v242", "v243", "s86", "vcc", "scc", "memory"

#define LD9 \
    "global_load_dwordx4 v[240:243], %5, %6 sc1\n\tglobal_load_dwordx4 v[208:211], %4, %6 sc1\n\tv_add_u32 %2, 8192, %4\n\tglobal_load_dwordx4 v[212:215], %2, %6 sc1\n\t" \
    "v_add_u32 %2, 16384, %4\n\tglobal_load_dwordx4 v[216:219], %2, %6 sc1\n\tv_add_u32 %2, 24576, %4\n\tglobal_load_dwordx4 v[220:223], %2, %6 sc1\n\tv_add_u32 %2, 32768, %4\n\t" \
    "global_load_dwordx4 v[224:227], %2, %6 sc1\n\tv_add_u32 %2, 40960, %4\n\tglobal_load_dwordx4 v[228:231], %2, %6 sc1\n\tv_add_u32 %2, 49152, %4\n\t" \
    "global_load_dwordx4 v[232:235], %2, %6 sc1\n\tv_add_u32 %2, 57344, %4\n\tglobal_load_dwordx4 v[236:239], %2, %6 sc1\n\t"
#define POLL9 \
    "s_mov_b64 s[82:83], 0\n\tv_max3_u32 %2, v208, v209, v210\n\tv_max_u32 %2, %2, v211\n\tv_cmp_eq_u32 vcc, -1, %2\n\tv_add_u32 %2, 0, %4\n\ts_or_b64 s[82:83], s[82:83], vcc\n\t" \
    "s_cbranch_vccz 2f\n\ts_mov_b64 exec, vcc\n\tglobal_load_dwordx4 v[208:211], %2, %6 sc1\n\ts_mov_b64 exec, -1\n\t2:\nv_max3_u32 %2, v212, v213, v214\n\t" \
    "v_max_u32 %2, %2, v215\n\tv_cmp_eq_u32 vcc, -1, %2\n\tv_add_u32 %2, 8192, %4\n\ts_or_b64 s[82:83], s[82:83], vcc\n\ts_cbranch_vccz 2f\n\ts_mov_b64 exec, vcc\n\t" \
    "global_load_dwordx4 v[212:215], %2, %6 sc1\n\ts_mov_b64 exec, -1\n\t2:\nv_max3_u32 %2, v216, v217, v218\n\tv_max_u32 %2, %2, v219\n\tv_cmp_eq_u32 vcc, -1, %2\n\t" \
    "v_add_u32 %2, 16384, %4\n\ts_or_b64 s[82:83], s[82:83], vcc\n\ts_cbranch_vccz 2f\n\ts_mov_b64 exec, vcc\n\tglobal_load_dwordx4 v[216:219], %2, %6 sc1\n\t" \
    "s_mov_b64 exec, -1\n\t2:\nv_max3_u32 %2, v220, v221, v222\n\tv_max_u32 %2, %2, v223\n\tv_cmp_eq_u32 vcc, -1, %2\n\tv_add_u32 %2, 24576, %4\n\t" \
    "s_or_b64 s[82:83], s[82:83], vcc\n\ts_cbranch_vccz 2f\n\ts_mov_b64 exec, vcc\n\tglobal_load_dwordx4 v[220:223], %2, %6 sc1\n\ts_mov_b64 exec, -1\n\t2:\n" \
    "v_max3_u32 %2, v224, v225, v226\n\tv_max_u32 %2, %2, v227\n\tv_cmp_eq_u32 vcc, -1, %2\n\tv_add_u32 %2, 32768, %4\n\ts_or_b64 s[82:83], s[82:83], vcc\n\ts_cbranch_vccz 2f\n\t" \
    "s_mov_b64 exec, vcc\n\tglobal_load_dwordx4 v[224:227], %2, %6 sc1\n\ts_mov_b64 exec, -1\n\t2:\nv_max3_u32 %2, v228, v229, v230\n\tv_max_u32 %2, %2, v231\n\t" \
    "v_cmp_eq_u32 vcc, -1, %2\n\tv_add_u32 %2, 40960, %4\n\ts_or_b64 s[82:83], s[82:83], vcc\n\ts_cbranch_vccz 2f\n\ts_mov_b64 exec, vcc\n\t" \
    "global_load_dwordx4 v[228:231], %2, %6 sc1\n\ts_mov_b64 exec, -1\n\t2:\nv_max3_u32 %2, v232, v233, v234\n\tv_max_u32 %2, %2, v235\n\tv_cmp_eq_u32 vcc, -1, %2\n\t" \
    "v_add_u32 %2, 49152, %4\n\ts_or_b64 s[82:83], s[82:83], vcc\n\ts_cbranch_vccz 2f\n\ts_mov_b64 exec, vcc\n\tglobal_load_dwordx4 v[232:235], %2, %6 sc1\n\t" \
    "s_mov_b64 exec, -1\n\t2:\nv_max3_u32 %2, v236, v237, v238\n\tv_max_u32 %2, %2, v239\n\tv_cmp_eq_u32 vcc, -1, %2\n\tv_add_u32 %2, 57344, %4\n\t" \
    "s_or_b64 s[82:83], s[82:83], vcc\n\ts_cbranch_vccz 2f\n\ts_mov_b64 exec, vcc\n\tglobal_load_dwordx4 v[236:239], %2, %6 sc1\n\ts_mov_b64 exec, -1\n\t2:\n" \
    "v_max3_u32 %2, v240, v241, v242\n\tv_max_u32 %2, %2, v243\n\tv_cmp_eq_u32 vcc, -1, %2\n\tv_mov_b32 %2, %5\n\ts_or_b64 s[82:83], s[82:83], vcc\n\ts_cbranch_vccz 2f\n\t" \
    "s_mov_b64 exec, vcc\n\tglobal_load_dwordx4 v[240:243], %2, %6 sc1\n\ts_mov_b64 exec, -1\n\t2:\ns_cmp_lg_u64 s[82:83], 0\n\ts_cselect_b32 %3, 1, 0\n\t" \
    "s_waitcnt vmcnt(0)\n\t"
#define STG8 \
    "s_mov_b32 %3, 0x10000\n\tv_add_u32 %2, 4096, %4\n\tglobal_load_dwordx4 v[208:211], %2, %6 offset:-4096 sc1\n\tglobal_load_dwordx4 v[212:215], %2, %6 offset:0 sc1\n\t" \
    "v_add_u32 %2, 12288, %4\n\tglobal_load_dwordx4 v[216:219], %2, %6 offset:-4096 sc1\n\tglobal_load_dwordx4 v[220:223], %2, %6 offset:0 sc1\n\tv_add_u32 %2, 20480, %4\n\t" \
    "global_load_dwordx4 v[224:227], %2, %6 offset:-4096 sc1\n\tglobal_load_dwordx4 v[228:231], %2, %6 offset:0 sc1\n\tv_add_u32 %2, 28672, %4\n\t" \
    "global_load_dwordx4 v[232:235], %2, %6 offset:-4096 sc1\n\tglobal_load_dwordx4 v[236:239], %2, %6 offset:0 sc1\n\t1:\ns_waitcnt vmcnt(7)\n\t" \
    "ds_write_b128 %5, v[208:211] offset:0\n\tv_max3_u32 %1, v208, v209, 0\n\tv_max3_u32 %1, v210, v211, %1\n\ts_waitcnt vmcnt(6)\n\tds_write_b128 %5, v[212:215] offset:4096\n\t" \
    "v_max3_u32 %1, v212, v213, %1\n\tv_max3_u32 %1, v214, v215, %1\n\ts_waitcnt vmcnt(5)\n\tds_write_b128 %5, v[216:219] offset:8192\n\tv_max3_u32 %1, v216, v217, %1\n\t" \
    "v_max3_u32 %1, v218, v219, %1\n\ts_waitcnt vmcnt(4)\n\tds_write_b128 %5, v[220:223] offset:12288\n\tv_max3_u32 %1, v220, v221, %1\n\tv_max3_u32 %1, v222, v223, %1\n\t" \
    "s_waitcnt vmcnt(3)\n\tds_write_b128 %5, v[224:227] offset:16384\n\tv_max3_u32 %1, v224, v225, %1\n\tv_max3_u32 %1, v226, v227, %1\n\ts_waitcnt vmcnt(2)\n\t" \
    "ds_write_b128 %5, v[228:231] offset:20480\n\tv_max3_u32 %1, v228, v229, %1\n\tv_max3_u32 %1, v230, v231, %1\n\ts_waitcnt vmcnt(1)\n\t" \
    "ds_write_b128 %5, v[232:235] offset:24576\n\tv_max3_u32 %1, v232, v233, %1\n\tv_max3_u32 %1, v234, v235, %1\n\ts_waitcnt vmcnt(0)\n\t" \
    "ds_write_b128 %5, v[236:239] offset:28672\n\tv_max3_u32 %1, v236, v237, %1\n\tv_max3_u32 %1, v238, v239, %1\n\tv_cmp_eq_u32 vcc, -1, %1\n\ts_cbranch_vccz 4f\n\t" \
    "s_sub_u32 %3, %3, 1\n\ts_cbranch_scc1 4f\n\ts_waitcnt lgkmcnt(0)\n\ts_mov_b64 s[82:83], 0\n\tv_max3_u32 %2, v208, v209, v210\n\tv_max_u32 %2, %2, v211\n\t" \
    "v_cmp_eq_u32 vcc, -1, %2\n\tv_add_u32 %2, 0, %4\n\ts_or_b64 s[82:83], s[82:83], vcc\n\ts_cbranch_vccz 2f\n\ts_mov_b64 exec, vcc\n\t" \
    "global_load_dwordx4 v[208:211], %2, %6 sc1\n\ts_mov_b64 exec, -1\n\t2:\nv_max3_u32 %2, v212, v213, v214\n\tv_max_u32 %2, %2, v215\n\tv_cmp_eq_u32 vcc, -1, %2\n\t" \
    "v_add_u32 %2, 4096, %4\n\ts_or_b64 s[82:83], s[82:83], vcc\n\ts_cbranch_vccz 2f\n\ts_mov_b64 exec, vcc\n\tglobal_load_dwordx4 v[212:215], %2, %6 sc1\n\t" \
    "s_mov_b64 exec, -1\n\t2:\nv_max3_u32 %2, v216, v217, v218\n\tv_max_u32 %2, %2, v219\n\tv_cmp_eq_u32 vcc, -1, %2\n\tv_add_u32 %2, 8192, %4\n\t" \
    "s_or_b64 s[82:83], s[82:83], vcc\n\ts_cbranch_vccz 2f\n\ts_mov_b64 exec, vcc\n\tglobal_load_dwordx4 v[216:219], %2, %6 sc1\n\ts_mov_b64 exec, -1\n\t2:\n" \
    "v_max3_u32 %2, v220, v221, v222\n\tv_max_u32 %2, %2, v223\n\tv_cmp_eq_u32 vcc, -1, %2\n\tv_add_u32 %2, 12288, %4\n\ts_or_b64 s[82:83], s[82:83], vcc\n\ts_cbranch_vccz 2f\n\t" \
    "s_mov_b64 exec, vcc\n\tglobal_load_dwordx4 v[220:223], %2, %6 sc1\n\ts_mov_b64 exec, -1\n\t2:\nv_max3_u32 %2, v224, v225, v226\n\tv_max_u32 %2, %2, v227\n\t" \
    "v_cmp_eq_u32 vcc, -1, %2\n\tv_add_u32 %2, 16384, %4\n\ts_or_b64 s[82:83], s[82:83], vcc\n\ts_cbranch_vccz 2f\n\ts_mov_b64 exec, vcc\n\t" \
    "global_load_dwordx4 v[224:227], %2, %6 sc1\n\ts_mov_b64 exec, -1\n\t2:\nv_max3_u32 %2, v228, v229, v230\n\tv_max_u32 %2, %2, v231\n\tv_cmp_eq_u32 vcc, -1, %2\n\t" \
    "v_add_u32 %2, 20480, %4\n\ts_or_b64 s[82:83], s[82:83], vcc\n\ts_cbranch_vccz 2f\n\ts_mov_b64 exec, vcc\n\tglobal_load_dwordx4 v[228:231], %2, %6 sc1\n\t" \
    "s_mov_b64 exec, -1\n\t2:\nv_max3_u32 %2, v232, v233, v234\n\tv_max_u32 %2, %2, v235\n\tv_cmp_eq_u32 vcc, -1, %2\n\tv_add_u32 %2, 24576, %4\n\t" \
    "s_or_b64 s[82:83], s[82:83], vcc\n\ts_cbranch_vccz 2f\n\ts_mov_b64 exec, vcc\n\tglobal_load_dwordx4 v[232:235], %2, %6 sc1\n\ts_mov_b64 exec, -1\n\t2:\n" \
    "v_max3_u32 %2, v236, v237, v238\n\tv_max_u32 %2, %2, v239\n\tv_cmp_eq_u32 vcc, -1, %2\n\tv_add_u32 %2, 28672, %4\n\ts_or_b64 s[82:83], s[82:83], vcc\n\ts_cbranch_vccz 2f\n\t" \
    "s_mov_b64 exec, vcc\n\tglobal_load_dwordx4 v[236:239], %2, %6 sc1\n\ts_mov_b64 exec, -1\n\t2:\ns_waitcnt vmcnt(0)\n\ts_branch 1b\n\t4:\n"

#define GM(x) x
#define GTS tid
template <bool gw> __device__ __forceinline__ void body(const Args& ar, const uint32_t sb, const uint32_t wu) {
#define ATTP { const int an = r16 & 7, ah = r16 >> 3; \
    _Pragma("unroll") for (int t = 0; t < 4; ++t) { const int i = 4 * g + t; bias[t] = (i > an && i <= an + 8 && (i >= 8 || rb + wu != 0)) ? 0.f : -1e30f; } \
    kp = sb + L_QT + (16 * wu + 2 * max(r16, 1)) * QP + 64 + g * 16; \
    qp = sb + L_QT + (16 + 16 * wu + 2 * an) * QP + g * 16; \
    vp = sb + L_QT + (16 * wu + 2 * max(4 * g + (r16 >> 2), 1)) * QP + 128 + (lane & 3) * 8; \
    const int orow = rb * 128 + 16 * wu + (g & 1) + 2 * an; \
    ooff = (((orow >> 5) * 32 + cg * 2 + ah) * 64 + (g >> 1) * 32 + (orow & 31)) * 16 + lb + W_OT; \
    asm volatile("" : "+v"(bias), "+v"(kp), "+v"(qp), "+v"(vp), "+v"(ooff)); SB0 }
  uint32_t tp[6] = {0, 0, 0, 0, 0, 0};
  float isc0 = 0.f, isc1 = 0.f, f1k0 = 0.f, f1k1 = 0.f;
  uint32_t dsk = 0;
  f32x4 kacc[4];
  float pm[7];
#define KGT const float tA0 = (float)(r16 < 8 ? r16 : 256 * ((r16 >> 2) - 1) + 2 * (r16 & 3)); \
      const float tA1 = (float)(r16 < 8 ? 768 + 256 * (r16 >> 1) + 4 * (r16 & 1) : 1792 + 256 * (r16 & 1)); \
      const float tB0 = (float)(8 * r16);
#define KGI(X) { u32x4 o[4]; \
      kgen_mode<0>(pm, tA0, tA1, tB0, o); SB0 X kgen_mode<1>(pm, tA0, tA1, tB0, o); SB0 X kgen_mode<2>(pm, tA0, tA1, tB0, o); SB0 X kgen_mode<3>(pm, tA0, tA1, tB0, o); \
      kacc[0] = MFK(o[0], o[2], kacc[0]); kacc[1] = MFK(o[0], o[3], kacc[1]); kacc[2] = MFK(o[1], o[2], kacc[2]); kacc[3] = MFK(o[1], o[3], kacc[3]); \
      _Pragma("unroll") for (int i = 0; i < 6; ++i) pm[i] = dppf<0x124>(pm[i]);     \
      SB0 }
  f32x4 bias;
  uint32_t kp, qp, vp, ooff;
  uint32_t st = 0, kg = 0, tag = 0;
#define FLD(v, q) asm volatile("global_load_dwordx4 v[252:255], %0, %1 sc1" :: "v"(W_SYN + v * 8192 + ((q) >> 2) * 128 + ((q) & 3) * 16), "s"(ws) : "memory");
#define FCHK(n, X) { uint32_t t_; asm volatile("s_waitcnt vmcnt(" #n ")\n\tv_xor_b32 v252, %1, v252\n\tv_xor_b32 v253, %1, v253\n\tv_xor_b32 v254, %1, v254\n\tv_xor_b32 v255, %1, v255\n\t" \
    "v_or3_b32 %0, v252, v253, v254\n\tv_or_b32 %0, %0, v255" : "=v"(t_) : "s"(tag) : "memory"); if (__all(t_ == 0u)) { st = 2; X } }
#define FST(v) asm volatile("s_mov_b64 exec, 1\n\tglobal_store_dword %0, %1, %2 sc1\n\ts_mov_b64 exec, -1" :: "v"(W_SYN + v * 8192 + (bx >> 4) * 128 + (bx & 15) * 4), "v"(tag), "s"(ws) : "memory");
#define FX1 LAS(int, fl4 + FX) = 1;
#define KGP(ch) { const float dt = rbf(expf((ch) ? hi2f(pr[7]) : lo2f(pr[7]))); \
      float q[6]; \
      _Pragma("unroll") for (int i = 0; i < 6; ++i) q[i] = __uint_as_float((lane & 1) ? (pr[i] & 0xffff0000u) : (pr[i] << 16)); \
      const float dx = dt * q[4], dy = dt * q[5], cx = q[2] * dx - q[3] * dy, cy = q[2] * dy + q[3] * dx; \
      pm[0] = dt * q[0] * 1.4426950408889634f; \
      pm[1] = dt * q[1] * 0.15915494309189535f; \
      pm[2] = 0.5f * __builtin_amdgcn_logf(cx * cx + cy * cy); \
      pm[3] = atan2f(cy, cx) * -0.15915494309189535f; \
      float m = fmaxf(pm[2], -60.f); \
      m = fmaxf(m, dppf<0x121>(m)); m = fmaxf(m, dppf<0x122>(m)); m = fmaxf(m, dppf<0x124>(m)); m = fmaxf(m, dppf<0x128>(m)); \
      { auto r = __builtin_amdgcn_permlane16_swap(__float_as_uint(m), __float_as_uint(m), false, false); m = fmaxf(__uint_as_float(r[0]), __uint_as_float(r[1])); } \
      { auto r = __builtin_amdgcn_permlane32_swap(__float_as_uint(m), __float_as_uint(m), false, false); m = fmaxf(__uint_as_float(r[0]), __uint_as_float(r[1])); } \
      m = ceilf(m) - 14.f; \
      pm[2] -= m; \
      pm[6] = __builtin_amdgcn_exp2f(m); \
      cexp2(pm[0] * 128.f, pm[1] * 128.f, pm[4], pm[5]); \
      }
  u32x8 pr;
#define GIN "v"(pw32), "v"(fl4 + FW), "v"(flw + FW), "s"(rot), "v"(sb + L_QT + (16 + wj * 32 + n) * QP + hh * 8), "v"(sb + L_CS + n * 4)
#define HDECL const int hp = 1 + (wj >> 1), hd = wj & 1;       \
      const uint32_t hr = sb + L_HST + r16 * 1024; \
      f32x4 hacc = {0.f, 0.f, 0.f, 0.f}; \
      u32x4 hv[16]; \
      f2 sr = {0.f, 0.f};
#define HPART(h0, h1, h2, h3, qu) { float p0 = 0.f, p1 = 0.f, p2 = 0.f, p3 = 0.f; \
        DSV(h0, p0, p1) DSV(h1, p2, p3) DSV(h2, p0, p1) DSV(h3, p2, p3) \
        p0 += p2; OPQ(p0); p1 += p3; OPQ(p1); \
        f2 t; t.x = xsum32(xsum16(p0)); t.y = xsum32(xsum16(p1)); \
        if (lane < 16) LAS(f2, sb + L_HS + (qu) * 128 + lane * 8) = t; }
#define HSTAT { const uint32_t a = sb + L_HS + r16 * 8; const f2 t0 = LAS(f2, a), t1 = LAS(f2, a + 128), t2 = LAS(f2, a + 256), t3 = LAS(f2, a + 384); \
        float s1 = t0.x + t1.x, s2 = t0.y + t1.y, s3 = t2.x + t3.x, s4 = t2.y + t3.y; OPQ(s1); OPQ(s2); OPQ(s3); OPQ(s4); s1 += s3; OPQ(s1); s2 += s4; \
        LNSTAT(sr, s1, s2) asm volatile("" : "+v"(sr)); SB0 }
  u32x4 af[32];
  uint32_t early = 0;
#define AFO(x) "=&v"(x)
#define AFB(x) "+v"(x)
#define LDAQ(C, h, cond, val) LDAQ_(C, h, cond, val)
#define LDAQ_(C, h, cond, val) { const char* wp = ws + W_WOP + (cb * 4 + wj) * 32768 + h * 8192; \
      asm volatile("s_cmp_eq_u32 %11, " #val "\n\ts_cbranch_scc0 1f\n\tglobal_load_dwordx4 %0, %8, %9 offset:0 sc1\n\tglobal_load_dwordx4 %1, %8, %9 offset:1024 sc1\n\tglobal_load_dwordx4 %2, %8, %9 offset:2048 sc1\n\tglobal_load_dwordx4 %3, %8, %9 offset:3072 sc1\n\tglobal_load_dwordx4 %4, %8, %10 offset:0 sc1\n\tglobal_load_dwordx4 %5, %8, %10 offset:1024 sc1\n\tglobal_load_dwordx4 %6, %8, %10 offset:2048 sc1\n\tglobal_load_dwordx4 %7, %8, %10 offset:3072 sc1\n1:" \
        : C(af[h * 8 + 0]), C(af[h * 8 + 1]), C(af[h * 8 + 2]), C(af[h * 8 + 3]), C(af[h * 8 + 4]), C(af[h * 8 + 5]), C(af[h * 8 + 6]), C(af[h * 8 + 7]) : "v"(lane * 16), "s"(wp), "s"(wp + 4096), "s"(cond) : "scc"); }
  {
    const uint32_t lb = 0;
#define HLN(ac, hp, hd) { const u32x4 cc = LAS(u32x4, sb + L_CS + ((hp) * 32 + (hd) * 16 + 4 * g) * 4); float t[4]; \
        _Pragma("unroll") for (int i = 0; i < 4; ++i) { float c = lo2f(cc[i]) + hi2f(cc[i]); OPQ(c); t[i] = fmaf(-sr.x, c, ac[i]); OPQ(t[i]); t[i] *= sr.y; OPQ(t[i]); } \
        LAS(u32x2, sb + L_QT + r16 * QP + (hp) * 64 + (hd) * 32 + g * 8) = (u32x2){pack2(t[0], t[1]), pack2(t[2], t[3])}; }
    if (gw) {
        IDS
        ATTP
        u32x4 wv;
        asm volatile("s_waitcnt lgkmcnt(0)\n\ts_barrier\n\ts_waitcnt vmcnt(8)" : "={v[168:171]}"(wv) :: "memory");
        asm volatile("global_store_dwordx2 %0, %1, %2 sc1" :: "v"(W_WOP + p * 2048 + tid * 8), "v"((u32x2){wv[0] | (wv[1] << 16), wv[2] | (wv[3] << 16)}), "s"(ws) : "memory");
        asm volatile("s_waitcnt vmcnt(1)" : "={v[160:167]}"(pr) :: "memory");
        tag = __builtin_amdgcn_readfirstlane(pr[6]) * 0x9E3779B1u + 0x7F4A7C15u;
        KGP(0)
        const uint32_t ra = sb + SC + wj * 12288 + n * 128 + ((hh ^ ((n >> 1) & 7)) * 16);
        asm volatile(GM(GEMM1) : "+v"(GTS)
                     : "v"(pw32), "v"(sb + L_FL + FW), "v"(flw + FR), "s"(rot), "v"(sb + L_QT + (16 + wj * 32 + n) * QP + hh * 8), "v"(sb + L_CS + n * 4), "v"(ra),
                       "v"((lane & 4) ? fl4 + FC : fl4 + FR), "v"((lane & 4) ? 1 : 16),
                       "v"(W_SYN + 8192 + (bx >> 4) * 128 + (bx & 15) * 4), "s"(ws),
                       "v"(tag), "v"(fl4 + FR), "s"(wj) : GEMM1_CLOB);
    } else {
      u32x16 ls, lv;
      u32x4 lt, hq;
      { int tid = threadIdx.x; asm volatile(GM(LSTA) : "+v"(GTS), "=&{s[48:63]}"(ls), "=&{s[64:67]}"(lt), "=&{v[64:79]}"(lv) : "s"(__builtin_amdgcn_kernarg_segment_ptr()), "s"(blockIdx.x), "s"(wu), "v"(threadIdx.x) : LSTA_CLOB); }
      SB0
      IDS
      if (wj == 0) asm volatile("s_barrier" ::: "memory");
      ATTP
      uint32_t qt = sb + L_QT + r16 * QP + (wj - 1) * 64 + g * 8;
      asm volatile("" : "+v"(qt));
      asm volatile(GM(LSTB) : "+v"(GTS), "+{s[48:63]}"(ls), "+{s[64:67]}"(lt), "+{v[64:79]}"(lv), "=&s"(tag), "=&s"(st) : "s"(wj) : LSTB_CLOB);
      asm volatile("" : "=v"(kacc[0]), "=v"(kacc[1]), "=v"(kacc[2]), "=v"(kacc[3]), "=v"(pm[0]), "=v"(pm[1]), "=v"(pm[2]), "=v"(pm[3]), "=v"(pm[4]), "=v"(pm[5]), "=v"(pm[6]), "={v[144:147]}"(hq));
      kg = 4;
      if (wj) {
        kg = 0;
        const uint32_t hp = wj - 1, fw = sb + L_FL + FW;
        u32x16 aw, ax;
        uint32_t cs = sb + L_CS + hp * 128 + g * 16, fc = flw + FC;
#pragma unroll
        for (int t = 0; t < 4; ++t) kacc[t] = (f32x4){0.f, 0.f, 0.f, 0.f};
        asm volatile(HADR : "=&{v[192:207]}"(aw), "=&{v[208:223]}"(ax)
                     : "v"(sb + hp * 1024 + (g >> 1) * 3072 + (8 * (g & 1) + (r16 >> 2)) * 64 + (lane & 3) * 8 + (g & 1) * 32), "v"(sb + L_HST + r16 * 1024 + (g ^ r16) * 16), "s"(rot) : "s44", "s45", "scc", "memory");
        asm volatile("s_barrier\n\ts_waitcnt vmcnt(0)" : "={v[160:167]}"(pr), "+v"(cs), "+v"(fc) :: "memory");
        KGP(wj >> 1)
        KGT
#pragma unroll 1
        do {
          KGI()
          ++kg;
        } while (kg < 4 && __builtin_amdgcn_readfirstlane(LAS(volatile int, fw)) < KGW);
        asm volatile(GM(HALO) : "+v"(GTS), "=&{v[144:147]}"(hq) : "{v[192:207]}"(aw), "{v[208:223]}"(ax), "v"(fw), "v"(cs), "v"(fc) : HALO_CLOB);
      }
      early = __builtin_amdgcn_readfirstlane(LAS(volatile uint32_t, sb + L_FL + FX));
      LDAQ(AFO, 0, early, 1) LDAQ(AFO, 1, early, 1)
      if (wj == 0) {
        asm volatile("s_cmp_eq_u32 %0, 0\n\ts_cbranch_scc1 1f\n\ts_waitcnt vmcnt(16)\n\ts_branch 2f\n1:\n\ts_waitcnt vmcnt(0)\n2:" :: "s"(early) : "scc", "memory");
        FST(2)
      }
      LDAQ(AFO, 2, early, 1) LDAQ(AFO, 3, early, EQ3)
      if (wj == 0) {
        const u32x4 sent = {SENT, SENT, SENT, SENT};
#pragma unroll
        for (int i = 0; i < 8; ++i) { ST16(p * 8192 + lane * 16, sent, ws + W_X1 + i * 1024) }
        ST16(W_ST + bx * 256 + r16 * 16, sent, ws)
      } else if (wj > 1) {
        flag_wait(fl4 + FR, 16);
        LAS(u32x2, qt) = (u32x2){hq[0], hq[1]}; LAS(u32x2, qt + 32) = (u32x2){hq[2], hq[3]};
      }
    }
  }
#pragma unroll 1
  for (int layer = 0;; layer = 1) {
    const uint32_t lb = layer * 8 * MB;

#define XQ2 "global_load_dword v240, %3, %4 offset:0 sc1\n\tglobal_load_dword v241, %3, %4 offset:128 sc1\n\tglobal_load_dword v242, %3, %4 offset:512 sc1\n\tglobal_load_dword v243, %3, %4 offset:640 sc1\n\tglobal_load_dword v244, %3, %4 offset:1024 sc1\n\tglobal_load_dword v245, %3, %4 offset:1152 sc1\n\tglobal_load_dword v246, %3, %4 offset:1536 sc1\n\tglobal_load_dword v247, %3, %4 offset:1664 sc1\n\t"
    {
      IDS
      const int ah = r16 >> 3;
      u32x4 qf[2];
      { u32x2 z[4]; asm volatile("v_mov_b64 %0, 0\n\tv_mov_b64 %1, 0\n\tv_mov_b64 %2, 0\n\tv_mov_b64 %3, 0" : "=v"(z[0]), "=v"(z[1]), "=v"(z[2]), "=v"(z[3]));
        qf[0] = (u32x4){z[0][0], z[0][1], z[1][0], z[1][1]}; qf[1] = (u32x4){z[2][0], z[2][1], z[3][0], z[3][1]}; }
      __builtin_amdgcn_s_setprio(APR);
      BAR
      u32x4 kf[2];
      u32x2 vf[2][2];
      asm volatile("ds_read_b128 %0, %2\n\tds_read_b128 %1, %2 offset:%3" : "=&v"(kf[0]), "=&v"(kf[1]) : "v"(kp), "i"(QP) : "memory");
      if ((g >> 1) == ah) asm volatile("ds_read_b128 %0, %2\n\tds_read_b128 %1, %2 offset:%3" : "+v"(qf[0]), "+v"(qf[1]) : "v"(qp), "i"(QP) : "memory");
      asm volatile("ds_read_b64_tr_b16 %0, %4\n\tds_read_b64_tr_b16 %1, %4 offset:32\n\tds_read_b64_tr_b16 %2, %4 offset:%5\n\tds_read_b64_tr_b16 %3, %4 offset:%6\n\t"
                   "s_waitcnt lgkmcnt(4)"
                   : "=&v"(vf[0][0]), "=&v"(vf[0][1]), "=&v"(vf[1][0]), "=&v"(vf[1][1]) : "v"(vp), "i"(QP), "i"(QP + 32) : "memory");
      asm volatile("" : "+v"(kf[0]), "+v"(kf[1]), "+v"(qf[0]), "+v"(qf[1]));
      float l[2][4], m[2];
      f32x4 sc[2];
#pragma unroll
      for (int u = 0; u < 2; ++u) sc[u] = MFS(kf[u], qf[u], bias);
      SB0
#pragma unroll
      for (int u = 0; u < 2; ++u) { l[u][0] = rb1(sc[u][0]); l[u][1] = rb1(sc[u][1]); l[u][2] = rb1(sc[u][2]); l[u][3] = rb1(sc[u][3]); }
#define XRED(OP, FIN) "s_nop 1\n\tv_permlane16_swap_b32 %0, %1\n\t" OP " %2, %0, %1\n\t" OP " %1, %0, %1\n\ts_nop 1\n\tv_permlane32_swap_b32 %2, %1\n\t" \
        OP " %2, %2, %1\n\t" FIN("%0") FIN("%1") "s_nop 1\n\tv_permlane16_swap_b32 %0, %1"
#define FMUL(d) "v_mul_f32 " d ", 0xbeb8aa3b, %2\n\t"
#define FRCP(d) "v_rcp_f32 " d ", %2\n\t"
      const float SCL = 0.25f * 1.4426950408889634f;
      float c_;
      asm("v_max_f32 %0, %3, %4\n\tv_max_f32 %1, %7, %8\n\tv_max3_f32 %0, %0, %5, %6\n\tv_max3_f32 %1, %1, %9, %10\n\t" XRED("v_max_f32", FMUL)
          : "=&v"(m[0]), "=&v"(m[1]), "=&v"(c_) : "v"(l[0][0]), "v"(l[0][1]), "v"(l[0][2]), "v"(l[0][3]), "v"(l[1][0]), "v"(l[1][1]), "v"(l[1][2]), "v"(l[1][3]));
#pragma unroll
      for (int u = 0; u < 2; ++u) {
#pragma unroll
        for (int t = 0; t < 4; ++t) { float e = fmaf(l[u][t], SCL, m[u]); OPQ(e); l[u][t] = __builtin_amdgcn_exp2f(e); }
        float a0 = l[u][0] + l[u][1], a1 = l[u][2] + l[u][3]; OPQ(a0); OPQ(a1);
        m[u] = a0 + a1;
      }
      asm(XRED("v_add_f32", FRCP) : "+v"(m[0]), "+v"(m[1]), "=&v"(c_));
      uint32_t o[2][2];
      u32x4 pb[2];
#pragma unroll
      for (int u = 0; u < 2; ++u) {
#pragma unroll
        for (int t = 0; t < 4; ++t) { l[u][t] *= m[u]; OPQ(l[u][t]); }
        const uint32_t p0 = pack2(l[u][0], l[u][1]), p1 = pack2(l[u][2], l[u][3]);
        pb[u] = (u32x4){ah ? 0u : p0, ah ? 0u : p1, ah ? p0 : 0u, ah ? p1 : 0u};
      }
      asm volatile("s_waitcnt lgkmcnt(0)" : "+v"(vf[0][0]), "+v"(vf[0][1]), "+v"(vf[1][0]), "+v"(vf[1][1]), "+v"(pb[0]), "+v"(pb[1]));
      f32x4 ov[2];
#pragma unroll
      for (int u = 0; u < 2; ++u) {
        const u32x4 va = {vf[u][0][0], vf[u][0][1], vf[u][1][0], vf[u][1][1]};
        ov[u] = MFS(va, pb[u], ((f32x4){0.f, 0.f, 0.f, 0.f}));
      }
      SB0
#pragma unroll
      for (int u = 0; u < 2; ++u) { o[u][0] = min(pack2(ov[u][0], ov[u][1]), SENT - 1u); o[u][1] = min(pack2(ov[u][2], ov[u][3]), SENT - 1u); }
      const auto x0 = __builtin_amdgcn_permlane16_swap(o[0][0], o[1][0], false, false), x1 = __builtin_amdgcn_permlane16_swap(o[0][1], o[1][1], false, false);
      ST16(ooff, ((u32x4){x0[0], x1[0], x0[1], x1[1]}), ws)
      __builtin_amdgcn_s_setprio(0);
    }
    if (gw) {
      IDS
      const u32x4 sent = {SENT, SENT, SENT, SENT};
      u32x4 tb0, tb1;
      u32x2 tb2;
      if (layer == 0) {
        FLD(2, lane)
        tb0 = *(const u32x4*)(g_tabs.prd + (tid & 127) * 8);
        tb1 = *(const u32x4*)(g_tabs.img + (tid & 127) * 8);
        tb2 = *(const u32x2*)(g_tabs.img + (tid & 127) * 8 + 8);
        KGT
#pragma unroll
        for (int t = 0; t < 4; ++t) kacc[t] = (f32x4){0.f, 0.f, 0.f, 0.f};
        KGI()
#pragma unroll
        for (int t = 0; t < 4; ++t) kacc[t] *= pm[6];
      } else {
        asm volatile("" : "=v"(kacc[0]), "=v"(kacc[1]), "=v"(kacc[2]), "=v"(kacc[3]), "=v"(tb0), "=v"(tb1), "=v"(tb2));
        if (bx + wj == 0) asm volatile("s_mov_b64 exec, 1\n\tglobal_store_dword %0, %1, %2 sc1\n\ts_mov_b64 exec, -1" :: "v"(W_SYN), "v"(tag), "s"(ws) : "memory");
      }
      VM0
      if (layer == 0) {
#pragma unroll 1
        for (int it = 0; it < (1 << 16); ++it) {
          FCHK(0, )
          if (st == 2) break;
          __builtin_amdgcn_s_sleep(2);
          FLD(2, lane)
        }
      }
      if (layer) __builtin_amdgcn_s_sleep(DL1); else __builtin_amdgcn_s_sleep(DL1A);
      {
        u32x32 q;
        uint32_t t0, t1, t2;
        asm volatile(STG8 : "=&{v[208:239]}"(q), "=&v"(t0), "=&v"(t1), "=&s"(t2) : "v"(lb + W_OT + rt * 32768 + tid * 16), "v"(sb + L_OT + tid * 16), "s"(ws)
                     : "s82", "s83", "vcc", "scc", "memory");
      }
      BAR
      const uint32_t ch = wj >> 1;
      if (layer == 0) {
#pragma unroll
        for (int t = 0; t < 4; ++t) LAS(f32x4, sb + L_PR + wj * 4096 + t * 1024 + lane * 16) = kacc[t];
        FSET(flw + FT, 1)
        ST16(p * 8192 + wj * 2048 + lane * 16, sent, ws + W_X2) ST16(p * 8192 + wj * 2048 + lane * 16, sent, ws + W_X2 + 1024)
        if (wj == 3) ST16(W_PS + p * 1024 + lane * 16, sent, ws)
        FLD(3, wj * 16 + r16)
        flag_wait((lane & 4) ? fl4 + FT : fl4 + FK, 1);
        f32x4 v[2];
        const uint32_t pa = sb + L_PR + ch * 20480 + (wj & 1) * 2048 + lane * 16;
#pragma unroll
        for (int t = 0; t < 2; ++t) v[t] = LAS(f32x4, pa + t * 1024) + LAS(f32x4, pa + t * 1024 + 4096);
        if (!ch) {
#pragma unroll
          for (int t = 0; t < 2; ++t) v[t] += (LAS(f32x4, pa + t * 1024 + 8192) + LAS(f32x4, pa + t * 1024 + 12288)) + LAS(f32x4, pa + t * 1024 + 16384);
        }
        uint32_t mx = max(mx4(*(u32x4*)&v[0] & 0x7fffffffu), mx4(*(u32x4*)&v[1] & 0x7fffffffu));
        mx = max(mx, (uint32_t)__builtin_amdgcn_mov_dpp(mx, 0x121, 0xf, 0xf, true)); mx = max(mx, (uint32_t)__builtin_amdgcn_mov_dpp(mx, 0x122, 0xf, 0xf, true));
        mx = max(mx, (uint32_t)__builtin_amdgcn_mov_dpp(mx, 0x124, 0xf, 0xf, true)); mx = max(mx, (uint32_t)__builtin_amdgcn_mov_dpp(mx, 0x128, 0xf, 0xf, true));
        { auto r = __builtin_amdgcn_permlane16_swap(mx, mx, false, false); mx = max(r[0], r[1]); }
        { auto r = __builtin_amdgcn_permlane32_swap(mx, mx, false, false); mx = max(r[0], r[1]); }
        LAS(uint32_t, sb + L_HS + wj * 4) = mx;
        const uint32_t ke = sb + L_KE + ch * 1536;
        flag_set(flw + FT, 2);
        st = 0;
#pragma unroll 1
        for (int it = 0; it < (1 << 16); ++it) {
          FCHK(0, FSET(flw + FX, 2))
          if (st == 2) break;
          __builtin_amdgcn_s_sleep(4);
          FLD(3, wj * 16 + r16)
        }
        flag_wait(fl4 + FT, 2);
        LAS(u16, ke + 1400) = 0;
        const u32x2 m2 = LAS(u32x2, sb + L_HS + ch * 8);
        const int e = min(max(268 - (int)(max(m2[0], m2[1]) >> 23), 2), 252);
        const float scl = __uint_as_float(e << 23);
        LAS(float, sb + L_HS + 16 + ch * 4) = __uint_as_float((254 - e) << 23);
#pragma unroll
        for (int t = 0; t < 2; ++t) {
          const uint32_t h0 = packh(v[t][0] * scl, v[t][1] * scl), h1 = packh(v[t][2] * scl, v[t][3] * scl);
          LAS(u16, ke + (tb0[2 * t] & 0xffffu)) = (u16)h0; LAS(u16, ke + (tb0[2 * t] >> 16)) = (u16)(h0 >> 16);
          LAS(u16, ke + (tb0[2 * t + 1] & 0xffffu)) = (u16)h1; LAS(u16, ke + (tb0[2 * t + 1] >> 16)) = (u16)(h1 >> 16);
        }
        flag_set(flw + FT, 3);
        flag_wait(fl4 + FT, 3);
#pragma unroll
        for (int i = 0; i < 6; ++i) {
          const uint32_t t = i < 4 ? tb1[i & 3] : tb2[i & 1];
          tp[i] = LAS(u16, ke + (t & 0xffffu)) | ((uint32_t)LAS(u16, ke + (t >> 16)) << 16);
        }
      }
      {
        const uint32_t dc = sb + L_CP + ch * 4 * CPP + (tid & 127) * 16;
        const uint32_t e0 = AB(tp[0], tp[1]), e1 = AB(tp[1], tp[2]), e2 = AB(tp[2], tp[3]), e3 = AB(tp[3], tp[4]), e4 = AB(tp[4], tp[5]);
        LAS(u32x4, dc) = (u32x4){tp[0], tp[1], tp[2], tp[3]}; LAS(u32x4, dc + CPP) = (u32x4){e0, e1, e2, e3};
        LAS(u32x4, dc + 2 * CPP) = (u32x4){tp[1], tp[2], tp[3], tp[4]}; LAS(u32x4, dc + 3 * CPP) = (u32x4){e1, e2, e3, e4};
      }
    } else {
      IDS
      KGT
      if (layer == 0) {
        if (wj == 0) { asm volatile("s_waitcnt vmcnt(1)" ::: "memory"); FST(3) }
#pragma unroll 1
        for (int it = 0; it < (1 << 20); ++it) {
          if (__all(LAS(volatile int, fl4 + FX) >= 1)) break;
          if (st != 2) { FLD(1, lane) FCHK(0, FX1) }
          __builtin_amdgcn_s_sleep(1);
        }
      }
      LDAQ(AFB, 0, early, 0) LDAQ(AFB, 1, early, 0)
      LDAQ(AFB, 2, early, 0)
      LDAQ(AFB, 3, layer, 0)
      early = 2;
      u32x8 xq;
      const uint32_t xo = layer ? W_X2 + rt * 32768 + (cb * 4 + wj) * 2048 + hh * 256 + n * 4 : ((rt * 32 + n) * DM + cb * 128 + wj * 32 + 4 * hh) * 2;
      asm volatile("s_cmp_eq_u32 %3, 0\n\ts_cbranch_scc0 1f\n\tglobal_load_dwordx2 v[240:241], %1, %2\n\tglobal_load_dwordx2 v[242:243], %1, %2 offset:16\n\t"
                   "global_load_dwordx2 v[244:245], %1, %2 offset:32\n\tglobal_load_dwordx2 v[246:247], %1, %2 offset:48\n1:" : "=&{v[240:247]}"(xq) : "v"(xo), "s"(ar.x), "s"(layer) : "scc");
      if (layer == 0) {
#pragma unroll 1
        for (; kg < 4; ++kg) KGI()
#pragma unroll
        for (int t = 0; t < 4; ++t) kacc[t] *= pm[6];
      }
      asm volatile("s_waitcnt vmcnt(0)" : "+v"(af[0]), "+v"(af[1]), "+v"(af[2]), "+v"(af[3]), "+v"(af[4]), "+v"(af[5]), "+v"(af[6]), "+v"(af[7]),
                   "+v"(af[8]), "+v"(af[9]), "+v"(af[10]), "+v"(af[11]), "+v"(af[12]), "+v"(af[13]), "+v"(af[14]), "+v"(af[15]) :: "memory");
      asm volatile("" : "+v"(af[16]), "+v"(af[17]), "+v"(af[18]), "+v"(af[19]), "+v"(af[20]), "+v"(af[21]), "+v"(af[22]), "+v"(af[23]),
                   "+v"(af[24]), "+v"(af[25]), "+v"(af[26]), "+v"(af[27]), "+v"(af[28]), "+v"(af[29]), "+v"(af[30]), "+v"(af[31]) :: "memory");
      uint32_t tw = sb + L_TP + wj * 2048 + hh * 256 + n * 4, tr = sb + L_TP + wj * 2048 + lane * 16, pb = sb + L_OT + lane * 16;
      uint32_t wxo = lb + W_X1 + (cb * 64 + wj * 16 + (lane >> 3)) * 8192 + rt * 128 + (lane & 7) * 16, wx1 = wxo + 65536;
      uint32_t sw = sb + L_SP + wj * 256 + lane * 4, sr_ = sb + L_SP + r16 * 16, so_ = lb + W_ST + (cb * 2 + (r16 >> 3)) * 8192 + rt * 128 + (lane & 7) * 16;
      uint32_t fk = flw + FK, fk4 = fl4 + FK, fkv = 2 + layer, tw1 = tw + 1024;
      float p1 = 0.f, p2 = 0.f, p3 = 0.f, p4 = 0.f;
      asm volatile("" : "+v"(tw), "+v"(tr), "+v"(pb), "+v"(wxo), "+v"(wx1), "+v"(sw), "+v"(sr_), "+v"(so_), "+v"(fk), "+v"(fk4), "+v"(fkv), "+v"(tw1),
                   "+v"(p1), "+v"(p2), "+v"(p3), "+v"(p4));
      __builtin_amdgcn_s_setprio(WPR);
      BAR
      u32x4 bf[6];
#define LDB(i) asm volatile("ds_read_b128 %0, %1 offset:%2" : "=v"(bf[(i) % 6]) : "v"(pb), "i"((i) * 1024)); SB0
      LDB(0) LDB(1) LDB(2) LDB(3) LDB(4) LDB(5)
      asm volatile("s_cmp_eq_u32 %3, 0\n\ts_cbranch_scc1 1f\n\tglobal_load_dword v240, %1, %2 offset:0 sc1\n\tglobal_load_dword v241, %1, %2 offset:128 sc1\n\tglobal_load_dword v242, %1, %2 offset:512 sc1\n\tglobal_load_dword v243, %1, %2 offset:640 sc1\n\tglobal_load_dword v244, %1, %2 offset:1024 sc1\n\tglobal_load_dword v245, %1, %2 offset:1152 sc1\n\tglobal_load_dword v246, %1, %2 offset:1536 sc1\n\tglobal_load_dword v247, %1, %2 offset:1664 sc1\n1:" : "+{v[240:247]}"(xq) : "v"(xo), "s"(ws), "s"(layer) : "scc");
      f32x16 ac2;
      ZERO16(ac2)
      f2 xf[8];
      int fq;
      SB0
#pragma unroll
      for (int s = 0; s < 32; ++s) {
        asm volatile("s_waitcnt lgkmcnt(%1)" : "+v"(bf[s % 6]) : "i"(s < 27 ? 5 : (s < 29 ? 31 - s : 32 - s)));
        ac2 = MFB(af[s], bf[s % 6], ac2); SB0
        if (s < 26) { LDB(s + 6) }
        if (s == 0 && layer == 0 && wj) {
#pragma unroll
          for (int t = 0; t < 4; ++t) LAS(f32x4, sb + L_PR + 12288 + wj * 4096 + t * 1024 + lane * 16) = kacc[t];
          FSET(fk, 1)
        }
        if (s == 26) {
          uint32_t t_, c_;
          asm volatile("s_waitcnt vmcnt(0)\n\ts_lshl_b32 %2, %5, 16\n1:\n\tv_max3_u32 %1, v240, v241, v242\n\tv_max3_u32 %1, %1, v243, v244\n\tv_max3_u32 %1, %1, v245, v246\n\tv_max_u32 %1, %1, v247\n\t"
                       "v_cmp_eq_u32 vcc, -1, %1\n\ts_cbranch_vccz 2f\n\ts_sub_u32 %2, %2, 1\n\ts_cbranch_scc1 2f\n\t" XQ2 "s_waitcnt vmcnt(0)\n\ts_branch 1b\n2:"
                       : "+{v[240:247]}"(xq), "=&v"(t_), "=&s"(c_) : "v"(xo), "s"(ws), "s"(layer) : "vcc", "scc", "memory");
        }
        if (s >= 27 && s < 31) {
          const int q = 2 * (s - 27);
          xf[q] = (f2){lo2f(xq[q]), hi2f(xq[q])}; xf[q + 1] = (f2){lo2f(xq[q + 1]), hi2f(xq[q + 1])};
          asm volatile("" : "+v"(xf[q]), "+v"(xf[q + 1]));
        }
        if (s == 28) asm volatile("ds_read_b32 %0, %1" : "=v"(fq) : "v"(fl4 + FX) : "memory");
        SB0
      }
      uint32_t u[8], um[8];
      f2 t[8];
#define PIN8(x) asm volatile("" : "+v"(x[0]), "+v"(x[1]), "+v"(x[2]), "+v"(x[3]), "+v"(x[4]), "+v"(x[5]), "+v"(x[6]), "+v"(x[7]));
#pragma unroll
      for (int q = 0; q < 8; ++q) t[q] = (f2){rb1(ac2[2 * q]), rb1(ac2[2 * q + 1])};
      PIN8(t)
#pragma unroll
      for (int q = 0; q < 8; ++q) t[q] += xf[q];
      PIN8(t)
#pragma unroll
      for (int q = 0; q < 8; ++q) u[q] = pack2(t[q].x, t[q].y);
      PIN8(u)
#pragma unroll
      for (int q = 0; q < 8; ++q) um[q] = min(u[q], SENT - 1u);
      u32x4 w0, w1;
      asm volatile("s_waitcnt lgkmcnt(0)" : "+v"(fq));
      if (__builtin_expect(!__all(fq >= 2), 0)) flag_wait(fl4 + FX, 2);
      asm volatile("ds_write2_b32 %2, %5, %6 offset1:32\n\tds_write2_b32 %2, %7, %8 offset0:128 offset1:160\n\tds_write2_b32 %3, %9, %10 offset1:32\n\t"
                   "ds_write2_b32 %3, %11, %12 offset0:128 offset1:160\n\tds_read_b128 %0, %4\n\tds_read_b128 %1, %4 offset:1024"
                   : "=&v"(w0), "=&v"(w1) : "v"(tw), "v"(tw1), "v"(tr), "v"(um[0]), "v"(um[1]), "v"(um[2]), "v"(um[3]), "v"(um[4]), "v"(um[5]), "v"(um[6]), "v"(um[7]) : "memory");
      SB0
#pragma unroll
      for (int q = 0; q < 8; q += 2) { p1 = dot2(u[q], 0x3F803F80u, p1); p2 = dot2(u[q], u[q], p2); p3 = dot2(u[q + 1], 0x3F803F80u, p3); p4 = dot2(u[q + 1], u[q + 1], p4); }
      { auto r = __builtin_amdgcn_permlane32_swap(__float_as_uint(p1 + p3), __float_as_uint(p2 + p4), false, false);
        asm volatile("ds_write_b32 %0, %1\n\ts_mov_b64 exec, 1\n\tds_write_b32 %2, %3\n\ts_mov_b64 exec, -1" :: "v"(sw), "v"(__uint_as_float(r[0]) + __uint_as_float(r[1])), "v"(fk), "v"(fkv) : "memory"); }
      asm volatile("s_waitcnt lgkmcnt(2)" : "+v"(w0), "+v"(w1));
      ST16(wxo, w0, ws) ST16(wx1, w1, ws)
      if (wj == 0) {
        f32x4 v[4];
#pragma unroll 1
        for (int it = 0; it < (1 << 20); ++it) {
          asm volatile("ds_read_b32 %4, %5\n\tds_read_b128 %0, %6\n\tds_read_b128 %1, %6 offset:256\n\tds_read_b128 %2, %6 offset:512\n\tds_read_b128 %3, %6 offset:768\n\ts_waitcnt lgkmcnt(0)"
                       : "=&v"(v[0]), "=&v"(v[1]), "=&v"(v[2]), "=&v"(v[3]), "=&v"(fq) : "v"(fk4), "v"(sr_) : "memory");
          if (__all(fq >= (int)fkv)) break;
        }
        const f32x4 s = (v[0] + v[1]) + (v[2] + v[3]);
        const u32x4 su = *(const u32x4*)&s;
        asm volatile("s_mov_b64 exec, 0xffff\n\tglobal_store_dwordx4 %0, %1, %2 sc1\n\ts_mov_b64 exec, -1\n\ts_nop 0"
                     :: "v"(so_), "v"((u32x4){min(su[0], SENT - 1u), min(su[1], SENT - 1u), min(su[2], SENT - 1u), min(su[3], SENT - 1u)}), "s"(ws) : "memory");
      }
      __builtin_amdgcn_s_setprio(0);
    }
#define DLH 6
#define FPR 2
    {
      IDS
      const uint32_t ch = wj >> 1, wo = wj & 1;
      asm volatile("s_load_dword %0, %1, 0\n\ts_waitcnt lgkmcnt(0)" : "=s"(dsk) : "s"(ar.Dskip + 2 * p));
      const uint32_t sch = sb + L_S + ch * SCH;
      int off = lb + W_ST + tid * 16, offx = lb + W_X1 + p * 8192 + tid * 16;
      uint32_t du = sb + L_U + 17 * 80 + (tid >> 3) * 80 + (tid & 7) * 8;
      uint32_t e0 = sb + L_S + (tid >> 3) * 128 + (((tid ^ (tid >> 3)) & 7) * 16), e1 = sb + L_PF - 2048 + tid * 16;
      uint32_t oo = layer ? W_X3 + xcd * 262144 + (tid >> 4) * 8192 + bi * 256 + (tid & 15) * 16 : W_X2 + (tid >> 3) * 32768 + p * 128 + (tid & 7) * 16;
      uint32_t a1, a2, a3, ra, rc, d0, d1, d2 = 0, d3 = 0, fa, za, ta;
      {
        const int t2 = tid & 255;
        za = sb + (t2 < 85 ? L_U + (tid >> 8) * UCH + t2 * 16 : L_S + (tid >> 8) * SCH + (t2 < 123 ? (t2 - 85) * 16 : (t2 < 161 ? S8A + (t2 - 123) * 16 : 2 * S8A)));
        const uint32_t i0 = 1023 - n + 8 * hh;
        ta = sb + L_CP + (ch * 4 + (i0 & 3)) * CPP + (i0 >> 2) * 8 - 2048;
      }
      if (gw) {
        a1 = sb + L_U + ch * UCH + (n + 32 * wo) * 80 + 16 * hh;
        a2 = sch + (n + 32 * wo) * 128 + ((hh ^ (n & 7)) * 16);
        fa = sb + L_FL + FT + (ch * 2 + (lane & 1)) * 4;
        const uint32_t ur = sb + L_U + ch * UCH + 17 * 80;
        const int w = lane - 1, o = lane + 1, o1 = lane + 2;
        a3 = ur + (w >> 2) * 80 + (w & 3) * 16;
        ra = ur + ((w - 1) >> 2) * 80 + ((w - 1) & 3) * 16 + 8;
        rc = ur + (lane >> 2) * 80 + (lane & 3) * 16;
        if (!wo) {
          d0 = sch + 576 + (o >> 5) * 576 + (o & 31) * 16; d1 = sch + 576 + (o1 >> 5) * 576 + (o1 & 31) * 16;
          d2 = o < 64 ? d0 : sch + 1088 - 2304; d3 = o1 < 64 ? d1 : sch + 1088 - 2304;
        } else {
          d0 = sch + 2 * S8A + (o >> 6) * 1152 + (o & 63) * 16; d2 = o < 64 ? d0 : sch + 2 * S8A + 1024 - 1152;
          d1 = sch + 2 * S8A + S16A + lane * 16;
        }
        asm volatile("" : "+v"(d2), "+v"(d3));
      } else {
        const int c4 = lane & 3, i1 = tid - 257 + 136, i0 = i1 - 1;
        ra = sb + L_U + (i0 >> 3) * 80 + (i0 & 7) * 8 + 4; rc = sb + L_U + (i1 >> 3) * 80 + (i1 & 7) * 8;
        a1 = sch + (wo ? 0 : (g & 1) * 1152) + (c4 >> 1) * 576 + (8 * hh + (r16 >> 2)) * 16 + (c4 & 1) * 8;
        a3 = sch + 2 * S8A + (8 * hh + (r16 >> 2)) * 32 + c4 * 8; a2 = a3 + (g & 1) * 1152;
        d0 = sb + L_PF + ch * 10240 + n * 32 + hh * 16; d1 = sb + L_PF + ch * 10240 + 6144 + n * 64 + ((hh ^ ((n >> 1) & 2)) * 16);
        d2 = sb + L_PF + 6144 - 4096 + ((tid * 16) ^ ((tid & 16) * 2));
        fa = sb + L_FL + FS + (ch * 2 + (lane & wo)) * 4;
        asm volatile("" : "+v"(d2));
      }
      uint32_t fo = flw, fv = 4 + layer;
      asm volatile("" : "+v"(du), "+v"(e0), "+v"(e1), "+v"(oo), "+v"(a1), "+v"(a2), "+v"(a3), "+v"(ra), "+v"(rc), "+v"(d0), "+v"(d1), "+v"(fa), "+v"(fo), "+v"(fv),
                   "+v"(za), "+v"(ta), "+v"(off), "+v"(offx));
      if (!gw) { VM0 if (layer) __builtin_amdgcn_s_sleep(DL2); else __builtin_amdgcn_s_sleep(DL2A); }
      BAR
      u32x32 qs;
      u32x4 xa;
      uint32_t tq, ag;
      asm volatile(LD9 : "=&{v[208:239]}"(qs), "=&{v[240:243]}"(xa), "=&v"(tq), "=&s"(ag) : "v"(off), "v"(offx), "s"(ws) : "memory");
      if (layer == 0) {
#define RFL(x) __uint_as_float(__builtin_amdgcn_readfirstlane(__float_as_uint(x)))
        isc0 = RFL(LAS(float, sb + L_HS + 16)); isc1 = RFL(LAS(float, sb + L_HS + 20));
        f1k0 = RFL((float)LAS(_Float16, sb + L_KE + 1024)); f1k1 = RFL((float)LAS(_Float16, sb + L_KE + 1536 + 1024));
      }
      asm volatile("" ::: "memory");
      { uint32_t z0 = 0; asm volatile("" : "+v"(z0)); if ((tid & 255) < 162) LAS(u32x4, za) = (u32x4){z0, z0, z0, z0}; }
      u32x2 tf[16][2];
      u32x32 tg0, tg1, tg2, tg3;
      u32x16 tg4;
      {
#define LDT(i, a, n0) asm volatile("ds_read_b64 %0, %2 offset:%3\n\tds_read_b64 %1, %2 offset:%4" : "=&v"(tf[i][0]), "=&v"(tf[i][1]) : "v"(a), "i"(2048 - 2 * (n0)), "i"(2056 - 2 * (n0)));
        if (gw) {
          asm volatile(TAPSG : "=&{v[64:95]}"(tg0), "=&{v[96:127]}"(tg1), "=&{v[128:159]}"(tg2), "=&{v[160:191]}"(tg3), "=&{v[192:207]}"(tg4) : "v"(ta) : "memory");
        } else {
          const uint32_t tb = ta + (wo ? 0 : 256);
#pragma unroll
          for (int i = 0; i < 16; ++i) { LDT(i, i < 8 ? ta : tb, 8 * (74 + 12 * (i >> 2)) + 16 * ((i & 3) - 1)) }
        }
      }
      asm volatile("s_waitcnt lgkmcnt(0)" ::: "memory");
#pragma unroll
      for (int i = 0; i < (gw ? 0 : 16); ++i) asm volatile("" : "+v"(tf[i][0]), "+v"(tf[i][1]));
      asm volatile("s_waitcnt vmcnt(0)" : "+{v[208:239]}"(qs), "+{v[240:243]}"(xa) :: "memory");
      f32x4 mn, r;
#pragma unroll 1
      for (int it = 0; it < (1 << 16); ++it) {
#define QS(i) ((f32x4){__uint_as_float(qs[4 * i]), __uint_as_float(qs[4 * i + 1]), __uint_as_float(qs[4 * i + 2]), __uint_as_float(qs[4 * i + 3])})
        const f32x4 s1 = (QS(0) + QS(2)) + (QS(4) + QS(6)), s2 = (QS(1) + QS(3)) + (QS(5) + QS(7));
        mn = s1 * (1.f / 512.f);
        const f32x4 vr = s2 * (1.f / 512.f) - mn * mn;
        r = (f32x4){fmaxf(vr[0], 0.f), fmaxf(vr[1], 0.f), fmaxf(vr[2], 0.f), fmaxf(vr[3], 0.f)};
        if (!__builtin_expect(__any(__builtin_isunordered(vr[0], vr[1]) || __builtin_isunordered(vr[2], vr[3]) || mx4(xa) == SENT), 0)) break;
        asm volatile(POLL9 : "+{v[208:239]}"(qs), "+{v[240:243]}"(xa), "=&v"(tq), "=&s"(ag) : "v"(off), "v"(offx), "s"(ws) : "s82", "s83", "vcc", "scc", "memory");
        if (!__builtin_amdgcn_readfirstlane(ag)) break;
      }
      f2 xf[4], uf[4];
      {
#define PIN4(x) asm volatile("" : "+v"(x[0]), "+v"(x[1]), "+v"(x[2]), "+v"(x[3]));
#define EACH(e) _Pragma("unroll") for (int i = 0; i < 4; ++i) { e; }
        EACH(r[i] = rb1(r[i]); mn[i] = rb1(mn[i]))
        r += 1e-5f;
        asm volatile("" : "+v"(r), "+v"(mn));
        EACH(r[i] = __builtin_amdgcn_sqrtf(rb1(r[i])); xf[i] = ((f2){lo2f(xa[i]), hi2f(xa[i])}))
        asm volatile("" : "+v"(r)); PIN4(xf)
        EACH(r[i] = __builtin_amdgcn_rcpf(rb1(r[i])); uf[i] = xf[i] - mn[i])
        PIN4(uf)
        EACH(uf[i] = rb2(uf[i]))
        PIN4(uf)
        EACH(uf[i] *= r[i])
        PIN4(uf)
        EACH(uf[i] = rb2(uf[i]))
        PIN4(uf)
      }
      LAS(u32x2, du) = (u32x2){packh(uf[0].x, uf[1].x), packh(uf[2].x, uf[3].x)};
      LAS(u32x2, du + UCH) = (u32x2){packh(uf[0].y, uf[1].y), packh(uf[2].y, uf[3].y)};
      BAR
#define RT(r, a, o, st) asm volatile("ds_read_b64_tr_b16 %0, %2 offset:%3\n\tds_read_b64_tr_b16 %1, %2 offset:%4" : "=&v"(r[0]), "=&v"(r[1]) : "v"(a), "i"(o), "i"((o) + (st))); SB0
#define TF(i) ((u32x4){tf[i][0][0], tf[i][0][1], tf[i][1][0], tf[i][1][1]})
#define WQ(a, z, q) LAS(f4, a) = (f4){z[4 * (q)], z[4 * (q) + 1], z[4 * (q) + 2], z[4 * (q) + 3]};
#define SKP(i) { float k0 = uf[i].x * lo2f(dsk), k1 = uf[i].y * hi2f(dsk); OPQ(k0); OPQ(k1); uf[i] = (f2){rb1(k0), rb1(k1)}; asm volatile("" : "+v"(uf[i])); SB0 }
      f32x4 ya = {0.f, 0.f, 0.f, 0.f}, yb = {0.f, 0.f, 0.f, 0.f};
#define WFL(of) asm volatile("s_mov_b64 exec, 1\n\tds_write_b32 %0, %1 offset:%2\n\ts_mov_b64 exec, -1" :: "v"(fo), "v"(fv), "i"(of) : "memory");
      if (gw) {
        asm volatile(CONVG : "+{v[244:245]}"(uf[0]), "+{v[246:247]}"(uf[1]), "+{v[248:249]}"(uf[2]), "+{v[250:251]}"(uf[3]), "+{v[64:95]}"(tg0), "+{v[96:127]}"(tg1), "+{v[128:159]}"(tg2), "+{v[160:191]}"(tg3), "+{v[192:207]}"(tg4)
                     : "v"(a1), "v"(a2), "v"(a3), "v"(ra), "v"(rc), "v"(d0), "v"(d1), "v"(d2), "v"(d3), "v"(fa), "v"(fo), "v"(fv),
                       "s"(dsk << 16), "s"(dsk & 0xffff0000u), "s"(wo) : CONVG_CLOB);
      } else {
        {
          const h2 p0 = LAS(h2, ra), p1 = LAS(h2, ra + UCH);
          const u32x2 k0 = LAS(u32x2, rc), k1 = LAS(u32x2, rc + UCH);
          const uint32_t q0 = k0[0], q1 = k0[1], q2 = k1[0], q3 = k1[1];
          SKP(0) SKP(1) SKP(2) SKP(3)
          ya = (f32x4){(float)p0[1], (float)(*(const h2*)&q0)[0], (float)(*(const h2*)&q0)[1], (float)(*(const h2*)&q1)[0]} * -f1k0;
          yb = (f32x4){(float)p1[1], (float)(*(const h2*)&q2)[0], (float)(*(const h2*)&q2)[1], (float)(*(const h2*)&q3)[0]} * -f1k1;
          asm volatile("" : "+v"(ya), "+v"(yb));
        }
        if (layer == 0) {
          const u32x4 sent = {SENT, SENT, SENT, SENT};
          const uint32_t o = p * 8192 + wj * 2048 + lane * 16;
#pragma unroll
          for (int i = 0; i < 6; ++i) { ST16(o, sent, ws + ((i >> 1) == 2 ? W_X3 : 8 * MB + (i >> 1) * 2 * MB) + (i & 1) * 1024) }
          if (wj == 2) ST16(W_ST + 8 * MB + bx * 256 + r16 * 16, sent, ws)
        }
        u32x2 D[4][2];
        flag_wait(fa, fv);
        __builtin_amdgcn_s_setprio(FPR);
        if (!wo) {
          f32x16 z;
          ZERO16(z)
#define L8(u) RT(D[(u) & 3], a1, ((u) / 6) * S8A + (3 - (((u) % 6) >> 1)) * 576 + (((u) & 1) ? 0 : 256), 64)
          L8(0) L8(1) L8(2) L8(3)
#pragma unroll
          for (int u = 0; u < 12; ++u) {
            asm volatile("s_waitcnt lgkmcnt(%2)" : "+v"(D[u & 3][0]), "+v"(D[u & 3][1]) : "i"(u < 8 ? 6 : 2 * (11 - u) + 1));
            const u32x4 d = {D[u & 3][0][0], D[u & 3][0][1], D[u & 3][1][0], D[u & 3][1][1]}, t = TF((u / 6) * 4 + u % 6 + (u % 6 < 4 ? 0 : 4));
            z = MFH(d, t, z); SB0
            if (u + 4 < 12) { L8(u + 4) }
            if (u == 7) WFL(FT)
          }
#pragma unroll
          for (int q4 = 0; q4 < 4; ++q4) { WQ(d0 + 2048 + q4 * 1024, z, q4) }
        } else {
          f32x16 z0, z1, z2;
          ZERO16(z0) ZERO16(z1) ZERO16(z2)
#define L16U(u) if ((u) >= 8) { RT(D[(u) & 3], a1, (((u) >> 2) & 1) * S8A + (1 - (((u) & 3) >> 1)) * 576 + (((u) & 1) ? 0 : 256), 64) } \
                else if ((u) & 2) { RT(D[(u) & 3], a3, ((u) >> 2) * S16A + (((u) & 1) ? 0 : 512), 128) } else { RT(D[(u) & 3], a2, ((u) >> 2) * S16A + (((u) & 1) ? 0 : 512), 128) }
          L16U(0) L16U(1) L16U(2) L16U(3)
#pragma unroll
          for (int u = 0; u < 16; ++u) {
            asm volatile("s_waitcnt lgkmcnt(%2)" : "+v"(D[u & 3][0]), "+v"(D[u & 3][1]) : "i"(u < 12 ? 6 : 2 * (15 - u) + 1));
            const u32x4 d = {D[u & 3][0][0], D[u & 3][0][1], D[u & 3][1][0], D[u & 3][1][1]}, t = TF(u ^ 8);
            if (u >= 8) z0 = MFH(d, t, z0); else if (u & 2) z1 = MFH(d, t, z1); else z2 = MFH(d, t, z2);
            SB0
            if (u + 4 < 16) { L16U(u + 4) }
            if (u == 11) WFL(FT)
            if (u == 12 || u == 13) {
#pragma unroll
              for (int j = 4 * (u - 12); j < 4 * (u - 11); ++j) { z2[8 + j] += z1[8 + j]; OPQ(z2[8 + j]); }
              SB0
            }
          }
#pragma unroll
          for (int q4 = 0; q4 < 4; ++q4) { WQ((d1 ^ ((q4 & 1) * 32)) + (q4 >> 1) * 2048, z2, q4) }
          WQ(d0, z0, 0) WQ(d0 + 1024, z0, 1)
        }
        __builtin_amdgcn_s_setprio(0);
        VM0
      }
      BAR
      {
        f32x4 v[6];
        v[0] = LAS(f32x4, e0); v[1] = LAS(f32x4, e0 + SCH);
        if (!gw) {
          v[2] = LAS(f32x4, e1); v[3] = LAS(f32x4, e1 + 10240); v[4] = LAS(f32x4, d2); v[5] = LAS(f32x4, d2 + 10240);
          ya = (ya + v[0]) + (v[2] + v[4]); yb = (yb + v[1]) + (v[3] + v[5]);
        } else {
          if (wu >= 2) { ya = LAS(f32x4, e1); yb = LAS(f32x4, e1 + 10240); }
          ya += v[0]; yb += v[1];
        }
        ya *= isc0; yb *= isc1;
        asm volatile("" : "+v"(ya), "+v"(yb));
      }
      uint32_t o[4];
      EACH(uf[i] += ((f2){rb1(ya[i]), rb1(yb[i])}))
      PIN4(uf)
      EACH(xf[i] += rb2(uf[i]))
      PIN4(xf)
      EACH(o[i] = pack2(xf[i].x, xf[i].y))
      asm volatile("" : "=v"(bias), "=v"(kp), "=v"(qp), "=v"(vp), "=v"(ooff));
      if (!gw) asm volatile("" : "=v"(kacc[0]), "=v"(kacc[1]), "=v"(kacc[2]), "=v"(kacc[3]), "=v"(af[16]), "=v"(af[17]), "=v"(af[18]), "=v"(af[19]), "=v"(af[20]), "=v"(af[21]), "=v"(af[22]), "=v"(af[23]),
                            "=v"(af[24]), "=v"(af[25]), "=v"(af[26]), "=v"(af[27]), "=v"(af[28]), "=v"(af[29]), "=v"(af[30]), "=v"(af[31]));
      asm volatile("" : "=v"(pm[0]), "=v"(pm[1]), "=v"(pm[2]), "=v"(pm[3]), "=v"(pm[4]), "=v"(pm[5]), "=v"(pm[6]));
      ST16(oo, ((u32x4){min(o[0], SENT - 1u), min(o[1], SENT - 1u), min(o[2], SENT - 1u), min(o[3], SENT - 1u)}), ws)
      if (layer) {
        const uint32_t la = W_X3 + p * 8192 + (lane & 7) * 1024 + (tid >> 3) * 4, lo = (bi * 64 + (tid >> 3)) * 1024 + xcd * 128 + (lane & 7) * 16;
        uint32_t t_, c_;
        asm volatile("s_waitcnt vmcnt(0)\n\ts_sleep %7\n\ts_mov_b32 %2, 0x10000\n"
                     "1:\n\tglobal_load_dword v252, %3, %5 sc1\n\tglobal_load_dword v253, %3, %5 offset:256 sc1\n\tglobal_load_dword v254, %3, %5 offset:512 sc1\n\t"
                     "global_load_dword v255, %3, %5 offset:768 sc1\n\ts_waitcnt vmcnt(0)\n\tv_max3_u32 %1, v252, v253, v254\n\tv_max_u32 %1, %1, v255\n\t"
                     "v_cmp_eq_u32 vcc, -1, %1\n\ts_cbranch_vccz 2f\n\ts_sub_u32 %2, %2, 1\n\ts_cbranch_scc0 1b\n"
                     "2:\n\tglobal_store_dwordx4 %4, v[252:255], %6 " OAUX
                     : "+v"(oo), "=&v"(t_), "=&s"(c_) : "v"(la), "v"(lo), "s"(ws), "s"(ar.out), "i"(DL4) : "vcc", "scc", "memory");
        break;
      }
    }
    {
      const uint32_t lb = 8 * MB;
      IDS
      if (gw) {
        ATTP
        asm volatile(GM(GEMM2) : "+v"(GTS), "=&s"(st) : GIN, "v"(hh * 512 + n * 4), "s"(ws + W_X2 + (size_t)(rb * 4 + wj) * 32768 + 2048),
                     "v"(W_PS + xcd * 32768 + (bi & 3) * 8192 + wj * 2048 + lane * 4), "s"(ws) : GEMM2_CLOB);
      }
      if (!gw) {
        HDECL
        const char* xh = ws + W_X2 + (size_t)(rb ? rb * 4 - 1 : 0) * 32768 + wj * 8192 + 4096;
        const uint32_t xv = g * 512 + r16 * 4 + 64;
        uint32_t h[16];
        ATTP
        VM0
        if (DL3) __builtin_amdgcn_s_sleep(DL3);
        __builtin_amdgcn_s_setprio(3);
        BAR
        if (DLH) __builtin_amdgcn_s_sleep(DLH);
#pragma unroll
        for (int i = 0; i < 16; ++i) asm volatile("global_load_dword %0, %1, %2 offset:%3 sc1" : "=v"(h[i]) : "v"(xv), "s"(xh), "i"((i >> 2) * 2048 + (i & 3) * 128 - 4096));
        CHK16(h, 0, xv, xh, 2048, 128)
#pragma unroll
        for (int i = 0; i < 4; ++i) LAS(u32x4, hr + (((wj * 16 + i * 4 + g) ^ r16) * 16)) = (u32x4){h[4 * i], h[4 * i + 1], h[4 * i + 2], h[4 * i + 3]};
        HPART((h + 0), (h + 4), (h + 8), (h + 12), wj)
        flag_set(flw + FH, 3);
        flag_wait(fl4 + FH, 3);
#pragma unroll
        for (int s = 0; s < 16; ++s) hv[s] = LAS(u32x4, hr + (s >> 2) * 256 + ((((s & 3) * 4 + g) ^ r16) * 16));
        HSTAT
        const uint32_t pw = sb + hp * 1024 + (g >> 1) * 3072 + (8 * (g & 1) + (r16 >> 2)) * 64 + (lane & 3) * 8 + ((hd ^ (g & 1)) * 32), pw2 = pw + 49152;
#pragma unroll
        for (int k = 0; k < 4; ++k) {
          u32x2 e[8];
#pragma unroll
          for (int s = 0; s < 8; ++s) asm volatile("ds_read_b64_tr_b16 %0, %1 offset:%2" : "=v"(e[s]) : "v"(k < 2 ? pw : pw2), "i"((k & 1) * 24576 + (s >> 1) * 6144 + (s & 1) * 256));
          asm volatile("s_waitcnt lgkmcnt(0)" : "+v"(e[0]), "+v"(e[1]), "+v"(e[2]), "+v"(e[3]), "+v"(e[4]), "+v"(e[5]), "+v"(e[6]), "+v"(e[7]));
#pragma unroll
          for (int s = 0; s < 4; ++s) { const u32x4 wv = {e[2 * s][0], e[2 * s][1], e[2 * s + 1][0], e[2 * s + 1][1]}; hacc = MFS(wv, hv[k * 4 + s], hacc); }
          SB0
        }
        HLN(hacc, hp, hd)
        __builtin_amdgcn_s_setprio(0);
        flag_wait(fl4 + FX, 3);
        LDAQ(AFO, 2, layer, 0) LDAQ(AFO, 3, layer, 0)
      }
    }
  }
}

__global__ __launch_bounds__(512, 2) __attribute__((amdgpu_num_vgpr(126))) void k_all(const Args ar) {
  __shared__ __attribute__((aligned(128))) char smem[163840];
  const uint32_t wu = __builtin_amdgcn_readfirstlane(threadIdx.x >> 6);
  const uint32_t sb = (uint32_t)(uintptr_t)LDSP(smem);
  {
    uint32_t t0, t1;
    u32x4 t2;
    const uint32_t tid = threadIdx.x, p = (blockIdx.x & 7) * 32 + (blockIdx.x >> 3);
    asm volatile("s_cmp_lt_u32 %3, 4\n\ts_cbranch_scc0 1f\n\ts_load_dwordx16 s[44:59], %8, 0x10\n\ts_load_dwordx2 s[60:61], %8, 0x60\n\t"
                 "s_getpc_b64 vcc\n\tv_min_u32 %0, .Lk_all_end-1f-4, %4\n\ts_mov_b64 exec, 15\n\tglobal_load_dword v255, %0, vcc\n\t"
                 "s_cmp_gt_u32 %3, 1\n\ts_cbranch_scc1 2f\n\ts_cmp_eq_u32 %3, 0\n\ts_cselect_b64 exec, 0xff, -1\n\ts_cselect_b32 %1, 0, 0x3f803f80\n\tv_mov_b32 v64, %1\n\tv_mov_b32 v65, %1\n\t"
                 "v_mov_b32 v66, %1\n\tv_mov_b32 v67, %1\n\ts_cselect_b32 %1, %6, 0\n\ts_add_u32 %1, %1, %7\n\tv_lshl_add_u32 %0, %5, 4, %1\n\tds_write_b128 %0, v[64:67]\n"
                 "2:\n\ts_mov_b64 exec, -1\n\tv_mov_b32 %0, %10\n\tv_lshlrev_b32 v68, 1, %5\n\ts_lshl_b32 %1, %3, 3\n\tv_subrev_u32 v69, %1, v68\n\tv_and_b32 v68, 0x60, v68\n\tv_and_b32 v69, 28, v69\n\t"
                 "s_lshl_b32 %1, %11, 9\n\tv_or3_b32 v68, v68, v69, %1\n\ts_lshl_b32 %1, %11, 2\n\tv_mov_b32 v69, %1\n\ts_waitcnt lgkmcnt(0)\n\t"
                 "global_load_ushort v168, %9, s[44:45]\n\tglobal_load_ushort v169, %9, s[44:45] offset:1024\n\t"
                 "global_load_ushort v170, %9, s[44:45] offset:2048\n\tglobal_load_ushort v171, %9, s[44:45] offset:3072\n\tglobal_load_dword v166, %0, s[60:61] sc1\n\t"
                 "global_load_dword v160, v68, s[46:47]\n\tglobal_load_dword v161, v68, s[48:49]\n\tglobal_load_dword v162, v68, s[54:55]\n\tglobal_load_dword v163, v68, s[56:57]\n\t"
                 "global_load_dword v164, v68, s[50:51]\n\tglobal_load_dword v165, v68, s[52:53]\n\tglobal_load_dword v167, v69, s[58:59]\n1:"
                 : "=&v"(t0), "=&s"(t1), "=&{v[64:67]}"(t2) : "s"(wu), "v"(((tid & 3) * 128 + wu * 32 + (blockIdx.x >> 3)) * 128), "v"(tid), "i"(L_FL - L_HST), "i"(L_HST),
                   "s"(__builtin_amdgcn_kernarg_segment_ptr()), "v"(((p & 15) * 32 + wu * 8 + (tid & 1) * 4) * 1024 + (p >> 4) * 64 + (tid & 62)), "i"(W_SYN), "s"(p)
                 : "s44", "s45", "s46", "s47", "s48", "s49", "s50", "s51", "s52", "s53", "s54", "s55", "s56", "s57", "s58", "s59", "s60", "s61", "v68", "v69",
                   "v160", "v161", "v162", "v163", "v164", "v165", "v166", "v167", "v168", "v169", "v170", "v171", "scc", "vcc", "memory");
  }
  if (wu < 4) body<true>(ar, sb, wu); else body<false>(ar, sb, wu);
  asm volatile(".Lk_all_end:" ::: "v255");
}

extern "C" void kernel_launch(void* const* d_in, const int* in_sizes, int n_in,
                              void* d_out, int out_size, void* d_ws, size_t ws_size,
                              hipStream_t stream) {
  Args a;
  a.x = (const u16*)d_in[0]; a.w = (const u16*)d_in[1]; a.wo = (const u16*)d_in[2];
  a.A_re = (const u16*)d_in[4]; a.A_im = (const u16*)d_in[5]; a.B_re = (const u16*)d_in[6]; a.B_im = (const u16*)d_in[7];
  a.C_re = (const u16*)d_in[8]; a.C_im = (const u16*)d_in[9]; a.log_dt = (const u16*)d_in[10]; a.Dskip = (const u16*)d_in[11];
  a.out = (u16*)d_out; a.ws = (char*)d_ws;
  k_all<<<256, 512, 0, stream>>>(a);
}
```

```cpp
#include <hip/hip_runtime.h>
#include <stdint.h>

typedef unsigned short u16;
constexpr int rnd_l(int v) { int e = 0; while ((v >> e) >= 256) ++e; if (!e) return v; const int h = 1 << (e - 1), q = v >> e, r = v & ((1 << e) - 1); return (q + ((r > h || (r == h && (q & 1))) ? 1 : 0)) << e; }
constexpr int slot_l(int v) { v = rnd_l(v); int e = 0; while ((v >> e) >= 256) ++e; return e * 128 + (v >> e); }
struct Tabs { u16 img[1032]; u16 prd[1024]; };
constexpr Tabs mk_tabs() {
  Tabs t{};
  for (int i = 0; i < 1032; ++i) {
    const int f = 1023 - i, o = f >> 3, a = (o - 74) / 12, m = f - 8 * (74 + 12 * a);
    int v = -1;
    if (f >= 32 && f - 32 <= 525) v = f - 32;
    if (o >= 74 && o < 122 && m < 64) v = a >= 2 ? 16 * (65 + m) - 8 * (a - 2) : (m < 62 ? 8 * (66 + m) + 4 * a : (m == 62 ? 1024 : -1));
    t.img[i] = (u16)(2 * (v < 0 ? 700 : slot_l(v)));
  }
  for (int w = 0; w < 1024; ++w) {
    const int tile = (w >> 9) * 2 + ((w >> 2) & 1), lane = (w >> 3) & 63;
    const int r = (lane >> 4) * 4 + (w & 3), C = (tile & 1) * 16 + (lane & 15);
    const int lr = 8 * C + (r < 4 ? r : (r == 4 ? 258 : 256 * (r - 4))) + 4 * (tile >> 1);
    const bool ok = r <= 12 && lr <= 2048 && rnd_l(lr) == lr;
    t.prd[w] = (u16)(2 * (ok ? slot_l(lr) : 704 + (lane & 63)));
  }
  return t;
}
extern "C" { extern __device__ __attribute__((aligned(16), used)) const Tabs g_tabs = mk_tabs(); }

struct Args { const u16 *x, *w, *wo, *A_re, *A_im, *B_re, *B_im, *C_re, *C_im, *log_dt, *Dskip; u16* out; char* ws; };

__global__ __launch_bounds__(512, 2) __attribute__((aligned(4096))) void k_all(const Args ar) {
  __shared__ __attribute__((aligned(128))) char smem[163840];
  asm volatile(
    ".Lk_all_beg:\ns_load_dwordx16 s[64:79], s[0:1], 0\ns_load_dwordx8 s[80:87], s[0:1], 0x40\ns_load_dwordx2 s[4:5], s[0:1], 0x60\nv_readfirstlane_b32 s3, v0\n"
    "s_lshr_b32 s25, s2, 3\ns_lshr_b32 s3, s3, 6\ns_cmp_lg_u32 s3, 4\ns_cbranch_scc1 .Loth1\ns_lshr_b32 s51, s2, 5\ns_and_b32 s60, s25, 3\nv_bfe_u32 v64, v0, 2, 4\n"
    "s_lshl1_add_u32 s50, s60, s51\nv_lshrrev_b32 v65, 4, v0\nv_mul_u32_u24 v64, 0xc00, v64\ns_and_b32 s50, s50, 7\nv_and_b32 v66, 2, v65\ns_and_b32 s61, s2, 1\n"
    "v_xor_b32 v66, v66, v0\ns_lshl3_add_u32 s51, s61, s51\nv_and_b32 v66, 3, v66\ns_lshl_b32 s51, s51, 6\nv_lshl_add_u32 v64, v66, 4, v64\ns_mul_i32 m0, s50, 12288\n"
    "v_add_u32 v64, s51, v64\ns_lshl_b32 s59, m0, 4\ns_mov_b32 s58, 3\ns_waitcnt lgkmcnt(0)\ns_add_u32 s52, s66, s59\n.Le5:\ns_addc_u32 s53, s67, 0\n"
    "global_load_lds_dwordx4 v64, s[52:53] offset:0\nglobal_load_lds_dwordx4 v64, s[52:53] offset:1024\nglobal_load_lds_dwordx4 v64, s[52:53] offset:2048\n"
    "s_add_u32 m0, m0, 3072\ns_add_u32 s59, s59, 49152\ns_sub_u32 s58, s58, 1\ns_cbranch_scc1 .Le5x\ns_add_u32 s52, s66, s59\ns_branch .Le5\n.Le5x:\n"
    "s_bfe_u32 s61, s2, 0x20001\nv_and_b32 v65, 3, v65\ns_lshl2_add_u32 s61, s61, s60\nv_xor_b32 v65, v65, v0\ns_lshl_b32 s61, s61, 17\nv_and_b32 v65, 7, v65\n"
    "s_add_u32 s48, s64, s61\nv_bfe_u32 v66, v0, 3, 3\ns_addc_u32 s49, s65, 0\nv_lshlrev_b32 v66, 10, v66\ns_lshl_b32 s59, s50, 7\nv_lshl_add_u32 v65, v65, 4, v66\n"
    "s_add_u32 s54, s48, s59\nv_xor_b32 v66, 64, v65\ns_addc_u32 s55, s49, 0\nv_add_u32 v67, 14336, v65\ns_mov_b32 m0, 98304\nv_add_u32 v68, 21504, v66\n"
    "v_add_u32 v66, 7168, v66\n.Le6:\nglobal_load_lds_dwordx4 v65, s[54:55] offset:0\nglobal_load_lds_dwordx4 v66, s[54:55] offset:1024\n"
    "global_load_lds_dwordx4 v67, s[54:55] offset:2048\nglobal_load_lds_dwordx4 v68, s[54:55] offset:3072\ns_add_u32 s54, s54, 32768\ns_addc_u32 s55, s55, 0\n"
    "s_add_u32 m0, m0, 12288\ns_cmp_lt_u32 m0, 147456\ns_cbranch_scc1 .Le6\ns_barrier\ns_branch .Lcom2\n.Lhop:\ns_sub_u32 s89, s89, 1\ns_cbranch_scc1 .Lhpx\n"
    "s_add_u32 s90, s90, 1024\ns_addc_u32 s91, s91, 0\ns_setpc_b64 s[90:91]\n.Lhpx:\ns_mov_b64 exec, -1\ns_setpc_b64 s[94:95]\n"
    ".Loth1:\ns_and_b32 s24, s2, 7\ns_lshl_b32 s44, s3, 2\ns_lshl_b32 s18, s24, 5\ns_add_u32 s18, s18, s25\ns_getpc_b64 s[92:93]\n.Lk_pc:\n"
    "s_sub_u32 s92, s92, .Lk_pc-.Lk_all_beg+4\ns_subb_u32 s93, s93, 0\ns_cmp_gt_u32 s3, 3\ns_cbranch_scc1 .Loth3\nv_and_b32 v65, 63, v0\ns_lshl3_add_u32 s47, s44, s25\n"
    "v_lshl_add_u32 v64, v65, 2, s3\ns_sub_u32 s47, s47, 1984\nv_cmp_gt_u32 vcc, 16, v65\nv_lshl_add_u32 v65, v65, 7, s47\ns_and_b32 s47, s18, 15\n"
    "v_cndmask_b32 v64, v65, v64, vcc\ns_lshl1_add_u32 s47, s47, s3\nv_min_u32 v64, (.Lk_all_end-.Lk_all_beg)/128-8, v64\ns_lshl_b32 s47, s47, 14\nv_lshlrev_b32 v64, 7, v64\n"
    "s_mov_b64 exec, 0x3ffff\nglobal_load_dword v255, v64, s[92:93] offset:1024\ns_mov_b64 exec, -1\ns_and_b32 s48, s18, 0xf0\nv_and_b32 v69, 32, v0\n"
    "s_lshl2_add_u32 s47, s48, s47\nv_and_b32 v71, 31, v0\ns_sub_u32 s47, s47, 28672\nv_lshl_add_u32 v69, v69, 8, s47\nv_mov_b32 v70, 16777216\n"
    "v_lshl_add_u32 v69, v71, 1, v69\n.Loth3:\nv_lshlrev_b32 v64, 1, v0\ns_lshl_b32 s47, s18, 9\nv_and_b32 v64, 0x1fc, v64\ns_lshl_b32 s46, s18, 2\nv_or_b32 v64, s47, v64\n"
    "v_mov_b32 v65, s46\ns_mov_b32 s27, 0\ns_cmp_gt_u32 s3, 3\ns_waitcnt lgkmcnt(0)\ns_cbranch_scc1 .Loth3x\nglobal_load_dword v166, v70, s[4:5] sc1\ns_cmp_eq_u32 s3, 0\n"
    "s_cbranch_scc1 .Loth3x\ns_cmp_eq_u32 s3, 1\ns_cbranch_scc1 .Loth3y\nglobal_load_ushort v168, v69, s[68:69] offset:-4096\n"
    "global_load_ushort v169, v69, s[68:69] offset:-3072\nglobal_load_ushort v170, v69, s[68:69] offset:-2048\nglobal_load_ushort v171, v69, s[68:69] offset:-1024\n"
    "global_load_ushort v172, v69, s[68:69] offset:0\nglobal_load_ushort v173, v69, s[68:69] offset:1024\nglobal_load_ushort v174, v69, s[68:69] offset:2048\n"
    "global_load_ushort v175, v69, s[68:69] offset:3072\ns_branch .Lcom2\n.Loth3x:\nglobal_load_dword v160, v64, s[70:71]\nglobal_load_dword v161, v64, s[72:73]\n"
    "global_load_dword v162, v64, s[78:79]\nglobal_load_dword v163, v64, s[80:81]\nglobal_load_dword v164, v64, s[74:75]\nglobal_load_dword v165, v64, s[76:77]\n"
    "global_load_dword v167, v65, s[82:83]\ns_mov_b32 s27, 4\n.Loth3y:\ns_cmp_gt_u32 s3, 1\ns_cbranch_scc1 .Lcom2\ns_cmp_eq_u32 s3, 0\ns_cselect_b64 exec, 0xff, -1\n"
    "s_cselect_b32 s47, 0, 0x3f803f80\nv_mov_b32 v80, s47\nv_mov_b32 v81, s47\nv_mov_b32 v82, s47\nv_mov_b32 v83, s47\ns_cselect_b32 s47, 17280, 0\n"
    "s_add_u32 s47, s47, 146432\nv_lshl_add_u32 v68, v0, 4, s47\nds_write_b128 v68, v[80:83]\ns_mov_b64 exec, -1\n"
    ".Lcom2:\ns_and_b32 s24, s2, 7\ns_lshr_b32 s25, s2, 3\ns_and_b32 s13, s3, 3\ns_lshl_b32 s43, s24, 5\ns_add_u32 s18, s43, s25\ns_lshr_b32 s43, s24, 1\n"
    "s_lshl_b32 s42, s43, 2\ns_and_b32 s41, s25, 3\ns_add_u32 s19, s42, s41\ns_and_b32 s43, s24, 1\ns_lshl_b32 s42, s43, 3\ns_lshr_b32 s41, s25, 2\ns_add_u32 s20, s42, s41\n"
    "s_lshl_b32 s43, s24, 3\ns_and_b32 s42, s25, 7\ns_add_u32 s21, s43, s42\ns_lshr_b32 s22, s25, 3\ns_lshr_b32 s43, s25, 2\ns_and_b32 s42, s25, 3\ns_lshl_b32 s41, s42, 1\n"
    "s_add_u32 s40, s43, s41\ns_and_b32 s23, s40, 7\ns_lshl_b32 s44, s18, 2\ns_mov_b64 s[6:7], s[86:87]\ns_mov_b64 s[8:9], s[64:65]\ns_mov_b32 s12, 0\n"
    "v_bfe_u32 v251, v0, 4, 2\nv_lshlrev_b32 v250, 2, v251\ns_add_u32 s43, s19, s3\ns_cmp_lt_u32 s43, 1\ns_cselect_b32 s42, 8, 0\nv_and_b32 v249, 7, v0\n"
    "v_add_u32 v248, 1, v249\nv_max_u32 v247, s42, v248\nv_sub_u32 v2, v250, v247\nv_add_u32 v246, 9, v249\nv_sub_u32 v40, v246, v247\nv_add_u32 v3, 1, v2\n"
    "v_add_u32 v4, 2, v2\ns_cbranch_execz .Lhop\nv_add_u32 v5, 3, v2\nv_cmp_lt_u32 s[36:37], v2, v40\nv_cmp_lt_u32 s[38:39], v3, v40\nv_cmp_lt_u32 s[40:41], v4, v40\n"
    "v_cmp_lt_u32 s[42:43], v5, v40\nv_mov_b32 v40, 0xf149f2ca\nv_cndmask_b32 v2, v40, 0, s[36:37]\nv_cndmask_b32 v3, v40, 0, s[38:39]\nv_cndmask_b32 v4, v40, 0, s[40:41]\n"
    "v_cndmask_b32 v5, v40, 0, s[42:43]\nv_and_b32 v251, 15, v0\nv_max_u32 v250, 1, v251\nv_mul_u32_u24 v249, 0x1a0, v250\nv_bfe_u32 v248, v0, 4, 2\ns_mul_i32 s43, s3, 0xd00\n"
    "s_add_u32 s42, s43, 0x18040\nv_add_u32 v6, s42, v249\nv_lshl_add_u32 v6, v248, 4, v6\nv_and_b32 v247, 7, v0\nv_mul_u32_u24 v246, 0x1a0, v247\n"
    "s_add_u32 s41, s43, 0x18d00\nv_add_u32 v7, s41, v246\nv_lshl_add_u32 v7, v248, 4, v7\nv_bfe_u32 v245, v0, 2, 2\nv_lshl_add_u32 v244, v248, 2, v245\n"
    "v_max_u32 v243, 1, v244\nv_mul_u32_u24 v242, 0x1a0, v243\nv_and_b32 v241, 3, v0\ns_add_u32 s40, s43, 0x18080\nv_add_u32 v8, s40, v242\nv_lshl_add_u32 v8, v241, 3, v8\n"
    "v_bfe_u32 v240, v0, 4, 1\ns_lshl_b32 s39, s19, 7\ns_lshl_b32 s38, s3, 4\ns_add_u32 s37, s39, s38\nv_add_u32 v239, s37, v240\nv_lshl_add_u32 v239, v247, 1, v239\n"
    "v_lshrrev_b32 v238, 5, v239\nv_bfe_u32 v237, v0, 3, 1\nv_bfe_u32 v236, v0, 5, 1\nv_and_b32 v235, 31, v239\ns_lshl_b32 s36, s20, 11\nv_lshl_add_u32 v9, v238, 15, s36\n"
    "v_lshl_add_u32 v9, v237, 10, v9\nv_lshl_add_u32 v9, v236, 9, v9\nv_lshl_add_u32 v9, v235, 4, v9\nv_mov_b64 v[40:41], 0\nv_mov_b64 v[42:43], 0\nv_mov_b64 v[44:45], 0\n"
    "v_mov_b64 v[46:47], 0\nv_mov_b64 v[48:49], 0\nv_mov_b64 v[50:51], 0\nv_mov_b64 v[52:53], 0\nv_mov_b64 v[54:55], 0\nv_mov_b32 v56, 163712\ns_add_u32 s16, s84, s44\n"
    "s_addc_u32 s17, s85, 0\ns_waitcnt lgkmcnt(0)\ns_cmp_eq_u32 s3, 4\ns_cbranch_scc1 .Llstb7\ns_barrier\ns_cmp_lt_u32 s3, 4\ns_cbranch_scc1 .Lgset8\n"
    "v_bfe_u32 v251, v0, 5, 1\nv_mul_u32_u24 v250, 0xc00, v251\nv_bfe_u32 v249, v0, 4, 1\nv_bfe_u32 v248, v0, 2, 2\nv_and_b32 v247, 3, v0\ns_lshl_b32 s43, s13, 10\n"
    "s_add_u32 s42, s43, 0xfffffc00\nv_add_u32 v27, s42, v250\nv_lshl_add_u32 v27, v249, 9, v27\nv_lshl_add_u32 v27, v248, 6, v27\nv_lshl_add_u32 v27, v247, 3, v27\n"
    "v_lshl_add_u32 v27, v249, 5, v27\nv_and_b32 v246, 15, v0\nv_bfe_u32 v245, v0, 4, 2\nv_xor_b32 v244, v245, v246\ns_mov_b32 s41, 0x23c00\n"
    "v_lshl_add_u32 v32, v246, 10, s41\nv_lshl_add_u32 v32, v244, 4, v32\ns_lshl_b32 s40, s13, 7\ns_add_u32 s39, s40, 0x27b80\nv_lshl_add_u32 v58, v245, 4, s39\n"
    "s_lshl_b32 s38, s13, 2\ns_add_u32 s37, s38, 0x27f80\nv_mov_b32 v37, s37\nv_mul_u32_u24 v243, 0xd0, v246\ns_lshl_b32 s36, s13, 6\ns_add_u32 s35, s36, 0x17fc0\n"
    "v_add_u32 v38, s35, v243\nv_lshl_add_u32 v38, v245, 3, v38\ns_mov_b32 s34, 0x27f80\nv_lshl_add_u32 v39, v247, 2, s34\ns_add_u32 s44, s23, 0\ns_and_b32 s44, s44, 7\n"
    "s_mul_i32 s45, s44, 12288\ns_lshl_b32 s44, s44, 7\nv_add_u32 v192, s45, v27\nv_xor_b32 v208, s44, v32\nv_xor_b32 v193, 32, v192\nv_xor_b32 v209, 64, v208\n"
    "s_add_u32 s44, s23, 1\ns_and_b32 s44, s44, 7\ns_mul_i32 s45, s44, 12288\ns_lshl_b32 s44, s44, 7\nv_add_u32 v194, s45, v27\nv_xor_b32 v210, s44, v32\n"
    "v_xor_b32 v195, 32, v194\nv_xor_b32 v211, 64, v210\ns_add_u32 s44, s23, 2\ns_and_b32 s44, s44, 7\ns_mul_i32 s45, s44, 12288\ns_lshl_b32 s44, s44, 7\n"
    "v_add_u32 v196, s45, v27\nv_xor_b32 v212, s44, v32\nv_xor_b32 v197, 32, v196\nv_xor_b32 v213, 64, v212\ns_add_u32 s44, s23, 3\ns_and_b32 s44, s44, 7\n"
    "s_mul_i32 s45, s44, 12288\ns_lshl_b32 s44, s44, 7\nv_add_u32 v198, s45, v27\nv_xor_b32 v214, s44, v32\nv_xor_b32 v199, 32, v198\nv_xor_b32 v215, 64, v214\n"
    "s_add_u32 s44, s23, 4\ns_and_b32 s44, s44, 7\ns_mul_i32 s45, s44, 12288\ns_lshl_b32 s44, s44, 7\nv_add_u32 v200, s45, v27\nv_xor_b32 v216, s44, v32\n"
    "v_xor_b32 v201, 32, v200\nv_xor_b32 v217, 64, v216\ns_add_u32 s44, s23, 5\ns_and_b32 s44, s44, 7\ns_mul_i32 s45, s44, 12288\ns_lshl_b32 s44, s44, 7\n"
    "v_add_u32 v202, s45, v27\nv_xor_b32 v218, s44, v32\nv_xor_b32 v203, 32, v202\nv_xor_b32 v219, 64, v218\ns_add_u32 s44, s23, 6\ns_and_b32 s44, s44, 7\n"
    "s_mul_i32 s45, s44, 12288\ns_lshl_b32 s44, s44, 7\nv_add_u32 v204, s45, v27\nv_xor_b32 v220, s44, v32\nv_xor_b32 v205, 32, v204\nv_xor_b32 v221, 64, v220\n"
    "s_add_u32 s44, s23, 7\ns_and_b32 s44, s44, 7\ns_mul_i32 s45, s44, 12288\ns_lshl_b32 s44, s44, 7\nv_add_u32 v206, s45, v27\nv_xor_b32 v222, s44, v32\n"
    "v_xor_b32 v207, 32, v206\nv_xor_b32 v223, 64, v222\ns_mov_b32 s10, 64\ns_waitcnt vmcnt(0)\nv_and_b32 v224, 127, v0\ns_getpc_b64 s[44:45]\n"
    "s_add_u32 s44, s44, g_tabs@rel32@lo+4\ns_addc_u32 s45, s45, g_tabs@rel32@hi+12\nv_lshlrev_b32 v224, 4, v224\nglobal_load_dwordx4 v[28:31], v224, s[44:45] offset:2064\n"
    "global_load_dwordx4 v[10:13], v224, s[44:45]\nglobal_load_dwordx2 v[14:15], v224, s[44:45] offset:16\ns_branch .Lkgp9\n"
    ".Lgset8:\nv_bfe_u32 v251, v0, 5, 1\nv_bfe_u32 v250, v0, 2, 2\nv_bfe_u32 v249, v0, 4, 1\nv_xor_b32 v248, v249, v251\nv_and_b32 v247, 3, v0\nv_lshlrev_b32 v36, 9, v251\n"
    "v_lshl_add_u32 v36, v250, 6, v36\nv_lshl_add_u32 v36, v248, 5, v36\nv_lshl_add_u32 v36, v247, 3, v36\nv_and_b32 v246, 31, v0\nv_bfe_u32 v245, v0, 1, 3\n"
    "v_xor_b32 v244, v251, v245\ns_nop 0\ns_cbranch_execz .Lhop\ns_mul_i32 s43, s13, 0x3000\ns_add_u32 s42, s43, 0x18000\nv_lshl_add_u32 v27, v246, 7, s42\n"
    "v_lshl_add_u32 v27, v244, 4, v27\nv_mul_u32_u24 v243, 0xd0, v246\ns_mul_i32 s41, s13, 0x1a00\ns_add_u32 s40, s41, 0x18d00\nv_add_u32 v37, s40, v243\n"
    "v_lshl_add_u32 v37, v251, 3, v37\ns_mov_b32 s39, 0x27c00\nv_lshl_add_u32 v38, v246, 2, s39\ns_mov_b32 s38, 0x27fe0\nv_lshl_add_u32 v242, v247, 2, s38\n"
    "v_and_b32 v241, 4, v0\nv_lshlrev_b32 v240, 4, v241\nv_sub_u32 v28, v242, v240\nv_mov_b32 v239, 16\nv_cmp_gt_u32 vcc, 1, v241\ns_nop 1\nv_cndmask_b32 v29, 1, v239, vcc\n"
    "s_lshl_b32 s37, s13, 2\ns_add_u32 s36, s37, 0x27fe0\nv_mov_b32 v30, s36\nv_mov_b32 v31, 0x27fe8\ns_lshr_b32 s35, s2, 4\ns_lshl_b32 s34, s35, 7\ns_and_b32 s33, s2, 15\n"
    "s_lshl_b32 s32, s33, 2\ns_add_u32 s31, s34, s32\ns_add_u32 s31, s31, 0x1002000\nv_mov_b32 v32, s31\nv_and_b32 v251, 0x7f, v0\ns_lshl_b32 s43, s18, 11\n"
    "s_add_u32 s42, s43, 0xe00000\nv_lshl_add_u32 v57, v251, 4, s42\ns_mov_b32 s10, 4\nv_mov_b64 v[64:65], 0\nv_mov_b64 v[66:67], 0\nv_mov_b64 v[68:69], 0\n"
    "v_mov_b64 v[70:71], 0\nv_mov_b64 v[72:73], 0\nv_mov_b64 v[74:75], 0\nv_mov_b64 v[76:77], 0\nv_mov_b64 v[78:79], 0\nv_mov_b64 v[80:81], 0\nv_mov_b64 v[82:83], 0\n"
    "v_mov_b64 v[84:85], 0\nv_mov_b64 v[86:87], 0\nv_mov_b64 v[88:89], 0\nv_mov_b64 v[90:91], 0\nv_mov_b64 v[92:93], 0\nv_mov_b64 v[94:95], 0\nv_mov_b64 v[96:97], 0\n"
    "v_mov_b64 v[98:99], 0\nv_mov_b64 v[100:101], 0\nv_mov_b64 v[102:103], 0\nv_mov_b64 v[104:105], 0\nv_mov_b64 v[106:107], 0\nv_mov_b64 v[108:109], 0\n"
    "v_mov_b64 v[110:111], 0\nv_mov_b64 v[112:113], 0\nv_mov_b64 v[114:115], 0\nv_mov_b64 v[116:117], 0\nv_mov_b64 v[118:119], 0\nv_mov_b64 v[120:121], 0\n"
    "v_mov_b64 v[122:123], 0\nv_mov_b64 v[124:125], 0\nv_mov_b64 v[126:127], 0\nv_mov_b64 v[128:129], 0\nv_mov_b64 v[130:131], 0\nv_mov_b64 v[132:133], 0\n"
    "v_mov_b64 v[134:135], 0\ns_waitcnt vmcnt(0)\nv_readfirstlane_b32 s11, v166\ns_mul_i32 s11, s11, 0x9E3779B1\ns_add_u32 s11, s11, 0x7F4A7C15\ns_cmp_lg_u32 s3, 0\n"
    "s_cbranch_scc1 .Lgm14w\n"
    ".Lkgp9:\ns_bitcmp1_b32 s3, 1\ns_cselect_b32 s44, 0, 16\nv_lshlrev_b32 v240, s44, v167\nv_and_b32 v224, 1, v0\nv_and_b32 v240, 0xffff0000, v240\n"
    "v_lshlrev_b32 v224, 4, v224\ns_mov_b32 s45, 0x3fb8aa3b\nv_mul_f32 v241, s45, v240\nv_xor_b32 v224, 16, v224\nv_fma_f32 v242, v240, s45, -v241\nv_rndne_f32 v243, v241\n"
    "v_fmamk_f32 v242, v240, 0x32a5705f, v242\nv_sub_f32 v241, v241, v243\nv_add_f32 v241, v241, v242\nv_exp_f32 v241, v241\nv_cvt_i32_f32 v243, v243\n"
    "v_lshlrev_b32 v160, v224, v160\nv_lshlrev_b32 v161, v224, v161\nv_lshlrev_b32 v162, v224, v162\nv_lshlrev_b32 v163, v224, v163\nv_lshlrev_b32 v164, v224, v164\n"
    "v_lshlrev_b32 v165, v224, v165\nv_ldexp_f32 v241, v241, v243\nv_and_b32 v160, 0xffff0000, v160\nv_and_b32 v161, 0xffff0000, v161\nv_and_b32 v162, 0xffff0000, v162\n"
    "v_and_b32 v163, 0xffff0000, v163\nv_and_b32 v164, 0xffff0000, v164\nv_and_b32 v165, 0xffff0000, v165\nv_cvt_pk_bf16_f32 v241, 0, v241\nv_mul_f32 v164, v241, v164\n"
    "v_mul_f32 v165, v241, v165\nv_mul_f32 v16, v241, v160\nv_mul_f32 v17, v241, v161\nv_mul_f32 v242, v162, v164\nv_mul_f32 v243, v163, v165\nv_mul_f32 v244, v162, v165\n"
    "v_mul_f32 v245, v163, v164\nv_sub_f32 v242, v242, v243\nv_add_f32 v243, v244, v245\nv_mul_f32 v16, s45, v16\nv_mul_f32 v17, 0.15915494, v17\nv_mul_f32 v244, v242, v242\n"
    "v_mul_f32 v245, v243, v243\nv_add_f32 v244, v244, v245\nv_log_f32 v244, v244\nv_max_f32 v246, abs(v242), abs(v243)\nv_min_f32 v247, abs(v242), abs(v243)\n"
    "v_mul_f32 v18, 0.5, v244\nv_frexp_mant_f32 v248, v246\nv_max_f32 v244, 0xc2700000, v18\nv_rcp_f32 v248, v248\nv_frexp_exp_i32_f32 v246, v246\n"
    "v_max_f32_dpp v244, v244, v244 row_ror:1 row_mask:0xf bank_mask:0xf\nv_frexp_exp_i32_f32 v249, v247\nv_frexp_mant_f32 v247, v247\n"
    "v_max_f32_dpp v244, v244, v244 row_ror:2 row_mask:0xf bank_mask:0xf\nv_mul_f32 v248, v247, v248\nv_sub_u32 v246, v249, v246\n"
    "v_max_f32_dpp v244, v244, v244 row_ror:4 row_mask:0xf bank_mask:0xf\nv_ldexp_f32 v246, v248, v246\nv_mov_b32 v248, 0xbc7a590c\n"
    "v_max_f32_dpp v244, v244, v244 row_ror:8 row_mask:0xf bank_mask:0xf\nv_mul_f32 v247, v246, v246\nv_mov_b32 v245, v244\nv_fmac_f32 v248, 0x3b2d2a58, v247\n"
    "v_fmaak_f32 v248, v247, v248, 0x3d29fb3f\nv_permlane16_swap_b32 v244, v245\nv_fmaak_f32 v248, v247, v248, 0xbd97d4d7\nv_max_f32 v244, v244, v245\n"
    "v_fmaak_f32 v248, v247, v248, 0x3dd931b2\nv_mov_b32 v245, v244\nv_fmaak_f32 v248, v247, v248, 0xbe1160e6\nv_fmaak_f32 v248, v247, v248, 0x3e4cb8bf\n"
    "v_permlane32_swap_b32 v244, v245\nv_fmaak_f32 v248, v247, v248, 0xbeaaaa62\nv_max_f32 v244, v244, v245\nv_mul_f32 v247, v247, v248\nv_ceil_f32 v244, v244\n"
    "v_fmac_f32 v246, v246, v247\nv_add_f32 v244, 0xc1600000, v244\nv_cmp_gt_f32 vcc, abs(v243), abs(v242)\nv_sub_f32 v247, 0x3fc90fdb, v246\nv_sub_f32 v18, v18, v244\n"
    "v_cndmask_b32 v246, v246, v247, vcc\nv_cmp_gt_f32 vcc, 0, v242\nv_sub_f32 v247, 0x40490fdb, v246\nv_exp_f32 v24, v244\nv_cndmask_b32 v246, v246, v247, vcc\n"
    "s_brev_b32 s44, -2\nv_max_f32 v246, 0, v246\nv_mul_f32 v244, 0x43000000, v16\nv_bfi_b32 v246, s44, v246, v243\nv_mul_f32 v245, 0x43000000, v17\n"
    "v_mul_f32 v19, 0xbe22f983, v246\nv_mul_f32 v247, 4.0, v16\nv_mul_f32 v248, 4.0, v17\nv_fract_f32 v245, v245\nv_fract_f32 v248, v248\nv_exp_f32 v244, v244\n"
    "v_exp_f32 v247, v247\nv_cos_f32 v20, v245\nv_sin_f32 v21, v245\nv_cos_f32 v22, v248\nv_sin_f32 v23, v248\nv_mul_f32 v20, v244, v20\nv_mul_f32 v21, v244, v21\n"
    "v_mul_f32 v22, v247, v22\nv_mul_f32 v23, v247, v23\nv_and_b32 v251, 15, v0\nv_mov_b32 v250, 0x102\nv_min_u32 v249, 12, v251\ns_mov_b32 s43, 0xfffffc00\n"
    "v_lshl_add_u32 v248, v249, 8, s43\nv_cmp_gt_u32 vcc, 5, v251\ns_nop 1\nv_cndmask_b32 v247, v248, v250, vcc\nv_cmp_gt_u32 vcc, 4, v251\ns_cbranch_execz .Lhop\ns_nop 1\n"
    "v_cndmask_b32 v25, v247, v251, vcc\nv_lshlrev_b32 v26, 3, v251\nv_cvt_f32_u32 v25, v25\nv_cvt_f32_u32 v26, v26\ns_cmp_gt_u32 s3, 3\n"
    "s_cselect_b32 s94, .Lhalo10-.Lgm14, 0\ns_add_u32 s94, s94, .Lgm14-.Lk_all_beg+4\ns_add_u32 s94, s94, s92\ns_addc_u32 s95, s93, 0\n"
    ".Lkgl13:\nv_mov_b32_dpp v240, v18 row_newbcast:0 row_mask:0xf bank_mask:0xf\nv_mov_b32_dpp v241, v19 row_newbcast:0 row_mask:0xf bank_mask:0xf\n"
    "v_mul_f32_dpp v242, v16, v26 row_newbcast:0 row_mask:0xf bank_mask:0xf\nv_mul_f32_dpp v243, v17, v26 row_newbcast:0 row_mask:0xf bank_mask:0xf\n"
    "v_fmac_f32_dpp v240, v16, v25 row_newbcast:0 row_mask:0xf bank_mask:0xf\nv_fmac_f32_dpp v241, -v17, v25 row_newbcast:0 row_mask:0xf bank_mask:0xf\n"
    "v_fract_f32 v243, v243\nv_exp_f32 v242, v242\nv_fract_f32 v241, v241\nv_exp_f32 v240, v240\nv_cos_f32 v244, v243\nv_sin_f32 v245, v243\nv_cos_f32 v246, v241\n"
    "v_sin_f32 v247, v241\nv_mul_f32 v244, v242, v244\nv_mul_f32 v245, v242, v245\nv_mul_f32 v246, v240, v246\nv_mul_f32 v247, v240, v247\n"
    "v_mul_f32_dpp v248, v20, v244 row_newbcast:0 row_mask:0xf bank_mask:0xf\nv_mul_f32_dpp v249, v20, v245 row_newbcast:0 row_mask:0xf bank_mask:0xf\n"
    "v_mul_f32_dpp v250, v22, v246 row_newbcast:0 row_mask:0xf bank_mask:0xf\nv_mul_f32_dpp v251, v22, v247 row_newbcast:0 row_mask:0xf bank_mask:0xf\n"
    "v_fmac_f32_dpp v248, -v21, v245 row_newbcast:0 row_mask:0xf bank_mask:0xf\nv_fmac_f32_dpp v249, v21, v244 row_newbcast:0 row_mask:0xf bank_mask:0xf\n"
    "v_fmac_f32_dpp v250, v23, v247 row_newbcast:0 row_mask:0xf bank_mask:0xf\nv_fmac_f32_dpp v251, -v23, v246 row_newbcast:0 row_mask:0xf bank_mask:0xf\n"
    "v_cvt_pk_f16_f32 v232, v244, v245\nv_cvt_pk_f16_f32 v224, v246, v247\nv_cvt_pk_f16_f32 v236, v248, v249\nv_cvt_pk_f16_f32 v228, v250, v251\n"
    "v_mov_b32_dpp v240, v18 row_newbcast:1 row_mask:0xf bank_mask:0xf\nv_mov_b32_dpp v241, v19 row_newbcast:1 row_mask:0xf bank_mask:0xf\n"
    "v_mul_f32_dpp v242, v16, v26 row_newbcast:1 row_mask:0xf bank_mask:0xf\nv_mul_f32_dpp v243, v17, v26 row_newbcast:1 row_mask:0xf bank_mask:0xf\n"
    "v_fmac_f32_dpp v240, v16, v25 row_newbcast:1 row_mask:0xf bank_mask:0xf\nv_fmac_f32_dpp v241, -v17, v25 row_newbcast:1 row_mask:0xf bank_mask:0xf\n"
    "v_fract_f32 v243, v243\nv_exp_f32 v242, v242\nv_fract_f32 v241, v241\nv_exp_f32 v240, v240\nv_cos_f32 v244, v243\nv_sin_f32 v245, v243\nv_cos_f32 v246, v241\n"
    "v_sin_f32 v247, v241\nv_mul_f32 v244, v242, v244\nv_mul_f32 v245, v242, v245\nv_mul_f32 v246, v240, v246\nv_mul_f32 v247, v240, v247\n"
    "v_mul_f32_dpp v248, v20, v244 row_newbcast:1 row_mask:0xf bank_mask:0xf\nv_mul_f32_dpp v249, v20, v245 row_newbcast:1 row_mask:0xf bank_mask:0xf\n"
    "v_mul_f32_dpp v250, v22, v246 row_newbcast:1 row_mask:0xf bank_mask:0xf\nv_mul_f32_dpp v251, v22, v247 row_newbcast:1 row_mask:0xf bank_mask:0xf\n"
    "v_fmac_f32_dpp v248, -v21, v245 row_newbcast:1 row_mask:0xf bank_mask:0xf\nv_fmac_f32_dpp v249, v21, v244 row_newbcast:1 row_mask:0xf bank_mask:0xf\n"
    "v_fmac_f32_dpp v250, v23, v247 row_newbcast:1 row_mask:0xf bank_mask:0xf\nv_fmac_f32_dpp v251, -v23, v246 row_newbcast:1 row_mask:0xf bank_mask:0xf\n"
    "v_cvt_pk_f16_f32 v233, v244, v245\nv_cvt_pk_f16_f32 v225, v246, v247\nv_cvt_pk_f16_f32 v237, v248, v249\nv_cvt_pk_f16_f32 v229, v250, v251\n"
    "v_mov_b32_dpp v240, v18 row_newbcast:2 row_mask:0xf bank_mask:0xf\nv_mov_b32_dpp v241, v19 row_newbcast:2 row_mask:0xf bank_mask:0xf\n"
    "v_mul_f32_dpp v242, v16, v26 row_newbcast:2 row_mask:0xf bank_mask:0xf\nv_mul_f32_dpp v243, v17, v26 row_newbcast:2 row_mask:0xf bank_mask:0xf\n"
    "v_fmac_f32_dpp v240, v16, v25 row_newbcast:2 row_mask:0xf bank_mask:0xf\nv_fmac_f32_dpp v241, -v17, v25 row_newbcast:2 row_mask:0xf bank_mask:0xf\n"
    "v_fract_f32 v243, v243\nv_exp_f32 v242, v242\nv_fract_f32 v241, v241\nv_exp_f32 v240, v240\nv_cos_f32 v244, v243\nv_sin_f32 v245, v243\nv_cos_f32 v246, v241\n"
    "v_sin_f32 v247, v241\nv_mul_f32 v244, v242, v244\nv_mul_f32 v245, v242, v245\nv_mul_f32 v246, v240, v246\nv_mul_f32 v247, v240, v247\n"
    "v_mul_f32_dpp v248, v20, v244 row_newbcast:2 row_mask:0xf bank_mask:0xf\nv_mul_f32_dpp v249, v20, v245 row_newbcast:2 row_mask:0xf bank_mask:0xf\n"
    "v_mul_f32_dpp v250, v22, v246 row_newbcast:2 row_mask:0xf bank_mask:0xf\nv_mul_f32_dpp v251, v22, v247 row_newbcast:2 row_mask:0xf bank_mask:0xf\n"
    "v_fmac_f32_dpp v248, -v21, v245 row_newbcast:2 row_mask:0xf bank_mask:0xf\nv_fmac_f32_dpp v249, v21, v244 row_newbcast:2 row_mask:0xf bank_mask:0xf\n"
    "v_fmac_f32_dpp v250, v23, v247 row_newbcast:2 row_mask:0xf bank_mask:0xf\nv_fmac_f32_dpp v251, -v23, v246 row_newbcast:2 row_mask:0xf bank_mask:0xf\n"
    "v_cvt_pk_f16_f32 v234, v244, v245\nv_cvt_pk_f16_f32 v226, v246, v247\nv_cvt_pk_f16_f32 v238, v248, v249\nv_cvt_pk_f16_f32 v230, v250, v251\nds_read_b32 v57, v56\n"
    "v_mov_b32_dpp v240, v18 row_newbcast:3 row_mask:0xf bank_mask:0xf\nv_mov_b32_dpp v241, v19 row_newbcast:3 row_mask:0xf bank_mask:0xf\n"
    "v_mul_f32_dpp v242, v16, v26 row_newbcast:3 row_mask:0xf bank_mask:0xf\nv_mul_f32_dpp v243, v17, v26 row_newbcast:3 row_mask:0xf bank_mask:0xf\n"
    "v_fmac_f32_dpp v240, v16, v25 row_newbcast:3 row_mask:0xf bank_mask:0xf\nv_fmac_f32_dpp v241, -v17, v25 row_newbcast:3 row_mask:0xf bank_mask:0xf\n"
    "v_fract_f32 v243, v243\nv_exp_f32 v242, v242\nv_fract_f32 v241, v241\nv_exp_f32 v240, v240\nv_cos_f32 v244, v243\nv_sin_f32 v245, v243\nv_cos_f32 v246, v241\n"
    "v_sin_f32 v247, v241\nv_mul_f32 v244, v242, v244\nv_mul_f32 v245, v242, v245\nv_mul_f32 v246, v240, v246\nv_mul_f32 v247, v240, v247\n"
    "v_mul_f32_dpp v248, v20, v244 row_newbcast:3 row_mask:0xf bank_mask:0xf\nv_mul_f32_dpp v249, v20, v245 row_newbcast:3 row_mask:0xf bank_mask:0xf\n"
    "v_mul_f32_dpp v250, v22, v246 row_newbcast:3 row_mask:0xf bank_mask:0xf\nv_mul_f32_dpp v251, v22, v247 row_newbcast:3 row_mask:0xf bank_mask:0xf\n"
    "v_fmac_f32_dpp v248, -v21, v245 row_newbcast:3 row_mask:0xf bank_mask:0xf\nv_fmac_f32_dpp v249, v21, v244 row_newbcast:3 row_mask:0xf bank_mask:0xf\n"
    "v_fmac_f32_dpp v250, v23, v247 row_newbcast:3 row_mask:0xf bank_mask:0xf\nv_fmac_f32_dpp v251, -v23, v246 row_newbcast:3 row_mask:0xf bank_mask:0xf\n"
    "v_cvt_pk_f16_f32 v235, v244, v245\nv_cvt_pk_f16_f32 v227, v246, v247\nv_cvt_pk_f16_f32 v239, v248, v249\nv_cvt_pk_f16_f32 v231, v250, v251\ns_waitcnt lgkmcnt(0)\n"
    "v_readfirstlane_b32 s44, v57\nv_mov_b32_dpp v16, v16 row_ror:12 row_mask:0xf bank_mask:0xf\nv_mov_b32_dpp v17, v17 row_ror:12 row_mask:0xf bank_mask:0xf\n"
    "v_mfma_f32_16x16x32_f16 v[40:43], v[224:227], v[232:235], v[40:43]\nv_mov_b32_dpp v18, v18 row_ror:12 row_mask:0xf bank_mask:0xf\n"
    "v_mov_b32_dpp v19, v19 row_ror:12 row_mask:0xf bank_mask:0xf\nv_mfma_f32_16x16x32_f16 v[44:47], v[224:227], v[236:239], v[44:47]\n"
    "v_mov_b32_dpp v20, v20 row_ror:12 row_mask:0xf bank_mask:0xf\nv_mov_b32_dpp v21, v21 row_ror:12 row_mask:0xf bank_mask:0xf\n"
    "v_mfma_f32_16x16x32_f16 v[48:51], v[228:231], v[232:235], v[48:51]\nv_mov_b32_dpp v22, v22 row_ror:12 row_mask:0xf bank_mask:0xf\n"
    "v_mov_b32_dpp v23, v23 row_ror:12 row_mask:0xf bank_mask:0xf\nv_mfma_f32_16x16x32_f16 v[52:55], v[228:231], v[236:239], v[52:55]\ns_sub_u32 s27, s27, 1\n"
    "s_cmp_eq_u32 s27, 0\ns_cbranch_scc1 .Lkx15\ns_cmp_lt_i32 s44, s10\ns_cbranch_scc1 .Lkgl13\ns_setpc_b64 s[94:95]\n.Lkx15:\nv_and_b32 v251, 63, v0\n"
    "s_lshl_b32 s43, s13, 12\ns_add_u32 s42, s43, 0x1f600\nv_lshl_add_u32 v33, v251, 4, s42\ns_lshl_b32 s41, s13, 2\ns_add_u32 s40, s41, 0x27d80\nv_mov_b32 v242, s40\n"
    "v_mul_f32 v40, v40, v24\nv_mul_f32 v41, v41, v24\nv_mul_f32 v42, v42, v24\nv_mul_f32 v43, v43, v24\nv_mul_f32 v44, v44, v24\nv_mul_f32 v45, v45, v24\n"
    "v_mul_f32 v46, v46, v24\ns_cbranch_execz .Lhop\nv_mul_f32 v47, v47, v24\nv_mul_f32 v48, v48, v24\nv_mul_f32 v49, v49, v24\nv_mul_f32 v50, v50, v24\n"
    "v_mul_f32 v51, v51, v24\nv_mul_f32 v52, v52, v24\nv_mul_f32 v53, v53, v24\nv_mul_f32 v54, v54, v24\nv_mul_f32 v55, v55, v24\n"
    "v_max3_f32 v240, abs(v40), abs(v41), abs(v42)\nv_max3_f32 v241, abs(v43), abs(v44), abs(v45)\nv_max3_f32 v240, v240, abs(v46), abs(v47)\n"
    "v_max3_f32 v241, v241, abs(v48), abs(v49)\nv_max3_f32 v240, v240, abs(v50), abs(v51)\nv_max3_f32 v241, v241, abs(v52), abs(v53)\n"
    "v_max3_f32 v240, v240, abs(v54), abs(v55)\nv_max_f32 v240, v240, v241\ns_nop 1\nv_max_f32_dpp v240, v240, v240 row_ror:1 row_mask:0xf bank_mask:0xf\ns_nop 1\n"
    "v_max_f32_dpp v240, v240, v240 row_ror:2 row_mask:0xf bank_mask:0xf\ns_nop 1\nv_max_f32_dpp v240, v240, v240 row_ror:4 row_mask:0xf bank_mask:0xf\ns_nop 1\n"
    "v_max_f32_dpp v240, v240, v240 row_ror:8 row_mask:0xf bank_mask:0xf\nv_mov_b32 v241, v240\ns_nop 1\nv_permlane16_swap_b32 v240, v241\nv_max_f32 v240, v240, v241\n"
    "v_mov_b32 v241, v240\ns_nop 1\nv_permlane32_swap_b32 v240, v241\nv_max_f32 v240, v240, v241\ns_mov_b64 exec, 1\nds_write_b32 v242, v240\ns_mov_b64 exec, -1\n"
    "s_setpc_b64 s[94:95]\n"
    ".Lgm14w:\ns_cmp_eq_u32 s3, 1\ns_cbranch_scc1 .Lgm14v\nv_lshl_or_b32 v168, v169, 16, v168\nv_lshl_or_b32 v169, v171, 16, v170\nv_lshl_or_b32 v170, v173, 16, v172\n"
    "v_lshl_or_b32 v171, v175, 16, v174\nglobal_store_dwordx4 v57, v[168:171], s[4:5] sc1\n.Lgm14v:\ns_mul_i32 s88, s13, 9\ns_add_u32 s88, s88, -4\ns_lshl_b32 s90, s88, 10\n"
    "s_add_u32 s90, s92, s90\ns_addc_u32 s91, s93, 0\ns_sub_u32 s89, 9, 1\ns_mov_b64 exec, 0\ns_swappc_b64 s[94:95], s[90:91]\n.Lz162:\nv_mov_b32 v240, 1\n"
    "v_mov_b32 v164, s11\ns_mov_b64 exec, 1\ns_waitcnt vmcnt(0)\nds_write_b32 v30, v240\ns_mov_b64 exec, -1\ns_cmp_lg_u32 s13, 3\ns_cbranch_scc1 .Lgm14\n"
    "s_mov_b32 s86, 0x100000\n.Ls18:\nds_read_b32 v243, v31 offset:0\ns_waitcnt lgkmcnt(0)\nv_cmp_gt_i32 vcc, 1, v243\ns_sub_u32 s86, s86, 1\ns_cbranch_scc1 .Ls19\n"
    "s_cbranch_vccz .Ls19\ns_sleep 1\ns_branch .Ls18\n.Ls19:\ns_mov_b64 exec, 1\nglobal_store_dword v32, v164, s[4:5] sc1\ns_mov_b64 exec, -1\n.Lgm14:\n"
    "s_mov_b32 s81, 0x3b000000\ns_mov_b32 s84, -1\ns_mov_b32 s85, 0\nv_mov_b32 v241, 0x3f803f80\ns_mov_b32 s46, 0\ns_mov_b32 s47, s23\ns_mul_i32 s44, s23, 12288\n"
    "v_add_u32 v160, s46, v27\nv_xor_b32 v161, 32, v160\nv_xor_b32 v162, 64, v160\nv_xor_b32 v163, 0x60, v160\nv_add_u32 v242, s44, v36\n.Lgf16:\nv_mov_b32 v240, 2\n"
    "s_mov_b32 s45, 3\ns_mov_b32 s86, 0x100000\n.Ls20:\nds_read_b32 v243, v56 offset:0\ns_waitcnt lgkmcnt(0)\nv_cmp_gt_i32 vcc, 1, v243\ns_sub_u32 s86, s86, 1\n"
    "s_cbranch_scc1 .Ls21\ns_cbranch_vccz .Ls21\ns_sleep 2\ns_branch .Ls20\n.Ls21:\nds_read_b64_tr_b16 v[208:209], v242 offset:0\n"
    "ds_read_b64_tr_b16 v[210:211], v242 offset:256\nds_read_b64_tr_b16 v[212:213], v242 offset:3072\nds_read_b64_tr_b16 v[214:215], v242 offset:3328\n"
    "v_cmp_gt_i32 vcc, 2, v243\ns_cbranch_vccz .Ls23\ns_mov_b32 s86, 0x100000\n.Ls22:\nds_read_b32 v243, v56 offset:0\ns_waitcnt lgkmcnt(0)\nv_cmp_gt_i32 vcc, 2, v243\n"
    "s_sub_u32 s86, s86, 1\ns_cbranch_scc1 .Ls23\ns_cbranch_vccz .Ls23\ns_sleep 1\ns_branch .Ls22\n.Ls23:\nds_read_b128 v[144:147], v160\n"
    "ds_read_b64_tr_b16 v[216:217], v242 offset:6144\nds_read_b64_tr_b16 v[218:219], v242 offset:6400\nds_read_b128 v[148:151], v161\n"
    "ds_read_b64_tr_b16 v[220:221], v242 offset:9216\nds_read_b64_tr_b16 v[222:223], v242 offset:9472\nds_read_b128 v[152:155], v162\n"
    "ds_read_b64_tr_b16 v[224:225], v242 offset:1024\nds_read_b64_tr_b16 v[226:227], v242 offset:1280\nds_read_b128 v[156:159], v163\n"
    "ds_read_b64_tr_b16 v[228:229], v242 offset:4096\nds_read_b64_tr_b16 v[230:231], v242 offset:4352\n.Lgl17:\ns_waitcnt lgkmcnt(11)\nv_dot2c_f32_bf16 v112, v241, v144\n"
    "v_dot2c_f32_bf16 v113, v144, v144\nv_dot2c_f32_bf16 v114, v241, v145\nv_dot2c_f32_bf16 v115, v145, v145\nv_dot2c_f32_bf16 v116, v241, v146\n"
    "v_dot2c_f32_bf16 v117, v146, v146\nv_dot2c_f32_bf16 v118, v241, v147\nv_dot2c_f32_bf16 v119, v147, v147\ns_waitcnt lgkmcnt(8)\nv_dot2c_f32_bf16 v112, v241, v148\n"
    "v_dot2c_f32_bf16 v113, v148, v148\nv_dot2c_f32_bf16 v114, v241, v149\nv_dot2c_f32_bf16 v115, v149, v149\nv_dot2c_f32_bf16 v116, v241, v150\n"
    "v_dot2c_f32_bf16 v117, v150, v150\nv_dot2c_f32_bf16 v118, v241, v151\nv_dot2c_f32_bf16 v119, v151, v151\ns_waitcnt lgkmcnt(5)\nv_dot2c_f32_bf16 v112, v241, v152\n"
    "v_dot2c_f32_bf16 v113, v152, v152\nv_dot2c_f32_bf16 v114, v241, v153\nv_dot2c_f32_bf16 v115, v153, v153\nv_dot2c_f32_bf16 v116, v241, v154\n"
    "v_dot2c_f32_bf16 v117, v154, v154\nv_dot2c_f32_bf16 v118, v241, v155\nv_dot2c_f32_bf16 v119, v155, v155\ns_waitcnt lgkmcnt(2)\nv_dot2c_f32_bf16 v112, v241, v156\n"
    "v_dot2c_f32_bf16 v113, v156, v156\nv_dot2c_f32_bf16 v114, v241, v157\nv_dot2c_f32_bf16 v115, v157, v157\nv_dot2c_f32_bf16 v116, v241, v158\n"
    "v_dot2c_f32_bf16 v117, v158, v158\nv_dot2c_f32_bf16 v118, v241, v159\nv_dot2c_f32_bf16 v119, v159, v159\n"
    "v_mfma_f32_32x32x16_bf16 v[64:79], v[208:211], v[144:147], v[64:79]\nds_read_b64_tr_b16 v[208:209], v242 offset:7168\nds_read_b64_tr_b16 v[210:211], v242 offset:7424\n"
    "s_mov_b64 exec, 1\nds_write_b32 v30, v240\ns_mov_b64 exec, -1\nv_mfma_f32_32x32x16_bf16 v[64:79], v[212:215], v[148:151], v[64:79]\n"
    "ds_read_b64_tr_b16 v[212:213], v242 offset:10240\nds_read_b64_tr_b16 v[214:215], v242 offset:10496\nds_read_b32 v243, v56\ns_add_u32 s47, s47, 1\n"
    "s_add_u32 s46, s46, 4096\nv_mfma_f32_32x32x16_bf16 v[64:79], v[216:219], v[152:155], v[64:79]\nds_read_b64_tr_b16 v[216:217], v242 offset:2048\n"
    "ds_read_b64_tr_b16 v[218:219], v242 offset:2304\ns_and_b32 s47, s47, 7\ns_cmp_eq_u32 s46, 12288\ns_cselect_b32 s46, 0, s46\n"
    "v_mfma_f32_32x32x16_bf16 v[64:79], v[220:223], v[156:159], v[64:79]\nds_read_b64_tr_b16 v[220:221], v242 offset:5120\nds_read_b64_tr_b16 v[222:223], v242 offset:5376\n"
    "s_mul_i32 s44, s47, 12288\nv_add_u32 v160, s46, v27\nv_xor_b32 v161, 32, v160\nv_mfma_f32_32x32x16_bf16 v[80:95], v[224:227], v[144:147], v[80:95]\n"
    "ds_read_b64_tr_b16 v[224:225], v242 offset:8192\nds_read_b64_tr_b16 v[226:227], v242 offset:8448\ns_waitcnt lgkmcnt(6)\nv_readfirstlane_b32 s49, v243\n"
    "ds_read_b32 v243, v56\ns_nop 0\ns_cbranch_execz .Lhop\nv_mfma_f32_32x32x16_bf16 v[80:95], v[228:231], v[148:151], v[80:95]\n"
    "ds_read_b64_tr_b16 v[228:229], v242 offset:11264\nds_read_b64_tr_b16 v[230:231], v242 offset:11520\nv_add_u32 v242, s44, v36\ns_cmp_lt_i32 s49, s45\n"
    "s_cbranch_scc1 .Lc24\n.Lc25:\nv_mfma_f32_32x32x16_bf16 v[80:95], v[208:211], v[152:155], v[80:95]\nds_read_b64_tr_b16 v[208:209], v242 offset:0\n"
    "ds_read_b64_tr_b16 v[210:211], v242 offset:256\ns_add_u32 s45, s45, 1\nv_xor_b32 v162, 64, v160\nv_xor_b32 v163, 0x60, v160\n"
    "v_mfma_f32_32x32x16_bf16 v[80:95], v[212:215], v[156:159], v[80:95]\nds_read_b64_tr_b16 v[212:213], v242 offset:3072\nds_read_b64_tr_b16 v[214:215], v242 offset:3328\n"
    "s_waitcnt lgkmcnt(6)\nv_readfirstlane_b32 s49, v243\nv_add_u32 v240, 2, v240\nv_mfma_f32_32x32x16_bf16 v[96:111], v[216:219], v[144:147], v[96:111]\n"
    "s_cmp_lt_i32 s49, s45\ns_cbranch_scc1 .Lc27\n.Lc28:\nds_read_b128 v[144:147], v160\nds_read_b64_tr_b16 v[216:217], v242 offset:6144\n"
    "ds_read_b64_tr_b16 v[218:219], v242 offset:6400\nv_mfma_f32_32x32x16_bf16 v[96:111], v[220:223], v[148:151], v[96:111]\nds_read_b128 v[148:151], v161\n"
    "ds_read_b64_tr_b16 v[220:221], v242 offset:9216\nds_read_b64_tr_b16 v[222:223], v242 offset:9472\ns_add_u32 s45, s45, 1\n"
    "v_mfma_f32_32x32x16_bf16 v[96:111], v[224:227], v[152:155], v[96:111]\nds_read_b128 v[152:155], v162\nds_read_b64_tr_b16 v[224:225], v242 offset:1024\n"
    "ds_read_b64_tr_b16 v[226:227], v242 offset:1280\ns_cmp_lt_u32 s45, 17\ns_waitcnt lgkmcnt(13)\nv_mfma_f32_32x32x16_bf16 v[96:111], v[228:231], v[156:159], v[96:111]\n"
    "ds_read_b128 v[156:159], v163\nds_read_b64_tr_b16 v[228:229], v242 offset:4096\nds_read_b64_tr_b16 v[230:231], v242 offset:4352\ns_cbranch_scc1 .Lgl17\n"
    "s_waitcnt lgkmcnt(11)\nv_dot2c_f32_bf16 v112, v241, v144\nv_dot2c_f32_bf16 v113, v144, v144\nv_dot2c_f32_bf16 v114, v241, v145\nv_dot2c_f32_bf16 v115, v145, v145\n"
    "v_dot2c_f32_bf16 v116, v241, v146\nv_dot2c_f32_bf16 v117, v146, v146\nv_dot2c_f32_bf16 v118, v241, v147\nv_dot2c_f32_bf16 v119, v147, v147\ns_waitcnt lgkmcnt(8)\n"
    "v_dot2c_f32_bf16 v112, v241, v148\nv_dot2c_f32_bf16 v113, v148, v148\nv_dot2c_f32_bf16 v114, v241, v149\nv_dot2c_f32_bf16 v115, v149, v149\n"
    "v_dot2c_f32_bf16 v116, v241, v150\nv_dot2c_f32_bf16 v117, v150, v150\nv_dot2c_f32_bf16 v118, v241, v151\nv_dot2c_f32_bf16 v119, v151, v151\ns_waitcnt lgkmcnt(5)\n"
    "v_dot2c_f32_bf16 v112, v241, v152\nv_dot2c_f32_bf16 v113, v152, v152\nv_dot2c_f32_bf16 v114, v241, v153\nv_dot2c_f32_bf16 v115, v153, v153\n"
    "v_dot2c_f32_bf16 v116, v241, v154\nv_dot2c_f32_bf16 v117, v154, v154\nv_dot2c_f32_bf16 v118, v241, v155\nv_dot2c_f32_bf16 v119, v155, v155\ns_waitcnt lgkmcnt(2)\n"
    "v_dot2c_f32_bf16 v112, v241, v156\nv_dot2c_f32_bf16 v113, v156, v156\nv_dot2c_f32_bf16 v114, v241, v157\nv_dot2c_f32_bf16 v115, v157, v157\n"
    "v_dot2c_f32_bf16 v116, v241, v158\nv_dot2c_f32_bf16 v117, v158, v158\nv_dot2c_f32_bf16 v118, v241, v159\nv_dot2c_f32_bf16 v119, v159, v159\n"
    "v_mfma_f32_32x32x16_bf16 v[64:79], v[208:211], v[144:147], v[64:79]\nds_read_b64_tr_b16 v[208:209], v242 offset:7168\nds_read_b64_tr_b16 v[210:211], v242 offset:7424\n"
    "s_mov_b64 exec, 1\nds_write_b32 v30, v240\ns_mov_b64 exec, -1\nv_mfma_f32_32x32x16_bf16 v[64:79], v[212:215], v[148:151], v[64:79]\n"
    "ds_read_b64_tr_b16 v[212:213], v242 offset:10240\nds_read_b64_tr_b16 v[214:215], v242 offset:10496\nv_add_f32 v112, v112, v114\nv_add_f32 v113, v113, v115\n"
    "v_add_f32 v116, v116, v118\nv_add_f32 v117, v117, v119\nv_mfma_f32_32x32x16_bf16 v[64:79], v[216:219], v[152:155], v[64:79]\n"
    "ds_read_b64_tr_b16 v[216:217], v242 offset:2048\nds_read_b64_tr_b16 v[218:219], v242 offset:2304\nv_add_f32 v112, v112, v116\nv_add_f32 v113, v113, v117\n"
    "v_mfma_f32_32x32x16_bf16 v[64:79], v[220:223], v[156:159], v[64:79]\nds_read_b32 v243, v28\nds_read_b32 v124, v38 offset:0\nds_read_b32 v128, v38 offset:128\n"
    "ds_read_b32 v132, v38 offset:256\nds_read_b64_tr_b16 v[220:221], v242 offset:5120\nds_read_b64_tr_b16 v[222:223], v242 offset:5376\nv_permlane32_swap_b32 v112, v113\n"
    "v_add_f32 v138, v112, v113\nv_add_f32 v139, v112, v113\nv_mfma_f32_32x32x16_bf16 v[80:95], v[224:227], v[144:147], v[80:95]\n"
    "ds_read_b64_tr_b16 v[224:225], v242 offset:8192\nds_read_b64_tr_b16 v[226:227], v242 offset:8448\nv_permlane32_swap_b32 v138, v139\nv_mul_f32 v138, s81, v138\n"
    "v_mul_f32 v112, v138, v138\nv_cvt_pk_bf16_f32 v138, -v138, -v138\ns_waitcnt lgkmcnt(12)\nv_mfma_f32_32x32x16_bf16 v[80:95], v[228:231], v[148:151], v[80:95]\n"
    "ds_read_b64_tr_b16 v[228:229], v242 offset:11264\nds_read_b64_tr_b16 v[230:231], v242 offset:11520\nv_cndmask_b32 v120, 0, v138, s[84:85]\n"
    "v_fma_f32 v139, v139, s81, -v112\nv_max_f32 v139, 0, v139\nv_cvt_pk_bf16_f32 v139, 0, v139\nv_mfma_f32_32x32x16_bf16 v[80:95], v[208:211], v[152:155], v[80:95]\n"
    "s_waitcnt lgkmcnt(6)\nv_cmp_gt_i32 vcc, v29, v243\nv_add_f32 v139, 0x3727c5ac, v139\nv_cvt_pk_bf16_f32 v139, 0, v139\nv_sqrt_f32 v139, v139\n"
    "v_mfma_f32_32x32x16_bf16 v[80:95], v[212:215], v[156:159], v[80:95]\nv_cvt_pk_bf16_f32 v139, 0, v139\nv_rcp_f32 v136, v139\nv_mov_b64 v[232:233], 0\n"
    "v_mov_b64 v[234:235], 0\ns_cbranch_vccnz .Lt30\n.Lt31:\nv_mfma_f32_32x32x16_bf16 v[64:79], v[124:127], v[120:123], v[64:79]\nv_mov_b32 v137, v136\n"
    "v_mov_b64 v[236:237], 0\nv_mov_b64 v[238:239], 0\nv_mov_b64 v[244:245], 0\nv_mov_b64 v[246:247], 0\nv_mfma_f32_32x32x16_bf16 v[80:95], v[128:131], v[120:123], v[80:95]\n"
    "v_mov_b64 v[248:249], 0\nv_mov_b64 v[250:251], 0\ns_mov_b32 s44, 0x00ff00ff\ns_mov_b32 s45, 0xff00ff00\ns_mov_b32 s46, s44\ns_mov_b32 s47, s44\ns_waitcnt lgkmcnt(0)\n"
    "v_mfma_f32_32x32x16_bf16 v[96:111], v[216:219], v[144:147], v[96:111]\nv_mul_f32 v64, v64, v136\nv_mul_f32 v65, v65, v136\nv_mul_f32 v66, v66, v136\n"
    "v_mul_f32 v67, v67, v136\nv_cvt_pk_bf16_f32 v64, v64, v65\nv_cvt_pk_bf16_f32 v65, v66, v67\nds_write_b64 v37, v[64:65] offset:0\n"
    "v_mfma_f32_32x32x16_bf16 v[96:111], v[220:223], v[148:151], v[96:111]\nv_mul_f32 v68, v68, v136\nv_mul_f32 v69, v69, v136\nv_mul_f32 v70, v70, v136\n"
    "v_mul_f32 v71, v71, v136\nv_cvt_pk_bf16_f32 v68, v68, v69\nv_cvt_pk_bf16_f32 v69, v70, v71\nds_write_b64 v37, v[68:69] offset:16\n"
    "v_mfma_f32_32x32x16_bf16 v[96:111], v[224:227], v[152:155], v[96:111]\nv_mul_f32 v72, v72, v136\nv_mul_f32 v73, v73, v136\nv_mul_f32 v74, v74, v136\n"
    "v_mul_f32 v75, v75, v136\nv_cvt_pk_bf16_f32 v72, v72, v73\nv_cvt_pk_bf16_f32 v73, v74, v75\nds_write_b64 v37, v[72:73] offset:32\n"
    "v_mfma_f32_32x32x16_bf16 v[96:111], v[228:231], v[156:159], v[96:111]\nv_mul_f32 v76, v76, v136\nv_mul_f32 v77, v77, v136\nv_mul_f32 v78, v78, v136\n"
    "v_mul_f32 v79, v79, v136\nv_cvt_pk_bf16_f32 v76, v76, v77\nv_cvt_pk_bf16_f32 v77, v78, v79\nds_write_b64 v37, v[76:77] offset:48\ns_cbranch_execz .Lhop\n"
    "v_mfma_f32_32x32x16_bf16 v[96:111], v[132:135], v[120:123], v[96:111]\nv_pk_mul_f32 v[80:81], v[80:81], v[136:137]\nv_pk_mul_f32 v[82:83], v[82:83], v[136:137]\n"
    "v_cvt_pk_bf16_f32 v80, v80, v81\nv_cvt_pk_bf16_f32 v81, v82, v83\nds_write_b64 v37, v[80:81] offset:64\nv_pk_mul_f32 v[84:85], v[84:85], v[136:137]\n"
    "v_pk_mul_f32 v[86:87], v[86:87], v[136:137]\nv_cvt_pk_bf16_f32 v84, v84, v85\nv_cvt_pk_bf16_f32 v85, v86, v87\nds_write_b64 v37, v[84:85] offset:80\n"
    "v_pk_mul_f32 v[88:89], v[88:89], v[136:137]\nv_pk_mul_f32 v[90:91], v[90:91], v[136:137]\nv_cvt_pk_bf16_f32 v88, v88, v89\nv_cvt_pk_bf16_f32 v89, v90, v91\n"
    "ds_write_b64 v37, v[88:89] offset:96\nv_pk_mul_f32 v[92:93], v[92:93], v[136:137]\nv_pk_mul_f32 v[94:95], v[94:95], v[136:137]\nv_cvt_pk_bf16_f32 v92, v92, v93\n"
    "v_cvt_pk_bf16_f32 v93, v94, v95\nds_write_b64 v37, v[92:93] offset:112\nv_pk_mul_f32 v[96:97], v[96:97], v[136:137]\nv_pk_mul_f32 v[98:99], v[98:99], v[136:137]\n"
    "v_cvt_pk_bf16_f32 v96, v96, v97\nv_cvt_pk_bf16_f32 v97, v98, v99\nds_write_b64 v37, v[96:97] offset:128\nv_pk_mul_f32 v[100:101], v[100:101], v[136:137]\n"
    "v_pk_mul_f32 v[102:103], v[102:103], v[136:137]\nv_cvt_pk_bf16_f32 v100, v100, v101\nv_cvt_pk_bf16_f32 v101, v102, v103\nds_write_b64 v37, v[100:101] offset:144\n"
    "v_pk_mul_f32 v[104:105], v[104:105], v[136:137]\nv_pk_mul_f32 v[106:107], v[106:107], v[136:137]\nv_cvt_pk_bf16_f32 v104, v104, v105\nv_cvt_pk_bf16_f32 v105, v106, v107\n"
    "ds_write_b64 v37, v[104:105] offset:160\nv_pk_mul_f32 v[108:109], v[108:109], v[136:137]\nv_pk_mul_f32 v[110:111], v[110:111], v[136:137]\n"
    "v_cvt_pk_bf16_f32 v108, v108, v109\nv_cvt_pk_bf16_f32 v109, v110, v111\nds_write_b64 v37, v[108:109] offset:176\ns_branch .Latt12\n.Lc24:\ns_mov_b32 s86, 0x100000\n"
    ".Lc26:\nds_read_b32 v243, v56\ns_waitcnt lgkmcnt(0)\nv_readfirstlane_b32 s49, v243\ns_sub_u32 s86, s86, 1\ns_cbranch_scc1 .Lc25\ns_cmp_ge_i32 s49, s45\n"
    "s_cbranch_scc1 .Lc25\ns_branch .Lc26\n.Lc27:\ns_mov_b32 s86, 0x100000\n.Lc29:\nds_read_b32 v243, v56\ns_waitcnt lgkmcnt(0)\nv_readfirstlane_b32 s49, v243\n"
    "s_sub_u32 s86, s86, 1\ns_cbranch_scc1 .Lc28\ns_cmp_ge_i32 s49, s45\ns_cbranch_scc1 .Lc28\ns_branch .Lc29\n.Lt30:\ns_mov_b32 s86, 0x100000\n.Ls32:\n"
    "ds_read_b32 v243, v28 offset:0\ns_waitcnt lgkmcnt(0)\nv_cmp_gt_i32 vcc, v29, v243\ns_sub_u32 s86, s86, 1\ns_cbranch_scc1 .Ls33\ns_cbranch_vccz .Ls33\ns_branch .Ls32\n"
    ".Ls33:\nds_read_b32 v124, v38 offset:0\nds_read_b32 v128, v38 offset:128\nds_read_b32 v132, v38 offset:256\ns_waitcnt lgkmcnt(0)\ns_branch .Lt31\n"
    ".Llstb7:\nv_mov_b32 v72, 1\nds_write_b32 v56, v72 offset:32\ns_mov_b32 s58, 2\ns_mov_b32 s57, 98304\ns_add_u32 s50, s50, 1\ns_and_b32 s50, s50, 7\n"
    "s_sub_u32 s62, s61, 16384\ns_max_i32 s62, s62, 0\ns_add_u32 s62, s8, s62\ns_addc_u32 s63, s9, 0\nv_and_b32 v251, 63, v0\nv_lshlrev_b32 v71, 4, v251\n"
    "v_bfe_u32 v250, v0, 2, 4\nv_and_b32 v249, 3, v0\ns_mov_b32 s43, 0x1002000\nv_lshl_add_u32 v57, v250, 7, s43\nv_lshl_add_u32 v57, v249, 4, v57\ns_lshr_b32 s42, s2, 4\n"
    "s_lshl_b32 s41, s42, 7\ns_and_b32 s40, s2, 15\ns_lshl_b32 s39, s40, 2\ns_add_u32 s38, s41, s39\ns_add_u32 s38, s38, 0x1004000\nv_mov_b32 v58, s38\n"
    "s_lshl_b32 s37, s19, 17\ns_lshl_b32 s36, s20, 11\ns_add_u32 s35, s37, s36\nv_lshl_add_u32 v79, v251, 4, s35\nv_mov_b32 v80, 0x1000000\ns_lshl_b32 s34, s25, 7\n"
    "s_add_u32 s33, s34, 0xe04000\nv_lshl_add_u32 v38, v250, 15, s33\nv_lshl_add_u32 v38, v249, 12, v38\nv_and_b32 v251, 63, v0\ns_lshl_b32 s43, s18, 13\n"
    "s_add_u32 s42, s43, 0x201000\nv_lshl_add_u32 v39, v251, 4, s42\nv_and_b32 v250, 7, v0\ns_lshl_b32 s41, s2, 7\ns_add_u32 s40, s41, 0x400000\n"
    "v_lshl_add_u32 v33, v250, 4, s40\ns_lshl_b32 s39, s13, 2\ns_add_u32 s38, s39, 0x27f80\nv_mov_b32 v37, s38\nv_mov_b64 v[60:61], -1\nv_mov_b64 v[62:63], -1\n"
    "v_and_b32 v224, 127, v0\ns_getpc_b64 s[44:45]\ns_add_u32 s44, s44, g_tabs@rel32@lo+4\ns_addc_u32 s45, s45, g_tabs@rel32@hi+12\nv_lshlrev_b32 v224, 4, v224\n"
    "global_load_dwordx4 v[28:31], v224, s[44:45] offset:2064\nglobal_load_dwordx4 v[10:13], v224, s[44:45]\nglobal_load_dwordx2 v[14:15], v224, s[44:45] offset:16\n"
    "s_lshl_b32 s43, s22, 17\ns_lshl_b32 s42, s13, 15\ns_add_u32 s44, s43, s42\ns_add_u32 s44, s44, 0xe01000\nv_and_b32 v231, 63, v0\nv_lshlrev_b32 v59, 4, v231\n"
    "s_add_u32 s68, s4, s44\ns_addc_u32 s69, s5, 0\ns_add_u32 s70, s68, 8192\ns_addc_u32 s71, s69, 0\ns_add_u32 s72, s68, 16384\ns_addc_u32 s73, s69, 0\n"
    "s_add_u32 s74, s68, 24576\ns_addc_u32 s75, s69, 0\nv_bfe_u32 v251, v0, 5, 1\nv_and_b32 v250, 31, v0\ns_lshl_b32 s43, s13, 11\ns_add_u32 s42, s43, 0x1d000\n"
    "v_lshl_add_u32 v17, v251, 8, s42\nv_lshl_add_u32 v17, v250, 2, v17\nv_and_b32 v249, 63, v0\nv_lshl_add_u32 v18, v249, 4, s42\ns_mov_b32 s41, 0x1f600\n"
    "v_lshl_add_u32 v19, v249, 4, s41\nv_bfe_u32 v248, v0, 3, 3\nv_and_b32 v247, 7, v0\ns_lshl_b32 s40, s22, 19\ns_lshl_b32 s39, s13, 17\ns_lshl_b32 s38, s21, 7\n"
    "s_add_u32 s37, s12, s40\ns_add_u32 s37, s37, s39\ns_add_u32 s37, s37, s38\ns_add_u32 s37, s37, 0x200000\nv_lshl_add_u32 v20, v248, 13, s37\n"
    "v_lshl_add_u32 v20, v247, 4, v20\ns_add_u32 s36, s12, s40\ns_add_u32 s36, s36, s39\ns_add_u32 s36, s36, s38\ns_nop 0\ns_cbranch_execz .Lhop\n"
    "s_add_u32 s36, s36, 0x210000\nv_lshl_add_u32 v21, v248, 13, s36\nv_lshl_add_u32 v21, v247, 4, v21\ns_lshl_b32 s35, s13, 8\ns_add_u32 s34, s35, 0x27600\n"
    "v_lshl_add_u32 v22, v249, 2, s34\ns_lshl_b32 s43, s13, 2\ns_add_u32 s42, s43, 0x27ff0\nv_mov_b32 v23, s42\ns_lshr_b32 s41, s12, 23\ns_add_u32 s40, s41, 2\n"
    "v_mov_b32 v24, s40\nv_bfe_u32 v251, v0, 5, 1\nv_and_b32 v250, 31, v0\ns_lshl_b32 s39, s13, 11\ns_add_u32 s38, s39, 0x1d400\nv_lshl_add_u32 v26, v251, 8, s38\n"
    "v_lshl_add_u32 v26, v250, 2, v26\nv_mov_b32 v25, 0x27fb4\ns_lshl_b32 s37, s21, 15\ns_lshl_b32 s36, s22, 8\ns_lshl_b32 s35, s13, 6\ns_add_u32 s34, s37, s36\n"
    "s_add_u32 s34, s34, s35\nv_lshl_add_u32 v16, v250, 10, s34\nv_lshl_add_u32 v16, v251, 4, v16\nv_and_b32 v251, 63, v0\ns_lshr_b32 s43, s13, 1\ns_lshl_b32 s42, s43, 13\n"
    "s_and_b32 s41, s13, 1\ns_lshl_b32 s40, s41, 11\ns_add_u32 s39, s42, s40\ns_add_u32 s39, s39, 0x1f600\nv_lshl_add_u32 v27, v251, 4, s39\nv_and_b32 v250, 1, v0\n"
    "s_lshl_b32 s38, s43, 3\ns_add_u32 s37, s38, 0x27fc0\nv_lshl_add_u32 v32, v250, 2, s37\ns_add_u32 s36, s38, 0x27d80\nv_mov_b32 v34, s36\nv_and_b32 v249, 0x7f, v0\n"
    "s_mul_i32 s35, s43, 0x2100\ns_add_u32 s34, s35, 0x18000\nv_lshl_add_u32 v35, v249, 4, s34\ns_lshr_b32 s43, s13, 1\ns_mul_i32 s42, s43, 0x600\n"
    "s_add_u32 s41, s42, 0x1c200\nv_mov_b32 v36, s41\nv_mov_b64 v[232:233], 0\nv_mov_b64 v[234:235], 0\nv_mov_b64 v[236:237], 0\nv_mov_b64 v[238:239], 0\n"
    "v_mov_b64 v[244:245], 0\nv_mov_b64 v[246:247], 0\nv_mov_b64 v[248:249], 0\nv_mov_b64 v[250:251], 0\n.Ll34:\nds_read_b128 v[74:77], v56 offset:96\n"
    "s_mul_i32 s59, s50, 196608\ns_mul_i32 s56, s50, 12288\ns_add_u32 s52, s66, s59\ns_addc_u32 s53, s67, 0\ns_mov_b32 m0, s56\ns_nop 0\n"
    "global_load_lds_dwordx4 v64, s[52:53] offset:0\nglobal_load_lds_dwordx4 v64, s[52:53] offset:1024\nglobal_load_lds_dwordx4 v64, s[52:53] offset:2048\n"
    "s_add_u32 s56, s56, 3072\ns_mov_b32 m0, s56\ns_add_u32 s52, s52, 49152\ns_addc_u32 s53, s53, 0\nglobal_load_lds_dwordx4 v64, s[52:53] offset:0\n"
    "global_load_lds_dwordx4 v64, s[52:53] offset:1024\nglobal_load_lds_dwordx4 v64, s[52:53] offset:2048\ns_add_u32 s56, s56, 3072\ns_mov_b32 m0, s56\n"
    "s_add_u32 s52, s52, 49152\ns_addc_u32 s53, s53, 0\nglobal_load_lds_dwordx4 v64, s[52:53] offset:0\nglobal_load_lds_dwordx4 v64, s[52:53] offset:1024\n"
    "global_load_lds_dwordx4 v64, s[52:53] offset:2048\ns_add_u32 s56, s56, 3072\ns_add_u32 s59, s58, -1\ns_cmp_lg_u32 s58, 4\ns_cbranch_scc1 .Ll38\ns_mov_b32 s59, s58\n"
    "s_waitcnt vmcnt(23)\n.Ll38:\nv_mov_b32 v70, s59\ns_mov_b64 exec, 1\ns_waitcnt vmcnt(25)\nds_max_i32 v56, v70\ns_mov_b64 exec, -1\ns_mov_b32 m0, s56\n"
    "s_add_u32 s52, s52, 49152\ns_addc_u32 s53, s53, 0\nglobal_load_lds_dwordx4 v64, s[52:53] offset:0\nglobal_load_lds_dwordx4 v64, s[52:53] offset:1024\n"
    "global_load_lds_dwordx4 v64, s[52:53] offset:2048\ns_add_u32 s56, s56, 3072\ns_lshl_b32 s59, s50, 7\ns_add_u32 s54, s48, s59\ns_addc_u32 s55, s49, 0\n"
    "s_add_u32 s57, s57, 4096\ns_cmp_eq_u32 s57, 110592\ns_cselect_b32 s57, 98304, s57\ns_sub_u32 s59, s58, 4\ns_mov_b32 s60, 0x100000\ns_waitcnt lgkmcnt(0)\n.Ll35:\n"
    "v_min3_i32 v74, v74, v75, v76\nv_min_i32 v74, v74, v77\nv_cmp_gt_i32 vcc, s59, v74\ns_cbranch_vccz .Ll36\ns_sub_u32 s60, s60, 1\ns_cbranch_scc1 .Ll36\n"
    "v_mov_b32 v70, s58\ns_mov_b64 exec, 1\ns_waitcnt vmcnt(12)\nds_max_i32 v56, v70\ns_mov_b64 exec, -1\ns_sleep 1\nds_read_b128 v[74:77], v56 offset:96\n"
    "s_waitcnt lgkmcnt(0)\ns_branch .Ll35\n.Ll36:\ns_add_u32 m0, s57, 0\ns_nop 0\nglobal_load_lds_dwordx4 v65, s[54:55] offset:0\n"
    "global_load_lds_dwordx4 v66, s[54:55] offset:1024\nglobal_load_lds_dwordx4 v67, s[54:55] offset:2048\nglobal_load_lds_dwordx4 v68, s[54:55] offset:3072\n"
    "s_add_u32 m0, s57, 12288\ns_add_u32 s54, s54, 32768\ns_addc_u32 s55, s55, 0\nglobal_load_lds_dwordx4 v65, s[54:55] offset:0\n"
    "global_load_lds_dwordx4 v66, s[54:55] offset:1024\nglobal_load_lds_dwordx4 v67, s[54:55] offset:2048\nglobal_load_lds_dwordx4 v68, s[54:55] offset:3072\n"
    "s_add_u32 m0, s57, 24576\ns_add_u32 s54, s54, 32768\ns_addc_u32 s55, s55, 0\nglobal_load_lds_dwordx4 v65, s[54:55] offset:0\n"
    "global_load_lds_dwordx4 v66, s[54:55] offset:1024\nglobal_load_lds_dwordx4 v67, s[54:55] offset:2048\nglobal_load_lds_dwordx4 v68, s[54:55] offset:3072\n"
    "s_add_u32 s59, s58, 0\nv_mov_b32 v70, s59\ns_mov_b64 exec, 1\ns_waitcnt vmcnt(24)\nds_max_i32 v56, v70\ns_mov_b64 exec, -1\ns_add_u32 m0, s57, 36864\n"
    "s_add_u32 s54, s54, 32768\ns_addc_u32 s55, s55, 0\nglobal_load_lds_dwordx4 v65, s[54:55] offset:0\nglobal_load_lds_dwordx4 v66, s[54:55] offset:1024\n"
    "global_load_lds_dwordx4 v67, s[54:55] offset:2048\nglobal_load_lds_dwordx4 v68, s[54:55] offset:3072\ns_cmp_lg_u32 s58, 2\ns_cbranch_scc1 .Ll37\ns_mov_b32 m0, 146432\n"
    "v_xor_b32 v72, 32, v71\nglobal_load_lds_dwordx4 v72, s[62:63] offset:2048\nv_xor_b32 v73, 48, v71\nglobal_load_lds_dwordx4 v73, s[62:63] offset:3072\n"
    "s_mov_b32 m0, 150528\nv_xor_b32 v72, 4160, v71\nglobal_load_lds_dwordx4 v72, s[62:63] offset:0\nv_xor_b32 v73, 4176, v71\n"
    "global_load_lds_dwordx4 v73, s[62:63] offset:1024\nv_xor_b32 v72, 4192, v71\ns_nop 0\ns_cbranch_execz .Lhop\nglobal_load_lds_dwordx4 v72, s[62:63] offset:2048\n"
    "v_xor_b32 v73, 4208, v71\nglobal_load_lds_dwordx4 v73, s[62:63] offset:3072\ns_mov_b32 m0, 154624\nv_xor_b32 v72, 8320, v71\n"
    "global_load_lds_dwordx4 v72, s[62:63] offset:0\nv_xor_b32 v73, 8336, v71\nglobal_load_lds_dwordx4 v73, s[62:63] offset:1024\ns_add_u32 s59, s58, 1\nv_mov_b32 v70, s59\n"
    "s_mov_b64 exec, 1\ns_waitcnt vmcnt(24)\nds_max_i32 v56, v70\ns_mov_b64 exec, -1\nv_xor_b32 v72, 8352, v71\nglobal_load_lds_dwordx4 v72, s[62:63] offset:2048\n"
    "v_xor_b32 v73, 8368, v71\nglobal_load_lds_dwordx4 v73, s[62:63] offset:3072\ns_mov_b32 m0, 158720\nv_xor_b32 v72, 12480, v71\n"
    "global_load_lds_dwordx4 v72, s[62:63] offset:0\nv_xor_b32 v73, 12496, v71\nglobal_load_lds_dwordx4 v73, s[62:63] offset:1024\nv_xor_b32 v72, 12512, v71\n"
    "global_load_lds_dwordx4 v72, s[62:63] offset:2048\nv_xor_b32 v73, 12528, v71\nglobal_load_lds_dwordx4 v73, s[62:63] offset:3072\n.Ll37:\ns_add_u32 s50, s50, 1\n"
    "s_and_b32 s50, s50, 7\ns_add_u32 s58, s58, 2\ns_cmp_lt_u32 s58, 16\ns_cbranch_scc1 .Ll34\nglobal_load_dword v78, v80, s[4:5] sc1\n"
    "global_load_dwordx4 v[252:255], v57, s[4:5] sc1\ns_add_u32 s59, s58, -1\nv_mov_b32 v70, s59\ns_mov_b64 exec, 1\ns_waitcnt vmcnt(18)\nds_max_i32 v56, v70\n"
    "s_mov_b64 exec, -1\nglobal_store_dwordx4 v79, v[60:63], s[4:5] offset:0 sc1\nglobal_store_dwordx4 v79, v[60:63], s[4:5] offset:1024 sc1\nv_add_u32 v79, 0x8000, v79\n"
    "global_store_dwordx4 v79, v[60:63], s[4:5] offset:0 sc1\nglobal_store_dwordx4 v79, v[60:63], s[4:5] offset:1024 sc1\nv_add_u32 v79, 0x8000, v79\n"
    "global_store_dwordx4 v79, v[60:63], s[4:5] offset:0 sc1\nglobal_store_dwordx4 v79, v[60:63], s[4:5] offset:1024 sc1\nv_add_u32 v79, 0x8000, v79\n"
    "global_store_dwordx4 v79, v[60:63], s[4:5] offset:0 sc1\nglobal_store_dwordx4 v79, v[60:63], s[4:5] offset:1024 sc1\ns_add_u32 s59, s58, 0\nv_mov_b32 v70, s59\n"
    "s_mov_b64 exec, 1\ns_waitcnt vmcnt(10)\nds_max_i32 v56, v70\ns_mov_b64 exec, -1\ns_waitcnt vmcnt(8)\nv_readfirstlane_b32 s11, v78\ns_mul_i32 s11, s11, 0x9E3779B1\n"
    "s_add_u32 s11, s11, 0x7F4A7C15\nv_xor_b32 v252, s11, v252\nv_xor_b32 v253, s11, v253\nv_xor_b32 v254, s11, v254\nv_xor_b32 v255, s11, v255\n"
    "v_or3_b32 v78, v252, v253, v254\nv_or_b32 v78, v78, v255\nv_cmp_ne_u32 vcc, 0, v78\ns_cmp_eq_u64 vcc, 0\ns_cselect_b32 s48, 18, 17\nv_mov_b32 v70, s48\n"
    "s_mov_b64 exec, 1\nds_write_b32 v56, v70\ns_mov_b64 exec, -1\nglobal_store_dwordx4 v39, v[60:63], s[4:5] offset:-4096 sc1\n"
    "global_store_dwordx4 v39, v[60:63], s[4:5] offset:-3072 sc1\nglobal_store_dwordx4 v39, v[60:63], s[4:5] offset:-2048 sc1\n"
    "global_store_dwordx4 v39, v[60:63], s[4:5] offset:-1024 sc1\nglobal_store_dwordx4 v39, v[60:63], s[4:5] offset:0 sc1\n"
    "global_store_dwordx4 v39, v[60:63], s[4:5] offset:1024 sc1\nglobal_store_dwordx4 v39, v[60:63], s[4:5] offset:2048 sc1\n"
    "global_store_dwordx4 v39, v[60:63], s[4:5] offset:3072 sc1\nglobal_store_dwordx4 v33, v[60:63], s[4:5] sc1\ns_branch .Lafl11\n"
    ".Lhalo10:\ns_lshl_b32 s43, s22, 17\ns_lshl_b32 s42, s13, 15\ns_add_u32 s44, s43, s42\ns_add_u32 s44, s44, 0xe01000\nv_and_b32 v231, 63, v0\nv_lshlrev_b32 v59, 4, v231\n"
    "s_add_u32 s68, s4, s44\ns_addc_u32 s69, s5, 0\ns_add_u32 s70, s68, 8192\ns_addc_u32 s71, s69, 0\ns_add_u32 s72, s68, 16384\ns_addc_u32 s73, s69, 0\n"
    "s_add_u32 s74, s68, 24576\ns_addc_u32 s75, s69, 0\nv_bfe_u32 v251, v0, 5, 1\nv_and_b32 v250, 31, v0\ns_lshl_b32 s43, s13, 11\ns_add_u32 s42, s43, 0x1d000\n"
    "v_lshl_add_u32 v17, v251, 8, s42\nv_lshl_add_u32 v17, v250, 2, v17\nv_and_b32 v249, 63, v0\nv_lshl_add_u32 v18, v249, 4, s42\ns_mov_b32 s41, 0x1f600\n"
    "v_lshl_add_u32 v19, v249, 4, s41\nv_bfe_u32 v248, v0, 3, 3\nv_and_b32 v247, 7, v0\ns_lshl_b32 s40, s22, 19\ns_lshl_b32 s39, s13, 17\ns_lshl_b32 s38, s21, 7\n"
    "s_add_u32 s37, s12, s40\ns_add_u32 s37, s37, s39\ns_add_u32 s37, s37, s38\ns_add_u32 s37, s37, 0x200000\nv_lshl_add_u32 v20, v248, 13, s37\n"
    "v_lshl_add_u32 v20, v247, 4, v20\ns_add_u32 s36, s12, s40\ns_add_u32 s36, s36, s39\ns_add_u32 s36, s36, s38\ns_add_u32 s36, s36, 0x210000\n"
    "v_lshl_add_u32 v21, v248, 13, s36\nv_lshl_add_u32 v21, v247, 4, v21\ns_lshl_b32 s35, s13, 8\ns_add_u32 s34, s35, 0x27600\nv_lshl_add_u32 v22, v249, 2, s34\n"
    "s_lshl_b32 s43, s13, 2\ns_add_u32 s42, s43, 0x27ff0\nv_mov_b32 v23, s42\ns_lshr_b32 s41, s12, 23\ns_add_u32 s40, s41, 2\nv_mov_b32 v24, s40\nv_bfe_u32 v251, v0, 5, 1\n"
    "v_and_b32 v250, 31, v0\ns_lshl_b32 s39, s13, 11\ns_add_u32 s38, s39, 0x1d400\nv_lshl_add_u32 v26, v251, 8, s38\nv_lshl_add_u32 v26, v250, 2, v26\nv_mov_b32 v25, 0x27fb4\n"
    "s_lshl_b32 s37, s21, 15\ns_lshl_b32 s36, s22, 8\ns_lshl_b32 s35, s13, 6\ns_add_u32 s34, s37, s36\ns_add_u32 s34, s34, s35\nv_lshl_add_u32 v16, v250, 10, s34\n"
    "v_lshl_add_u32 v16, v251, 4, v16\nv_and_b32 v251, 63, v0\ns_lshr_b32 s43, s13, 1\ns_lshl_b32 s42, s43, 13\ns_and_b32 s41, s13, 1\ns_lshl_b32 s40, s41, 11\n"
    "s_add_u32 s39, s42, s40\ns_add_u32 s39, s39, 0x1f600\nv_lshl_add_u32 v27, v251, 4, s39\nv_and_b32 v250, 1, v0\ns_lshl_b32 s38, s43, 3\ns_add_u32 s37, s38, 0x27fc0\n"
    "v_lshl_add_u32 v32, v250, 2, s37\ns_add_u32 s36, s38, 0x27d80\nv_mov_b32 v34, s36\nv_and_b32 v249, 0x7f, v0\ns_mul_i32 s35, s43, 0x2100\ns_add_u32 s34, s35, 0x18000\n"
    "v_lshl_add_u32 v35, v249, 4, s34\ns_lshr_b32 s43, s13, 1\ns_nop 0\ns_cbranch_execz .Lhop\ns_mul_i32 s42, s43, 0x600\ns_add_u32 s41, s42, 0x1c200\nv_mov_b32 v36, s41\n"
    "v_mov_b64 v[232:233], 0\nv_mov_b64 v[234:235], 0\nv_mov_b64 v[236:237], 0\nv_mov_b64 v[238:239], 0\nv_mov_b64 v[244:245], 0\nv_mov_b64 v[246:247], 0\n"
    "v_mov_b64 v[248:249], 0\nv_mov_b64 v[250:251], 0\ns_mov_b32 s48, 0\ns_cmp_lt_i32 s48, 5\ns_cbranch_scc1 .Lc40\n.Lc41:\nds_read_b128 v[64:67], v208\n"
    "ds_read_b128 v[68:71], v209\nds_read_b128 v[72:75], v210\nds_read_b128 v[76:79], v211\nds_read_b64_tr_b16 v[128:129], v192 offset:0\n"
    "ds_read_b64_tr_b16 v[130:131], v192 offset:256\nds_read_b64_tr_b16 v[132:133], v193 offset:0\nds_read_b64_tr_b16 v[134:135], v193 offset:256\n"
    "ds_read_b64_tr_b16 v[136:137], v192 offset:6144\nds_read_b64_tr_b16 v[138:139], v192 offset:6400\nds_read_b64_tr_b16 v[140:141], v193 offset:6144\n"
    "ds_read_b64_tr_b16 v[142:143], v193 offset:6400\ns_waitcnt lgkmcnt(4)\nv_mfma_f32_16x16x32_bf16 v[144:147], v[128:131], v[64:67], 0\n"
    "v_mfma_f32_16x16x32_bf16 v[148:151], v[132:135], v[64:67], 0\ns_waitcnt lgkmcnt(0)\nv_mfma_f32_16x16x32_bf16 v[144:147], v[136:139], v[68:71], v[144:147]\n"
    "v_mfma_f32_16x16x32_bf16 v[148:151], v[140:143], v[68:71], v[148:151]\ns_cmp_lt_i32 s48, 3\ns_cbranch_scc1 .Lc43\n.Lc44:\nds_read_b128 v[80:83], v212\n"
    "ds_read_b128 v[84:87], v213\nds_read_b128 v[88:91], v214\nds_read_b128 v[92:95], v215\nds_read_b64_tr_b16 v[128:129], v194 offset:0\n"
    "ds_read_b64_tr_b16 v[130:131], v194 offset:256\nds_read_b64_tr_b16 v[132:133], v195 offset:0\nds_read_b64_tr_b16 v[134:135], v195 offset:256\n"
    "ds_read_b64_tr_b16 v[136:137], v194 offset:6144\nds_read_b64_tr_b16 v[138:139], v194 offset:6400\nds_read_b64_tr_b16 v[140:141], v195 offset:6144\n"
    "ds_read_b64_tr_b16 v[142:143], v195 offset:6400\ns_waitcnt lgkmcnt(4)\nv_mfma_f32_16x16x32_bf16 v[144:147], v[128:131], v[72:75], v[144:147]\n"
    "v_mfma_f32_16x16x32_bf16 v[148:151], v[132:135], v[72:75], v[148:151]\ns_waitcnt lgkmcnt(0)\nv_mfma_f32_16x16x32_bf16 v[144:147], v[136:139], v[76:79], v[144:147]\n"
    "v_mfma_f32_16x16x32_bf16 v[148:151], v[140:143], v[76:79], v[148:151]\ns_cmp_lt_i32 s48, 5\ns_cbranch_scc1 .Lc46\n.Lc47:\nds_read_b128 v[96:99], v216\n"
    "ds_read_b128 v[100:103], v217\nds_read_b128 v[104:107], v218\nds_read_b128 v[108:111], v219\nds_read_b64_tr_b16 v[128:129], v196 offset:0\n"
    "ds_read_b64_tr_b16 v[130:131], v196 offset:256\nds_read_b64_tr_b16 v[132:133], v197 offset:0\nds_read_b64_tr_b16 v[134:135], v197 offset:256\n"
    "ds_read_b64_tr_b16 v[136:137], v196 offset:6144\nds_read_b64_tr_b16 v[138:139], v196 offset:6400\nds_read_b64_tr_b16 v[140:141], v197 offset:6144\n"
    "ds_read_b64_tr_b16 v[142:143], v197 offset:6400\nv_mfma_f32_16x16x32_bf16 v[152:155], v[64:67], v[64:67], 0\n"
    "v_mfma_f32_16x16x32_bf16 v[152:155], v[68:71], v[68:71], v[152:155]\nv_mfma_f32_16x16x32_bf16 v[152:155], v[72:75], v[72:75], v[152:155]\n"
    "v_mfma_f32_16x16x32_bf16 v[152:155], v[76:79], v[76:79], v[152:155]\ns_waitcnt lgkmcnt(4)\nv_mfma_f32_16x16x32_bf16 v[144:147], v[128:131], v[80:83], v[144:147]\n"
    "v_mfma_f32_16x16x32_bf16 v[148:151], v[132:135], v[80:83], v[148:151]\ns_waitcnt lgkmcnt(0)\nv_mfma_f32_16x16x32_bf16 v[144:147], v[136:139], v[84:87], v[144:147]\n"
    "v_mfma_f32_16x16x32_bf16 v[148:151], v[140:143], v[84:87], v[148:151]\ns_cmp_lt_i32 s48, 7\ns_cbranch_scc1 .Lc49\n.Lc50:\nds_read_b128 v[112:115], v220\n"
    "ds_read_b128 v[116:119], v221\nds_read_b128 v[120:123], v222\nds_read_b128 v[124:127], v223\nds_read_b64_tr_b16 v[128:129], v198 offset:0\n"
    "ds_read_b64_tr_b16 v[130:131], v198 offset:256\nds_read_b64_tr_b16 v[132:133], v199 offset:0\nds_read_b64_tr_b16 v[134:135], v199 offset:256\n"
    "ds_read_b64_tr_b16 v[136:137], v198 offset:6144\nds_read_b64_tr_b16 v[138:139], v198 offset:6400\nds_read_b64_tr_b16 v[140:141], v199 offset:6144\n"
    "ds_read_b64_tr_b16 v[142:143], v199 offset:6400\nv_mfma_f32_16x16x32_bf16 v[152:155], v[80:83], v[80:83], v[152:155]\n"
    "v_mfma_f32_16x16x32_bf16 v[152:155], v[84:87], v[84:87], v[152:155]\nv_mfma_f32_16x16x32_bf16 v[152:155], v[88:91], v[88:91], v[152:155]\n"
    "v_mfma_f32_16x16x32_bf16 v[152:155], v[92:95], v[92:95], v[152:155]\ns_waitcnt lgkmcnt(4)\nv_mfma_f32_16x16x32_bf16 v[144:147], v[128:131], v[88:91], v[144:147]\n"
    "v_mfma_f32_16x16x32_bf16 v[148:151], v[132:135], v[88:91], v[148:151]\ns_waitcnt lgkmcnt(0)\nv_mfma_f32_16x16x32_bf16 v[144:147], v[136:139], v[92:95], v[144:147]\n"
    "v_mfma_f32_16x16x32_bf16 v[148:151], v[140:143], v[92:95], v[148:151]\ns_cmp_lt_i32 s48, 9\ns_cbranch_scc1 .Lc52\n.Lc53:\nds_read_b64_tr_b16 v[128:129], v200 offset:0\n"
    "ds_read_b64_tr_b16 v[130:131], v200 offset:256\nds_read_b64_tr_b16 v[132:133], v201 offset:0\nds_read_b64_tr_b16 v[134:135], v201 offset:256\n"
    "ds_read_b64_tr_b16 v[136:137], v200 offset:6144\nds_read_b64_tr_b16 v[138:139], v200 offset:6400\nds_read_b64_tr_b16 v[140:141], v201 offset:6144\n"
    "ds_read_b64_tr_b16 v[142:143], v201 offset:6400\nv_mfma_f32_16x16x32_bf16 v[152:155], v[96:99], v[96:99], v[152:155]\n"
    "v_mfma_f32_16x16x32_bf16 v[152:155], v[100:103], v[100:103], v[152:155]\nv_mfma_f32_16x16x32_bf16 v[152:155], v[104:107], v[104:107], v[152:155]\n"
    "v_mfma_f32_16x16x32_bf16 v[152:155], v[108:111], v[108:111], v[152:155]\ns_waitcnt lgkmcnt(4)\nv_mfma_f32_16x16x32_bf16 v[144:147], v[128:131], v[96:99], v[144:147]\n"
    "v_mfma_f32_16x16x32_bf16 v[148:151], v[132:135], v[96:99], v[148:151]\ns_waitcnt lgkmcnt(0)\nv_mfma_f32_16x16x32_bf16 v[144:147], v[136:139], v[100:103], v[144:147]\n"
    "v_mfma_f32_16x16x32_bf16 v[148:151], v[140:143], v[100:103], v[148:151]\ns_cmp_lt_i32 s48, 11\ns_cbranch_scc1 .Lc55\n.Lc56:\n"
    "ds_read_b64_tr_b16 v[128:129], v202 offset:0\nds_read_b64_tr_b16 v[130:131], v202 offset:256\nds_read_b64_tr_b16 v[132:133], v203 offset:0\n"
    "ds_read_b64_tr_b16 v[134:135], v203 offset:256\nds_read_b64_tr_b16 v[136:137], v202 offset:6144\nds_read_b64_tr_b16 v[138:139], v202 offset:6400\n"
    "ds_read_b64_tr_b16 v[140:141], v203 offset:6144\nds_read_b64_tr_b16 v[142:143], v203 offset:6400\n"
    "v_mfma_f32_16x16x32_bf16 v[152:155], v[112:115], v[112:115], v[152:155]\nv_mfma_f32_16x16x32_bf16 v[152:155], v[116:119], v[116:119], v[152:155]\n"
    "v_mfma_f32_16x16x32_bf16 v[152:155], v[120:123], v[120:123], v[152:155]\nv_mfma_f32_16x16x32_bf16 v[152:155], v[124:127], v[124:127], v[152:155]\ns_waitcnt lgkmcnt(4)\n"
    "v_mfma_f32_16x16x32_bf16 v[144:147], v[128:131], v[104:107], v[144:147]\nv_mfma_f32_16x16x32_bf16 v[148:151], v[132:135], v[104:107], v[148:151]\ns_waitcnt lgkmcnt(0)\n"
    "v_mfma_f32_16x16x32_bf16 v[144:147], v[136:139], v[108:111], v[144:147]\nv_mfma_f32_16x16x32_bf16 v[148:151], v[140:143], v[108:111], v[148:151]\ns_cmp_lt_i32 s48, 13\n"
    "s_cbranch_scc1 .Lc58\n.Lc59:\nds_read_b64_tr_b16 v[128:129], v204 offset:0\nds_read_b64_tr_b16 v[130:131], v204 offset:256\nds_read_b64_tr_b16 v[132:133], v205 offset:0\n"
    "s_nop 0\ns_cbranch_execz .Lhop\nds_read_b64_tr_b16 v[134:135], v205 offset:256\nds_read_b64_tr_b16 v[136:137], v204 offset:6144\n"
    "ds_read_b64_tr_b16 v[138:139], v204 offset:6400\nds_read_b64_tr_b16 v[140:141], v205 offset:6144\nds_read_b64_tr_b16 v[142:143], v205 offset:6400\ns_waitcnt lgkmcnt(4)\n"
    "v_mfma_f32_16x16x32_bf16 v[144:147], v[128:131], v[112:115], v[144:147]\nv_mfma_f32_16x16x32_bf16 v[148:151], v[132:135], v[112:115], v[148:151]\ns_waitcnt lgkmcnt(0)\n"
    "v_mfma_f32_16x16x32_bf16 v[144:147], v[136:139], v[116:119], v[144:147]\nv_mfma_f32_16x16x32_bf16 v[148:151], v[140:143], v[116:119], v[148:151]\n"
    "s_mov_b32 s44, 0xaaaaaaaa\ns_mov_b32 s45, s44\ns_mov_b32 s46, 0xcccccccc\ns_mov_b32 s47, s46\nv_cndmask_b32 v156, v152, v153, s[44:45]\n"
    "v_cndmask_b32 v157, v154, v155, s[44:45]\ns_mov_b32 s44, 0x00f0000f\ns_mov_b32 s45, 0xf0000f00\nv_cndmask_b32 v156, v156, v157, s[46:47]\ns_mov_b64 s[46:47], 0xffff\n"
    "v_cndmask_b32 v156, 0, v156, s[44:45]\nv_cndmask_b32 v157, 0, v153, s[46:47]\ns_mov_b32 s44, 0x3b000000\ns_nop 0\nv_permlane32_swap_b32 v156, v157\n"
    "v_add_f32 v158, v156, v157\nv_add_f32 v159, v156, v157\ns_nop 1\nv_permlane16_swap_b32 v158, v159\nv_add_f32 v156, v158, v159\nv_add_f32 v157, v158, v159\ns_nop 1\n"
    "v_permlane32_swap_b32 v156, v157\nv_mul_f32 v157, s44, v157\nv_mul_f32 v158, v157, v157\nv_fma_f32 v158, v156, s44, -v158\nv_max_f32 v158, 0, v158\n"
    "v_cvt_pk_bf16_f32 v158, 0, v158\nv_add_f32 v158, 0x3727c5ac, v158\nv_cvt_pk_bf16_f32 v158, 0, v158\nv_sqrt_f32 v158, v158\nv_cvt_pk_bf16_f32 v157, 0, -v157\n"
    "v_cvt_pk_bf16_f32 v158, 0, v158\nv_rcp_f32 v158, v158\nv_mov_b32 v159, 1\ns_mov_b32 s46, 0xffff0000\ns_mov_b32 s44, 0x00020002\ns_mov_b32 s45, s44\ns_cmp_lt_i32 s48, 15\n"
    "s_cbranch_scc1 .Lc61\n.Lc62:\ns_setprio 3\nds_read_b64_tr_b16 v[128:129], v206 offset:0\nds_read_b64_tr_b16 v[130:131], v206 offset:256\n"
    "ds_read_b64_tr_b16 v[132:133], v207 offset:0\nds_read_b64_tr_b16 v[134:135], v207 offset:256\nds_read_b64_tr_b16 v[136:137], v206 offset:6144\n"
    "ds_read_b64_tr_b16 v[138:139], v206 offset:6400\nds_read_b64_tr_b16 v[140:141], v207 offset:6144\nds_read_b64_tr_b16 v[142:143], v207 offset:6400\ns_waitcnt lgkmcnt(4)\n"
    "v_mfma_f32_16x16x32_bf16 v[144:147], v[128:131], v[120:123], v[144:147]\nv_mfma_f32_16x16x32_bf16 v[148:151], v[132:135], v[120:123], v[148:151]\ns_waitcnt lgkmcnt(0)\n"
    "v_mfma_f32_16x16x32_bf16 v[144:147], v[136:139], v[124:127], v[144:147]\nv_mfma_f32_16x16x32_bf16 v[148:151], v[140:143], v[124:127], v[148:151]\ns_nop 6\n"
    "v_and_b32 v160, s46, v144\nv_and_b32 v161, s46, v145\nv_and_b32 v162, s46, v146\nv_and_b32 v163, s46, v147\nv_and_b32 v164, s46, v148\nv_and_b32 v165, s46, v149\n"
    "v_and_b32 v166, s46, v150\nv_and_b32 v167, s46, v151\nv_sub_f32 v168, v144, v160\nv_sub_f32 v169, v145, v161\nv_sub_f32 v170, v146, v162\nv_sub_f32 v171, v147, v163\n"
    "v_sub_f32 v172, v148, v164\nv_sub_f32 v173, v149, v165\nv_sub_f32 v174, v150, v166\nv_sub_f32 v175, v151, v167\nv_cvt_pk_bf16_f32 v160, v160, v168\n"
    "v_cvt_pk_bf16_f32 v161, v161, v169\nv_cvt_pk_bf16_f32 v162, v162, v170\nv_cvt_pk_bf16_f32 v163, v163, v171\nv_cvt_pk_bf16_f32 v164, v164, v172\n"
    "v_cvt_pk_bf16_f32 v165, v165, v173\nv_cvt_pk_bf16_f32 v166, v166, v174\nv_cvt_pk_bf16_f32 v167, v167, v175\ns_mov_b64 exec, s[44:45]\nds_write_b128 v58, v[160:163]\n"
    "ds_write_b128 v58, v[164:167] offset:64\nds_write_b32 v37, v159 offset:32\ns_mov_b64 exec, -1\ns_setprio 0\nds_read_b32 v156, v56\n"
    "v_fmac_f32_dpp v144, v144, v157 row_newbcast:1 row_mask:0xf bank_mask:0xf\nv_fmac_f32_dpp v145, v145, v157 row_newbcast:1 row_mask:0xf bank_mask:0xf\n"
    "v_fmac_f32_dpp v146, v146, v157 row_newbcast:1 row_mask:0xf bank_mask:0xf\nv_fmac_f32_dpp v147, v147, v157 row_newbcast:1 row_mask:0xf bank_mask:0xf\n"
    "v_fmac_f32_dpp v148, v148, v157 row_newbcast:1 row_mask:0xf bank_mask:0xf\nv_fmac_f32_dpp v149, v149, v157 row_newbcast:1 row_mask:0xf bank_mask:0xf\n"
    "v_fmac_f32_dpp v150, v150, v157 row_newbcast:1 row_mask:0xf bank_mask:0xf\nv_fmac_f32_dpp v151, v151, v157 row_newbcast:1 row_mask:0xf bank_mask:0xf\n"
    "v_mul_f32 v144, v144, v158\nv_mul_f32 v145, v145, v158\nv_mul_f32 v146, v146, v158\nv_mul_f32 v147, v147, v158\nv_mul_f32 v148, v148, v158\nv_mul_f32 v149, v149, v158\n"
    "s_waitcnt lgkmcnt(0)\nv_readfirstlane_b32 s48, v156\nv_mul_f32 v150, v150, v158\nv_mul_f32 v151, v151, v158\nv_cvt_pk_bf16_f32 v60, v144, v145\n"
    "v_cvt_pk_bf16_f32 v61, v146, v147\nv_cvt_pk_bf16_f32 v62, v148, v149\nv_cvt_pk_bf16_f32 v63, v150, v151\ns_cmp_lt_i32 s48, 17\ns_cbranch_scc1 .Lc64\n.Lc65:\n"
    ".Lafl11:\ns_mov_b32 s44, 0x00ff00ff\ns_mov_b32 s45, 0xff00ff00\ns_mov_b32 s46, s44\ns_mov_b32 s47, s44\ns_sub_u32 s26, s48, 17\ns_cmp_eq_u32 s26, 0\n"
    "s_cbranch_scc1 .La67\nglobal_load_dwordx4 v[64:67], v59, s[68:69] offset:-4096 sc1\nglobal_load_dwordx4 v[68:71], v59, s[68:69] offset:-3072 sc1\n"
    "global_load_dwordx4 v[72:75], v59, s[68:69] offset:-2048 sc1\nglobal_load_dwordx4 v[76:79], v59, s[68:69] offset:-1024 sc1\n"
    "global_load_dwordx4 v[80:83], v59, s[68:69] offset:0 sc1\nglobal_load_dwordx4 v[84:87], v59, s[68:69] offset:1024 sc1\n"
    "global_load_dwordx4 v[88:91], v59, s[68:69] offset:2048 sc1\nglobal_load_dwordx4 v[92:95], v59, s[68:69] offset:3072 sc1\n"
    "global_load_dwordx4 v[96:99], v59, s[70:71] offset:-4096 sc1\nglobal_load_dwordx4 v[100:103], v59, s[70:71] offset:-3072 sc1\n"
    "global_load_dwordx4 v[104:107], v59, s[70:71] offset:-2048 sc1\nglobal_load_dwordx4 v[108:111], v59, s[70:71] offset:-1024 sc1\n"
    "global_load_dwordx4 v[112:115], v59, s[70:71] offset:0 sc1\nglobal_load_dwordx4 v[116:119], v59, s[70:71] offset:1024 sc1\n"
    "global_load_dwordx4 v[120:123], v59, s[70:71] offset:2048 sc1\nglobal_load_dwordx4 v[124:127], v59, s[70:71] offset:3072 sc1\n.La69:\ns_cmp_eq_u32 s13, 1\n"
    "s_cbranch_scc1 .La71\ns_cmp_eq_u32 s13, 0\ns_cbranch_scc1 .La70\ns_mov_b32 s86, 0x100000\n.Ls72:\nds_read_b32 v224, v39 offset:96\ns_waitcnt lgkmcnt(0)\n"
    "v_cmp_gt_i32 vcc, 16, v224\ns_sub_u32 s86, s86, 1\ns_cbranch_scc1 .Ls73\ns_cbranch_vccz .Ls73\ns_sleep 1\ns_branch .Ls72\n.Ls73:\n"
    "ds_write2_b64 v38, v[60:61], v[62:63] offset1:4\ns_branch .La71\n.Lc40:\ns_mov_b32 s86, 0x100000\n.Lc42:\nds_read_b32 v156, v56\ns_waitcnt lgkmcnt(0)\n"
    "v_readfirstlane_b32 s48, v156\ns_sub_u32 s86, s86, 1\ns_cbranch_scc1 .Lc41\ns_cmp_ge_i32 s48, 5\ns_cbranch_scc1 .Lc41\ns_cbranch_execz .Lhop\ns_sleep 1\ns_branch .Lc42\n"
    ".Lc43:\ns_mov_b32 s86, 0x100000\n.Lc45:\nds_read_b32 v176, v56\ns_waitcnt lgkmcnt(0)\nv_readfirstlane_b32 s48, v176\ns_sub_u32 s86, s86, 1\ns_cbranch_scc1 .Lc44\n"
    "s_cmp_ge_i32 s48, 3\ns_cbranch_scc1 .Lc44\ns_sleep 1\ns_branch .Lc45\n.Lc46:\ns_mov_b32 s86, 0x100000\n.Lc48:\nds_read_b32 v176, v56\ns_waitcnt lgkmcnt(0)\n"
    "v_readfirstlane_b32 s48, v176\ns_sub_u32 s86, s86, 1\ns_cbranch_scc1 .Lc47\ns_cmp_ge_i32 s48, 5\ns_cbranch_scc1 .Lc47\ns_sleep 1\ns_branch .Lc48\n.Lc49:\n"
    "s_mov_b32 s86, 0x100000\n.Lc51:\nds_read_b32 v176, v56\ns_waitcnt lgkmcnt(0)\nv_readfirstlane_b32 s48, v176\ns_sub_u32 s86, s86, 1\ns_cbranch_scc1 .Lc50\n"
    "s_cmp_ge_i32 s48, 7\ns_cbranch_scc1 .Lc50\ns_sleep 1\ns_branch .Lc51\n.Lc52:\ns_mov_b32 s86, 0x100000\n.Lc54:\nds_read_b32 v176, v56\ns_waitcnt lgkmcnt(0)\n"
    "v_readfirstlane_b32 s48, v176\ns_sub_u32 s86, s86, 1\ns_cbranch_scc1 .Lc53\ns_cmp_ge_i32 s48, 9\ns_cbranch_scc1 .Lc53\ns_sleep 1\ns_branch .Lc54\n.Lc55:\n"
    "s_mov_b32 s86, 0x100000\n.Lc57:\nds_read_b32 v176, v56\ns_waitcnt lgkmcnt(0)\nv_readfirstlane_b32 s48, v176\ns_sub_u32 s86, s86, 1\ns_cbranch_scc1 .Lc56\n"
    "s_cmp_ge_i32 s48, 11\ns_cbranch_scc1 .Lc56\ns_sleep 1\ns_branch .Lc57\n.Lc58:\ns_mov_b32 s86, 0x100000\n.Lc60:\nds_read_b32 v176, v56\ns_waitcnt lgkmcnt(0)\n"
    "v_readfirstlane_b32 s48, v176\ns_sub_u32 s86, s86, 1\ns_cbranch_scc1 .Lc59\ns_cmp_ge_i32 s48, 13\ns_cbranch_scc1 .Lc59\ns_sleep 1\ns_branch .Lc60\n.Lc61:\n"
    "s_mov_b32 s86, 0x100000\n.Lc63:\nds_read_b32 v176, v56\ns_waitcnt lgkmcnt(0)\nv_readfirstlane_b32 s48, v176\ns_sub_u32 s86, s86, 1\ns_cbranch_scc1 .Lc62\n"
    "s_cmp_ge_i32 s48, 15\ns_cbranch_scc1 .Lc62\ns_branch .Lc63\n.Lc64:\ns_mov_b32 s86, 0x100000\n.Lc66:\nds_read_b32 v156, v56\ns_waitcnt lgkmcnt(0)\n"
    "v_readfirstlane_b32 s48, v156\ns_sub_u32 s86, s86, 1\ns_cbranch_scc1 .Lc65\ns_cmp_ge_i32 s48, 17\ns_cbranch_scc1 .Lc65\ns_sleep 1\ns_branch .Lc66\n.La70:\n"
    "v_mov_b32 v224, s11\ns_mov_b64 exec, 1\ns_waitcnt vmcnt(16)\nglobal_store_dword v58, v224, s[4:5] sc1\ns_mov_b64 exec, -1\ns_cmp_eq_u32 s26, 0\ns_cbranch_scc1 .La71\n"
    "global_load_dword v255, v38, s[4:5] sc1\n.La71:\n"
    ".Latt12:\ns_setprio 1\ns_waitcnt lgkmcnt(0)\ns_barrier\nds_read_b128 v[192:195], v6\nds_read_b128 v[196:199], v6 offset:208\ns_mov_b64 exec, s[44:45]\n"
    "ds_read_b128 v[232:235], v7\nds_read_b128 v[236:239], v7 offset:208\ns_mov_b64 exec, -1\nds_read_b64_tr_b16 v[200:201], v8 offset:0\n"
    "ds_read_b64_tr_b16 v[202:203], v8 offset:32\nds_read_b64_tr_b16 v[204:205], v8 offset:208\nds_read_b64_tr_b16 v[206:207], v8 offset:240\ns_mov_b32 s48, 0x3eb8aa3b\n"
    "s_waitcnt lgkmcnt(5)\nv_mfma_f32_16x16x32_bf16 v[208:211], v[192:195], v[232:235], v[2:5]\ns_waitcnt lgkmcnt(4)\n"
    "v_mfma_f32_16x16x32_bf16 v[212:215], v[196:199], v[236:239], v[2:5]\ns_nop 5\nv_max3_f32 v216, v208, v209, v210\ns_nop 0\nv_max3_f32 v217, v212, v213, v214\n"
    "v_max_f32 v216, v216, v211\nv_max_f32 v217, v217, v215\ns_nop 1\nv_permlane16_swap_b32 v216, v217\nv_max_f32 v218, v216, v217\nv_max_f32 v217, v216, v217\ns_nop 1\n"
    "v_permlane32_swap_b32 v218, v217\nv_max_f32 v218, v218, v217\nv_mul_f32 v216, 0xbeb8aa3b, v218\nv_mul_f32 v217, 0xbeb8aa3b, v218\ns_nop 1\n"
    "v_permlane16_swap_b32 v216, v217\nv_fma_f32 v208, v208, s48, v216\nv_fma_f32 v209, v209, s48, v216\nv_fma_f32 v210, v210, s48, v216\nv_fma_f32 v211, v211, s48, v216\n"
    "v_fma_f32 v212, v212, s48, v217\nv_fma_f32 v213, v213, s48, v217\nv_fma_f32 v214, v214, s48, v217\nv_fma_f32 v215, v215, s48, v217\nv_exp_f32 v208, v208\n"
    "v_exp_f32 v209, v209\nv_exp_f32 v210, v210\nv_exp_f32 v211, v211\nv_exp_f32 v212, v212\nv_exp_f32 v213, v213\nv_exp_f32 v214, v214\nv_exp_f32 v215, v215\n"
    "s_mov_b64 exec, s[46:47]\nv_cvt_pk_bf16_f32 v244, v208, v209\nv_cvt_pk_bf16_f32 v245, v210, v211\nv_cvt_pk_bf16_f32 v248, v212, v213\nv_cvt_pk_bf16_f32 v249, v214, v215\n"
    "s_not_b64 exec, exec\nv_cvt_pk_bf16_f32 v246, v208, v209\nv_cvt_pk_bf16_f32 v247, v210, v211\nv_cvt_pk_bf16_f32 v250, v212, v213\nv_cvt_pk_bf16_f32 v251, v214, v215\n"
    "s_mov_b64 exec, -1\nv_add_f32 v208, v208, v209\nv_add_f32 v209, v210, v211\ns_waitcnt lgkmcnt(0)\nv_mfma_f32_16x16x32_bf16 v[192:195], v[200:203], v[244:247], 0\n"
    "v_add_f32 v212, v212, v213\nv_add_f32 v214, v214, v215\nv_mfma_f32_16x16x32_bf16 v[196:199], v[204:207], v[248:251], 0\nv_add_f32 v216, v208, v209\n"
    "v_add_f32 v217, v212, v214\ns_nop 1\nv_permlane16_swap_b32 v216, v217\nv_add_f32 v218, v216, v217\nv_add_f32 v217, v216, v217\ns_nop 1\nv_permlane32_swap_b32 v218, v217\n"
    "v_add_f32 v218, v218, v217\nv_rcp_f32 v216, v218\nv_rcp_f32 v217, v218\ns_nop 1\nv_permlane16_swap_b32 v216, v217\n"
    "v_pk_mul_f32 v[192:193], v[192:193], v[216:217] op_sel:[0,0] op_sel_hi:[1,0]\nv_pk_mul_f32 v[194:195], v[194:195], v[216:217] op_sel:[0,0] op_sel_hi:[1,0]\n"
    "v_pk_mul_f32 v[196:197], v[196:197], v[216:217] op_sel:[0,1] op_sel_hi:[1,1]\nv_pk_mul_f32 v[198:199], v[198:199], v[216:217] op_sel:[0,1] op_sel_hi:[1,1]\n"
    "v_cvt_pk_bf16_f32 v200, v192, v193\nv_cvt_pk_bf16_f32 v201, v194, v195\nv_cvt_pk_bf16_f32 v202, v196, v197\nv_cvt_pk_bf16_f32 v203, v198, v199\ns_nop 0\n"
    "v_permlane16_swap_b32 v200, v202\nv_permlane16_swap_b32 v201, v203\nglobal_store_dwordx4 v9, v[200:203], s[4:5] sc1\ns_setprio 0\ns_cmp_gt_u32 s3, 3\n"
    "s_cbranch_scc1 .Lhb75\n"
    "s_or_b32 s44, s12, s13\ns_cbranch_scc1 .Lb74\ns_cmp_eq_u32 s27, 0\ns_cbranch_scc1 .Lb83\ns_mov_b32 s10, 64\ns_call_b64 s[94:95], .Lkgl13\n.Lb83:\n"
    "ds_write_b128 v33, v[40:43] offset:0\ns_nop 0\ns_cbranch_execz .Lhop\nds_write_b128 v33, v[44:47] offset:1024\nds_write_b128 v33, v[48:51] offset:2048\n"
    "ds_write_b128 v33, v[52:55] offset:3072\n.Lb74:\ns_lshl_b32 s43, s21, 15\ns_add_u32 s42, s12, s43\ns_add_u32 s42, s42, 0x1000\nv_lshl_add_u32 v60, v0, 4, s42\n"
    "s_mov_b32 s41, 0x1f600\nv_lshl_add_u32 v57, v0, 4, s41\nv_and_b32 v239, 3, v0\ns_mov_b32 s40, 0x27f80\nv_lshl_add_u32 v58, v239, 2, s40\nv_and_b32 v238, 0x7f, v0\n"
    "s_lshr_b32 s39, s13, 1\ns_mul_i32 s38, s39, 0x2100\ns_add_u32 s37, s38, 0x18000\nv_lshl_add_u32 v59, v238, 4, s37\nv_and_b32 v237, 31, v0\ns_mov_b32 s36, 0x27600\n"
    "v_lshl_add_u32 v39, v237, 2, s36\ns_lshl_b32 s35, s22, 13\ns_lshl_b32 s34, s21, 7\ns_add_u32 s33, s12, s35\ns_add_u32 s33, s33, s34\ns_add_u32 s33, s33, 0x400000\n"
    "v_lshl_add_u32 v56, v237, 2, s33\ns_lshr_b32 s43, s12, 23\ns_add_u32 s50, s43, 2\nv_bfe_u32 v239, v0, 2, 4\nv_and_b32 v238, 3, v0\ns_mov_b32 s42, 0x1004000\n"
    "v_lshl_add_u32 v240, v239, 7, s42\nv_lshl_add_u32 v240, v238, 4, v240\nv_add_u32 v61, 8192, v60\nv_add_u32 v62, 16384, v60\nv_add_u32 v63, 24576, v60\n"
    "s_mov_b32 s80, 0x10000\ns_cmp_lg_u32 s12, 0\ns_cbranch_scc1 .Lgb76\ns_waitcnt lgkmcnt(0)\ns_barrier\nglobal_load_dwordx4 v[252:255], v240, s[4:5] sc1\n"
    "s_add_u32 s43, s12, 0x400000\nv_lshl_add_u32 v16, v0, 4, s43\ns_lshl_b32 s42, s18, 13\ns_add_u32 s41, s12, s42\ns_add_u32 s41, s41, 0x200000\n"
    "v_lshl_add_u32 v17, v0, 4, s41\nv_lshrrev_b32 v127, 3, v0\nv_mul_u32_u24 v126, 0x50, v127\nv_and_b32 v125, 7, v0\nv_add_u32 v18, 0x1d550, v126\n"
    "v_lshl_add_u32 v18, v125, 3, v18\nv_bfe_u32 v124, v0, 4, 3\nv_xor_b32 v123, v0, v124\ns_mov_b32 s40, 0x19400\nv_lshl_add_u32 v19, v123, 3, s40\nv_bfe_u32 v122, v0, 4, 1\n"
    "v_xor_b32 v121, v0, v122\ns_mov_b32 s39, 0x17c00\nv_lshl_add_u32 v20, v121, 3, s39\ns_lshl_b32 s38, s18, 7\ns_add_u32 s37, s38, 0x600000\n"
    "v_lshl_add_u32 v120, v127, 15, s37\nv_lshl_add_u32 v120, v125, 4, v120\nv_lshrrev_b32 v119, 4, v0\nv_and_b32 v118, 15, v0\ns_lshl_b32 s36, s24, 18\n"
    "s_lshl_b32 s35, s25, 8\ns_add_u32 s34, s36, s35\ns_add_u32 s34, s34, 0x1200000\nv_lshl_add_u32 v117, v119, 13, s34\nv_lshl_add_u32 v117, v118, 4, v117\n"
    "s_cmp_lt_u32 s12, 1\ns_cselect_b64 vcc, -1, 0\nv_cndmask_b32 v21, v117, v120, vcc\nv_lshrrev_b32 v127, 8, v0\nv_mul_u32_u24 v126, 0x1950, v127\nv_and_b32 v125, 0xff, v0\n"
    "v_add_u32 v124, 0x1d000, v126\nv_lshl_add_u32 v124, v125, 4, v124\nv_mul_u32_u24 v123, 0x3180, v127\ns_mov_b32 s43, 0xfffffab0\nv_lshl_add_u32 v122, v125, 4, s43\n"
    "s_mov_b32 s42, 0x810\nv_lshl_add_u32 v121, v125, 4, s42\nv_mov_b32 v120, 0x1f80\nv_cmp_gt_u32 vcc, 0xa1, v125\ns_nop 1\nv_cndmask_b32 v119, v120, v121, vcc\n"
    "v_cmp_gt_u32 vcc, 0x7b, v125\ns_nop 1\nv_cndmask_b32 v118, v119, v122, vcc\nv_add_u32 v117, 0x20300, v123\nv_add_u32 v117, v117, v118\nv_cmp_gt_u32 vcc, 0x55, v125\n"
    "s_nop 1\nv_cndmask_b32 v22, v117, v124, vcc\nv_and_b32 v116, 31, v0\nv_sub_u32 v115, 0x3ff, v116\nv_bfe_u32 v114, v0, 5, 1\nv_lshl_add_u32 v113, v114, 3, v115\n"
    "v_and_b32 v112, 3, v113\nv_mul_u32_u24 v111, 0x840, v112\nv_lshrrev_b32 v110, 2, v113\ns_lshr_b32 s41, s13, 1\ns_mul_i32 s40, s41, 0x2100\ns_add_u32 s39, s40, 0x17800\n"
    "v_add_u32 v23, s39, v111\nv_lshl_add_u32 v23, v110, 3, v23\ns_lshl_b32 s38, s13, 2\ns_add_u32 s37, s38, 0x27f80\nv_mov_b32 v25, s37\ns_lshr_b32 s36, s12, 23\n"
    "s_add_u32 s35, s36, 4\nv_mov_b32 v26, s35\nv_mul_u32_u24 v109, 0x50, v116\ns_mul_i32 s34, s41, 0x1950\ns_and_b32 s33, s13, 1\ns_mul_i32 s32, s33, 0xa00\n"
    "s_add_u32 s31, s34, s32\ns_add_u32 s31, s31, 0x1d000\nv_add_u32 v27, s31, v109\nv_lshl_add_u32 v27, v114, 4, v27\nv_and_b32 v127, 31, v0\nv_bfe_u32 v126, v0, 5, 1\n"
    "v_bfe_u32 v125, v0, 1, 3\nv_xor_b32 v124, v126, v125\ns_lshr_b32 s43, s13, 1\ns_mul_i32 s42, s43, 0x2800\ns_and_b32 s41, s13, 1\ns_lshl_b32 s40, s41, 11\n"
    "s_add_u32 s39, s42, s40\ns_add_u32 s39, s39, 0x19400\nv_lshl_add_u32 v28, v127, 6, s39\nv_lshl_add_u32 v28, v124, 3, v28\nv_xor_b32 v29, 16, v28\nv_xor_b32 v30, 32, v28\n"
    "v_xor_b32 v31, 48, v28\n.Lg80:\ns_waitcnt vmcnt(0)\nv_xor_b32 v252, s11, v252\nv_xor_b32 v253, s11, v253\nv_xor_b32 v254, s11, v254\nv_xor_b32 v255, s11, v255\n"
    "v_or3_b32 v224, v252, v253, v254\nv_or_b32 v224, v224, v255\nv_cmp_ne_u32 vcc, 0, v224\ns_cbranch_vccz .Lg81\ns_sub_u32 s80, s80, 1\ns_cbranch_scc1 .Lg81\ns_sleep 2\n"
    "global_load_dwordx4 v[252:255], v240, s[4:5] sc1\ns_branch .Lg80\n.Lg81:\ns_mov_b32 s80, 0x10000\ns_sleep 2\ns_branch .Lgst77\n.Lgb76:\ns_barrier\ns_or_b32 s44, s2, s13\n"
    "s_cbranch_scc1 .Lg82\nv_mov_b32 v224, 16777216\nv_mov_b32 v225, s11\ns_mov_b64 exec, 1\nglobal_store_dword v224, v225, s[4:5] sc1\ns_mov_b64 exec, -1\n.Lg82:\n"
    "v_alignbit_b32 v228, v11, v10, 16\nv_alignbit_b32 v229, v12, v11, 16\nv_alignbit_b32 v230, v13, v12, 16\nv_alignbit_b32 v231, v14, v13, 16\ns_cbranch_execz .Lhop\n"
    "ds_write_b128 v59, v[10:13]\nv_mov_b32 v232, v11\nv_mov_b32 v233, v12\nv_mov_b32 v234, v13\nv_mov_b32 v235, v14\nds_write_b128 v59, v[228:231] offset:2112\n"
    "v_alignbit_b32 v236, v12, v11, 16\nv_alignbit_b32 v237, v13, v12, 16\nv_alignbit_b32 v238, v14, v13, 16\nv_alignbit_b32 v239, v15, v14, 16\n"
    "ds_write_b128 v59, v[232:235] offset:4224\nds_write_b128 v59, v[236:239] offset:6336\ns_add_u32 s43, s12, 0x400000\nv_lshl_add_u32 v16, v0, 4, s43\n"
    "s_lshl_b32 s42, s18, 13\ns_add_u32 s41, s12, s42\ns_add_u32 s41, s41, 0x200000\nv_lshl_add_u32 v17, v0, 4, s41\nv_lshrrev_b32 v127, 3, v0\nv_and_b32 v126, 7, v0\n"
    "s_lshl_b32 s40, s18, 7\ns_add_u32 s39, s40, 0x600000\nv_lshl_add_u32 v125, v127, 15, s39\nv_lshl_add_u32 v125, v126, 4, v125\nv_lshrrev_b32 v124, 4, v0\n"
    "v_and_b32 v123, 15, v0\ns_lshl_b32 s38, s24, 18\ns_lshl_b32 s37, s25, 8\ns_add_u32 s36, s38, s37\ns_add_u32 s36, s36, 0x1200000\nv_lshl_add_u32 v122, v124, 13, s36\n"
    "v_lshl_add_u32 v122, v123, 4, v122\ns_cmp_lt_u32 s12, 1\ns_cselect_b64 vcc, -1, 0\nv_cndmask_b32 v21, v122, v125, vcc\ns_lshr_b32 s35, s12, 23\ns_add_u32 s34, s35, 4\n"
    "v_mov_b32 v26, s34\ns_waitcnt vmcnt(0)\ns_sleep 18\n.Lgst77:\nglobal_load_dwordx4 v[208:211], v60, s[4:5] offset:-4096 sc1\n"
    "global_load_dwordx4 v[212:215], v60, s[4:5] offset:0 sc1\nglobal_load_dwordx4 v[216:219], v61, s[4:5] offset:-4096 sc1\n"
    "global_load_dwordx4 v[220:223], v61, s[4:5] offset:0 sc1\nglobal_load_dwordx4 v[224:227], v62, s[4:5] offset:-4096 sc1\n"
    "global_load_dwordx4 v[228:231], v62, s[4:5] offset:0 sc1\nglobal_load_dwordx4 v[232:235], v63, s[4:5] offset:-4096 sc1\n"
    "global_load_dwordx4 v[236:239], v63, s[4:5] offset:0 sc1\ns_cmp_lg_u32 s12, 0\ns_cbranch_scc1 .Lb83x\ns_mov_b32 s86, 0x100000\n.Ls84:\nds_read_b32 v244, v58 offset:64\n"
    "s_waitcnt lgkmcnt(0)\nv_cmp_gt_i32 vcc, 3, v244\ns_sub_u32 s86, s86, 1\ns_cbranch_scc1 .Ls85\ns_cbranch_vccz .Ls85\ns_sleep 1\ns_branch .Ls84\n.Ls85:\n.Lb83x:\n"
    "s_waitcnt vmcnt(7)\n.Lr86:\nds_write_b128 v57, v[208:211] offset:0\nv_max3_u32 v244, v208, v209, v210\ns_waitcnt vmcnt(6)\nv_max3_u32 v245, v212, v213, v214\n"
    "v_max3_u32 v244, v244, v211, v215\nv_max_u32 v244, v244, v245\nv_cmp_eq_u32 vcc, -1, v244\nds_write_b128 v57, v[212:215] offset:4096\ns_waitcnt lgkmcnt(0)\n"
    "s_cbranch_vccnz .Lw87\n.Lw87x:\ns_barrier\ns_waitcnt vmcnt(5)\n.Lr88:\nds_write_b128 v57, v[216:219] offset:8192\nv_max3_u32 v244, v216, v217, v218\ns_waitcnt vmcnt(4)\n"
    "v_max3_u32 v245, v220, v221, v222\nv_max3_u32 v244, v244, v219, v223\nv_max_u32 v244, v244, v245\nv_cmp_eq_u32 vcc, -1, v244\nds_write_b128 v57, v[220:223] offset:12288\n"
    "s_waitcnt lgkmcnt(0)\ns_cbranch_vccnz .Lw89\n.Lw89x:\ns_barrier\ns_waitcnt vmcnt(3)\n.Lr90:\nds_write_b128 v57, v[224:227] offset:16384\n"
    "v_max3_u32 v244, v224, v225, v226\ns_waitcnt vmcnt(2)\nv_max3_u32 v245, v228, v229, v230\nv_max3_u32 v244, v244, v227, v231\nv_max_u32 v244, v244, v245\n"
    "v_cmp_eq_u32 vcc, -1, v244\nds_write_b128 v57, v[228:231] offset:20480\ns_cbranch_vccnz .Lw91\n.Lw91x:\ns_waitcnt vmcnt(1)\n.Lr92:\n"
    "ds_write_b128 v57, v[232:235] offset:24576\nv_max3_u32 v244, v232, v233, v234\ns_waitcnt vmcnt(0)\nv_max3_u32 v245, v236, v237, v238\nv_max3_u32 v244, v244, v235, v239\n"
    "v_max_u32 v244, v244, v245\nv_cmp_eq_u32 vcc, -1, v244\nds_write_b128 v57, v[236:239] offset:28672\ns_waitcnt lgkmcnt(0)\ns_cbranch_vccnz .Lw93\n.Lw93x:\ns_barrier\n"
    "s_or_b32 s44, s12, s13\ns_cbranch_scc1 .Lg94\nv_and_b32 v239, 63, v0\ns_lshl_b32 s43, s18, 13\ns_add_u32 s42, s43, 0x601000\nv_lshl_add_u32 v40, v239, 4, s42\n"
    "s_lshl_b32 s41, s18, 10\ns_add_u32 s40, s41, 0xd00000\nv_lshl_add_u32 v41, v239, 4, s40\nv_mov_b64 v[52:53], -1\nv_mov_b64 v[54:55], -1\n"
    "global_store_dwordx4 v40, v[52:55], s[4:5] offset:-4096 sc1\nglobal_store_dwordx4 v40, v[52:55], s[4:5] offset:-3072 sc1\n"
    "global_store_dwordx4 v40, v[52:55], s[4:5] offset:-2048 sc1\nglobal_store_dwordx4 v40, v[52:55], s[4:5] offset:-1024 sc1\n"
    "global_store_dwordx4 v40, v[52:55], s[4:5] offset:0 sc1\nglobal_store_dwordx4 v40, v[52:55], s[4:5] offset:1024 sc1\n"
    "global_store_dwordx4 v40, v[52:55], s[4:5] offset:2048 sc1\nglobal_store_dwordx4 v40, v[52:55], s[4:5] offset:3072 sc1\nglobal_store_dwordx4 v41, v[52:55], s[4:5] sc1\n"
    ".Lg94:\nds_read_b128 v[10:13], v59\nds_read_b32 v14, v59 offset:4236\nds_read_u16 v15, v59 offset:6350\nds_read_b64 v[64:65], v23 offset:2016\n"
    "ds_read_b64 v[66:67], v23 offset:2024\nds_read_b64 v[68:69], v23 offset:1984\nds_read_b64 v[70:71], v23 offset:1992\nds_read_b64 v[72:73], v23 offset:1952\n"
    "ds_read_b64 v[74:75], v23 offset:1960\nds_read_b64 v[76:77], v23 offset:1920\nds_read_b64 v[78:79], v23 offset:1928\nds_read_b64 v[80:81], v23 offset:1888\n"
    "ds_read_b64 v[82:83], v23 offset:1896\nds_read_b64 v[84:85], v23 offset:1856\nds_read_b64 v[86:87], v23 offset:1864\nds_read_b64 v[88:89], v23 offset:1824\n"
    "ds_read_b64 v[90:91], v23 offset:1832\nds_read_b64 v[92:93], v23 offset:1792\nds_read_b64 v[94:95], v23 offset:1800\nds_read_b64 v[96:97], v23 offset:1760\n"
    "ds_read_b64 v[98:99], v23 offset:1768\nds_read_b64 v[100:101], v23 offset:1728\nds_read_b64 v[102:103], v23 offset:1736\nds_read_b64 v[104:105], v23 offset:1696\n"
    "ds_read_b64 v[106:107], v23 offset:1704\nds_read_b64 v[108:109], v23 offset:1664\nds_read_b64 v[110:111], v23 offset:1672\nds_read_b64 v[112:113], v23 offset:1632\n"
    "ds_read_b64 v[114:115], v23 offset:1640\nds_read_b64 v[116:117], v23 offset:1600\nds_read_b64 v[118:119], v23 offset:1608\nds_read_b64 v[120:121], v23 offset:1568\n"
    "ds_read_b64 v[122:123], v23 offset:1576\nds_read_b64 v[124:125], v23 offset:1536\ns_nop 0\ns_cbranch_execz .Lhop\nds_read_b64 v[126:127], v23 offset:1544\n"
    "ds_read_b64 v[128:129], v23 offset:1504\nds_read_b64 v[130:131], v23 offset:1512\nds_read_b64 v[132:133], v23 offset:1472\nds_read_b64 v[134:135], v23 offset:1480\n"
    "ds_read_b64 v[136:137], v23 offset:1440\nds_read_b64 v[138:139], v23 offset:1448\nds_read_b64 v[140:141], v23 offset:1408\nds_read_b64 v[142:143], v23 offset:1416\n"
    "ds_read_b64 v[144:145], v23 offset:1376\nds_read_b64 v[146:147], v23 offset:1384\nds_read_b64 v[148:149], v23 offset:1344\nds_read_b64 v[150:151], v23 offset:1352\n"
    "ds_read_b64 v[152:153], v23 offset:1312\nds_read_b64 v[154:155], v23 offset:1320\nds_read_b64 v[156:157], v23 offset:1280\nds_read_b64 v[158:159], v23 offset:1288\n"
    "ds_read_b64 v[160:161], v23 offset:1248\nds_read_b64 v[162:163], v23 offset:1256\nds_read_b64 v[164:165], v23 offset:1216\nds_read_b64 v[166:167], v23 offset:1224\n"
    "ds_read_b64 v[168:169], v23 offset:1184\nds_read_b64 v[170:171], v23 offset:1192\nds_read_b64 v[172:173], v23 offset:1152\nds_read_b64 v[174:175], v23 offset:1160\n"
    "ds_read_b64 v[176:177], v23 offset:1120\nds_read_b64 v[178:179], v23 offset:1128\nds_read_b64 v[180:181], v23 offset:1088\nds_read_b64 v[182:183], v23 offset:1096\n"
    "ds_read_b64 v[184:185], v23 offset:1056\nds_read_b64 v[186:187], v23 offset:1064\nds_read_b64 v[188:189], v23 offset:1024\nds_read_b64 v[190:191], v23 offset:1032\n"
    "ds_read_b64 v[192:193], v23 offset:992\nds_read_b64 v[194:195], v23 offset:1000\nds_read_b64 v[196:197], v23 offset:960\nds_read_b64 v[198:199], v23 offset:968\n"
    "ds_read_b64 v[200:201], v23 offset:928\nds_read_b64 v[202:203], v23 offset:936\ns_cmp_lg_u32 s13, 0\ns_cbranch_scc1 .Lln79\ns_mov_b32 s86, 0x100000\n"
    "s_mov_b32 s45, 0x3c000000\ns_waitcnt vmcnt(0)\n.Lg98:\nds_read_b32 v43, v58 offset:112\nds_read2_b32 v[44:45], v39 offset0:0 offset1:32\n"
    "ds_read2_b32 v[46:47], v39 offset0:64 offset1:96\nds_read2_b32 v[48:49], v39 offset0:128 offset1:160\nds_read2_b32 v[50:51], v39 offset0:192 offset1:224\n"
    "s_sub_u32 s86, s86, 1\ns_waitcnt lgkmcnt(0)\nv_cmp_gt_i32 vcc, s50, v43\nv_pk_add_f32 v[44:45], v[44:45], v[46:47]\nv_pk_add_f32 v[48:49], v[48:49], v[50:51]\n"
    "s_cbranch_scc1 .Lg99\nv_pk_add_f32 v[44:45], v[44:45], v[48:49]\nv_mul_f32 v44, s45, v44\nv_mul_f32 v46, v44, v44\ns_cbranch_vccnz .Lg98\n.Lg99:\n"
    "v_fma_f32 v45, v45, s45, -v46\nv_max_f32 v45, 0, v45\nv_sqrt_f32 v45, v45\ns_mov_b32 exec_hi, 0\nv_cvt_pk_bf16_f32 v44, v44, v45\n"
    "global_store_dword v56, v44, s[4:5] sc1\ns_mov_b32 exec_hi, -1\ns_waitcnt vmcnt(0)\ns_sleep 5\ns_branch .Lln79\n"
    ".Lhb75:\ns_cmp_lg_u32 s12, 0\ns_cbranch_scc1 .Lh100\ns_setprio 1\ns_cmp_eq_u32 s13, 0\ns_cbranch_scc1 .Lh100y\nds_write_b128 v33, v[40:43] offset:0\n"
    "ds_write_b128 v33, v[44:47] offset:1024\nds_write_b128 v33, v[48:51] offset:2048\nds_write_b128 v33, v[52:55] offset:3072\n.Lh100x:\ns_cmp_eq_u32 s26, 0\n"
    "s_waitcnt lgkmcnt(0)\ns_barrier\ns_cbranch_scc1 .Lh101\n.Lh102:\nds_read_b64 v[48:49], v34\nds_read_b128 v[192:195], v27 offset:0\n"
    "ds_read_b128 v[196:199], v27 offset:1024\nds_read_b128 v[200:203], v27 offset:4096\nds_read_b128 v[204:207], v27 offset:5120\nv_mov_b32 v52, 0\n"
    "ds_write_b16 v36, v52 offset:1400\ns_waitcnt vmcnt(17)\nglobal_load_dwordx4 v[240:243], v16, s[8:9] offset:0\n"
    "v_add_u32_sdwa v224, v36, v28 dst_sel:DWORD dst_unused:UNUSED_PAD src0_sel:DWORD src1_sel:WORD_0\n"
    "v_add_u32_sdwa v225, v36, v28 dst_sel:DWORD dst_unused:UNUSED_PAD src0_sel:DWORD src1_sel:WORD_1\n"
    "v_add_u32_sdwa v226, v36, v29 dst_sel:DWORD dst_unused:UNUSED_PAD src0_sel:DWORD src1_sel:WORD_0\n"
    "v_add_u32_sdwa v227, v36, v29 dst_sel:DWORD dst_unused:UNUSED_PAD src0_sel:DWORD src1_sel:WORD_1\nglobal_load_dwordx4 v[244:247], v16, s[8:9] offset:32\n"
    "v_add_u32_sdwa v228, v36, v30 dst_sel:DWORD dst_unused:UNUSED_PAD src0_sel:DWORD src1_sel:WORD_0\n"
    "v_add_u32_sdwa v229, v36, v30 dst_sel:DWORD dst_unused:UNUSED_PAD src0_sel:DWORD src1_sel:WORD_1\n"
    "v_add_u32_sdwa v230, v36, v31 dst_sel:DWORD dst_unused:UNUSED_PAD src0_sel:DWORD src1_sel:WORD_0\n"
    "v_add_u32_sdwa v231, v36, v31 dst_sel:DWORD dst_unused:UNUSED_PAD src0_sel:DWORD src1_sel:WORD_1\ns_waitcnt lgkmcnt(5)\nv_mov_b32 v50, 3\n"
    "global_load_dwordx4 v[128:131], v59, s[72:73] offset:-4096 sc1\nv_add_f32 v48, v48, v49\nv_lshrrev_b32 v48, 23, v48\nv_sub_u32 v48, 0x10c, v48\nv_min_u32 v48, 0xfc, v48\n"
    "global_load_dwordx4 v[132:135], v59, s[72:73] offset:-3072 sc1\nv_sub_u32 v49, 0xfe, v48\nv_lshlrev_b32 v48, 23, v48\nv_lshlrev_b32 v49, 23, v49\ns_waitcnt lgkmcnt(1)\n"
    "v_pk_add_f32 v[40:41], v[192:193], v[200:201]\nv_pk_add_f32 v[42:43], v[194:195], v[202:203]\nglobal_load_dwordx4 v[136:139], v59, s[72:73] offset:-2048 sc1\n"
    "v_pk_add_f32 v[44:45], v[196:197], v[204:205]\nv_pk_add_f32 v[46:47], v[198:199], v[206:207]\nv_pk_mul_f32 v[40:41], v[40:41], v[48:49] op_sel_hi:[1,0]\n"
    "v_pk_mul_f32 v[42:43], v[42:43], v[48:49] op_sel_hi:[1,0]\nglobal_load_dwordx4 v[140:143], v59, s[72:73] offset:-1024 sc1\n"
    "v_pk_mul_f32 v[44:45], v[44:45], v[48:49] op_sel_hi:[1,0]\nv_pk_mul_f32 v[46:47], v[46:47], v[48:49] op_sel_hi:[1,0]\nv_cvt_pk_f16_f32 v40, v40, v41\n"
    "v_cvt_pk_f16_f32 v41, v42, v43\nv_cvt_pk_f16_f32 v42, v44, v45\nglobal_load_dwordx4 v[144:147], v59, s[72:73] offset:0 sc1\nv_cvt_pk_f16_f32 v43, v46, v47\n"
    "ds_write_b16 v224, v40\nds_write_b16_d16_hi v225, v40\nds_write_b16 v226, v41\nglobal_load_dwordx4 v[148:151], v59, s[72:73] offset:1024 sc1\n"
    "ds_write_b16_d16_hi v227, v41\nds_write_b16 v228, v42\nds_write_b16_d16_hi v229, v42\nds_write_b16 v230, v43\nds_write_b16_d16_hi v231, v43\n"
    "global_load_dwordx4 v[152:155], v59, s[72:73] offset:2048 sc1\ns_mov_b64 exec, 1\nds_write_b32 v37, v50 offset:64\nds_write_b32 v34, v49 offset:32\ns_mov_b64 exec, -1\n"
    "v_add_u32_sdwa v232, v36, v10 dst_sel:DWORD dst_unused:UNUSED_PAD src0_sel:DWORD src1_sel:WORD_0\n"
    "v_add_u32_sdwa v233, v36, v10 dst_sel:DWORD dst_unused:UNUSED_PAD src0_sel:DWORD src1_sel:WORD_1\n"
    "v_add_u32_sdwa v234, v36, v11 dst_sel:DWORD dst_unused:UNUSED_PAD src0_sel:DWORD src1_sel:WORD_0\n"
    "v_add_u32_sdwa v235, v36, v11 dst_sel:DWORD dst_unused:UNUSED_PAD src0_sel:DWORD src1_sel:WORD_1\nglobal_load_dwordx4 v[156:159], v59, s[72:73] offset:3072 sc1\n"
    "s_cbranch_execz .Lhop\nv_add_u32_sdwa v236, v36, v12 dst_sel:DWORD dst_unused:UNUSED_PAD src0_sel:DWORD src1_sel:WORD_0\n"
    "v_add_u32_sdwa v237, v36, v12 dst_sel:DWORD dst_unused:UNUSED_PAD src0_sel:DWORD src1_sel:WORD_1\n"
    "v_add_u32_sdwa v238, v36, v13 dst_sel:DWORD dst_unused:UNUSED_PAD src0_sel:DWORD src1_sel:WORD_0\n"
    "v_add_u32_sdwa v239, v36, v13 dst_sel:DWORD dst_unused:UNUSED_PAD src0_sel:DWORD src1_sel:WORD_1\nglobal_load_dwordx4 v[160:163], v59, s[74:75] offset:-4096 sc1\n"
    "v_add_u32_sdwa v248, v36, v14 dst_sel:DWORD dst_unused:UNUSED_PAD src0_sel:DWORD src1_sel:WORD_0\n"
    "v_add_u32_sdwa v249, v36, v14 dst_sel:DWORD dst_unused:UNUSED_PAD src0_sel:DWORD src1_sel:WORD_1\n"
    "v_add_u32_sdwa v250, v36, v15 dst_sel:DWORD dst_unused:UNUSED_PAD src0_sel:DWORD src1_sel:WORD_0\ns_mov_b32 s86, 0x100000\n.Ls108:\nds_read_b32 v48, v32 offset:0\n"
    "s_waitcnt lgkmcnt(0)\nv_cmp_gt_i32 vcc, 3, v48\ns_sub_u32 s86, s86, 1\ns_cbranch_scc1 .Ls109\ns_cbranch_vccz .Ls109\ns_branch .Ls108\n.Ls109:\nds_read_u16 v200, v232\n"
    "global_load_dwordx4 v[164:167], v59, s[74:75] offset:-3072 sc1\nds_read_u16 v201, v233\nds_read_u16 v202, v234\nds_read_u16 v203, v235\nds_read_u16 v204, v236\n"
    "global_load_dwordx4 v[168:171], v59, s[74:75] offset:-2048 sc1\nds_read_u16 v205, v237\nds_read_u16 v206, v238\nds_read_u16 v207, v239\nds_read_u16 v208, v248\n"
    "ds_read_u16 v209, v249\nglobal_load_dwordx4 v[172:175], v59, s[74:75] offset:-1024 sc1\nds_read_u16 v210, v250\ns_waitcnt lgkmcnt(0)\nv_lshl_or_b32 v192, v201, 16, v200\n"
    "v_lshl_or_b32 v193, v203, 16, v202\nv_lshl_or_b32 v194, v205, 16, v204\nglobal_load_dwordx4 v[176:179], v59, s[74:75] offset:0 sc1\nv_lshl_or_b32 v195, v207, 16, v206\n"
    "v_lshl_or_b32 v196, v209, 16, v208\nv_mov_b32 v197, v210\nv_alignbit_b32 v212, v193, v192, 16\nv_alignbit_b32 v213, v194, v193, 16\n"
    "global_load_dwordx4 v[180:183], v59, s[74:75] offset:1024 sc1\nv_alignbit_b32 v214, v195, v194, 16\nv_alignbit_b32 v215, v196, v195, 16\nds_write_b128 v35, v[192:195]\n"
    "v_mov_b32 v216, v193\nglobal_load_dwordx4 v[184:187], v59, s[74:75] offset:2048 sc1\nv_mov_b32 v217, v194\nv_mov_b32 v218, v195\nv_mov_b32 v219, v196\n"
    "ds_write_b128 v35, v[212:215] offset:2112\nv_alignbit_b32 v220, v194, v193, 16\nglobal_load_dwordx4 v[188:191], v59, s[74:75] offset:3072 sc1\n"
    "v_alignbit_b32 v221, v195, v194, 16\nv_alignbit_b32 v222, v196, v195, 16\nv_alignbit_b32 v223, v197, v196, 16\nds_write_b128 v35, v[216:219] offset:4224\n"
    "ds_write_b128 v35, v[220:223] offset:6336\ns_waitcnt vmcnt(16)\nv_permlane32_swap_b32 v240, v242\nv_permlane32_swap_b32 v241, v243\nv_permlane32_swap_b32 v244, v246\n"
    "v_permlane32_swap_b32 v245, v247\nv_and_b32 v239, 63, v0\nv_add_u32 v238, 3, v239\nv_lshrrev_b32 v237, 2, v238\nv_mul_u32_u24 v236, 0x50, v237\nv_and_b32 v235, 3, v238\n"
    "s_lshr_b32 s43, s13, 1\ns_mul_i32 s42, s43, 0x1950\ns_add_u32 s41, s42, 0x1d500\nv_add_u32 v10, s41, v236\nv_lshl_add_u32 v10, v235, 4, v10\nv_add_u32 v234, 2, v239\n"
    "v_lshrrev_b32 v233, 2, v234\nv_mul_u32_u24 v232, 0x50, v233\nv_and_b32 v231, 3, v234\ns_add_u32 s40, s42, 0x1d508\nv_add_u32 v11, s40, v232\n"
    "v_lshl_add_u32 v11, v231, 4, v11\nv_bfe_u32 v230, v0, 2, 4\nv_mul_u32_u24 v229, 0x50, v230\nv_and_b32 v228, 3, v0\ns_add_u32 s39, s42, 0x1d550\nv_add_u32 v12, s39, v229\n"
    "v_lshl_add_u32 v12, v228, 4, v12\ns_and_b32 s38, s13, 1\nv_add_u32 v227, 1, v239\nv_lshrrev_b32 v226, 5, v227\nv_mul_u32_u24 v225, 0x240, v226\nv_and_b32 v224, 31, v227\n"
    "s_mul_i32 s37, s43, 0x3180\ns_add_u32 s36, s37, 0x20540\nv_add_u32 v223, s36, v225\nv_lshl_add_u32 v223, v224, 4, v223\nv_lshrrev_b32 v222, 6, v227\n"
    "v_mul_u32_u24 v221, 0x480, v222\nv_and_b32 v220, 63, v227\ns_add_u32 s35, s37, 0x22280\nv_add_u32 v219, s35, v221\nv_lshl_add_u32 v219, v220, 4, v219\n"
    "s_cmp_lt_u32 s38, 1\ns_cselect_b64 vcc, -1, 0\nv_cndmask_b32 v13, v219, v223, vcc\nv_lshrrev_b32 v218, 5, v234\nv_mul_u32_u24 v217, 0x240, v218\n"
    "v_and_b32 v216, 31, v234\nv_add_u32 v215, s36, v217\nv_lshl_add_u32 v215, v216, 4, v215\ns_add_u32 s34, s37, 0x22b80\nv_lshl_add_u32 v214, v239, 4, s34\n"
    "s_cmp_lt_u32 s38, 1\ns_cselect_b64 vcc, -1, 0\nv_cndmask_b32 v14, v214, v215, vcc\ns_and_b32 s43, s13, 1\nv_and_b32 v239, 63, v0\nv_add_u32 v238, 1, v239\n"
    "v_lshrrev_b32 v237, 5, v238\nv_mul_u32_u24 v236, 0x240, v237\nv_and_b32 v235, 31, v238\ns_lshr_b32 s42, s13, 1\ns_mul_i32 s41, s42, 0x3180\ns_add_u32 s40, s41, 0x20540\n"
    "v_add_u32 v234, s40, v236\nv_lshl_add_u32 v234, v235, 4, v234\nv_lshrrev_b32 v233, 6, v238\nv_mul_u32_u24 v232, 0x480, v233\nv_and_b32 v231, 63, v238\n"
    "s_add_u32 s39, s41, 0x22280\nv_add_u32 v230, s39, v232\nv_lshl_add_u32 v230, v231, 4, v230\ns_cmp_lt_u32 s43, 1\ns_cselect_b64 vcc, -1, 0\n"
    "v_cndmask_b32 v229, v230, v234, vcc\ns_mov_b32 s38, 0xfffffb40\ns_cmp_lt_u32 s43, 1\ns_cselect_b32 s37, s38, 0x1f00\ns_add_u32 s36, s41, s37\n"
    "s_add_u32 s36, s36, 0x20300\nv_mov_b32 v228, s36\nv_cmp_gt_u32 vcc, 64, v238\ns_nop 1\nv_cndmask_b32 v15, v228, v229, vcc\nv_add_u32 v227, 2, v239\n"
    "v_lshrrev_b32 v226, 5, v227\nv_mul_u32_u24 v225, 0x240, v226\nv_and_b32 v224, 31, v227\nv_add_u32 v223, s40, v225\nv_lshl_add_u32 v223, v224, 4, v223\n"
    "s_add_u32 s35, s41, 0x1fe40\nv_mov_b32 v222, s35\nv_cmp_gt_u32 vcc, 64, v227\ns_nop 1\nv_cndmask_b32 v35, v222, v223, vcc\ns_lshl_b32 s34, s18, 13\n"
    "s_lshl_b32 s33, s13, 11\ns_add_u32 s32, s34, s33\ns_add_u32 s32, s32, 0x800000\nv_lshl_add_u32 v37, v239, 4, s32\nv_and_b32 v239, 7, v0\ns_lshl_b32 s43, s2, 7\n"
    "s_add_u32 s42, s43, 0xc00000\nv_lshl_add_u32 v38, v239, 4, s42\ns_branch .Lhwo78\n.Lh100y:\ns_branch .Lh100x\n.Lh100:\ns_barrier\n"
    "global_load_dwordx4 v[128:131], v59, s[72:73] offset:-4096 sc1\nv_bfe_u32 v223, v0, 5, 1\nv_and_b32 v222, 31, v0\ns_lshl_b32 s43, s13, 11\ns_cbranch_execz .Lhop\n"
    "global_load_dwordx4 v[132:135], v59, s[72:73] offset:-3072 sc1\ns_add_u32 s42, s43, 0x1d000\nv_lshl_add_u32 v17, v223, 8, s42\nv_lshl_add_u32 v17, v222, 2, v17\n"
    "global_load_dwordx4 v[136:139], v59, s[72:73] offset:-2048 sc1\nv_and_b32 v221, 63, v0\nv_lshl_add_u32 v18, v221, 4, s42\ns_mov_b32 s41, 0x1f600\n"
    "global_load_dwordx4 v[140:143], v59, s[72:73] offset:-1024 sc1\nv_lshl_add_u32 v19, v221, 4, s41\nv_bfe_u32 v220, v0, 3, 3\nv_and_b32 v219, 7, v0\n"
    "global_load_dwordx4 v[144:147], v59, s[72:73] offset:0 sc1\ns_lshl_b32 s40, s22, 19\ns_lshl_b32 s39, s13, 17\ns_lshl_b32 s38, s21, 7\n"
    "global_load_dwordx4 v[148:151], v59, s[72:73] offset:1024 sc1\ns_add_u32 s37, s12, s40\ns_add_u32 s37, s37, s39\ns_add_u32 s37, s37, s38\n"
    "global_load_dwordx4 v[152:155], v59, s[72:73] offset:2048 sc1\ns_add_u32 s37, s37, 0x200000\nv_lshl_add_u32 v20, v220, 13, s37\nv_lshl_add_u32 v20, v219, 4, v20\n"
    "global_load_dwordx4 v[156:159], v59, s[72:73] offset:3072 sc1\ns_add_u32 s36, s12, s40\ns_add_u32 s36, s36, s39\ns_add_u32 s36, s36, s38\ns_add_u32 s36, s36, 0x210000\n"
    "global_load_dwordx4 v[160:163], v59, s[74:75] offset:-4096 sc1\nv_lshl_add_u32 v21, v220, 13, s36\nv_lshl_add_u32 v21, v219, 4, v21\ns_lshl_b32 s35, s13, 8\n"
    "global_load_dwordx4 v[164:167], v59, s[74:75] offset:-3072 sc1\ns_add_u32 s34, s35, 0x27600\nv_lshl_add_u32 v22, v221, 2, s34\ns_lshl_b32 s43, s13, 2\n"
    "global_load_dwordx4 v[168:171], v59, s[74:75] offset:-2048 sc1\ns_add_u32 s42, s43, 0x27ff0\nv_mov_b32 v23, s42\ns_lshr_b32 s41, s12, 23\n"
    "global_load_dwordx4 v[172:175], v59, s[74:75] offset:-1024 sc1\ns_add_u32 s40, s41, 2\nv_mov_b32 v24, s40\nv_bfe_u32 v223, v0, 5, 1\n"
    "global_load_dwordx4 v[176:179], v59, s[74:75] offset:0 sc1\nv_and_b32 v222, 31, v0\ns_lshl_b32 s39, s13, 11\ns_add_u32 s38, s39, 0x1d400\n"
    "global_load_dwordx4 v[180:183], v59, s[74:75] offset:1024 sc1\nv_lshl_add_u32 v26, v223, 8, s38\nv_lshl_add_u32 v26, v222, 2, v26\nv_mov_b32 v25, 0x27fb4\n"
    "global_load_dwordx4 v[184:187], v59, s[74:75] offset:2048 sc1\ns_lshl_b32 s37, s21, 15\ns_lshl_b32 s36, s22, 13\ns_add_u32 s35, s37, s36\n"
    "global_load_dwordx4 v[188:191], v59, s[74:75] offset:3072 sc1\ns_add_u32 s35, s35, s39\ns_add_u32 s35, s35, 0x600000\nv_lshl_add_u32 v16, v223, 8, s35\n"
    "v_lshl_add_u32 v16, v222, 2, v16\n"
    ".Lhwo78:\ns_mov_b32 s48, 0x3f803f80\ns_lshr_b32 s80, s12, 7\ns_waitcnt lgkmcnt(0)\ns_setprio 2\ns_cmp_eq_u32 s12, 0\ns_barrier\nds_read_b128 v[208:211], v19 offset:0\n"
    "ds_read_b128 v[212:215], v19 offset:1024\nds_read_b128 v[216:219], v19 offset:2048\nds_read_b128 v[220:223], v19 offset:3072\nds_read_b128 v[224:227], v19 offset:4096\n"
    "ds_read_b128 v[228:231], v19 offset:5120\ns_cbranch_scc1 .Lw110\nglobal_load_dword v240, v16, s[4:5] offset:0 sc1\nglobal_load_dword v241, v16, s[4:5] offset:128 sc1\n"
    "global_load_dword v242, v16, s[4:5] offset:512 sc1\nglobal_load_dword v243, v16, s[4:5] offset:640 sc1\nglobal_load_dword v244, v16, s[4:5] offset:1024 sc1\n"
    "global_load_dword v245, v16, s[4:5] offset:1152 sc1\nglobal_load_dword v246, v16, s[4:5] offset:1536 sc1\nglobal_load_dword v247, v16, s[4:5] offset:1664 sc1\n.Lw110:\n"
    "s_waitcnt lgkmcnt(5)\nv_mfma_f32_32x32x16_bf16 v[192:207], v[64:67], v[208:211], 0\nds_read_b128 v[208:211], v19 offset:6144\ns_waitcnt lgkmcnt(5)\n"
    "v_mfma_f32_32x32x16_bf16 v[192:207], v[68:71], v[212:215], v[192:207]\nds_read_b128 v[212:215], v19 offset:7168\ns_waitcnt lgkmcnt(5)\n"
    "v_mfma_f32_32x32x16_bf16 v[192:207], v[72:75], v[216:219], v[192:207]\ns_barrier\nds_read_b128 v[216:219], v19 offset:8192\ns_waitcnt lgkmcnt(5)\n"
    "v_mfma_f32_32x32x16_bf16 v[192:207], v[76:79], v[220:223], v[192:207]\nds_read_b128 v[220:223], v19 offset:9216\ns_waitcnt lgkmcnt(5)\n"
    "v_mfma_f32_32x32x16_bf16 v[192:207], v[80:83], v[224:227], v[192:207]\nds_read_b128 v[224:227], v19 offset:10240\ns_waitcnt lgkmcnt(5)\n"
    "v_mfma_f32_32x32x16_bf16 v[192:207], v[84:87], v[228:231], v[192:207]\nds_read_b128 v[228:231], v19 offset:11264\ns_waitcnt lgkmcnt(5)\n"
    "v_mfma_f32_32x32x16_bf16 v[192:207], v[88:91], v[208:211], v[192:207]\nds_read_b128 v[208:211], v19 offset:12288\ns_waitcnt lgkmcnt(5)\n"
    "v_mfma_f32_32x32x16_bf16 v[192:207], v[92:95], v[212:215], v[192:207]\nds_read_b128 v[212:215], v19 offset:13312\ns_waitcnt lgkmcnt(5)\n"
    "v_mfma_f32_32x32x16_bf16 v[192:207], v[96:99], v[216:219], v[192:207]\nds_read_b128 v[216:219], v19 offset:14336\ns_waitcnt lgkmcnt(5)\n"
    "v_mfma_f32_32x32x16_bf16 v[192:207], v[100:103], v[220:223], v[192:207]\nds_read_b128 v[220:223], v19 offset:15360\ns_waitcnt lgkmcnt(5)\n"
    "v_mfma_f32_32x32x16_bf16 v[192:207], v[104:107], v[224:227], v[192:207]\ns_barrier\nds_read_b128 v[224:227], v19 offset:16384\ns_waitcnt lgkmcnt(5)\n"
    "v_mfma_f32_32x32x16_bf16 v[192:207], v[108:111], v[228:231], v[192:207]\nds_read_b128 v[228:231], v19 offset:17408\ns_waitcnt lgkmcnt(5)\n"
    "v_mfma_f32_32x32x16_bf16 v[192:207], v[112:115], v[208:211], v[192:207]\nds_read_b128 v[208:211], v19 offset:18432\ns_waitcnt lgkmcnt(5)\n"
    "v_mfma_f32_32x32x16_bf16 v[192:207], v[116:119], v[212:215], v[192:207]\nds_read_b128 v[212:215], v19 offset:19456\ns_waitcnt lgkmcnt(5)\n"
    "v_mfma_f32_32x32x16_bf16 v[192:207], v[120:123], v[216:219], v[192:207]\nds_read_b128 v[216:219], v19 offset:20480\ns_waitcnt lgkmcnt(5)\n"
    "v_mfma_f32_32x32x16_bf16 v[192:207], v[124:127], v[220:223], v[192:207]\nds_read_b128 v[220:223], v19 offset:21504\ns_waitcnt vmcnt(8) lgkmcnt(5)\n"
    "v_mfma_f32_32x32x16_bf16 v[192:207], v[128:131], v[224:227], v[192:207]\nds_read_b128 v[224:227], v19 offset:22528\ns_waitcnt lgkmcnt(5)\n"
    "v_mfma_f32_32x32x16_bf16 v[192:207], v[132:135], v[228:231], v[192:207]\nds_read_b128 v[228:231], v19 offset:23552\ns_waitcnt lgkmcnt(5)\n"
    "v_mfma_f32_32x32x16_bf16 v[192:207], v[136:139], v[208:211], v[192:207]\nds_read_b128 v[208:211], v19 offset:24576\ns_waitcnt lgkmcnt(5)\n"
    "v_mfma_f32_32x32x16_bf16 v[192:207], v[140:143], v[212:215], v[192:207]\nds_read_b128 v[212:215], v19 offset:25600\ns_waitcnt lgkmcnt(5)\n"
    "v_mfma_f32_32x32x16_bf16 v[192:207], v[144:147], v[216:219], v[192:207]\nds_read_b128 v[216:219], v19 offset:26624\ns_waitcnt lgkmcnt(5)\n"
    "v_mfma_f32_32x32x16_bf16 v[192:207], v[148:151], v[220:223], v[192:207]\nds_read_b128 v[220:223], v19 offset:27648\ns_waitcnt lgkmcnt(5)\n"
    "v_mfma_f32_32x32x16_bf16 v[192:207], v[152:155], v[224:227], v[192:207]\nds_read_b128 v[224:227], v19 offset:28672\ns_waitcnt lgkmcnt(5)\ns_cbranch_execz .Lhop\n"
    "v_mfma_f32_32x32x16_bf16 v[192:207], v[156:159], v[228:231], v[192:207]\nds_read_b128 v[228:231], v19 offset:29696\ns_waitcnt vmcnt(0) lgkmcnt(5)\n"
    "v_mfma_f32_32x32x16_bf16 v[192:207], v[160:163], v[208:211], v[192:207]\nds_read_b128 v[208:211], v19 offset:30720\ns_waitcnt lgkmcnt(5)\n"
    "v_mfma_f32_32x32x16_bf16 v[192:207], v[164:167], v[212:215], v[192:207]\nds_read_b128 v[212:215], v19 offset:31744\nv_max3_u32 v232, v240, v241, v242\n"
    "v_max3_u32 v233, v243, v244, v245\nv_max3_u32 v232, v232, v246, v247\ns_waitcnt lgkmcnt(5)\nv_mfma_f32_32x32x16_bf16 v[192:207], v[168:171], v[216:219], v[192:207]\n"
    "v_max_u32 v232, v232, v233\nv_cmp_eq_u32 vcc, -1, v232\ns_waitcnt lgkmcnt(4)\nv_mfma_f32_32x32x16_bf16 v[192:207], v[172:175], v[220:223], v[192:207]\n"
    "s_cbranch_vccnz .Lw111\n.Lw112:\nv_lshlrev_b32 v40, 16, v240\nv_and_b32 v41, 0xffff0000, v240\ns_waitcnt lgkmcnt(3)\n"
    "v_mfma_f32_32x32x16_bf16 v[192:207], v[176:179], v[224:227], v[192:207]\nv_lshlrev_b32 v42, 16, v241\nv_and_b32 v43, 0xffff0000, v241\nv_lshlrev_b32 v44, 16, v242\n"
    "v_and_b32 v45, 0xffff0000, v242\ns_waitcnt lgkmcnt(2)\nv_mfma_f32_32x32x16_bf16 v[192:207], v[180:183], v[228:231], v[192:207]\nv_lshlrev_b32 v46, 16, v243\n"
    "v_and_b32 v47, 0xffff0000, v243\nv_lshlrev_b32 v48, 16, v244\nv_and_b32 v49, 0xffff0000, v244\ns_waitcnt lgkmcnt(1)\n"
    "v_mfma_f32_32x32x16_bf16 v[192:207], v[184:187], v[208:211], v[192:207]\nv_lshlrev_b32 v50, 16, v245\nv_and_b32 v51, 0xffff0000, v245\nv_lshlrev_b32 v52, 16, v246\n"
    "v_and_b32 v53, 0xffff0000, v246\ns_waitcnt lgkmcnt(0)\nv_mfma_f32_32x32x16_bf16 v[192:207], v[188:191], v[212:215], v[192:207]\nv_lshlrev_b32 v54, 16, v247\n"
    "v_and_b32 v55, 0xffff0000, v247\ns_nop 9\nv_pk_add_f32 v[192:193], v[192:193], v[40:41]\nv_pk_add_f32 v[194:195], v[194:195], v[42:43]\n"
    "v_pk_add_f32 v[196:197], v[196:197], v[44:45]\nv_pk_add_f32 v[198:199], v[198:199], v[46:47]\nv_pk_add_f32 v[200:201], v[200:201], v[48:49]\n"
    "v_pk_add_f32 v[202:203], v[202:203], v[50:51]\nv_pk_add_f32 v[204:205], v[204:205], v[52:53]\nv_pk_add_f32 v[206:207], v[206:207], v[54:55]\n"
    "v_cvt_pk_bf16_f32 v208, v192, v193\nv_cvt_pk_bf16_f32 v209, v194, v195\nv_cvt_pk_bf16_f32 v210, v196, v197\nv_cvt_pk_bf16_f32 v211, v198, v199\n"
    "v_cvt_pk_bf16_f32 v212, v200, v201\nv_cvt_pk_bf16_f32 v213, v202, v203\nv_cvt_pk_bf16_f32 v214, v204, v205\nv_cvt_pk_bf16_f32 v215, v206, v207\n"
    "v_dot2_f32_bf16 v236, v208, s48, 0\nv_dot2_f32_bf16 v237, v208, v208, 0\nv_dot2_f32_bf16 v238, v209, s48, 0\nv_dot2_f32_bf16 v239, v209, v209, 0\n"
    "v_dot2_f32_bf16 v236, v210, s48, v236\nv_dot2_f32_bf16 v237, v210, v210, v237\nv_dot2_f32_bf16 v238, v211, s48, v238\nv_dot2_f32_bf16 v239, v211, v211, v239\n"
    "v_dot2_f32_bf16 v236, v212, s48, v236\nv_dot2_f32_bf16 v237, v212, v212, v237\nv_dot2_f32_bf16 v238, v213, s48, v238\nv_dot2_f32_bf16 v239, v213, v213, v239\n"
    "v_dot2_f32_bf16 v236, v214, s48, v236\nv_dot2_f32_bf16 v237, v214, v214, v237\nv_dot2_f32_bf16 v238, v215, s48, v238\nv_dot2_f32_bf16 v239, v215, v215, v239\n"
    "ds_write2_b32 v17, v208, v209 offset1:32\nds_write2_b32 v17, v210, v211 offset0:128 offset1:160\nds_write2_b32 v26, v212, v213 offset1:32\n"
    "v_pk_add_f32 v[236:237], v[236:237], v[238:239]\nds_write2_b32 v26, v214, v215 offset0:128 offset1:160\nds_read_b128 v[216:219], v18\nv_permlane32_swap_b32 v236, v237\n"
    "v_add_f32 v236, v236, v237\nds_write_b32 v22, v236\ns_mov_b64 exec, 1\nds_write_b32 v23, v24\ns_mov_b64 exec, -1\nds_read_b128 v[220:223], v18 offset:1024\n"
    "s_waitcnt lgkmcnt(3)\nglobal_store_dwordx4 v20, v[216:219], s[4:5] sc1\ns_waitcnt lgkmcnt(0)\nglobal_store_dwordx4 v21, v[220:223], s[4:5] sc1\ns_setprio 0\n"
    "s_add_u32 s43, s12, 0x400000\nv_lshl_add_u32 v16, v0, 4, s43\ns_lshl_b32 s42, s18, 13\ns_add_u32 s41, s12, s42\ns_add_u32 s41, s41, 0x200000\n"
    "v_lshl_add_u32 v17, v0, 4, s41\nv_lshrrev_b32 v239, 3, v0\nv_mul_u32_u24 v238, 0x50, v239\nv_and_b32 v237, 7, v0\nv_add_u32 v18, 0x1d550, v238\n"
    "v_lshl_add_u32 v18, v237, 3, v18\nv_bfe_u32 v236, v0, 4, 3\nv_xor_b32 v235, v0, v236\ns_mov_b32 s40, 0x19400\nv_lshl_add_u32 v19, v235, 3, s40\nv_bfe_u32 v234, v0, 4, 1\n"
    "v_xor_b32 v233, v0, v234\ns_mov_b32 s39, 0x17c00\nv_lshl_add_u32 v20, v233, 3, s39\ns_lshl_b32 s38, s18, 7\ns_add_u32 s37, s38, 0x600000\n"
    "v_lshl_add_u32 v232, v239, 15, s37\nv_lshl_add_u32 v232, v237, 4, v232\nv_lshrrev_b32 v231, 4, v0\nv_and_b32 v230, 15, v0\ns_lshl_b32 s36, s24, 18\n"
    "s_lshl_b32 s35, s25, 8\ns_add_u32 s34, s36, s35\ns_add_u32 s34, s34, 0x1200000\nv_lshl_add_u32 v229, v231, 13, s34\nv_lshl_add_u32 v229, v230, 4, v229\n"
    "s_cmp_lt_u32 s12, 1\ns_cselect_b64 vcc, -1, 0\nv_cndmask_b32 v21, v229, v232, vcc\nv_lshrrev_b32 v239, 8, v0\nv_mul_u32_u24 v238, 0x1950, v239\nv_and_b32 v237, 0xff, v0\n"
    "v_add_u32 v236, 0x1d000, v238\nv_lshl_add_u32 v236, v237, 4, v236\nv_mul_u32_u24 v235, 0x3180, v239\ns_mov_b32 s43, 0xfffffab0\nv_lshl_add_u32 v234, v237, 4, s43\n"
    "s_mov_b32 s42, 0x810\nv_lshl_add_u32 v233, v237, 4, s42\nv_mov_b32 v232, 0x1f80\nv_cmp_gt_u32 vcc, 0xa1, v237\ns_nop 1\nv_cndmask_b32 v231, v232, v233, vcc\n"
    "v_cmp_gt_u32 vcc, 0x7b, v237\ns_nop 1\nv_cndmask_b32 v230, v231, v234, vcc\nv_add_u32 v229, 0x20300, v235\nv_add_u32 v229, v229, v230\nv_cmp_gt_u32 vcc, 0x55, v237\n"
    "s_nop 1\nv_cndmask_b32 v22, v229, v236, vcc\nv_and_b32 v228, 31, v0\nv_sub_u32 v227, 0x3ff, v228\nv_bfe_u32 v226, v0, 5, 1\nv_lshl_add_u32 v225, v226, 3, v227\n"
    "s_cbranch_execz .Lhop\nv_and_b32 v224, 3, v225\nv_mul_u32_u24 v223, 0x840, v224\nv_lshrrev_b32 v222, 2, v225\ns_lshr_b32 s41, s13, 1\ns_mul_i32 s40, s41, 0x2100\n"
    "s_add_u32 s39, s40, 0x17800\nv_add_u32 v23, s39, v223\nv_lshl_add_u32 v23, v222, 3, v23\ns_lshl_b32 s38, s13, 2\ns_add_u32 s37, s38, 0x27f80\nv_mov_b32 v25, s37\n"
    "s_lshr_b32 s36, s12, 23\ns_add_u32 s35, s36, 4\nv_mov_b32 v26, s35\ns_and_b32 s34, s13, 1\nv_and_b32 v221, 1, v0\ns_mov_b32 s33, 0x50\nv_lshl_add_u32 v220, v221, 2, s33\n"
    "v_mov_b32 v219, 48\ns_cmp_lt_u32 s34, 1\ns_cselect_b64 vcc, -1, 0\nv_cndmask_b32 v218, v219, v220, vcc\ns_lshl_b32 s32, s41, 3\ns_add_u32 s31, s32, 0x27f80\n"
    "v_add_u32 v24, s31, v218\nv_add_u32 v239, 0xffffff86, v0\nv_lshrrev_b32 v238, 3, v239\nv_mul_u32_u24 v237, 0x50, v238\nv_and_b32 v236, 7, v239\n"
    "v_add_u32 v30, 0x1d004, v237\nv_lshl_add_u32 v30, v236, 3, v30\nv_add_u32 v235, 0xffffff87, v0\nv_lshrrev_b32 v234, 3, v235\nv_mul_u32_u24 v233, 0x50, v234\n"
    "v_and_b32 v232, 7, v235\nv_add_u32 v31, 0x1d000, v233\nv_lshl_add_u32 v31, v232, 3, v31\ns_and_b32 s43, s13, 1\nv_bfe_u32 v231, v0, 4, 1\n"
    "v_mul_u32_u24 v230, 0x480, v231\nv_mov_b32 v229, 0\ns_cmp_lt_u32 s43, 1\ns_cselect_b64 vcc, -1, 0\nv_cndmask_b32 v228, v229, v230, vcc\nv_bfe_u32 v227, v0, 1, 1\n"
    "v_mul_u32_u24 v226, 0x240, v227\nv_bfe_u32 v225, v0, 5, 1\nv_bfe_u32 v224, v0, 2, 2\nv_and_b32 v223, 1, v0\ns_lshr_b32 s42, s13, 1\ns_mul_i32 s41, s42, 0x3180\n"
    "s_add_u32 s40, s41, 0x20300\nv_add_u32 v27, s40, v228\nv_add_u32 v27, v27, v226\nv_lshl_add_u32 v27, v225, 7, v27\nv_lshl_add_u32 v27, v224, 4, v27\n"
    "v_lshl_add_u32 v27, v223, 3, v27\nv_and_b32 v222, 3, v0\ns_add_u32 s39, s41, 0x22280\nv_lshl_add_u32 v29, v225, 8, s39\nv_lshl_add_u32 v29, v224, 5, v29\n"
    "v_lshl_add_u32 v29, v222, 3, v29\nv_add_u32 v28, s39, v230\nv_lshl_add_u32 v28, v225, 8, v28\nv_lshl_add_u32 v28, v224, 5, v28\nv_lshl_add_u32 v28, v222, 3, v28\n"
    "v_and_b32 v221, 31, v0\nv_bfe_u32 v220, v0, 3, 1\nv_xor_b32 v219, v225, v220\ns_mul_i32 s38, s42, 0x2800\ns_add_u32 s37, s38, 0x18000\nv_lshl_add_u32 v32, v221, 4, s37\n"
    "v_lshl_add_u32 v32, v219, 3, v32\nv_xor_b32 v218, v225, v224\ns_add_u32 s36, s38, 0x18c00\nv_lshl_add_u32 v33, v221, 5, s36\nv_lshl_add_u32 v33, v218, 3, v33\n"
    "v_bfe_u32 v217, v0, 4, 2\nv_xor_b32 v216, v0, v217\ns_mov_b32 s35, 0x18400\nv_lshl_add_u32 v34, v216, 3, s35\ns_add_u32 s54, s4, 2097152\ns_addc_u32 s55, s5, 0\n"
    "s_add_u32 s56, s4, 10485760\ns_addc_u32 s57, s5, 0\nv_mov_b64 v[252:253], -1\nv_mov_b64 v[254:255], -1\nv_xor_b32 v36, 16, v33\ns_bitcmp0_b32 s13, 0\n"
    "s_cselect_b32 s44, 256, 0\nv_add_u32 v232, s44, v23\nds_read_b64 v[128:129], v23 offset:896\nds_read_b64 v[130:131], v23 offset:904\n"
    "ds_read_b64 v[132:133], v23 offset:864\nds_read_b64 v[134:135], v23 offset:872\nds_read_b64 v[136:137], v23 offset:832\nds_read_b64 v[138:139], v23 offset:840\n"
    "ds_read_b64 v[140:141], v23 offset:800\nds_read_b64 v[142:143], v23 offset:808\nds_read_b64 v[144:145], v23 offset:704\nds_read_b64 v[146:147], v23 offset:712\n"
    "ds_read_b64 v[148:149], v23 offset:672\nds_read_b64 v[150:151], v23 offset:680\nds_read_b64 v[152:153], v23 offset:640\nds_read_b64 v[154:155], v23 offset:648\n"
    "ds_read_b64 v[156:157], v23 offset:608\nds_read_b64 v[158:159], v23 offset:616\nds_read_b64 v[160:161], v232 offset:512\nds_read_b64 v[162:163], v232 offset:520\n"
    "ds_read_b64 v[164:165], v232 offset:480\nds_read_b64 v[166:167], v232 offset:488\nds_read_b64 v[168:169], v232 offset:448\nds_read_b64 v[170:171], v232 offset:456\n"
    "ds_read_b64 v[172:173], v232 offset:416\nds_read_b64 v[174:175], v232 offset:424\nds_read_b64 v[176:177], v232 offset:320\nds_read_b64 v[178:179], v232 offset:328\n"
    "ds_read_b64 v[180:181], v232 offset:288\nds_read_b64 v[182:183], v232 offset:296\nds_read_b64 v[184:185], v232 offset:256\nds_read_b64 v[186:187], v232 offset:264\n"
    "ds_read_b64 v[188:189], v232 offset:224\nds_read_b64 v[190:191], v232 offset:232\ns_waitcnt vmcnt(0)\ns_sleep 5\n"
    ".Lln79:\ns_mov_b64 s[68:69], s[4:5]\ns_add_u32 s70, s4, 8192\ns_addc_u32 s71, s5, 0\ns_add_u32 s72, s4, 16384\ns_addc_u32 s73, s5, 0\ns_add_u32 s74, s4, 24576\n"
    "s_addc_u32 s75, s5, 0\ns_cmp_lg_u32 s12, 0\ns_waitcnt lgkmcnt(0)\ns_barrier\nglobal_load_dwordx4 v[240:243], v17, s[4:5] sc1\n"
    "global_load_dwordx4 v[208:211], v16, s[68:69] sc1\nglobal_load_dwordx4 v[212:215], v16, s[70:71] sc1\nglobal_load_dwordx4 v[216:219], v16, s[72:73] sc1\n"
    "global_load_dwordx4 v[220:223], v16, s[74:75] sc1\nv_mov_b64 v[236:237], 0\nv_mov_b64 v[238:239], 0\ns_cbranch_scc1 .Ln116\ns_load_dword s17, s[16:17], 0\n"
    "v_mov_b32 v232, 115200\nds_read_u16 v233, v232 offset:1024\nds_read_u16 v234, v232 offset:2560\nds_read_b32 v235, v232 offset:48032\nds_read_b32 v232, v232 offset:48040\n"
    "s_waitcnt lgkmcnt(0)\nv_fma_mix_f32 v233, v233, -v235, 0 op_sel_hi:[1,0,0]\nv_fma_mix_f32 v234, v234, -v232, 0 op_sel_hi:[1,0,0]\ns_lshl_b32 s16, s17, 16\n"
    "s_and_b32 s17, s17, 0xffff0000\nv_readfirstlane_b32 s14, v235\nv_readfirstlane_b32 s15, v232\nv_readfirstlane_b32 s28, v233\nv_readfirstlane_b32 s29, v234\n"
    "s_cbranch_execz .Lhop\ns_lshr_b32 s26, s14, 16\ns_lshr_b32 s27, s15, 16\n.Ln116:\nds_write_b128 v22, v[236:239]\ns_mov_b32 s44, 0x3f80\ns_mov_b32 s46, 0x3e800000\n"
    "s_mov_b32 s48, 0x3727c5ac\ns_mov_b32 s80, 0x10000\n.Ln118:\ns_waitcnt vmcnt(4)\nv_lshlrev_b32 v56, 16, v240\nv_lshlrev_b32 v57, 16, v241\nv_lshlrev_b32 v58, 16, v242\n"
    "v_lshlrev_b32 v59, 16, v243\nv_and_b32 v60, 0xffff0000, v240\nv_and_b32 v61, 0xffff0000, v241\nv_and_b32 v62, 0xffff0000, v242\nv_and_b32 v63, 0xffff0000, v243\n"
    "v_max3_u32 v232, v240, v241, v242\nv_max_u32 v232, v232, v243\nv_cmp_eq_u32 s[50:51], -1, v232\ns_waitcnt vmcnt(3)\nv_dot2_f32_bf16 v224, v208, s44, 0\n"
    "v_dot2_f32_bf16 v228, v208, v208, 0\nv_dot2_f32_bf16 v225, v209, s44, 0\nv_dot2_f32_bf16 v229, v209, v209, 0\nv_dot2_f32_bf16 v226, v210, s44, 0\n"
    "v_dot2_f32_bf16 v230, v210, v210, 0\nv_dot2_f32_bf16 v227, v211, s44, 0\nv_dot2_f32_bf16 v231, v211, v211, 0\ns_waitcnt vmcnt(2)\nv_dot2_f32_bf16 v224, v212, s44, v224\n"
    "v_dot2_f32_bf16 v228, v212, v212, v228\nv_dot2_f32_bf16 v225, v213, s44, v225\nv_dot2_f32_bf16 v229, v213, v213, v229\nv_dot2_f32_bf16 v226, v214, s44, v226\n"
    "v_dot2_f32_bf16 v230, v214, v214, v230\nv_dot2_f32_bf16 v227, v215, s44, v227\nv_dot2_f32_bf16 v231, v215, v215, v231\ns_waitcnt vmcnt(1)\n"
    "v_dot2_f32_bf16 v224, v216, s44, v224\nv_dot2_f32_bf16 v228, v216, v216, v228\nv_dot2_f32_bf16 v225, v217, s44, v225\nv_dot2_f32_bf16 v229, v217, v217, v229\n"
    "v_dot2_f32_bf16 v226, v218, s44, v226\nv_dot2_f32_bf16 v230, v218, v218, v230\nv_dot2_f32_bf16 v227, v219, s44, v227\nv_dot2_f32_bf16 v231, v219, v219, v231\n"
    "s_waitcnt vmcnt(0)\nv_dot2_f32_bf16 v224, v220, s44, v224\nv_dot2_f32_bf16 v228, v220, v220, v228\nv_dot2_f32_bf16 v225, v221, s44, v225\n"
    "v_dot2_f32_bf16 v229, v221, v221, v229\nv_dot2_f32_bf16 v226, v222, s44, v226\nv_dot2_f32_bf16 v230, v222, v222, v230\nv_dot2_f32_bf16 v227, v223, s44, v227\n"
    "v_dot2_f32_bf16 v231, v223, v223, v231\nv_pk_mul_f32 v[224:225], v[224:225], s[46:47] op_sel_hi:[1,0]\ns_nop 0\n"
    "v_pk_mul_f32 v[226:227], v[226:227], s[46:47] op_sel_hi:[1,0]\nv_cmp_u_f32 vcc, v228, v229\nv_cmp_u_f32 s[52:53], v230, v231\n"
    "v_pk_fma_f32 v[232:233], v[224:225], v[224:225], s[48:49] op_sel_hi:[1,1,0] neg_lo:[0,0,1] neg_hi:[0,0,1]\n"
    "v_pk_fma_f32 v[234:235], v[226:227], v[226:227], s[48:49] op_sel_hi:[1,1,0] neg_lo:[0,0,1] neg_hi:[0,0,1]\n"
    "v_pk_fma_f32 v[228:229], v[228:229], s[46:47], v[232:233] op_sel_hi:[1,0,1] neg_lo:[0,0,1] neg_hi:[0,0,1]\n"
    "v_pk_fma_f32 v[230:231], v[230:231], s[46:47], v[234:235] op_sel_hi:[1,0,1] neg_lo:[0,0,1] neg_hi:[0,0,1]\nv_max_f32 v228, s48, v228\nv_max_f32 v229, s48, v229\n"
    "v_max_f32 v230, s48, v230\nv_max_f32 v231, s48, v231\nv_rsq_f32 v228, v228\nv_rsq_f32 v229, v229\nv_rsq_f32 v230, v230\nv_rsq_f32 v231, v231\n"
    "v_pk_add_f32 v[244:245], v[56:57], v[224:225] neg_lo:[0,1] neg_hi:[0,1]\nv_pk_add_f32 v[246:247], v[58:59], v[226:227] neg_lo:[0,1] neg_hi:[0,1]\n"
    "v_pk_add_f32 v[248:249], v[60:61], v[224:225] neg_lo:[0,1] neg_hi:[0,1]\nv_pk_add_f32 v[250:251], v[62:63], v[226:227] neg_lo:[0,1] neg_hi:[0,1]\n"
    "s_or_b64 vcc, vcc, s[50:51]\nv_pk_mul_f32 v[244:245], v[244:245], v[228:229]\nv_pk_mul_f32 v[246:247], v[246:247], v[230:231]\n"
    "v_pk_mul_f32 v[248:249], v[248:249], v[228:229]\nv_pk_mul_f32 v[250:251], v[250:251], v[230:231]\ns_or_b64 vcc, vcc, s[52:53]\nv_cvt_pk_f16_f32 v232, v244, v245\n"
    "v_cvt_pk_f16_f32 v233, v246, v247\nv_cvt_pk_f16_f32 v234, v248, v249\nds_write_b64 v18, v[232:233]\nv_cvt_pk_f16_f32 v235, v250, v251\n"
    "ds_write_b64 v18, v[234:235] offset:6480\ns_cbranch_scc1 .Llw121\n.Llx122:\ns_cmp_gt_u32 s3, 3\ns_cbranch_scc1 .Lhcv119\n"
    "s_waitcnt lgkmcnt(0)\ns_barrier\nds_read_b128 v[220:223], v27 offset:1392\nds_read_b128 v[224:227], v27 offset:1360\nds_read_b128 v[228:231], v27 offset:1312\n"
    "v_fmac_f32 v56, s16, v244\nv_fmac_f32 v57, s16, v245\nv_fmac_f32 v58, s16, v246\nv_fmac_f32 v59, s16, v247\nv_fmac_f32 v60, s17, v248\nv_fmac_f32 v61, s17, v249\n"
    "v_fmac_f32 v62, s17, v250\nv_fmac_f32 v63, s17, v251\ns_waitcnt lgkmcnt(2)\nv_mfma_f32_32x32x16_f16 v[204:219], v[64:67], v[220:223], 0\n"
    "ds_read_b128 v[232:235], v27 offset:1280\nds_read_b128 v[236:239], v27 offset:1232\ns_waitcnt lgkmcnt(3)\n"
    "v_mfma_f32_32x32x16_f16 v[204:219], v[68:71], v[224:227], v[204:219]\nds_read_b128 v[240:243], v27 offset:1200\nds_read_b128 v[220:223], v27 offset:1152\n"
    "s_waitcnt lgkmcnt(4)\nv_mfma_f32_32x32x16_f16 v[204:219], v[72:75], v[228:231], v[204:219]\nds_read_b128 v[224:227], v27 offset:1120\n"
    "ds_read_b128 v[228:231], v27 offset:1072\ns_waitcnt lgkmcnt(5)\nv_mfma_f32_32x32x16_f16 v[204:219], v[76:79], v[232:235], v[204:219]\n"
    "ds_read_b128 v[232:235], v27 offset:1040\ns_waitcnt lgkmcnt(5)\nv_mfma_f32_32x32x16_f16 v[204:219], v[80:83], v[236:239], v[204:219]\n"
    "ds_read_b128 v[236:239], v27 offset:992\ns_waitcnt lgkmcnt(5)\nv_mfma_f32_32x32x16_f16 v[204:219], v[84:87], v[240:243], v[204:219]\n"
    "ds_read_b128 v[240:243], v27 offset:960\ns_waitcnt lgkmcnt(5)\nv_mfma_f32_32x32x16_f16 v[204:219], v[88:91], v[220:223], v[204:219]\n"
    "ds_read_b128 v[220:223], v27 offset:912\ns_waitcnt lgkmcnt(5)\nv_mfma_f32_32x32x16_f16 v[204:219], v[92:95], v[224:227], v[204:219]\n"
    "ds_read_b128 v[224:227], v27 offset:880\ns_waitcnt lgkmcnt(5)\nv_mfma_f32_32x32x16_f16 v[204:219], v[96:99], v[228:231], v[204:219]\n"
    "ds_read_b128 v[228:231], v27 offset:832\ns_waitcnt lgkmcnt(5)\nv_mfma_f32_32x32x16_f16 v[204:219], v[100:103], v[232:235], v[204:219]\n"
    "ds_read_b128 v[232:235], v27 offset:800\ns_waitcnt lgkmcnt(5)\nv_mfma_f32_32x32x16_f16 v[204:219], v[104:107], v[236:239], v[204:219]\n"
    "ds_read_b128 v[236:239], v27 offset:752\ns_waitcnt lgkmcnt(5)\nv_mfma_f32_32x32x16_f16 v[204:219], v[108:111], v[240:243], v[204:219]\n"
    "ds_read_b128 v[240:243], v27 offset:720\ns_waitcnt lgkmcnt(5)\nv_mfma_f32_32x32x16_f16 v[204:219], v[112:115], v[220:223], v[204:219]\n"
    "ds_read_b128 v[220:223], v27 offset:672\ns_waitcnt lgkmcnt(5)\nv_mfma_f32_32x32x16_f16 v[204:219], v[116:119], v[224:227], v[204:219]\n"
    "ds_read_b128 v[224:227], v27 offset:640\ns_waitcnt lgkmcnt(5)\nv_mfma_f32_32x32x16_f16 v[204:219], v[120:123], v[228:231], v[204:219]\n"
    "ds_read_b128 v[228:231], v27 offset:592\ns_waitcnt lgkmcnt(5)\nv_mfma_f32_32x32x16_f16 v[204:219], v[124:127], v[232:235], v[204:219]\ns_cbranch_execz .Lhop\n"
    "ds_read_b128 v[232:235], v27 offset:560\ns_waitcnt lgkmcnt(5)\nv_mfma_f32_32x32x16_f16 v[204:219], v[128:131], v[236:239], v[204:219]\n"
    "ds_read_b128 v[236:239], v27 offset:512\ns_waitcnt lgkmcnt(5)\nv_mfma_f32_32x32x16_f16 v[204:219], v[132:135], v[240:243], v[204:219]\n"
    "ds_read_b128 v[240:243], v27 offset:480\ns_waitcnt lgkmcnt(5)\nv_mfma_f32_32x32x16_f16 v[204:219], v[136:139], v[220:223], v[204:219]\n"
    "ds_read_b128 v[220:223], v27 offset:432\ns_waitcnt lgkmcnt(5)\nv_mfma_f32_32x32x16_f16 v[204:219], v[140:143], v[224:227], v[204:219]\n"
    "ds_read_b128 v[224:227], v27 offset:400\ns_waitcnt lgkmcnt(5)\nv_mfma_f32_32x32x16_f16 v[204:219], v[144:147], v[228:231], v[204:219]\n"
    "ds_read_b128 v[228:231], v27 offset:352\ns_waitcnt lgkmcnt(5)\nv_mfma_f32_32x32x16_f16 v[204:219], v[148:151], v[232:235], v[204:219]\n"
    "ds_read_b128 v[232:235], v27 offset:320\ns_waitcnt lgkmcnt(5)\nv_mfma_f32_32x32x16_f16 v[204:219], v[152:155], v[236:239], v[204:219]\n"
    "ds_read_b128 v[236:239], v27 offset:272\ns_waitcnt lgkmcnt(5)\nv_mfma_f32_32x32x16_f16 v[204:219], v[156:159], v[240:243], v[204:219]\n"
    "ds_read_b128 v[240:243], v27 offset:240\ns_waitcnt lgkmcnt(5)\nv_mfma_f32_32x32x16_f16 v[204:219], v[160:163], v[220:223], v[204:219]\n"
    "ds_read_b128 v[220:223], v27 offset:192\ns_waitcnt lgkmcnt(5)\nv_mfma_f32_32x32x16_f16 v[204:219], v[164:167], v[224:227], v[204:219]\n"
    "ds_read_b128 v[224:227], v27 offset:160\ns_waitcnt lgkmcnt(5)\nv_mfma_f32_32x32x16_f16 v[204:219], v[168:171], v[228:231], v[204:219]\n"
    "ds_read_b128 v[228:231], v27 offset:112\ns_waitcnt lgkmcnt(5)\nv_mfma_f32_32x32x16_f16 v[204:219], v[172:175], v[232:235], v[204:219]\n"
    "ds_read_b128 v[232:235], v27 offset:80\ns_waitcnt lgkmcnt(5)\nv_mfma_f32_32x32x16_f16 v[204:219], v[176:179], v[236:239], v[204:219]\n"
    "ds_read_b128 v[236:239], v27 offset:32\ns_waitcnt lgkmcnt(5)\nv_mfma_f32_32x32x16_f16 v[204:219], v[180:183], v[240:243], v[204:219]\ns_waitcnt lgkmcnt(4)\n"
    "v_mfma_f32_32x32x16_f16 v[204:219], v[184:187], v[220:223], v[204:219]\ns_waitcnt lgkmcnt(3)\nv_mfma_f32_32x32x16_f16 v[204:219], v[188:191], v[224:227], v[204:219]\n"
    "s_waitcnt lgkmcnt(2)\nv_mfma_f32_32x32x16_f16 v[204:219], v[192:195], v[228:231], v[204:219]\ns_waitcnt lgkmcnt(1)\n"
    "v_mfma_f32_32x32x16_f16 v[204:219], v[196:199], v[232:235], v[204:219]\ns_waitcnt lgkmcnt(0)\nv_mfma_f32_32x32x16_f16 v[204:219], v[200:203], v[236:239], v[204:219]\n"
    "s_cmp_lt_u32 s3, 2\ns_nop 10\nv_cvt_pk_bf16_f32 v204, v204, v205\nv_cvt_pk_bf16_f32 v205, v206, v207\nds_write_b64 v28, v[204:205] offset:0\n"
    "v_cvt_pk_bf16_f32 v208, v208, v209\nv_cvt_pk_bf16_f32 v209, v210, v211\nds_write_b64 v29, v[208:209] offset:0\nv_cvt_pk_bf16_f32 v212, v212, v213\n"
    "v_cvt_pk_bf16_f32 v213, v214, v215\nds_write_b64 v30, v[212:213] offset:0\nv_cvt_pk_bf16_f32 v216, v216, v217\nv_cvt_pk_bf16_f32 v217, v218, v219\n"
    "ds_write_b64 v31, v[216:217] offset:0\ns_waitcnt lgkmcnt(0)\ns_barrier\nds_read_b64 v[208:209], v19\nds_read_b64 v[210:211], v19 offset:10240\ns_cbranch_scc1 .Le123\n"
    "ds_read_b64 v[212:213], v20\nds_read_b64 v[214:215], v20 offset:10240\ns_waitcnt lgkmcnt(0)\nv_dot2c_f32_bf16 v56, s26, v212\nv_dot2c_f32_bf16 v57, s14, v212\n"
    "v_dot2c_f32_bf16 v58, s26, v213\nv_dot2c_f32_bf16 v59, s14, v213\nv_dot2c_f32_bf16 v60, s27, v214\nv_dot2c_f32_bf16 v61, s15, v214\nv_dot2c_f32_bf16 v62, s27, v215\n"
    "v_dot2c_f32_bf16 v63, s15, v215\n.Le123:\ns_waitcnt lgkmcnt(0)\nv_dot2c_f32_bf16 v56, s26, v208\nv_dot2c_f32_bf16 v57, s14, v208\nv_dot2c_f32_bf16 v58, s26, v209\n"
    "v_dot2c_f32_bf16 v59, s14, v209\nv_dot2c_f32_bf16 v60, s27, v210\nv_dot2c_f32_bf16 v61, s15, v210\nv_dot2c_f32_bf16 v62, s27, v211\nv_dot2c_f32_bf16 v63, s15, v211\n"
    "v_cvt_pk_bf16_f32 v40, v56, v60\nv_cvt_pk_bf16_f32 v41, v57, v61\nv_cvt_pk_bf16_f32 v42, v58, v62\nv_cvt_pk_bf16_f32 v43, v59, v63\n"
    "global_store_dwordx4 v21, v[40:43], s[4:5] sc1\ns_cmp_eq_u32 s12, 0\ns_cbranch_scc1 .Lqg125\n"
    ".Lfin124:\nv_and_b32 v239, 7, v0\nv_lshrrev_b32 v238, 3, v0\ns_lshl_b32 s43, s18, 13\ns_add_u32 s42, s43, 0x1200000\nv_lshl_add_u32 v16, v239, 10, s42\n"
    "v_lshl_add_u32 v16, v238, 2, v16\ns_lshl_b32 s41, s25, 16\ns_lshl_b32 s40, s24, 7\ns_add_u32 s39, s41, s40\nv_lshl_add_u32 v17, v238, 10, s39\n"
    "v_lshl_add_u32 v17, v239, 4, v17\ns_mov_b32 s80, 0x10000\ns_waitcnt vmcnt(0)\ns_sleep 12\n.Lf127:\nglobal_load_dword v44, v16, s[4:5] offset:0 sc1\n"
    "global_load_dword v45, v16, s[4:5] offset:256 sc1\nglobal_load_dword v46, v16, s[4:5] offset:512 sc1\nglobal_load_dword v47, v16, s[4:5] offset:768 sc1\n"
    "s_waitcnt vmcnt(0)\nv_max3_u32 v48, v44, v45, v46\nv_max_u32 v48, v48, v47\nv_cmp_eq_u32 vcc, -1, v48\ns_cbranch_vccnz .Lf128\n.Lf128x:\n"
    "global_store_dwordx4 v17, v[44:47], s[6:7] sc1\ns_endpgm\n.Lf128:\ns_sub_u32 s80, s80, 1\ns_cbranch_scc0 .Lf127\ns_branch .Lf128x\n"
    ".Lhcv119:\ns_setprio 0\ns_bitcmp1_b32 s13, 0\ns_cbranch_scc1 .Lk130\ns_cmp_lg_u32 s12, 0\ns_waitcnt lgkmcnt(0)\ns_barrier\nds_read_b64 v[192:193], v11 offset:0\n"
    "ds_read_b128 v[194:197], v10 offset:0\nds_read_b64 v[198:199], v12 offset:0\nds_read_b64 v[200:201], v11 offset:1280\nds_read_b128 v[202:205], v10 offset:1280\n"
    "ds_read_b64 v[206:207], v12 offset:1280\nds_read_b64 v[208:209], v11 offset:2560\nds_read_b128 v[210:213], v10 offset:2560\nds_read_b64 v[214:215], v12 offset:2560\n"
    "s_cbranch_scc1 .Lz133\nglobal_store_dwordx4 v37, v[252:255], s[4:5] offset:0 sc1\nglobal_store_dwordx4 v37, v[252:255], s[4:5] offset:1024 sc1\n"
    "global_store_dwordx4 v37, v[252:255], s[54:55] offset:0 sc1\nglobal_store_dwordx4 v37, v[252:255], s[54:55] offset:1024 sc1\n"
    "global_store_dwordx4 v37, v[252:255], s[56:57] offset:0 sc1\nglobal_store_dwordx4 v37, v[252:255], s[56:57] offset:1024 sc1\ns_cmp_lg_u32 s13, 2\ns_cbranch_scc1 .Lz133\n"
    "global_store_dwordx4 v38, v[252:255], s[4:5] sc1\n.Lz133:\ns_waitcnt lgkmcnt(6)\nv_alignbit_b32 v216, v194, v193, 16\nv_alignbit_b32 v217, v195, v194, 16\n"
    "v_alignbit_b32 v218, v196, v195, 16\nv_alignbit_b32 v219, v197, v196, 16\nv_alignbit_b32 v220, v198, v197, 16\nv_pk_add_f16 v224, v193, v216\n"
    "v_pk_add_f16 v225, v194, v217\nv_pk_add_f16 v226, v195, v218\nv_pk_add_f16 v227, v196, v219\nv_pk_add_f16 v228, v197, v220\ns_nop 0\ns_cbranch_execz .Lhop\n"
    "v_pk_add_f16 v236, v216, v225\nv_pk_add_f16 v237, v217, v226\nv_pk_add_f16 v238, v218, v227\nv_pk_add_f16 v239, v219, v228\nds_write_b64 v13, v[236:237] offset:4040\n"
    "v_pk_add_f16 v232, v224, v195\nv_pk_add_f16 v233, v225, v196\nv_pk_add_f16 v234, v226, v197\nv_pk_add_f16 v235, v227, v198\nds_write_b64 v14, v[238:239] offset:4032\n"
    "v_pk_add_f16 v232, v232, v225\nv_pk_add_f16 v233, v233, v226\nv_pk_add_f16 v234, v234, v227\nv_pk_add_f16 v235, v235, v228\nds_write_b128 v13, v[232:235] offset:0\n"
    "s_mov_b64 exec, 1\nds_write_b32 v25, v26 offset:48\ns_mov_b64 exec, -1\ns_waitcnt lgkmcnt(6)\nv_alignbit_b32 v216, v202, v201, 16\nv_alignbit_b32 v217, v203, v202, 16\n"
    "v_alignbit_b32 v218, v204, v203, 16\nv_alignbit_b32 v219, v205, v204, 16\nv_alignbit_b32 v220, v206, v205, 16\nv_pk_add_f16 v224, v201, v216\n"
    "v_pk_add_f16 v225, v202, v217\nv_pk_add_f16 v226, v203, v218\nv_pk_add_f16 v227, v204, v219\nv_pk_add_f16 v228, v205, v220\nv_pk_add_f16 v236, v216, v225\n"
    "v_pk_add_f16 v237, v217, v226\nv_pk_add_f16 v238, v218, v227\nv_pk_add_f16 v239, v219, v228\nds_write_b64 v13, v[236:237] offset:5192\nv_pk_add_f16 v232, v224, v203\n"
    "v_pk_add_f16 v233, v225, v204\nv_pk_add_f16 v234, v226, v205\nv_pk_add_f16 v235, v227, v206\nds_write_b64 v14, v[238:239] offset:5184\nv_pk_add_f16 v232, v232, v225\n"
    "v_pk_add_f16 v233, v233, v226\nv_pk_add_f16 v234, v234, v227\nv_pk_add_f16 v235, v235, v228\nds_write_b128 v13, v[232:235] offset:1152\ns_waitcnt lgkmcnt(6)\n"
    "v_alignbit_b32 v216, v210, v209, 16\nv_alignbit_b32 v217, v211, v210, 16\nv_alignbit_b32 v218, v212, v211, 16\nv_alignbit_b32 v219, v213, v212, 16\n"
    "v_alignbit_b32 v220, v214, v213, 16\nv_pk_add_f16 v224, v209, v216\nv_pk_add_f16 v225, v210, v217\nv_pk_add_f16 v226, v211, v218\nv_pk_add_f16 v227, v212, v219\n"
    "v_pk_add_f16 v228, v213, v220\nv_pk_add_f16 v236, v216, v225\nv_pk_add_f16 v237, v217, v226\nv_pk_add_f16 v238, v218, v227\nv_pk_add_f16 v239, v219, v228\n"
    "ds_write_b64 v15, v[236:237] offset:6344\nv_pk_add_f16 v232, v224, v211\nv_pk_add_f16 v233, v225, v212\nv_pk_add_f16 v234, v226, v213\nv_pk_add_f16 v235, v227, v214\n"
    "ds_write_b64 v35, v[238:239] offset:6336\nv_pk_add_f16 v232, v232, v225\nv_pk_add_f16 v233, v233, v226\nv_pk_add_f16 v234, v234, v227\nv_pk_add_f16 v235, v235, v228\n"
    "ds_write_b128 v15, v[232:235] offset:2304\ns_setprio 0\nds_read_b64_tr_b16 v[40:41], v27 offset:1984\nds_read_b64_tr_b16 v[42:43], v27 offset:2048\n"
    "ds_read_b64_tr_b16 v[44:45], v27 offset:1728\nds_read_b64_tr_b16 v[46:47], v27 offset:1792\nds_read_b64_tr_b16 v[48:49], v27 offset:1408\n"
    "ds_read_b64_tr_b16 v[50:51], v27 offset:1472\nds_read_b64_tr_b16 v[52:53], v27 offset:1152\nds_read_b64_tr_b16 v[54:55], v27 offset:1216\n"
    "ds_read_b64_tr_b16 v[240:241], v27 offset:832\nds_read_b64_tr_b16 v[242:243], v27 offset:896\ns_waitcnt lgkmcnt(8)\n"
    "v_mfma_f32_32x32x16_f16 v[192:207], v[40:43], v[128:131], 0\nds_read_b64_tr_b16 v[40:41], v27 offset:576\nds_read_b64_tr_b16 v[42:43], v27 offset:640\n"
    "s_waitcnt lgkmcnt(8)\nv_mfma_f32_32x32x16_f16 v[192:207], v[44:47], v[132:135], v[192:207]\nds_read_b64_tr_b16 v[44:45], v27 offset:6016\n"
    "ds_read_b64_tr_b16 v[46:47], v27 offset:6080\nv_fmac_f32 v56, s16, v244\nv_fmac_f32 v57, s16, v245\ns_waitcnt lgkmcnt(8)\n"
    "v_mfma_f32_32x32x16_f16 v[192:207], v[48:51], v[136:139], v[192:207]\nds_read_b64_tr_b16 v[48:49], v27 offset:5760\nds_read_b64_tr_b16 v[50:51], v27 offset:5824\n"
    "v_fmac_f32 v58, s16, v246\nv_fmac_f32 v59, s16, v247\ns_waitcnt lgkmcnt(8)\nv_mfma_f32_32x32x16_f16 v[192:207], v[52:55], v[140:143], v[192:207]\n"
    "ds_read_b64_tr_b16 v[52:53], v27 offset:5440\nds_read_b64_tr_b16 v[54:55], v27 offset:5504\nv_fmac_f32 v60, s17, v248\nv_fmac_f32 v61, s17, v249\ns_waitcnt lgkmcnt(8)\n"
    "v_mfma_f32_32x32x16_f16 v[192:207], v[240:243], v[160:163], v[192:207]\nds_read_b64_tr_b16 v[240:241], v27 offset:5184\nds_read_b64_tr_b16 v[242:243], v27 offset:5248\n"
    "v_fmac_f32 v62, s17, v250\nv_fmac_f32 v63, s17, v251\ns_waitcnt lgkmcnt(8)\nv_mfma_f32_32x32x16_f16 v[192:207], v[40:43], v[164:167], v[192:207]\n"
    "ds_read_b64_tr_b16 v[40:41], v27 offset:4864\nds_read_b64_tr_b16 v[42:43], v27 offset:4928\ns_waitcnt lgkmcnt(8)\n"
    "v_mfma_f32_32x32x16_f16 v[192:207], v[44:47], v[144:147], v[192:207]\nds_read_b64_tr_b16 v[44:45], v27 offset:4608\nds_read_b64_tr_b16 v[46:47], v27 offset:4672\n"
    "s_waitcnt lgkmcnt(8)\nv_mfma_f32_32x32x16_f16 v[192:207], v[48:51], v[148:151], v[192:207]\nds_read_b32 v244, v30\nds_read_b64 v[246:247], v31\n"
    "ds_read_b32 v245, v30 offset:6480\nds_read_b64 v[248:249], v31 offset:6480\ns_waitcnt lgkmcnt(10)\nv_mfma_f32_32x32x16_f16 v[192:207], v[52:55], v[152:155], v[192:207]\n"
    "s_waitcnt lgkmcnt(8)\nv_mfma_f32_32x32x16_f16 v[192:207], v[240:243], v[156:159], v[192:207]\ns_waitcnt lgkmcnt(6)\n"
    "v_mfma_f32_32x32x16_f16 v[192:207], v[40:43], v[176:179], v[192:207]\ns_waitcnt lgkmcnt(0)\nv_fma_mix_f32 v56, v244, s28, v56 op_sel:[1,0,0] op_sel_hi:[1,0,0]\n"
    "v_fma_mix_f32 v57, v246, s28, v57 op_sel:[0,0,0] op_sel_hi:[1,0,0]\nv_fma_mix_f32 v58, v246, s28, v58 op_sel:[1,0,0] op_sel_hi:[1,0,0]\ns_waitcnt lgkmcnt(4)\n"
    "v_mfma_f32_32x32x16_f16 v[192:207], v[44:47], v[180:183], v[192:207]\nv_fma_mix_f32 v59, v247, s28, v59 op_sel:[0,0,0] op_sel_hi:[1,0,0]\n"
    "v_fma_mix_f32 v60, v245, s29, v60 op_sel:[1,0,0] op_sel_hi:[1,0,0]\nv_fma_mix_f32 v61, v248, s29, v61 op_sel:[0,0,0] op_sel_hi:[1,0,0]\n"
    "v_fma_mix_f32 v62, v248, s29, v62 op_sel:[1,0,0] op_sel_hi:[1,0,0]\nv_fma_mix_f32 v63, v249, s29, v63 op_sel:[0,0,0] op_sel_hi:[1,0,0]\ns_nop 6\ns_cbranch_execz .Lhop\n"
    "v_cvt_pk_bf16_f32 v192, v192, v193\nv_cvt_pk_bf16_f32 v193, v194, v195\nds_write_b64 v32, v[192:193] offset:1024\nv_cvt_pk_bf16_f32 v196, v196, v197\n"
    "v_cvt_pk_bf16_f32 v197, v198, v199\nds_write_b64 v32, v[196:197] offset:1536\nv_cvt_pk_bf16_f32 v200, v200, v201\nv_cvt_pk_bf16_f32 v201, v202, v203\n"
    "ds_write_b64 v32, v[200:201] offset:2048\nv_cvt_pk_bf16_f32 v204, v204, v205\nv_cvt_pk_bf16_f32 v205, v206, v207\nds_write_b64 v32, v[204:205] offset:2560\n"
    "s_branch .Lk131\n.Lk130:\ns_cmp_lg_u32 s12, 0\ns_waitcnt lgkmcnt(0)\ns_barrier\nds_read_b64 v[192:193], v11 offset:0\nds_read_b128 v[194:197], v10 offset:0\n"
    "ds_read_b64 v[198:199], v12 offset:0\nds_read_b64 v[200:201], v11 offset:1280\nds_read_b128 v[202:205], v10 offset:1280\nds_read_b64 v[206:207], v12 offset:1280\n"
    "s_cbranch_scc1 .Lz134\nglobal_store_dwordx4 v37, v[252:255], s[4:5] offset:0 sc1\nglobal_store_dwordx4 v37, v[252:255], s[4:5] offset:1024 sc1\n"
    "global_store_dwordx4 v37, v[252:255], s[54:55] offset:0 sc1\nglobal_store_dwordx4 v37, v[252:255], s[54:55] offset:1024 sc1\n"
    "global_store_dwordx4 v37, v[252:255], s[56:57] offset:0 sc1\nglobal_store_dwordx4 v37, v[252:255], s[56:57] offset:1024 sc1\ns_cmp_lg_u32 s13, 2\ns_cbranch_scc1 .Lz134\n"
    "global_store_dwordx4 v38, v[252:255], s[4:5] sc1\n.Lz134:\ns_waitcnt lgkmcnt(3)\nv_alignbit_b32 v216, v193, v192, 16\nv_alignbit_b32 v217, v194, v193, 16\n"
    "v_alignbit_b32 v218, v195, v194, 16\nv_alignbit_b32 v219, v196, v195, 16\nv_alignbit_b32 v220, v197, v196, 16\nv_alignbit_b32 v221, v198, v197, 16\n"
    "v_alignbit_b32 v222, v199, v198, 16\nv_pk_add_f16 v224, v192, v216\nv_pk_add_f16 v225, v193, v217\nv_pk_add_f16 v226, v194, v218\nv_pk_add_f16 v227, v195, v219\n"
    "v_pk_add_f16 v228, v196, v220\nv_pk_add_f16 v229, v197, v221\nv_pk_add_f16 v230, v198, v222\nv_pk_add_f16 v223, v226, v227\nv_pk_add_f16 v231, v228, v229\n"
    "v_pk_add_f16 v232, v224, v196\nv_pk_add_f16 v233, v225, v197\nv_pk_add_f16 v234, v226, v198\nv_pk_add_f16 v235, v227, v199\nv_pk_add_f16 v208, v225, v223\n"
    "v_pk_add_f16 v209, v223, v228\nv_pk_add_f16 v210, v227, v231\nv_pk_add_f16 v211, v231, v230\nv_pk_add_f16 v236, v208, v216\nv_pk_add_f16 v237, v209, v217\n"
    "v_pk_add_f16 v238, v210, v218\nv_pk_add_f16 v239, v211, v219\nds_write_b128 v14, v[236:239] offset:0\nv_pk_add_f16 v232, v232, v208\nv_pk_add_f16 v233, v233, v209\n"
    "v_pk_add_f16 v234, v234, v210\nv_pk_add_f16 v235, v235, v211\nds_write_b128 v13, v[232:235] offset:0\ns_waitcnt lgkmcnt(2)\nv_alignbit_b32 v216, v201, v200, 16\n"
    "v_alignbit_b32 v217, v202, v201, 16\nv_alignbit_b32 v218, v203, v202, 16\nv_alignbit_b32 v219, v204, v203, 16\nv_alignbit_b32 v220, v205, v204, 16\n"
    "v_alignbit_b32 v221, v206, v205, 16\nv_alignbit_b32 v222, v207, v206, 16\nv_pk_add_f16 v224, v200, v216\nv_pk_add_f16 v225, v201, v217\nv_pk_add_f16 v226, v202, v218\n"
    "v_pk_add_f16 v227, v203, v219\nv_pk_add_f16 v228, v204, v220\nv_pk_add_f16 v229, v205, v221\nv_pk_add_f16 v230, v206, v222\nv_pk_add_f16 v223, v226, v227\n"
    "v_pk_add_f16 v231, v228, v229\nv_pk_add_f16 v232, v224, v204\nv_pk_add_f16 v233, v225, v205\nv_pk_add_f16 v234, v226, v206\nv_pk_add_f16 v235, v227, v207\n"
    "v_pk_add_f16 v208, v225, v223\nv_pk_add_f16 v209, v223, v228\nv_pk_add_f16 v210, v227, v231\nv_pk_add_f16 v211, v231, v230\nv_pk_add_f16 v236, v208, v216\n"
    "v_pk_add_f16 v237, v209, v217\nv_pk_add_f16 v238, v210, v218\nv_pk_add_f16 v239, v211, v219\nds_write_b128 v14, v[236:239] offset:1152\nv_pk_add_f16 v232, v232, v208\n"
    "v_pk_add_f16 v233, v233, v209\nv_pk_add_f16 v234, v234, v210\nv_pk_add_f16 v235, v235, v211\nds_write_b128 v15, v[232:235] offset:1152\nv_fmac_f32 v56, s16, v244\n"
    "v_fmac_f32 v57, s16, v245\nv_fmac_f32 v58, s16, v246\nv_fmac_f32 v59, s16, v247\nv_fmac_f32 v60, s17, v248\nv_fmac_f32 v61, s17, v249\nv_fmac_f32 v62, s17, v250\n"
    "v_fmac_f32 v63, s17, v251\nds_read_b32 v244, v30\nds_read_b64 v[246:247], v31\nds_read_b32 v245, v30 offset:6480\nds_read_b64 v[248:249], v31 offset:6480\ns_setprio 0\n"
    "ds_read_b32 v39, v24\nds_read_b64_tr_b16 v[40:41], v28 offset:512\nds_read_b64_tr_b16 v[42:43], v28 offset:640\nds_read_b64_tr_b16 v[44:45], v28 offset:0\n"
    "ds_read_b64_tr_b16 v[46:47], v28 offset:128\nds_read_b64_tr_b16 v[48:49], v29 offset:512\nds_read_b64_tr_b16 v[50:51], v29 offset:640\n"
    "ds_read_b64_tr_b16 v[52:53], v29 offset:0\nds_read_b64_tr_b16 v[54:55], v29 offset:128\nds_read_b64_tr_b16 v[240:241], v28 offset:2816\n"
    "ds_read_b64_tr_b16 v[242:243], v28 offset:2944\ns_waitcnt lgkmcnt(8)\nv_mfma_f32_32x32x16_f16 v[224:239], v[40:43], v[160:163], 0\nv_cmp_gt_i32 vcc, v26, v39\n"
    "ds_read_b64_tr_b16 v[40:41], v28 offset:2304\nds_read_b64_tr_b16 v[42:43], v28 offset:2432\ns_waitcnt lgkmcnt(8)\n"
    "v_mfma_f32_32x32x16_f16 v[224:239], v[44:47], v[164:167], v[224:239]\nds_read_b64_tr_b16 v[44:45], v29 offset:2816\nds_read_b64_tr_b16 v[46:47], v29 offset:2944\n"
    "s_waitcnt lgkmcnt(8)\nv_mfma_f32_32x32x16_f16 v[208:223], v[48:51], v[168:171], 0\nds_read_b64_tr_b16 v[48:49], v29 offset:2304\ns_nop 0\ns_cbranch_execz .Lhop\n"
    "ds_read_b64_tr_b16 v[50:51], v29 offset:2432\ns_waitcnt lgkmcnt(8)\nv_mfma_f32_32x32x16_f16 v[208:223], v[52:55], v[172:175], v[208:223]\ns_cbranch_vccnz .Lw135\n"
    ".Lr136:\nds_read_b64_tr_b16 v[52:53], v27 offset:832\nds_read_b64_tr_b16 v[54:55], v27 offset:896\ns_waitcnt lgkmcnt(8)\n"
    "v_mfma_f32_32x32x16_f16 v[224:239], v[240:243], v[176:179], v[224:239]\nds_read_b64_tr_b16 v[240:241], v27 offset:576\nds_read_b64_tr_b16 v[242:243], v27 offset:640\n"
    "v_fma_mix_f32 v56, v244, s28, v56 op_sel:[1,0,0] op_sel_hi:[1,0,0]\nv_fma_mix_f32 v57, v246, s28, v57 op_sel:[0,0,0] op_sel_hi:[1,0,0]\ns_waitcnt lgkmcnt(8)\n"
    "v_mfma_f32_32x32x16_f16 v[224:239], v[40:43], v[180:183], v[224:239]\nds_read_b64_tr_b16 v[40:41], v27 offset:256\nds_read_b64_tr_b16 v[42:43], v27 offset:320\n"
    "v_fma_mix_f32 v58, v246, s28, v58 op_sel:[1,0,0] op_sel_hi:[1,0,0]\nv_fma_mix_f32 v59, v247, s28, v59 op_sel:[0,0,0] op_sel_hi:[1,0,0]\ns_waitcnt lgkmcnt(8)\n"
    "v_mfma_f32_32x32x16_f16 v[208:223], v[44:47], v[184:187], v[208:223]\nds_read_b64_tr_b16 v[44:45], v27 offset:0\nds_read_b64_tr_b16 v[46:47], v27 offset:64\n"
    "v_fma_mix_f32 v60, v245, s29, v60 op_sel:[1,0,0] op_sel_hi:[1,0,0]\nv_fma_mix_f32 v61, v248, s29, v61 op_sel:[0,0,0] op_sel_hi:[1,0,0]\ns_waitcnt lgkmcnt(8)\n"
    "v_mfma_f32_32x32x16_f16 v[208:223], v[48:51], v[188:191], v[208:223]\nds_read_b64_tr_b16 v[48:49], v27 offset:4864\nds_read_b64_tr_b16 v[50:51], v27 offset:4928\n"
    "v_fma_mix_f32 v62, v248, s29, v62 op_sel:[1,0,0] op_sel_hi:[1,0,0]\nv_fma_mix_f32 v63, v249, s29, v63 op_sel:[0,0,0] op_sel_hi:[1,0,0]\ns_waitcnt lgkmcnt(8)\n"
    "v_mfma_f32_32x32x16_f16 v[192:207], v[52:55], v[128:131], 0\nds_read_b64_tr_b16 v[52:53], v27 offset:4608\nds_read_b64_tr_b16 v[54:55], v27 offset:4672\n"
    "s_waitcnt lgkmcnt(8)\nv_mfma_f32_32x32x16_f16 v[192:207], v[240:243], v[132:135], v[192:207]\nds_read_b64_tr_b16 v[240:241], v27 offset:4288\n"
    "ds_read_b64_tr_b16 v[242:243], v27 offset:4352\nv_cvt_pk_bf16_f32 v224, v224, v225\nv_cvt_pk_bf16_f32 v225, v226, v227\nds_write_b64 v33, v[224:225] offset:0\n"
    "s_waitcnt lgkmcnt(9)\nv_mfma_f32_32x32x16_f16 v[192:207], v[40:43], v[136:139], v[192:207]\nds_read_b64_tr_b16 v[40:41], v27 offset:4032\n"
    "ds_read_b64_tr_b16 v[42:43], v27 offset:4096\nv_cvt_pk_bf16_f32 v228, v228, v229\nv_cvt_pk_bf16_f32 v229, v230, v231\nds_write_b64 v36, v[228:229] offset:0\n"
    "s_waitcnt lgkmcnt(10)\nv_mfma_f32_32x32x16_f16 v[192:207], v[44:47], v[140:143], v[192:207]\nv_add_f32 v232, v232, v216\nv_add_f32 v233, v233, v217\n"
    "v_add_f32 v234, v234, v218\nv_add_f32 v235, v235, v219\ns_waitcnt lgkmcnt(8)\nv_mfma_f32_32x32x16_f16 v[192:207], v[48:51], v[144:147], v[192:207]\n"
    "v_add_f32 v236, v236, v220\nv_add_f32 v237, v237, v221\nv_add_f32 v238, v238, v222\nv_add_f32 v239, v239, v223\ns_waitcnt lgkmcnt(6)\n"
    "v_mfma_f32_32x32x16_f16 v[192:207], v[52:55], v[148:151], v[192:207]\nv_cvt_pk_bf16_f32 v232, v232, v233\nv_cvt_pk_bf16_f32 v233, v234, v235\n"
    "ds_write_b64 v33, v[232:233] offset:1024\ns_waitcnt lgkmcnt(5)\nv_mfma_f32_32x32x16_f16 v[192:207], v[240:243], v[152:155], v[192:207]\n"
    "v_cvt_pk_bf16_f32 v236, v236, v237\nv_cvt_pk_bf16_f32 v237, v238, v239\nds_write_b64 v36, v[236:237] offset:1024\ns_waitcnt lgkmcnt(3)\n"
    "v_mfma_f32_32x32x16_f16 v[192:207], v[40:43], v[156:159], v[192:207]\ns_nop 11\nv_cvt_pk_bf16_f32 v192, v192, v193\nv_cvt_pk_bf16_f32 v193, v194, v195\n"
    "ds_write_b64 v32, v[192:193] offset:0\nv_cvt_pk_bf16_f32 v196, v196, v197\nv_cvt_pk_bf16_f32 v197, v198, v199\nds_write_b64 v32, v[196:197] offset:512\n.Lk131:\n"
    "s_setprio 0\ns_waitcnt vmcnt(0) lgkmcnt(0)\ns_barrier\nds_read_b64 v[208:209], v19\nds_read_b64 v[210:211], v19 offset:10240\nds_read_b64 v[212:213], v20\n"
    "ds_read_b64 v[214:215], v20 offset:10240\nds_read_b64 v[216:217], v34\nds_read_b64 v[218:219], v34 offset:10240\ns_cmp_lg_u32 s12, 0\ns_waitcnt lgkmcnt(4)\n"
    "v_dot2c_f32_bf16 v56, s26, v208\nv_dot2c_f32_bf16 v57, s14, v208\nv_dot2c_f32_bf16 v58, s26, v209\nv_dot2c_f32_bf16 v59, s14, v209\nv_dot2c_f32_bf16 v60, s27, v210\n"
    "v_dot2c_f32_bf16 v61, s15, v210\nv_dot2c_f32_bf16 v62, s27, v211\nv_dot2c_f32_bf16 v63, s15, v211\ns_waitcnt lgkmcnt(2)\nv_dot2c_f32_bf16 v56, s26, v212\n"
    "v_dot2c_f32_bf16 v57, s14, v212\nv_dot2c_f32_bf16 v58, s26, v213\nv_dot2c_f32_bf16 v59, s14, v213\nv_dot2c_f32_bf16 v60, s27, v214\nv_dot2c_f32_bf16 v61, s15, v214\n"
    "v_dot2c_f32_bf16 v62, s27, v215\nv_dot2c_f32_bf16 v63, s15, v215\ns_waitcnt lgkmcnt(0)\nv_dot2c_f32_bf16 v56, s26, v216\nv_dot2c_f32_bf16 v57, s14, v216\n"
    "v_dot2c_f32_bf16 v58, s26, v217\nv_dot2c_f32_bf16 v59, s14, v217\nv_dot2c_f32_bf16 v60, s27, v218\nv_dot2c_f32_bf16 v61, s15, v218\nv_dot2c_f32_bf16 v62, s27, v219\n"
    "v_dot2c_f32_bf16 v63, s15, v219\nv_cvt_pk_bf16_f32 v40, v56, v60\nv_cvt_pk_bf16_f32 v41, v57, v61\nv_cvt_pk_bf16_f32 v42, v58, v62\nv_cvt_pk_bf16_f32 v43, v59, v63\n"
    "global_store_dwordx4 v21, v[40:43], s[4:5] sc1\ns_cbranch_scc1 .Lfin124\n"
    "s_mov_b32 s12, 8388608\nv_add_u32 v9, 8388608, v9\ns_lshl_b32 s43, s22, 17\ns_lshl_b32 s42, s13, 15\ns_add_u32 s44, s43, s42\ns_add_u32 s44, s44, 0xe01000\n"
    "v_and_b32 v231, 63, v0\nv_lshlrev_b32 v59, 4, v231\ns_add_u32 s68, s4, s44\ns_addc_u32 s69, s5, 0\ns_add_u32 s70, s68, 8192\ns_addc_u32 s71, s69, 0\n"
    "s_add_u32 s72, s68, 16384\ns_addc_u32 s73, s69, 0\ns_add_u32 s74, s68, 24576\ns_addc_u32 s75, s69, 0\ns_lshl_b32 s43, s19, 2\ns_max_u32 s42, s43, 1\n"
    "s_lshl_b32 s41, s42, 15\ns_lshl_b32 s40, s13, 13\ns_add_u32 s46, s41, s40\ns_add_u32 s46, s46, 0x5f9000\nv_bfe_u32 v251, v0, 4, 2\nv_and_b32 v250, 15, v0\n"
    "v_lshl_add_u32 v16, v251, 9, 64\nv_lshl_add_u32 v16, v250, 2, v16\nv_xor_b32 v249, v251, v250\ns_lshl_b32 s39, s13, 8\ns_add_u32 s38, s39, 0x23c00\n"
    "v_lshl_add_u32 v17, v250, 10, s38\nv_lshl_add_u32 v17, v249, 4, v17\ns_mov_b32 s37, 0x23c00\nv_lshl_add_u32 v21, v250, 10, s37\nv_lshl_add_u32 v21, v249, 4, v21\n"
    "s_lshl_b32 s36, s13, 7\ns_add_u32 s35, s36, 0x27d80\nv_lshl_add_u32 v25, v250, 3, s35\ns_mov_b32 s34, 0x27d80\nv_lshl_add_u32 v26, v250, 3, s34\n"
    "v_bfe_u32 v251, v0, 5, 1\nv_mul_u32_u24 v250, 0xc00, v251\nv_bfe_u32 v249, v0, 4, 1\ns_nop 0\ns_cbranch_execz .Lhop\nv_bfe_u32 v248, v0, 2, 2\nv_and_b32 v247, 3, v0\n"
    "s_and_b32 s43, s13, 1\nv_xor_b32 v246, s43, v249\ns_lshr_b32 s42, s13, 1\ns_lshl_b32 s41, s42, 10\ns_add_u32 s40, s41, 0x400\nv_add_u32 v27, s40, v250\n"
    "v_lshl_add_u32 v27, v249, 9, v27\nv_lshl_add_u32 v27, v248, 6, v27\nv_lshl_add_u32 v27, v247, 3, v27\nv_lshl_add_u32 v27, v246, 5, v27\ns_add_u32 s39, s41, 0xc400\n"
    "v_add_u32 v28, s39, v250\nv_lshl_add_u32 v28, v249, 9, v28\nv_lshl_add_u32 v28, v248, 6, v28\nv_lshl_add_u32 v28, v247, 3, v28\nv_lshl_add_u32 v28, v246, 5, v28\n"
    "v_bfe_u32 v245, v0, 4, 2\ns_lshl_b32 s38, s42, 7\ns_lshl_b32 s37, s43, 6\ns_add_u32 s36, s38, s37\ns_add_u32 s36, s36, 0x27c80\nv_lshl_add_u32 v29, v245, 4, s36\n"
    "v_and_b32 v244, 15, v0\nv_mul_u32_u24 v243, 0xd0, v244\ns_lshl_b32 s35, s42, 6\ns_lshl_b32 s34, s43, 5\ns_add_u32 s33, s35, s34\ns_add_u32 s33, s33, 0x18040\n"
    "v_add_u32 v30, s33, v243\nv_lshl_add_u32 v30, v245, 3, v30\ns_lshl_b32 s43, s13, 2\ns_add_u32 s42, s43, 0x27f80\nv_mov_b32 v31, s42\nv_and_b32 v251, 3, v0\n"
    "s_mov_b32 s41, 0x27f80\nv_lshl_add_u32 v32, v251, 2, s41\nv_xor_b32 v18, 64, v17\nv_xor_b32 v19, 128, v17\nv_xor_b32 v20, 192, v17\nv_xor_b32 v22, 64, v21\n"
    "v_xor_b32 v23, 128, v21\nv_xor_b32 v24, 192, v21\ns_add_u32 s44, s4, s46\ns_addc_u32 s45, s5, 0\ns_mov_b32 s46, 0x3f803f80\ns_mov_b32 s47, 0x3b000000\n"
    "s_mov_b32 s80, 0x10000\nv_mov_b32 v33, 3\nds_read_b128 v[40:43], v29\ns_waitcnt vmcnt(0) lgkmcnt(0)\nv_lshlrev_b32 v44, 16, v40\nv_lshlrev_b32 v45, 16, v41\n"
    "v_lshlrev_b32 v46, 16, v42\nv_lshlrev_b32 v47, 16, v43\nv_and_b32 v40, 0xffff0000, v40\nv_and_b32 v41, 0xffff0000, v41\nv_and_b32 v42, 0xffff0000, v42\n"
    "v_and_b32 v43, 0xffff0000, v43\nv_add_f32 v40, v40, v44\nv_add_f32 v41, v41, v45\nv_add_f32 v42, v42, v46\nv_add_f32 v43, v43, v47\ns_sleep 4\ns_setprio 3\ns_barrier\n"
    "s_sleep 6\n.Lq139:\nglobal_load_dword v192, v16, s[44:45] offset:-4096 sc1\nglobal_load_dword v193, v16, s[44:45] offset:-3968 sc1\n"
    "global_load_dword v194, v16, s[44:45] offset:-3840 sc1\nglobal_load_dword v195, v16, s[44:45] offset:-3712 sc1\nglobal_load_dword v196, v16, s[44:45] offset:-2048 sc1\n"
    "global_load_dword v197, v16, s[44:45] offset:-1920 sc1\nglobal_load_dword v198, v16, s[44:45] offset:-1792 sc1\nglobal_load_dword v199, v16, s[44:45] offset:-1664 sc1\n"
    "global_load_dword v200, v16, s[44:45] offset:0 sc1\nglobal_load_dword v201, v16, s[44:45] offset:128 sc1\nglobal_load_dword v202, v16, s[44:45] offset:256 sc1\n"
    "global_load_dword v203, v16, s[44:45] offset:384 sc1\nglobal_load_dword v204, v16, s[44:45] offset:2048 sc1\nglobal_load_dword v205, v16, s[44:45] offset:2176 sc1\n"
    "global_load_dword v206, v16, s[44:45] offset:2304 sc1\nglobal_load_dword v207, v16, s[44:45] offset:2432 sc1\ns_waitcnt vmcnt(0)\nv_max3_u32 v48, v192, v193, v194\n"
    "v_max3_u32 v48, v48, v195, v196\nv_max3_u32 v48, v48, v197, v198\nv_max3_u32 v48, v48, v199, v200\nv_max3_u32 v48, v48, v201, v202\nv_max3_u32 v48, v48, v203, v204\n"
    "v_max3_u32 v48, v48, v205, v206\nv_max_u32 v48, v48, v207\nv_cmp_eq_u32 vcc, -1, v48\nds_write_b128 v17, v[192:195]\nds_write_b128 v18, v[196:199]\n"
    "ds_write_b128 v19, v[200:203]\nds_write_b128 v20, v[204:207]\ns_cbranch_vccnz .Lq140\n.Lq140x:\nv_dot2_f32_bf16 v48, v192, s46, 0\nv_dot2_f32_bf16 v49, v192, v192, 0\n"
    "v_dot2_f32_bf16 v50, v193, s46, 0\nv_dot2_f32_bf16 v51, v193, v193, 0\nv_dot2_f32_bf16 v48, v194, s46, v48\nv_dot2_f32_bf16 v49, v194, v194, v49\n"
    "v_dot2_f32_bf16 v50, v195, s46, v50\nv_dot2_f32_bf16 v51, v195, v195, v51\nv_dot2_f32_bf16 v48, v196, s46, v48\nv_dot2_f32_bf16 v49, v196, v196, v49\n"
    "v_dot2_f32_bf16 v50, v197, s46, v50\nv_dot2_f32_bf16 v51, v197, v197, v51\nv_dot2_f32_bf16 v48, v198, s46, v48\nv_dot2_f32_bf16 v49, v198, v198, v49\n"
    "v_dot2_f32_bf16 v50, v199, s46, v50\nv_dot2_f32_bf16 v51, v199, v199, v51\nv_dot2_f32_bf16 v48, v200, s46, v48\nv_dot2_f32_bf16 v49, v200, v200, v49\n"
    "v_dot2_f32_bf16 v50, v201, s46, v50\nv_dot2_f32_bf16 v51, v201, v201, v51\nv_dot2_f32_bf16 v48, v202, s46, v48\nv_dot2_f32_bf16 v49, v202, v202, v49\n"
    "v_dot2_f32_bf16 v50, v203, s46, v50\nv_dot2_f32_bf16 v51, v203, v203, v51\nv_dot2_f32_bf16 v48, v204, s46, v48\nv_dot2_f32_bf16 v49, v204, v204, v49\n"
    "v_dot2_f32_bf16 v50, v205, s46, v50\nv_dot2_f32_bf16 v51, v205, v205, v51\nv_dot2_f32_bf16 v48, v206, s46, v48\nv_dot2_f32_bf16 v49, v206, v206, v49\n"
    "v_dot2_f32_bf16 v50, v207, s46, v50\nv_dot2_f32_bf16 v51, v207, v207, v51\ns_nop 1\nv_add_f32 v48, v48, v50\nv_add_f32 v49, v49, v51\ns_nop 1\n"
    "v_permlane16_swap_b32 v48, v49\nv_add_f32 v50, v48, v49\nv_add_f32 v49, v48, v49\ns_nop 1\nv_permlane32_swap_b32 v50, v49\nv_add_f32 v50, v50, v49\nv_mov_b32 v48, v50\n"
    "v_mov_b32 v49, v50\ns_nop 1\nv_permlane16_swap_b32 v48, v49\ns_mov_b64 exec, 0xffff\nds_write_b64 v25, v[48:49]\ns_mov_b64 exec, 1\nds_write_b32 v31, v33 offset:16\n"
    "s_mov_b64 exec, -1\ns_mov_b32 s86, 0x100000\n.Ls141:\nds_read_b32 v48, v32 offset:16\ns_waitcnt lgkmcnt(0)\nv_cmp_gt_i32 vcc, 3, v48\ns_sub_u32 s86, s86, 1\n"
    "s_cbranch_scc1 .Ls142\ns_cbranch_vccz .Ls142\ns_sleep 1\ns_branch .Ls141\ns_cbranch_execz .Lhop\n.Ls142:\nds_read_b64 v[48:49], v26 offset:0\n"
    "ds_read_b64 v[50:51], v26 offset:128\nds_read_b64 v[52:53], v26 offset:256\nds_read_b64 v[54:55], v26 offset:384\nds_read_b128 v[128:131], v21 offset:0\n"
    "ds_read_b128 v[132:135], v22 offset:0\nds_read_b128 v[136:139], v23 offset:0\nds_read_b128 v[140:143], v24 offset:0\nds_read_b128 v[144:147], v21 offset:256\n"
    "ds_read_b128 v[148:151], v22 offset:256\nds_read_b128 v[152:155], v23 offset:256\nds_read_b128 v[156:159], v24 offset:256\nds_read_b128 v[160:163], v21 offset:512\n"
    "ds_read_b128 v[164:167], v22 offset:512\nds_read_b128 v[168:171], v23 offset:512\nds_read_b128 v[172:175], v24 offset:512\nds_read_b128 v[176:179], v21 offset:768\n"
    "ds_read_b128 v[180:183], v22 offset:768\nds_read_b128 v[184:187], v23 offset:768\nds_read_b128 v[188:191], v24 offset:768\nds_read_b64_tr_b16 v[192:193], v27 offset:0\n"
    "ds_read_b64_tr_b16 v[194:195], v27 offset:256\nds_read_b64_tr_b16 v[196:197], v27 offset:6144\nds_read_b64_tr_b16 v[198:199], v27 offset:6400\n"
    "ds_read_b64_tr_b16 v[200:201], v27 offset:12288\nds_read_b64_tr_b16 v[202:203], v27 offset:12544\nds_read_b64_tr_b16 v[204:205], v27 offset:18432\n"
    "ds_read_b64_tr_b16 v[206:207], v27 offset:18688\ns_waitcnt lgkmcnt(15)\nv_add_f32 v48, v48, v50\nv_add_f32 v49, v49, v51\nv_add_f32 v52, v52, v54\n"
    "v_add_f32 v53, v53, v55\nv_add_f32 v48, v48, v52\nv_add_f32 v49, v49, v53\nv_mul_f32 v48, s47, v48\nv_mul_f32 v50, v48, v48\nv_fma_f32 v49, v49, s47, -v50\n"
    "v_max_f32 v49, 0, v49\nv_cvt_pk_bf16_f32 v49, 0, v49\nv_add_f32 v49, 0x3727c5ac, v49\nv_cvt_pk_bf16_f32 v49, 0, v49\nv_sqrt_f32 v49, v49\nv_cvt_pk_bf16_f32 v48, 0, -v48\n"
    "v_cvt_pk_bf16_f32 v49, 0, v49\nv_rcp_f32 v49, v49\nv_mul_f32 v44, v48, v40\nv_mul_f32 v45, v48, v41\nv_mul_f32 v46, v48, v42\nv_mul_f32 v47, v48, v43\n"
    "ds_read_b64_tr_b16 v[208:209], v27 offset:24576\nds_read_b64_tr_b16 v[210:211], v27 offset:24832\nds_read_b64_tr_b16 v[212:213], v27 offset:30720\n"
    "ds_read_b64_tr_b16 v[214:215], v27 offset:30976\nds_read_b64_tr_b16 v[216:217], v27 offset:36864\nds_read_b64_tr_b16 v[218:219], v27 offset:37120\n"
    "ds_read_b64_tr_b16 v[220:221], v27 offset:43008\nds_read_b64_tr_b16 v[222:223], v27 offset:43264\ns_waitcnt lgkmcnt(8)\n"
    "v_mfma_f32_16x16x32_bf16 v[44:47], v[192:195], v[128:131], v[44:47]\nv_mfma_f32_16x16x32_bf16 v[44:47], v[196:199], v[132:135], v[44:47]\n"
    "v_mfma_f32_16x16x32_bf16 v[44:47], v[200:203], v[136:139], v[44:47]\nv_mfma_f32_16x16x32_bf16 v[44:47], v[204:207], v[140:143], v[44:47]\n"
    "ds_read_b64_tr_b16 v[192:193], v28 offset:0\nds_read_b64_tr_b16 v[194:195], v28 offset:256\nds_read_b64_tr_b16 v[196:197], v28 offset:6144\n"
    "ds_read_b64_tr_b16 v[198:199], v28 offset:6400\nds_read_b64_tr_b16 v[200:201], v28 offset:12288\nds_read_b64_tr_b16 v[202:203], v28 offset:12544\n"
    "ds_read_b64_tr_b16 v[204:205], v28 offset:18432\nds_read_b64_tr_b16 v[206:207], v28 offset:18688\ns_waitcnt lgkmcnt(8)\n"
    "v_mfma_f32_16x16x32_bf16 v[44:47], v[208:211], v[144:147], v[44:47]\nv_mfma_f32_16x16x32_bf16 v[44:47], v[212:215], v[148:151], v[44:47]\n"
    "v_mfma_f32_16x16x32_bf16 v[44:47], v[216:219], v[152:155], v[44:47]\nv_mfma_f32_16x16x32_bf16 v[44:47], v[220:223], v[156:159], v[44:47]\n"
    "ds_read_b64_tr_b16 v[208:209], v28 offset:24576\nds_read_b64_tr_b16 v[210:211], v28 offset:24832\nds_read_b64_tr_b16 v[212:213], v28 offset:30720\n"
    "ds_read_b64_tr_b16 v[214:215], v28 offset:30976\nds_read_b64_tr_b16 v[216:217], v28 offset:36864\nds_read_b64_tr_b16 v[218:219], v28 offset:37120\n"
    "ds_read_b64_tr_b16 v[220:221], v28 offset:43008\nds_read_b64_tr_b16 v[222:223], v28 offset:43264\ns_waitcnt lgkmcnt(8)\n"
    "v_mfma_f32_16x16x32_bf16 v[44:47], v[192:195], v[160:163], v[44:47]\nv_mfma_f32_16x16x32_bf16 v[44:47], v[196:199], v[164:167], v[44:47]\n"
    "v_mfma_f32_16x16x32_bf16 v[44:47], v[200:203], v[168:171], v[44:47]\nv_mfma_f32_16x16x32_bf16 v[44:47], v[204:207], v[172:175], v[44:47]\ns_waitcnt lgkmcnt(0)\n"
    "v_mfma_f32_16x16x32_bf16 v[44:47], v[208:211], v[176:179], v[44:47]\nv_mfma_f32_16x16x32_bf16 v[44:47], v[212:215], v[180:183], v[44:47]\n"
    "v_mfma_f32_16x16x32_bf16 v[44:47], v[216:219], v[184:187], v[44:47]\nv_mfma_f32_16x16x32_bf16 v[44:47], v[220:223], v[188:191], v[44:47]\ns_nop 7\n"
    "v_mul_f32 v44, v44, v49\nv_mul_f32 v45, v45, v49\nv_mul_f32 v46, v46, v49\nv_mul_f32 v47, v47, v49\nv_cvt_pk_bf16_f32 v44, v44, v45\nv_cvt_pk_bf16_f32 v45, v46, v47\n"
    "ds_write_b64 v30, v[44:45]\ns_setprio 0\nv_mov_b64 v[232:233], 0\nv_mov_b64 v[234:235], 0\nv_mov_b64 v[236:237], 0\nv_mov_b64 v[238:239], 0\nv_mov_b64 v[244:245], 0\n"
    "v_mov_b64 v[246:247], 0\nv_mov_b64 v[248:249], 0\nv_mov_b64 v[250:251], 0\ns_mov_b32 s44, 0x00ff00ff\ns_mov_b32 s45, 0xff00ff00\ns_mov_b32 s46, s44\ns_mov_b32 s47, s44\n"
    "s_branch .Latt12\n"
    ".Lqg125:\ns_mov_b32 s12, 8388608\nv_add_u32 v9, 8388608, v9\nv_bfe_u32 v239, v0, 5, 1\nv_and_b32 v238, 31, v0\nv_lshlrev_b32 v39, 9, v239\n"
    "v_lshl_add_u32 v39, v238, 2, v39\nv_and_b32 v237, 63, v0\ns_lshl_b32 s43, s24, 15\ns_and_b32 s42, s25, 3\ns_lshl_b32 s41, s42, 13\ns_lshl_b32 s40, s13, 11\n"
    "s_add_u32 s39, s43, s41\ns_add_u32 s39, s39, s40\ns_add_u32 s39, s39, 0xd00000\nv_lshl_add_u32 v56, v237, 2, s39\ns_lshl_b32 s38, s19, 17\ns_lshl_b32 s37, s13, 15\n"
    "s_add_u32 s82, s38, s37\ns_add_u32 s82, s82, 0x600800\ns_lshl_b32 s36, s23, 8\ns_add_u32 s35, s43, s41\ns_add_u32 s35, s35, s40\ns_add_u32 s35, s35, s36\n"
    "s_add_u32 s35, s35, 0xd00000\nv_lshl_add_u32 v57, v237, 2, s35\ns_add_u32 s82, s4, s82\ns_addc_u32 s83, s5, 0\ns_add_u32 s86, s23, 0\ns_and_b32 s86, s86, 7\n"
    "s_lshl_b32 s44, s86, 12\ns_add_u32 s52, s82, s44\ns_addc_u32 s53, s83, 0\ns_add_u32 s86, s23, 1\ns_and_b32 s86, s86, 7\ns_lshl_b32 s45, s86, 12\ns_add_u32 s54, s82, s45\n"
    "s_addc_u32 s55, s83, 0\ns_add_u32 s86, s23, 2\ns_and_b32 s86, s86, 7\ns_lshl_b32 s46, s86, 12\ns_add_u32 s56, s82, s46\ns_addc_u32 s57, s83, 0\ns_add_u32 s86, s23, 3\n"
    "s_and_b32 s86, s86, 7\ns_lshl_b32 s47, s86, 12\ns_add_u32 s58, s82, s47\ns_addc_u32 s59, s83, 0\ns_add_u32 s86, s23, 4\ns_and_b32 s86, s86, 7\ns_lshl_b32 s48, s86, 12\n"
    "s_add_u32 s60, s82, s48\ns_cbranch_execz .Lhop\ns_addc_u32 s61, s83, 0\ns_add_u32 s86, s23, 5\ns_and_b32 s86, s86, 7\ns_lshl_b32 s49, s86, 12\ns_add_u32 s62, s82, s49\n"
    "s_addc_u32 s63, s83, 0\ns_add_u32 s86, s23, 6\ns_and_b32 s86, s86, 7\ns_lshl_b32 s50, s86, 12\ns_add_u32 s64, s82, s50\ns_addc_u32 s65, s83, 0\ns_add_u32 s86, s23, 7\n"
    "s_and_b32 s86, s86, 7\ns_lshl_b32 s51, s86, 12\ns_add_u32 s66, s82, s51\ns_addc_u32 s67, s83, 0\ns_mov_b32 s81, 0x3b000000\ns_mov_b32 s84, -1\ns_mov_b32 s85, 0\n"
    "v_mov_b32 v241, 0x3f803f80\nv_mov_b64 v[120:121], 0\nv_mov_b64 v[122:123], 0\nv_mov_b64 v[124:125], 0\nv_mov_b64 v[126:127], 0\nv_mov_b64 v[128:129], 0\n"
    "v_mov_b64 v[130:131], 0\nv_mov_b64 v[132:133], 0\nv_mov_b64 v[134:135], 0\nv_mad_u32_u24 v242, s44, 3, v36\ns_mov_b32 s80, 0x10000\n"
    "ds_read_b64_tr_b16 v[208:209], v242 offset:0\nds_read_b64_tr_b16 v[210:211], v242 offset:256\nds_read_b64_tr_b16 v[212:213], v242 offset:3072\n"
    "ds_read_b64_tr_b16 v[214:215], v242 offset:3328\nds_read_b64_tr_b16 v[216:217], v242 offset:6144\nds_read_b64_tr_b16 v[218:219], v242 offset:6400\n"
    "ds_read_b64_tr_b16 v[220:221], v242 offset:9216\nds_read_b64_tr_b16 v[222:223], v242 offset:9472\nds_read_b64_tr_b16 v[224:225], v242 offset:1024\n"
    "ds_read_b64_tr_b16 v[226:227], v242 offset:1280\nds_read_b64_tr_b16 v[228:229], v242 offset:4096\nds_read_b64_tr_b16 v[230:231], v242 offset:4352\n"
    "ds_read_b32 v124, v38 offset:0\nds_read_b32 v128, v38 offset:128\nds_read_b32 v132, v38 offset:256\ns_waitcnt vmcnt(0) lgkmcnt(0)\ns_barrier\n"
    "global_load_dword v144, v39, s[52:53] offset:-2048 sc1\nglobal_load_dword v145, v39, s[52:53] offset:-1920 sc1\nglobal_load_dword v146, v39, s[52:53] offset:-1792 sc1\n"
    "global_load_dword v147, v39, s[52:53] offset:-1664 sc1\nglobal_load_dword v148, v39, s[52:53] offset:-1024 sc1\nglobal_load_dword v149, v39, s[52:53] offset:-896 sc1\n"
    "global_load_dword v150, v39, s[52:53] offset:-768 sc1\nglobal_load_dword v151, v39, s[52:53] offset:-640 sc1\nglobal_load_dword v152, v39, s[52:53] offset:0 sc1\n"
    "global_load_dword v153, v39, s[52:53] offset:128 sc1\nglobal_load_dword v154, v39, s[52:53] offset:256 sc1\nglobal_load_dword v155, v39, s[52:53] offset:384 sc1\n"
    "global_load_dword v156, v39, s[52:53] offset:1024 sc1\nglobal_load_dword v157, v39, s[52:53] offset:1152 sc1\nglobal_load_dword v158, v39, s[52:53] offset:1280 sc1\n"
    "global_load_dword v159, v39, s[52:53] offset:1408 sc1\nglobal_load_dword v160, v39, s[54:55] offset:-2048 sc1\nglobal_load_dword v161, v39, s[54:55] offset:-1920 sc1\n"
    "global_load_dword v162, v39, s[54:55] offset:-1792 sc1\nglobal_load_dword v163, v39, s[54:55] offset:-1664 sc1\nglobal_load_dword v164, v39, s[54:55] offset:-1024 sc1\n"
    "global_load_dword v165, v39, s[54:55] offset:-896 sc1\nglobal_load_dword v166, v39, s[54:55] offset:-768 sc1\nglobal_load_dword v167, v39, s[54:55] offset:-640 sc1\n"
    "global_load_dword v168, v39, s[54:55] offset:0 sc1\nglobal_load_dword v169, v39, s[54:55] offset:128 sc1\nglobal_load_dword v170, v39, s[54:55] offset:256 sc1\n"
    "global_load_dword v171, v39, s[54:55] offset:384 sc1\nglobal_load_dword v172, v39, s[54:55] offset:1024 sc1\nglobal_load_dword v173, v39, s[54:55] offset:1152 sc1\n"
    "global_load_dword v174, v39, s[54:55] offset:1280 sc1\nglobal_load_dword v175, v39, s[54:55] offset:1408 sc1\nglobal_load_dword v176, v39, s[56:57] offset:-2048 sc1\n"
    "global_load_dword v177, v39, s[56:57] offset:-1920 sc1\nglobal_load_dword v178, v39, s[56:57] offset:-1792 sc1\nglobal_load_dword v179, v39, s[56:57] offset:-1664 sc1\n"
    "global_load_dword v180, v39, s[56:57] offset:-1024 sc1\nglobal_load_dword v181, v39, s[56:57] offset:-896 sc1\nglobal_load_dword v182, v39, s[56:57] offset:-768 sc1\n"
    "global_load_dword v183, v39, s[56:57] offset:-640 sc1\nglobal_load_dword v184, v39, s[56:57] offset:0 sc1\nglobal_load_dword v185, v39, s[56:57] offset:128 sc1\n"
    "global_load_dword v186, v39, s[56:57] offset:256 sc1\nglobal_load_dword v187, v39, s[56:57] offset:384 sc1\nglobal_load_dword v188, v39, s[56:57] offset:1024 sc1\n"
    "global_load_dword v189, v39, s[56:57] offset:1152 sc1\nglobal_load_dword v190, v39, s[56:57] offset:1280 sc1\nglobal_load_dword v191, v39, s[56:57] offset:1408 sc1\n"
    "s_waitcnt vmcnt(32)\n.Ld143:\nv_dot2_f32_bf16 v112, v144, v241, 0\nv_dot2_f32_bf16 v113, v144, v144, 0\nv_dot2_f32_bf16 v114, v145, v241, 0\n"
    "v_dot2_f32_bf16 v115, v145, v145, 0\nv_dot2_f32_bf16 v116, v146, v241, 0\nv_dot2_f32_bf16 v117, v146, v146, 0\nv_dot2_f32_bf16 v118, v147, v241, 0\n"
    "v_dot2_f32_bf16 v119, v147, v147, 0\nv_dot2_f32_bf16 v112, v148, v241, v112\nv_dot2_f32_bf16 v113, v148, v148, v113\nv_dot2_f32_bf16 v114, v149, v241, v114\n"
    "v_dot2_f32_bf16 v115, v149, v149, v115\nv_dot2_f32_bf16 v116, v150, v241, v116\nv_dot2_f32_bf16 v117, v150, v150, v117\nv_dot2_f32_bf16 v118, v151, v241, v118\n"
    "v_dot2_f32_bf16 v119, v151, v151, v119\nv_dot2_f32_bf16 v112, v152, v241, v112\nv_dot2_f32_bf16 v113, v152, v152, v113\nv_dot2_f32_bf16 v114, v153, v241, v114\n"
    "v_dot2_f32_bf16 v115, v153, v153, v115\nv_dot2_f32_bf16 v116, v154, v241, v116\nv_dot2_f32_bf16 v117, v154, v154, v117\nv_dot2_f32_bf16 v118, v155, v241, v118\n"
    "v_dot2_f32_bf16 v119, v155, v155, v119\nv_dot2_f32_bf16 v112, v156, v241, v112\nv_dot2_f32_bf16 v113, v156, v156, v113\nv_dot2_f32_bf16 v114, v157, v241, v114\n"
    "v_dot2_f32_bf16 v115, v157, v157, v115\nv_dot2_f32_bf16 v116, v158, v241, v116\nv_dot2_f32_bf16 v117, v158, v158, v117\nv_dot2_f32_bf16 v118, v159, v241, v118\n"
    "v_dot2_f32_bf16 v119, v159, v159, v119\nv_pk_add_f32 v[112:113], v[112:113], v[114:115]\ns_nop 1\nv_pk_add_f32 v[116:117], v[116:117], v[118:119]\n"
    "v_pk_add_f32 v[112:113], v[112:113], v[116:117]\nv_cmp_u_f32 vcc, v112, v113\ns_nop 0\nv_permlane32_swap_b32 v112, v113\nv_add_f32 v112, v112, v113\n"
    "s_cbranch_vccnz .Ld144\n.Ld145:\nglobal_store_dword v57, v112, s[4:5] sc1\nv_mfma_f32_32x32x16_bf16 v[64:79], v[208:211], v[144:147], 0\n"
    "ds_read_b64_tr_b16 v[208:209], v242 offset:7168\nds_read_b64_tr_b16 v[210:211], v242 offset:7424\nglobal_load_dword v192, v39, s[58:59] offset:-2048 sc1\n"
    "global_load_dword v193, v39, s[58:59] offset:-1920 sc1\nv_mfma_f32_32x32x16_bf16 v[64:79], v[212:215], v[148:151], v[64:79]\n"
    "ds_read_b64_tr_b16 v[212:213], v242 offset:10240\ns_cbranch_execz .Lhop\nds_read_b64_tr_b16 v[214:215], v242 offset:10496\n"
    "global_load_dword v194, v39, s[58:59] offset:-1792 sc1\nglobal_load_dword v195, v39, s[58:59] offset:-1664 sc1\n"
    "v_mfma_f32_32x32x16_bf16 v[64:79], v[216:219], v[152:155], v[64:79]\nds_read_b64_tr_b16 v[216:217], v242 offset:2048\nds_read_b64_tr_b16 v[218:219], v242 offset:2304\n"
    "global_load_dword v196, v39, s[58:59] offset:-1024 sc1\nglobal_load_dword v197, v39, s[58:59] offset:-896 sc1\ns_waitcnt lgkmcnt(6)\n"
    "v_mfma_f32_32x32x16_bf16 v[64:79], v[220:223], v[156:159], v[64:79]\nds_read_b64_tr_b16 v[220:221], v242 offset:5120\nds_read_b64_tr_b16 v[222:223], v242 offset:5376\n"
    "global_load_dword v198, v39, s[58:59] offset:-768 sc1\nglobal_load_dword v199, v39, s[58:59] offset:-640 sc1\n"
    "v_mfma_f32_32x32x16_bf16 v[80:95], v[224:227], v[144:147], 0\nds_read_b64_tr_b16 v[224:225], v242 offset:8192\nds_read_b64_tr_b16 v[226:227], v242 offset:8448\n"
    "global_load_dword v200, v39, s[58:59] offset:0 sc1\nglobal_load_dword v201, v39, s[58:59] offset:128 sc1\nv_mad_u32_u24 v240, s45, 3, v36\n"
    "v_mfma_f32_32x32x16_bf16 v[80:95], v[228:231], v[148:151], v[80:95]\nds_read_b64_tr_b16 v[228:229], v242 offset:11264\nds_read_b64_tr_b16 v[230:231], v242 offset:11520\n"
    "global_load_dword v202, v39, s[58:59] offset:256 sc1\nglobal_load_dword v203, v39, s[58:59] offset:384 sc1\ns_waitcnt lgkmcnt(6)\n"
    "v_mfma_f32_32x32x16_bf16 v[80:95], v[208:211], v[152:155], v[80:95]\nds_read_b64_tr_b16 v[208:209], v240 offset:0\nds_read_b64_tr_b16 v[210:211], v240 offset:256\n"
    "global_load_dword v204, v39, s[58:59] offset:1024 sc1\nglobal_load_dword v205, v39, s[58:59] offset:1152 sc1\n"
    "v_mfma_f32_32x32x16_bf16 v[80:95], v[212:215], v[156:159], v[80:95]\nds_read_b64_tr_b16 v[212:213], v240 offset:3072\nds_read_b64_tr_b16 v[214:215], v240 offset:3328\n"
    "global_load_dword v206, v39, s[58:59] offset:1280 sc1\nglobal_load_dword v207, v39, s[58:59] offset:1408 sc1\n"
    "v_mfma_f32_32x32x16_bf16 v[96:111], v[216:219], v[144:147], 0\nds_read_b64_tr_b16 v[216:217], v240 offset:6144\nds_read_b64_tr_b16 v[218:219], v240 offset:6400\n"
    "s_waitcnt vmcnt(33)\nv_max3_u32 v244, v160, v161, v162\nv_max3_u32 v243, v163, v164, v165\ns_waitcnt lgkmcnt(6)\n"
    "v_mfma_f32_32x32x16_bf16 v[96:111], v[220:223], v[148:151], v[96:111]\nds_read_b64_tr_b16 v[220:221], v240 offset:9216\nds_read_b64_tr_b16 v[222:223], v240 offset:9472\n"
    "v_max3_u32 v244, v244, v166, v167\nv_max3_u32 v243, v243, v168, v169\nv_max3_u32 v244, v244, v170, v171\n"
    "v_mfma_f32_32x32x16_bf16 v[96:111], v[224:227], v[152:155], v[96:111]\nds_read_b64_tr_b16 v[224:225], v240 offset:1024\nds_read_b64_tr_b16 v[226:227], v240 offset:1280\n"
    "v_max3_u32 v243, v243, v172, v173\nv_max3_u32 v244, v244, v174, v175\nv_max_u32 v243, v243, v244\nv_cmp_eq_u32 vcc, -1, v243\n"
    "v_mfma_f32_32x32x16_bf16 v[96:111], v[228:231], v[156:159], v[96:111]\nds_read_b64_tr_b16 v[228:229], v240 offset:4096\nds_read_b64_tr_b16 v[230:231], v240 offset:4352\n"
    "s_cbranch_vccnz .Lx148\n.Lx149:\ns_waitcnt lgkmcnt(6)\nv_mfma_f32_32x32x16_bf16 v[64:79], v[208:211], v[160:163], v[64:79]\n"
    "ds_read_b64_tr_b16 v[208:209], v240 offset:7168\nds_read_b64_tr_b16 v[210:211], v240 offset:7424\nglobal_load_dword v144, v39, s[60:61] offset:-2048 sc1\n"
    "global_load_dword v145, v39, s[60:61] offset:-1920 sc1\nv_mfma_f32_32x32x16_bf16 v[64:79], v[212:215], v[164:167], v[64:79]\n"
    "ds_read_b64_tr_b16 v[212:213], v240 offset:10240\nds_read_b64_tr_b16 v[214:215], v240 offset:10496\nglobal_load_dword v146, v39, s[60:61] offset:-1792 sc1\n"
    "global_load_dword v147, v39, s[60:61] offset:-1664 sc1\nv_mfma_f32_32x32x16_bf16 v[64:79], v[216:219], v[168:171], v[64:79]\n"
    "ds_read_b64_tr_b16 v[216:217], v240 offset:2048\nds_read_b64_tr_b16 v[218:219], v240 offset:2304\nglobal_load_dword v148, v39, s[60:61] offset:-1024 sc1\n"
    "global_load_dword v149, v39, s[60:61] offset:-896 sc1\ns_waitcnt lgkmcnt(6)\nv_mfma_f32_32x32x16_bf16 v[64:79], v[220:223], v[172:175], v[64:79]\n"
    "ds_read_b64_tr_b16 v[220:221], v240 offset:5120\nds_read_b64_tr_b16 v[222:223], v240 offset:5376\nglobal_load_dword v150, v39, s[60:61] offset:-768 sc1\n"
    "global_load_dword v151, v39, s[60:61] offset:-640 sc1\nv_mfma_f32_32x32x16_bf16 v[80:95], v[224:227], v[160:163], v[80:95]\n"
    "ds_read_b64_tr_b16 v[224:225], v240 offset:8192\nds_read_b64_tr_b16 v[226:227], v240 offset:8448\nglobal_load_dword v152, v39, s[60:61] offset:0 sc1\n"
    "global_load_dword v153, v39, s[60:61] offset:128 sc1\nv_mad_u32_u24 v242, s46, 3, v36\nv_mfma_f32_32x32x16_bf16 v[80:95], v[228:231], v[164:167], v[80:95]\n"
    "ds_read_b64_tr_b16 v[228:229], v240 offset:11264\nds_read_b64_tr_b16 v[230:231], v240 offset:11520\nglobal_load_dword v154, v39, s[60:61] offset:256 sc1\n"
    "global_load_dword v155, v39, s[60:61] offset:384 sc1\ns_waitcnt lgkmcnt(6)\nv_mfma_f32_32x32x16_bf16 v[80:95], v[208:211], v[168:171], v[80:95]\n"
    "ds_read_b64_tr_b16 v[208:209], v242 offset:0\nds_read_b64_tr_b16 v[210:211], v242 offset:256\nglobal_load_dword v156, v39, s[60:61] offset:1024 sc1\n"
    "global_load_dword v157, v39, s[60:61] offset:1152 sc1\nv_mfma_f32_32x32x16_bf16 v[80:95], v[212:215], v[172:175], v[80:95]\n"
    "ds_read_b64_tr_b16 v[212:213], v242 offset:3072\nds_read_b64_tr_b16 v[214:215], v242 offset:3328\nglobal_load_dword v158, v39, s[60:61] offset:1280 sc1\n"
    "global_load_dword v159, v39, s[60:61] offset:1408 sc1\nv_mfma_f32_32x32x16_bf16 v[96:111], v[216:219], v[160:163], v[96:111]\n"
    "ds_read_b64_tr_b16 v[216:217], v242 offset:6144\nds_read_b64_tr_b16 v[218:219], v242 offset:6400\ns_waitcnt vmcnt(33)\nv_max3_u32 v244, v176, v177, v178\n"
    "v_max3_u32 v243, v179, v180, v181\ns_waitcnt lgkmcnt(6)\nv_mfma_f32_32x32x16_bf16 v[96:111], v[220:223], v[164:167], v[96:111]\n"
    "ds_read_b64_tr_b16 v[220:221], v242 offset:9216\nds_read_b64_tr_b16 v[222:223], v242 offset:9472\nv_max3_u32 v244, v244, v182, v183\nv_max3_u32 v243, v243, v184, v185\n"
    "v_max3_u32 v244, v244, v186, v187\nv_mfma_f32_32x32x16_bf16 v[96:111], v[224:227], v[168:171], v[96:111]\nds_read_b64_tr_b16 v[224:225], v242 offset:1024\n"
    "ds_read_b64_tr_b16 v[226:227], v242 offset:1280\nv_max3_u32 v243, v243, v188, v189\nv_max3_u32 v244, v244, v190, v191\nv_max_u32 v243, v243, v244\n"
    "v_cmp_eq_u32 vcc, -1, v243\nv_mfma_f32_32x32x16_bf16 v[96:111], v[228:231], v[172:175], v[96:111]\nds_read_b64_tr_b16 v[228:229], v242 offset:4096\n"
    "ds_read_b64_tr_b16 v[230:231], v242 offset:4352\ns_cbranch_vccnz .Lx150\n.Lx151:\ns_waitcnt lgkmcnt(6)\n"
    "v_mfma_f32_32x32x16_bf16 v[64:79], v[208:211], v[176:179], v[64:79]\nds_read_b64_tr_b16 v[208:209], v242 offset:7168\nds_read_b64_tr_b16 v[210:211], v242 offset:7424\n"
    "global_load_dword v160, v39, s[62:63] offset:-2048 sc1\nglobal_load_dword v161, v39, s[62:63] offset:-1920 sc1\n"
    "v_mfma_f32_32x32x16_bf16 v[64:79], v[212:215], v[180:183], v[64:79]\ns_nop 0\ns_cbranch_execz .Lhop\nds_read_b64_tr_b16 v[212:213], v242 offset:10240\n"
    "ds_read_b64_tr_b16 v[214:215], v242 offset:10496\nglobal_load_dword v162, v39, s[62:63] offset:-1792 sc1\nglobal_load_dword v163, v39, s[62:63] offset:-1664 sc1\n"
    "v_mfma_f32_32x32x16_bf16 v[64:79], v[216:219], v[184:187], v[64:79]\nds_read_b64_tr_b16 v[216:217], v242 offset:2048\nds_read_b64_tr_b16 v[218:219], v242 offset:2304\n"
    "global_load_dword v164, v39, s[62:63] offset:-1024 sc1\nglobal_load_dword v165, v39, s[62:63] offset:-896 sc1\ns_waitcnt lgkmcnt(6)\n"
    "v_mfma_f32_32x32x16_bf16 v[64:79], v[220:223], v[188:191], v[64:79]\nds_read_b64_tr_b16 v[220:221], v242 offset:5120\nds_read_b64_tr_b16 v[222:223], v242 offset:5376\n"
    "global_load_dword v166, v39, s[62:63] offset:-768 sc1\nglobal_load_dword v167, v39, s[62:63] offset:-640 sc1\n"
    "v_mfma_f32_32x32x16_bf16 v[80:95], v[224:227], v[176:179], v[80:95]\nds_read_b64_tr_b16 v[224:225], v242 offset:8192\nds_read_b64_tr_b16 v[226:227], v242 offset:8448\n"
    "global_load_dword v168, v39, s[62:63] offset:0 sc1\nglobal_load_dword v169, v39, s[62:63] offset:128 sc1\nv_mad_u32_u24 v240, s47, 3, v36\n"
    "v_mfma_f32_32x32x16_bf16 v[80:95], v[228:231], v[180:183], v[80:95]\nds_read_b64_tr_b16 v[228:229], v242 offset:11264\nds_read_b64_tr_b16 v[230:231], v242 offset:11520\n"
    "global_load_dword v170, v39, s[62:63] offset:256 sc1\nglobal_load_dword v171, v39, s[62:63] offset:384 sc1\ns_waitcnt lgkmcnt(6)\n"
    "v_mfma_f32_32x32x16_bf16 v[80:95], v[208:211], v[184:187], v[80:95]\nds_read_b64_tr_b16 v[208:209], v240 offset:0\nds_read_b64_tr_b16 v[210:211], v240 offset:256\n"
    "global_load_dword v172, v39, s[62:63] offset:1024 sc1\nglobal_load_dword v173, v39, s[62:63] offset:1152 sc1\n"
    "v_mfma_f32_32x32x16_bf16 v[80:95], v[212:215], v[188:191], v[80:95]\nds_read_b64_tr_b16 v[212:213], v240 offset:3072\nds_read_b64_tr_b16 v[214:215], v240 offset:3328\n"
    "global_load_dword v174, v39, s[62:63] offset:1280 sc1\nglobal_load_dword v175, v39, s[62:63] offset:1408 sc1\n"
    "v_mfma_f32_32x32x16_bf16 v[96:111], v[216:219], v[176:179], v[96:111]\nds_read_b64_tr_b16 v[216:217], v240 offset:6144\nds_read_b64_tr_b16 v[218:219], v240 offset:6400\n"
    "s_waitcnt vmcnt(32)\nv_max3_u32 v244, v192, v193, v194\nv_max3_u32 v243, v195, v196, v197\ns_waitcnt lgkmcnt(6)\n"
    "v_mfma_f32_32x32x16_bf16 v[96:111], v[220:223], v[180:183], v[96:111]\nds_read_b64_tr_b16 v[220:221], v240 offset:9216\nds_read_b64_tr_b16 v[222:223], v240 offset:9472\n"
    "v_max3_u32 v244, v244, v198, v199\nv_max3_u32 v243, v243, v200, v201\nv_max3_u32 v244, v244, v202, v203\n"
    "v_mfma_f32_32x32x16_bf16 v[96:111], v[224:227], v[184:187], v[96:111]\nds_read_b64_tr_b16 v[224:225], v240 offset:1024\nds_read_b64_tr_b16 v[226:227], v240 offset:1280\n"
    "v_max3_u32 v243, v243, v204, v205\nv_max3_u32 v244, v244, v206, v207\nv_max_u32 v243, v243, v244\nv_cmp_eq_u32 vcc, -1, v243\n"
    "v_mfma_f32_32x32x16_bf16 v[96:111], v[228:231], v[188:191], v[96:111]\nds_read_b64_tr_b16 v[228:229], v240 offset:4096\nds_read_b64_tr_b16 v[230:231], v240 offset:4352\n"
    "s_cbranch_vccnz .Lx152\n.Lx153:\ns_waitcnt lgkmcnt(6)\nv_mfma_f32_32x32x16_bf16 v[64:79], v[208:211], v[192:195], v[64:79]\n"
    "ds_read_b64_tr_b16 v[208:209], v240 offset:7168\nds_read_b64_tr_b16 v[210:211], v240 offset:7424\nglobal_load_dword v176, v39, s[64:65] offset:-2048 sc1\n"
    "global_load_dword v177, v39, s[64:65] offset:-1920 sc1\nv_mfma_f32_32x32x16_bf16 v[64:79], v[212:215], v[196:199], v[64:79]\n"
    "ds_read_b64_tr_b16 v[212:213], v240 offset:10240\nds_read_b64_tr_b16 v[214:215], v240 offset:10496\nglobal_load_dword v178, v39, s[64:65] offset:-1792 sc1\n"
    "global_load_dword v179, v39, s[64:65] offset:-1664 sc1\nv_mfma_f32_32x32x16_bf16 v[64:79], v[216:219], v[200:203], v[64:79]\n"
    "ds_read_b64_tr_b16 v[216:217], v240 offset:2048\nds_read_b64_tr_b16 v[218:219], v240 offset:2304\nglobal_load_dword v180, v39, s[64:65] offset:-1024 sc1\n"
    "global_load_dword v181, v39, s[64:65] offset:-896 sc1\ns_waitcnt lgkmcnt(6)\nv_mfma_f32_32x32x16_bf16 v[64:79], v[220:223], v[204:207], v[64:79]\n"
    "ds_read_b64_tr_b16 v[220:221], v240 offset:5120\nds_read_b64_tr_b16 v[222:223], v240 offset:5376\nglobal_load_dword v182, v39, s[64:65] offset:-768 sc1\n"
    "global_load_dword v183, v39, s[64:65] offset:-640 sc1\nv_mfma_f32_32x32x16_bf16 v[80:95], v[224:227], v[192:195], v[80:95]\n"
    "ds_read_b64_tr_b16 v[224:225], v240 offset:8192\nds_read_b64_tr_b16 v[226:227], v240 offset:8448\nglobal_load_dword v184, v39, s[64:65] offset:0 sc1\n"
    "global_load_dword v185, v39, s[64:65] offset:128 sc1\nv_mad_u32_u24 v242, s48, 3, v36\nv_mfma_f32_32x32x16_bf16 v[80:95], v[228:231], v[196:199], v[80:95]\n"
    "ds_read_b64_tr_b16 v[228:229], v240 offset:11264\nds_read_b64_tr_b16 v[230:231], v240 offset:11520\nglobal_load_dword v186, v39, s[64:65] offset:256 sc1\n"
    "global_load_dword v187, v39, s[64:65] offset:384 sc1\ns_waitcnt lgkmcnt(6)\nv_mfma_f32_32x32x16_bf16 v[80:95], v[208:211], v[200:203], v[80:95]\n"
    "ds_read_b64_tr_b16 v[208:209], v242 offset:0\nds_read_b64_tr_b16 v[210:211], v242 offset:256\nglobal_load_dword v188, v39, s[64:65] offset:1024 sc1\n"
    "global_load_dword v189, v39, s[64:65] offset:1152 sc1\nv_mfma_f32_32x32x16_bf16 v[80:95], v[212:215], v[204:207], v[80:95]\n"
    "ds_read_b64_tr_b16 v[212:213], v242 offset:3072\nds_read_b64_tr_b16 v[214:215], v242 offset:3328\nglobal_load_dword v190, v39, s[64:65] offset:1280 sc1\n"
    "global_load_dword v191, v39, s[64:65] offset:1408 sc1\nv_mfma_f32_32x32x16_bf16 v[96:111], v[216:219], v[192:195], v[96:111]\n"
    "ds_read_b64_tr_b16 v[216:217], v242 offset:6144\nds_read_b64_tr_b16 v[218:219], v242 offset:6400\ns_waitcnt vmcnt(32)\nv_max3_u32 v244, v144, v145, v146\n"
    "v_max3_u32 v243, v147, v148, v149\ns_waitcnt lgkmcnt(6)\nv_mfma_f32_32x32x16_bf16 v[96:111], v[220:223], v[196:199], v[96:111]\n"
    "ds_read_b64_tr_b16 v[220:221], v242 offset:9216\nds_read_b64_tr_b16 v[222:223], v242 offset:9472\nv_max3_u32 v244, v244, v150, v151\nv_max3_u32 v243, v243, v152, v153\n"
    "v_max3_u32 v244, v244, v154, v155\nv_mfma_f32_32x32x16_bf16 v[96:111], v[224:227], v[200:203], v[96:111]\nds_read_b64_tr_b16 v[224:225], v242 offset:1024\n"
    "ds_read_b64_tr_b16 v[226:227], v242 offset:1280\nv_max3_u32 v243, v243, v156, v157\nv_max3_u32 v244, v244, v158, v159\nv_max_u32 v243, v243, v244\n"
    "v_cmp_eq_u32 vcc, -1, v243\nv_mfma_f32_32x32x16_bf16 v[96:111], v[228:231], v[204:207], v[96:111]\nds_read_b64_tr_b16 v[228:229], v242 offset:4096\n"
    "ds_read_b64_tr_b16 v[230:231], v242 offset:4352\ns_cbranch_vccnz .Lx154\n.Lx155:\ns_waitcnt lgkmcnt(6)\n"
    "v_mfma_f32_32x32x16_bf16 v[64:79], v[208:211], v[144:147], v[64:79]\nds_read_b64_tr_b16 v[208:209], v242 offset:7168\nds_read_b64_tr_b16 v[210:211], v242 offset:7424\n"
    "global_load_dword v192, v39, s[66:67] offset:-2048 sc1\nglobal_load_dword v193, v39, s[66:67] offset:-1920 sc1\ns_nop 0\ns_cbranch_execz .Lhop\n"
    "v_mfma_f32_32x32x16_bf16 v[64:79], v[212:215], v[148:151], v[64:79]\nds_read_b64_tr_b16 v[212:213], v242 offset:10240\nds_read_b64_tr_b16 v[214:215], v242 offset:10496\n"
    "global_load_dword v194, v39, s[66:67] offset:-1792 sc1\nglobal_load_dword v195, v39, s[66:67] offset:-1664 sc1\n"
    "v_mfma_f32_32x32x16_bf16 v[64:79], v[216:219], v[152:155], v[64:79]\nds_read_b64_tr_b16 v[216:217], v242 offset:2048\nds_read_b64_tr_b16 v[218:219], v242 offset:2304\n"
    "global_load_dword v196, v39, s[66:67] offset:-1024 sc1\nglobal_load_dword v197, v39, s[66:67] offset:-896 sc1\ns_waitcnt lgkmcnt(6)\n"
    "v_mfma_f32_32x32x16_bf16 v[64:79], v[220:223], v[156:159], v[64:79]\nds_read_b64_tr_b16 v[220:221], v242 offset:5120\nds_read_b64_tr_b16 v[222:223], v242 offset:5376\n"
    "global_load_dword v198, v39, s[66:67] offset:-768 sc1\nglobal_load_dword v199, v39, s[66:67] offset:-640 sc1\n"
    "v_mfma_f32_32x32x16_bf16 v[80:95], v[224:227], v[144:147], v[80:95]\nds_read_b64_tr_b16 v[224:225], v242 offset:8192\nds_read_b64_tr_b16 v[226:227], v242 offset:8448\n"
    "global_load_dword v200, v39, s[66:67] offset:0 sc1\nglobal_load_dword v201, v39, s[66:67] offset:128 sc1\nv_mad_u32_u24 v240, s49, 3, v36\n"
    "v_mfma_f32_32x32x16_bf16 v[80:95], v[228:231], v[148:151], v[80:95]\nds_read_b64_tr_b16 v[228:229], v242 offset:11264\nds_read_b64_tr_b16 v[230:231], v242 offset:11520\n"
    "global_load_dword v202, v39, s[66:67] offset:256 sc1\nglobal_load_dword v203, v39, s[66:67] offset:384 sc1\ns_waitcnt lgkmcnt(6)\n"
    "v_mfma_f32_32x32x16_bf16 v[80:95], v[208:211], v[152:155], v[80:95]\nds_read_b64_tr_b16 v[208:209], v240 offset:0\nds_read_b64_tr_b16 v[210:211], v240 offset:256\n"
    "global_load_dword v204, v39, s[66:67] offset:1024 sc1\nglobal_load_dword v205, v39, s[66:67] offset:1152 sc1\n"
    "v_mfma_f32_32x32x16_bf16 v[80:95], v[212:215], v[156:159], v[80:95]\nds_read_b64_tr_b16 v[212:213], v240 offset:3072\nds_read_b64_tr_b16 v[214:215], v240 offset:3328\n"
    "global_load_dword v206, v39, s[66:67] offset:1280 sc1\nglobal_load_dword v207, v39, s[66:67] offset:1408 sc1\n"
    "v_mfma_f32_32x32x16_bf16 v[96:111], v[216:219], v[144:147], v[96:111]\nds_read_b64_tr_b16 v[216:217], v240 offset:6144\nds_read_b64_tr_b16 v[218:219], v240 offset:6400\n"
    "s_waitcnt vmcnt(32)\nv_max3_u32 v244, v160, v161, v162\nv_max3_u32 v243, v163, v164, v165\ns_waitcnt lgkmcnt(6)\n"
    "v_mfma_f32_32x32x16_bf16 v[96:111], v[220:223], v[148:151], v[96:111]\nds_read_b64_tr_b16 v[220:221], v240 offset:9216\nds_read_b64_tr_b16 v[222:223], v240 offset:9472\n"
    "v_max3_u32 v244, v244, v166, v167\nv_max3_u32 v243, v243, v168, v169\nv_max3_u32 v244, v244, v170, v171\n"
    "v_mfma_f32_32x32x16_bf16 v[96:111], v[224:227], v[152:155], v[96:111]\nds_read_b64_tr_b16 v[224:225], v240 offset:1024\nds_read_b64_tr_b16 v[226:227], v240 offset:1280\n"
    "v_max3_u32 v243, v243, v172, v173\nv_max3_u32 v244, v244, v174, v175\nv_max_u32 v243, v243, v244\nv_cmp_eq_u32 vcc, -1, v243\n"
    "v_mfma_f32_32x32x16_bf16 v[96:111], v[228:231], v[156:159], v[96:111]\nds_read_b64_tr_b16 v[228:229], v240 offset:4096\nds_read_b64_tr_b16 v[230:231], v240 offset:4352\n"
    "s_cbranch_vccnz .Lx156\n.Lx157:\ns_waitcnt lgkmcnt(6)\nv_mfma_f32_32x32x16_bf16 v[64:79], v[208:211], v[160:163], v[64:79]\n"
    "ds_read_b64_tr_b16 v[208:209], v240 offset:7168\nds_read_b64_tr_b16 v[210:211], v240 offset:7424\nglobal_load_dword v112, v56, s[4:5] offset:0 sc1\n"
    "global_load_dword v113, v56, s[4:5] offset:256 sc1\nv_mfma_f32_32x32x16_bf16 v[64:79], v[212:215], v[164:167], v[64:79]\n"
    "ds_read_b64_tr_b16 v[212:213], v240 offset:10240\nds_read_b64_tr_b16 v[214:215], v240 offset:10496\nglobal_load_dword v114, v56, s[4:5] offset:512 sc1\n"
    "global_load_dword v115, v56, s[4:5] offset:768 sc1\nv_mfma_f32_32x32x16_bf16 v[64:79], v[216:219], v[168:171], v[64:79]\nds_read_b64_tr_b16 v[216:217], v240 offset:2048\n"
    "ds_read_b64_tr_b16 v[218:219], v240 offset:2304\nglobal_load_dword v116, v56, s[4:5] offset:1024 sc1\nglobal_load_dword v117, v56, s[4:5] offset:1280 sc1\n"
    "s_waitcnt lgkmcnt(6)\nv_mfma_f32_32x32x16_bf16 v[64:79], v[220:223], v[172:175], v[64:79]\nds_read_b64_tr_b16 v[220:221], v240 offset:5120\n"
    "ds_read_b64_tr_b16 v[222:223], v240 offset:5376\nglobal_load_dword v118, v56, s[4:5] offset:1536 sc1\nglobal_load_dword v119, v56, s[4:5] offset:1792 sc1\n"
    "v_mfma_f32_32x32x16_bf16 v[80:95], v[224:227], v[160:163], v[80:95]\nds_read_b64_tr_b16 v[224:225], v240 offset:8192\nds_read_b64_tr_b16 v[226:227], v240 offset:8448\n"
    "v_mad_u32_u24 v242, s50, 3, v36\nv_mfma_f32_32x32x16_bf16 v[80:95], v[228:231], v[164:167], v[80:95]\nds_read_b64_tr_b16 v[228:229], v240 offset:11264\n"
    "ds_read_b64_tr_b16 v[230:231], v240 offset:11520\ns_waitcnt lgkmcnt(6)\nv_mfma_f32_32x32x16_bf16 v[80:95], v[208:211], v[168:171], v[80:95]\n"
    "ds_read_b64_tr_b16 v[208:209], v242 offset:0\nds_read_b64_tr_b16 v[210:211], v242 offset:256\nv_mfma_f32_32x32x16_bf16 v[80:95], v[212:215], v[172:175], v[80:95]\n"
    "ds_read_b64_tr_b16 v[212:213], v242 offset:3072\nds_read_b64_tr_b16 v[214:215], v242 offset:3328\nv_mfma_f32_32x32x16_bf16 v[96:111], v[216:219], v[160:163], v[96:111]\n"
    "ds_read_b64_tr_b16 v[216:217], v242 offset:6144\nds_read_b64_tr_b16 v[218:219], v242 offset:6400\ns_waitcnt vmcnt(24)\nv_max3_u32 v244, v176, v177, v178\n"
    "v_max3_u32 v243, v179, v180, v181\ns_waitcnt lgkmcnt(6)\nv_mfma_f32_32x32x16_bf16 v[96:111], v[220:223], v[164:167], v[96:111]\n"
    "ds_read_b64_tr_b16 v[220:221], v242 offset:9216\nds_read_b64_tr_b16 v[222:223], v242 offset:9472\nv_max3_u32 v244, v244, v182, v183\nv_max3_u32 v243, v243, v184, v185\n"
    "v_max3_u32 v244, v244, v186, v187\nv_mfma_f32_32x32x16_bf16 v[96:111], v[224:227], v[168:171], v[96:111]\nds_read_b64_tr_b16 v[224:225], v242 offset:1024\n"
    "ds_read_b64_tr_b16 v[226:227], v242 offset:1280\nv_max3_u32 v243, v243, v188, v189\nv_max3_u32 v244, v244, v190, v191\nv_max_u32 v243, v243, v244\n"
    "v_cmp_eq_u32 vcc, -1, v243\nv_mfma_f32_32x32x16_bf16 v[96:111], v[228:231], v[172:175], v[96:111]\nds_read_b64_tr_b16 v[228:229], v242 offset:4096\n"
    "ds_read_b64_tr_b16 v[230:231], v242 offset:4352\ns_cbranch_vccnz .Lx158\n.Lx159:\ns_waitcnt lgkmcnt(6)\n"
    "v_mfma_f32_32x32x16_bf16 v[64:79], v[208:211], v[176:179], v[64:79]\nds_read_b64_tr_b16 v[208:209], v242 offset:7168\nds_read_b64_tr_b16 v[210:211], v242 offset:7424\n"
    "v_mfma_f32_32x32x16_bf16 v[64:79], v[212:215], v[180:183], v[64:79]\nds_read_b64_tr_b16 v[212:213], v242 offset:10240\nds_read_b64_tr_b16 v[214:215], v242 offset:10496\n"
    "v_mfma_f32_32x32x16_bf16 v[64:79], v[216:219], v[184:187], v[64:79]\nds_read_b64_tr_b16 v[216:217], v242 offset:2048\nds_read_b64_tr_b16 v[218:219], v242 offset:2304\n"
    "s_waitcnt lgkmcnt(6)\nv_mfma_f32_32x32x16_bf16 v[64:79], v[220:223], v[188:191], v[64:79]\nds_read_b64_tr_b16 v[220:221], v242 offset:5120\n"
    "ds_read_b64_tr_b16 v[222:223], v242 offset:5376\ns_cbranch_execz .Lhop\nv_mfma_f32_32x32x16_bf16 v[80:95], v[224:227], v[176:179], v[80:95]\n"
    "ds_read_b64_tr_b16 v[224:225], v242 offset:8192\nds_read_b64_tr_b16 v[226:227], v242 offset:8448\nv_mad_u32_u24 v240, s51, 3, v36\n"
    "v_mfma_f32_32x32x16_bf16 v[80:95], v[228:231], v[180:183], v[80:95]\nds_read_b64_tr_b16 v[228:229], v242 offset:11264\nds_read_b64_tr_b16 v[230:231], v242 offset:11520\n"
    "s_waitcnt lgkmcnt(6)\nv_mfma_f32_32x32x16_bf16 v[80:95], v[208:211], v[184:187], v[80:95]\nds_read_b64_tr_b16 v[208:209], v240 offset:0\n"
    "ds_read_b64_tr_b16 v[210:211], v240 offset:256\nv_mfma_f32_32x32x16_bf16 v[80:95], v[212:215], v[188:191], v[80:95]\nds_read_b64_tr_b16 v[212:213], v240 offset:3072\n"
    "ds_read_b64_tr_b16 v[214:215], v240 offset:3328\nv_mfma_f32_32x32x16_bf16 v[96:111], v[216:219], v[176:179], v[96:111]\nds_read_b64_tr_b16 v[216:217], v240 offset:6144\n"
    "ds_read_b64_tr_b16 v[218:219], v240 offset:6400\ns_waitcnt vmcnt(8)\nv_max3_u32 v244, v192, v193, v194\nv_max3_u32 v243, v195, v196, v197\ns_waitcnt lgkmcnt(6)\n"
    "v_mfma_f32_32x32x16_bf16 v[96:111], v[220:223], v[180:183], v[96:111]\nds_read_b64_tr_b16 v[220:221], v240 offset:9216\nds_read_b64_tr_b16 v[222:223], v240 offset:9472\n"
    "v_max3_u32 v244, v244, v198, v199\nv_max3_u32 v243, v243, v200, v201\nv_max3_u32 v244, v244, v202, v203\n"
    "v_mfma_f32_32x32x16_bf16 v[96:111], v[224:227], v[184:187], v[96:111]\nds_read_b64_tr_b16 v[224:225], v240 offset:1024\nds_read_b64_tr_b16 v[226:227], v240 offset:1280\n"
    "v_max3_u32 v243, v243, v204, v205\nv_max3_u32 v244, v244, v206, v207\nv_max_u32 v243, v243, v244\nv_cmp_eq_u32 vcc, -1, v243\n"
    "v_mfma_f32_32x32x16_bf16 v[96:111], v[228:231], v[188:191], v[96:111]\nds_read_b64_tr_b16 v[228:229], v240 offset:4096\nds_read_b64_tr_b16 v[230:231], v240 offset:4352\n"
    "s_cbranch_vccnz .Lx160\n.Lx161:\ns_waitcnt lgkmcnt(6)\nv_mfma_f32_32x32x16_bf16 v[64:79], v[208:211], v[192:195], v[64:79]\n"
    "ds_read_b64_tr_b16 v[208:209], v240 offset:7168\nds_read_b64_tr_b16 v[210:211], v240 offset:7424\ns_waitcnt vmcnt(0)\nv_add_f32 v138, v112, v113\n"
    "v_add_f32 v139, v114, v115\nv_add_f32 v244, v116, v117\nv_mfma_f32_32x32x16_bf16 v[64:79], v[212:215], v[196:199], v[64:79]\n"
    "ds_read_b64_tr_b16 v[212:213], v240 offset:10240\nds_read_b64_tr_b16 v[214:215], v240 offset:10496\nv_add_f32 v243, v118, v119\nv_add_f32 v138, v138, v139\n"
    "v_add_f32 v244, v244, v243\nv_add_f32 v139, v138, v244\nv_mfma_f32_32x32x16_bf16 v[64:79], v[216:219], v[200:203], v[64:79]\n"
    "ds_read_b64_tr_b16 v[216:217], v240 offset:2048\nds_read_b64_tr_b16 v[218:219], v240 offset:2304\nv_add_f32 v138, v138, v244\nv_cmp_u_f32 vcc, v138, v138\n"
    "s_waitcnt lgkmcnt(6)\nv_mfma_f32_32x32x16_bf16 v[64:79], v[220:223], v[204:207], v[64:79]\nds_read_b64_tr_b16 v[220:221], v240 offset:5120\n"
    "ds_read_b64_tr_b16 v[222:223], v240 offset:5376\ns_cbranch_vccnz .Lp146\n.Lp147:\nv_permlane32_swap_b32 v138, v139\nv_mul_f32 v138, s81, v138\n"
    "v_mul_f32 v112, v138, v138\nv_mfma_f32_32x32x16_bf16 v[80:95], v[224:227], v[192:195], v[80:95]\nds_read_b64_tr_b16 v[224:225], v240 offset:8192\n"
    "ds_read_b64_tr_b16 v[226:227], v240 offset:8448\nv_fma_f32 v139, v139, s81, -v112\nv_max_f32 v139, 0, v139\nv_cvt_pk_bf16_f32 v139, 0, v139\n"
    "v_mfma_f32_32x32x16_bf16 v[80:95], v[228:231], v[196:199], v[80:95]\nds_read_b64_tr_b16 v[228:229], v240 offset:11264\nds_read_b64_tr_b16 v[230:231], v240 offset:11520\n"
    "v_add_f32 v139, 0x3727c5ac, v139\nv_cvt_pk_bf16_f32 v139, 0, v139\ns_waitcnt lgkmcnt(8)\nv_mfma_f32_32x32x16_bf16 v[80:95], v[208:211], v[200:203], v[80:95]\n"
    "v_sqrt_f32 v139, v139\nv_cvt_pk_bf16_f32 v138, -v138, -v138\nv_mfma_f32_32x32x16_bf16 v[80:95], v[212:215], v[204:207], v[80:95]\nv_cndmask_b32 v120, 0, v138, s[84:85]\n"
    "v_cvt_pk_bf16_f32 v139, 0, v139\nv_rcp_f32 v136, v139\ns_waitcnt lgkmcnt(0)\nv_mov_b32 v137, v136\nv_mfma_f32_32x32x16_bf16 v[64:79], v[124:127], v[120:123], v[64:79]\n"
    "v_mfma_f32_32x32x16_bf16 v[80:95], v[128:131], v[120:123], v[80:95]\nv_mfma_f32_32x32x16_bf16 v[96:111], v[216:219], v[192:195], v[96:111]\nv_mov_b64 v[232:233], 0\n"
    "v_mov_b64 v[234:235], 0\nv_mov_b64 v[236:237], 0\nv_mov_b64 v[238:239], 0\nv_mov_b64 v[244:245], 0\nv_mov_b64 v[246:247], 0\nv_mov_b64 v[248:249], 0\n"
    "v_mov_b64 v[250:251], 0\ns_mov_b32 s44, 0x00ff00ff\ns_mov_b32 s45, 0xff00ff00\nv_mul_f32 v64, v64, v136\nv_mul_f32 v65, v65, v136\nv_mul_f32 v66, v66, v136\n"
    "v_mul_f32 v67, v67, v136\nv_cvt_pk_bf16_f32 v64, v64, v65\nv_cvt_pk_bf16_f32 v65, v66, v67\nds_write_b64 v37, v[64:65] offset:0\n"
    "v_mfma_f32_32x32x16_bf16 v[96:111], v[220:223], v[196:199], v[96:111]\nv_mul_f32 v68, v68, v136\nv_mul_f32 v69, v69, v136\nv_mul_f32 v70, v70, v136\n"
    "v_mul_f32 v71, v71, v136\nv_cvt_pk_bf16_f32 v68, v68, v69\nv_cvt_pk_bf16_f32 v69, v70, v71\nds_write_b64 v37, v[68:69] offset:16\n"
    "v_mfma_f32_32x32x16_bf16 v[96:111], v[224:227], v[200:203], v[96:111]\nv_mul_f32 v72, v72, v136\nv_mul_f32 v73, v73, v136\nv_mul_f32 v74, v74, v136\n"
    "v_mul_f32 v75, v75, v136\nv_cvt_pk_bf16_f32 v72, v72, v73\nv_cvt_pk_bf16_f32 v73, v74, v75\nds_write_b64 v37, v[72:73] offset:32\n"
    "v_mfma_f32_32x32x16_bf16 v[96:111], v[228:231], v[204:207], v[96:111]\nv_mul_f32 v76, v76, v136\nv_mul_f32 v77, v77, v136\nv_mul_f32 v78, v78, v136\n"
    "v_mul_f32 v79, v79, v136\nv_cvt_pk_bf16_f32 v76, v76, v77\nv_cvt_pk_bf16_f32 v77, v78, v79\nds_write_b64 v37, v[76:77] offset:48\n"
    "v_mfma_f32_32x32x16_bf16 v[96:111], v[132:135], v[120:123], v[96:111]\ns_mov_b32 s46, s44\ns_mov_b32 s47, s44\nv_pk_mul_f32 v[80:81], v[80:81], v[136:137]\n"
    "v_pk_mul_f32 v[82:83], v[82:83], v[136:137]\nv_cvt_pk_bf16_f32 v80, v80, v81\nv_cvt_pk_bf16_f32 v81, v82, v83\nds_write_b64 v37, v[80:81] offset:64\n"
    "v_pk_mul_f32 v[84:85], v[84:85], v[136:137]\nv_pk_mul_f32 v[86:87], v[86:87], v[136:137]\nv_cvt_pk_bf16_f32 v84, v84, v85\nv_cvt_pk_bf16_f32 v85, v86, v87\n"
    "ds_write_b64 v37, v[84:85] offset:80\nv_pk_mul_f32 v[88:89], v[88:89], v[136:137]\nv_pk_mul_f32 v[90:91], v[90:91], v[136:137]\nv_cvt_pk_bf16_f32 v88, v88, v89\n"
    "v_cvt_pk_bf16_f32 v89, v90, v91\nds_write_b64 v37, v[88:89] offset:96\nv_pk_mul_f32 v[92:93], v[92:93], v[136:137]\nv_pk_mul_f32 v[94:95], v[94:95], v[136:137]\n"
    "v_cvt_pk_bf16_f32 v92, v92, v93\nv_cvt_pk_bf16_f32 v93, v94, v95\nds_write_b64 v37, v[92:93] offset:112\ns_nop 0\ns_cbranch_execz .Lhop\n"
    "v_pk_mul_f32 v[96:97], v[96:97], v[136:137]\nv_pk_mul_f32 v[98:99], v[98:99], v[136:137]\nv_cvt_pk_bf16_f32 v96, v96, v97\nv_cvt_pk_bf16_f32 v97, v98, v99\n"
    "ds_write_b64 v37, v[96:97] offset:128\nv_pk_mul_f32 v[100:101], v[100:101], v[136:137]\nv_pk_mul_f32 v[102:103], v[102:103], v[136:137]\n"
    "v_cvt_pk_bf16_f32 v100, v100, v101\nv_cvt_pk_bf16_f32 v101, v102, v103\nds_write_b64 v37, v[100:101] offset:144\nv_pk_mul_f32 v[104:105], v[104:105], v[136:137]\n"
    "v_pk_mul_f32 v[106:107], v[106:107], v[136:137]\nv_cvt_pk_bf16_f32 v104, v104, v105\nv_cvt_pk_bf16_f32 v105, v106, v107\nds_write_b64 v37, v[104:105] offset:160\n"
    "v_pk_mul_f32 v[108:109], v[108:109], v[136:137]\nv_pk_mul_f32 v[110:111], v[110:111], v[136:137]\nv_cvt_pk_bf16_f32 v108, v108, v109\nv_cvt_pk_bf16_f32 v109, v110, v111\n"
    "ds_write_b64 v37, v[108:109] offset:176\ns_branch .Latt12\n"
    ".La67:\ns_waitcnt vmcnt(0)\ns_branch .La69\n.Lw87:\ns_sub_u32 s80, s80, 1\ns_cbranch_scc1 .Lw87x\ns_mov_b64 exec, vcc\n"
    "global_load_dwordx4 v[208:211], v60, s[4:5] offset:-4096 sc1\nglobal_load_dwordx4 v[212:215], v60, s[4:5] offset:0 sc1\ns_mov_b64 exec, -1\ns_waitcnt vmcnt(0)\n"
    "s_branch .Lr86\n.Lw89:\ns_sub_u32 s80, s80, 1\ns_cbranch_scc1 .Lw89x\ns_mov_b64 exec, vcc\nglobal_load_dwordx4 v[216:219], v61, s[4:5] offset:-4096 sc1\n"
    "global_load_dwordx4 v[220:223], v61, s[4:5] offset:0 sc1\ns_mov_b64 exec, -1\ns_waitcnt vmcnt(0)\ns_branch .Lr88\n.Lw91:\ns_sub_u32 s80, s80, 1\ns_cbranch_scc1 .Lw91x\n"
    "s_mov_b64 exec, vcc\nglobal_load_dwordx4 v[224:227], v62, s[4:5] offset:-4096 sc1\nglobal_load_dwordx4 v[228:231], v62, s[4:5] offset:0 sc1\ns_mov_b64 exec, -1\n"
    "s_waitcnt vmcnt(0)\ns_branch .Lr90\n.Lw93:\ns_sub_u32 s80, s80, 1\ns_cbranch_scc1 .Lw93x\ns_mov_b64 exec, vcc\n"
    "global_load_dwordx4 v[232:235], v63, s[4:5] offset:-4096 sc1\nglobal_load_dwordx4 v[236:239], v63, s[4:5] offset:0 sc1\ns_mov_b64 exec, -1\ns_waitcnt vmcnt(0)\n"
    "s_branch .Lr92\n.Lh101:\ns_cmp_lg_u32 s13, 0\ns_cbranch_scc1 .Lh104\ns_mov_b32 s80, 0x10000\nv_mov_b32 v225, 18\n.Lh103:\n"
    "global_load_dwordx4 v[252:255], v57, s[4:5] sc1\ns_waitcnt vmcnt(0)\nv_xor_b32 v252, s11, v252\nv_xor_b32 v253, s11, v253\nv_xor_b32 v254, s11, v254\n"
    "v_xor_b32 v255, s11, v255\nv_or3_b32 v226, v252, v253, v254\nv_or_b32 v226, v226, v255\nv_cmp_ne_u32 vcc, 0, v226\ns_cbranch_vccz .Lh105\ns_sub_u32 s80, s80, 1\n"
    "s_cbranch_scc1 .Lh105\ns_sleep 4\ns_branch .Lh103\n.Lh105:\ns_mov_b64 exec, 1\nds_write_b32 v56, v225\ns_mov_b64 exec, -1\n.Lh104:\ns_mov_b32 s86, 0x100000\n.Ls106:\n"
    "ds_read_b32 v226, v56 offset:0\ns_waitcnt lgkmcnt(0)\nv_cmp_gt_i32 vcc, 18, v226\ns_sub_u32 s86, s86, 1\ns_cbranch_scc1 .Ls107\ns_cbranch_vccz .Ls107\ns_sleep 1\n"
    "s_branch .Ls106\n.Ls107:\nglobal_load_dwordx4 v[64:67], v59, s[68:69] offset:-4096 sc1\nglobal_load_dwordx4 v[68:71], v59, s[68:69] offset:-3072 sc1\n"
    "global_load_dwordx4 v[72:75], v59, s[68:69] offset:-2048 sc1\nglobal_load_dwordx4 v[76:79], v59, s[68:69] offset:-1024 sc1\n"
    "global_load_dwordx4 v[80:83], v59, s[68:69] offset:0 sc1\nglobal_load_dwordx4 v[84:87], v59, s[68:69] offset:1024 sc1\n"
    "global_load_dwordx4 v[88:91], v59, s[68:69] offset:2048 sc1\nglobal_load_dwordx4 v[92:95], v59, s[68:69] offset:3072 sc1\n"
    "global_load_dwordx4 v[96:99], v59, s[70:71] offset:-4096 sc1\nglobal_load_dwordx4 v[100:103], v59, s[70:71] offset:-3072 sc1\n"
    "global_load_dwordx4 v[104:107], v59, s[70:71] offset:-2048 sc1\nglobal_load_dwordx4 v[108:111], v59, s[70:71] offset:-1024 sc1\n"
    "global_load_dwordx4 v[112:115], v59, s[70:71] offset:0 sc1\nglobal_load_dwordx4 v[116:119], v59, s[70:71] offset:1024 sc1\n"
    "global_load_dwordx4 v[120:123], v59, s[70:71] offset:2048 sc1\nglobal_load_dwordx4 v[124:127], v59, s[70:71] offset:3072 sc1\ns_branch .Lh102\n.Lw111:\n"
    "s_sub_u32 s80, s80, 1\ns_cbranch_scc1 .Lw112\nglobal_load_dword v240, v16, s[4:5] offset:0 sc1\nglobal_load_dword v241, v16, s[4:5] offset:128 sc1\n"
    "global_load_dword v242, v16, s[4:5] offset:512 sc1\nglobal_load_dword v243, v16, s[4:5] offset:640 sc1\nglobal_load_dword v244, v16, s[4:5] offset:1024 sc1\n"
    "global_load_dword v245, v16, s[4:5] offset:1152 sc1\nglobal_load_dword v246, v16, s[4:5] offset:1536 sc1\nglobal_load_dword v247, v16, s[4:5] offset:1664 sc1\n"
    "s_waitcnt vmcnt(0)\nv_max3_u32 v232, v240, v241, v242\nv_max3_u32 v233, v243, v244, v245\nv_max3_u32 v232, v232, v246, v247\nv_max_u32 v232, v232, v233\n"
    "v_cmp_eq_u32 vcc, -1, v232\ns_cbranch_vccnz .Lw111\ns_branch .Lw112\n.Llw121:\nv_max3_u32 v232, v208, v209, v210\nv_max3_u32 v232, v232, v211, v212\n"
    "v_max3_u32 v232, v232, v213, v214\nv_max3_u32 v232, v232, v215, v216\nv_max3_u32 v232, v232, v217, v218\nv_max3_u32 v232, v232, v219, v220\n"
    "v_max3_u32 v232, v232, v221, v222\nv_max_u32 v232, v232, v223\nv_cmp_eq_u32 vcc, -1, v232\ns_or_b64 vcc, vcc, s[50:51]\ns_cbranch_scc0 .Llx122\ns_sub_u32 s80, s80, 1\n"
    "s_cbranch_scc1 .Llx122\ns_mov_b64 exec, vcc\nglobal_load_dwordx4 v[240:243], v17, s[4:5] sc1\nglobal_load_dwordx4 v[208:211], v16, s[68:69] sc1\n"
    "global_load_dwordx4 v[212:215], v16, s[70:71] sc1\nglobal_load_dwordx4 v[216:219], v16, s[72:73] sc1\nglobal_load_dwordx4 v[220:223], v16, s[74:75] sc1\n"
    "s_mov_b64 exec, -1\ns_branch .Ln118\n.Lw135:\ns_mov_b32 s86, 0x100000\n.Ls137:\nds_read_b32 v39, v24 offset:0\ns_waitcnt lgkmcnt(0)\nv_cmp_gt_i32 vcc, v26, v39\n"
    "s_sub_u32 s86, s86, 1\ns_cbranch_scc1 .Ls138\ns_cbranch_vccz .Ls138\ns_sleep 1\ns_branch .Ls137\n.Ls138:\ns_branch .Lr136\n.Lq140:\ns_sub_u32 s80, s80, 1\n"
    "s_cbranch_scc1 .Lq140x\ns_sleep 2\ns_waitcnt lgkmcnt(0)\ns_branch .Lq139\n.Ld144:\nv_max3_u32 v244, v144, v145, v146\nv_max3_u32 v243, v147, v148, v149\n"
    "v_max3_u32 v244, v244, v150, v151\nv_max3_u32 v243, v243, v152, v153\nv_max3_u32 v244, v244, v154, v155\nv_max3_u32 v243, v243, v156, v157\n"
    "v_max3_u32 v244, v244, v158, v159\nv_max_u32 v243, v243, v244\nv_cmp_eq_u32 vcc, -1, v243\ns_cbranch_vccz .Ld145\ns_sub_u32 s80, s80, 1\ns_cbranch_scc1 .Ld145\n"
    "s_sleep 2\nglobal_load_dword v144, v39, s[52:53] offset:-2048 sc1\nglobal_load_dword v145, v39, s[52:53] offset:-1920 sc1\ns_cbranch_execz .Lhop\n"
    "global_load_dword v146, v39, s[52:53] offset:-1792 sc1\nglobal_load_dword v147, v39, s[52:53] offset:-1664 sc1\nglobal_load_dword v148, v39, s[52:53] offset:-1024 sc1\n"
    "global_load_dword v149, v39, s[52:53] offset:-896 sc1\nglobal_load_dword v150, v39, s[52:53] offset:-768 sc1\nglobal_load_dword v151, v39, s[52:53] offset:-640 sc1\n"
    "global_load_dword v152, v39, s[52:53] offset:0 sc1\nglobal_load_dword v153, v39, s[52:53] offset:128 sc1\nglobal_load_dword v154, v39, s[52:53] offset:256 sc1\n"
    "global_load_dword v155, v39, s[52:53] offset:384 sc1\nglobal_load_dword v156, v39, s[52:53] offset:1024 sc1\nglobal_load_dword v157, v39, s[52:53] offset:1152 sc1\n"
    "global_load_dword v158, v39, s[52:53] offset:1280 sc1\nglobal_load_dword v159, v39, s[52:53] offset:1408 sc1\ns_waitcnt vmcnt(0)\ns_branch .Ld143\n.Lp146:\n"
    "v_max3_u32 v243, v112, v113, v114\nv_max3_u32 v243, v243, v115, v116\nv_max3_u32 v243, v243, v117, v118\nv_max_u32 v243, v243, v119\nv_cmp_eq_u32 vcc, -1, v243\n"
    "s_cbranch_vccz .Lp147\ns_sub_u32 s80, s80, 1\ns_cbranch_scc1 .Lp147\nglobal_load_dword v112, v56, s[4:5] offset:0 sc1\n"
    "global_load_dword v113, v56, s[4:5] offset:256 sc1\nglobal_load_dword v114, v56, s[4:5] offset:512 sc1\nglobal_load_dword v115, v56, s[4:5] offset:768 sc1\n"
    "global_load_dword v116, v56, s[4:5] offset:1024 sc1\nglobal_load_dword v117, v56, s[4:5] offset:1280 sc1\nglobal_load_dword v118, v56, s[4:5] offset:1536 sc1\n"
    "global_load_dword v119, v56, s[4:5] offset:1792 sc1\ns_waitcnt vmcnt(0)\nv_add_f32 v138, v112, v113\nv_add_f32 v139, v114, v115\nv_add_f32 v244, v116, v117\n"
    "v_add_f32 v243, v118, v119\nv_add_f32 v138, v138, v139\nv_add_f32 v244, v244, v243\nv_add_f32 v139, v138, v244\nv_add_f32 v138, v138, v244\nv_cmp_u_f32 vcc, v138, v138\n"
    "s_nop 0\ns_branch .Lp146\n.Lx148:\ns_sub_u32 s80, s80, 1\ns_cbranch_scc1 .Lx149\ns_sleep 2\nglobal_load_dword v160, v39, s[54:55] offset:-2048 sc1\n"
    "global_load_dword v161, v39, s[54:55] offset:-1920 sc1\nglobal_load_dword v162, v39, s[54:55] offset:-1792 sc1\nglobal_load_dword v163, v39, s[54:55] offset:-1664 sc1\n"
    "global_load_dword v164, v39, s[54:55] offset:-1024 sc1\nglobal_load_dword v165, v39, s[54:55] offset:-896 sc1\nglobal_load_dword v166, v39, s[54:55] offset:-768 sc1\n"
    "global_load_dword v167, v39, s[54:55] offset:-640 sc1\nglobal_load_dword v168, v39, s[54:55] offset:0 sc1\nglobal_load_dword v169, v39, s[54:55] offset:128 sc1\n"
    "global_load_dword v170, v39, s[54:55] offset:256 sc1\nglobal_load_dword v171, v39, s[54:55] offset:384 sc1\nglobal_load_dword v172, v39, s[54:55] offset:1024 sc1\n"
    "global_load_dword v173, v39, s[54:55] offset:1152 sc1\nglobal_load_dword v174, v39, s[54:55] offset:1280 sc1\nglobal_load_dword v175, v39, s[54:55] offset:1408 sc1\n"
    "s_waitcnt vmcnt(0)\nv_max3_u32 v244, v160, v161, v162\nv_max3_u32 v243, v163, v164, v165\nv_max3_u32 v244, v244, v166, v167\nv_max3_u32 v243, v243, v168, v169\n"
    "v_max3_u32 v244, v244, v170, v171\nv_max3_u32 v243, v243, v172, v173\nv_max3_u32 v244, v244, v174, v175\nv_max_u32 v243, v243, v244\nv_cmp_eq_u32 vcc, -1, v243\n"
    "s_cbranch_vccnz .Lx148\ns_branch .Lx149\n.Lx150:\ns_sub_u32 s80, s80, 1\ns_cbranch_scc1 .Lx151\ns_sleep 2\nglobal_load_dword v176, v39, s[56:57] offset:-2048 sc1\n"
    "global_load_dword v177, v39, s[56:57] offset:-1920 sc1\nglobal_load_dword v178, v39, s[56:57] offset:-1792 sc1\nglobal_load_dword v179, v39, s[56:57] offset:-1664 sc1\n"
    "global_load_dword v180, v39, s[56:57] offset:-1024 sc1\nglobal_load_dword v181, v39, s[56:57] offset:-896 sc1\nglobal_load_dword v182, v39, s[56:57] offset:-768 sc1\n"
    "global_load_dword v183, v39, s[56:57] offset:-640 sc1\nglobal_load_dword v184, v39, s[56:57] offset:0 sc1\nglobal_load_dword v185, v39, s[56:57] offset:128 sc1\n"
    "global_load_dword v186, v39, s[56:57] offset:256 sc1\nglobal_load_dword v187, v39, s[56:57] offset:384 sc1\nglobal_load_dword v188, v39, s[56:57] offset:1024 sc1\n"
    "global_load_dword v189, v39, s[56:57] offset:1152 sc1\nglobal_load_dword v190, v39, s[56:57] offset:1280 sc1\nglobal_load_dword v191, v39, s[56:57] offset:1408 sc1\n"
    "s_waitcnt vmcnt(0)\nv_max3_u32 v244, v176, v177, v178\nv_max3_u32 v243, v179, v180, v181\nv_max3_u32 v244, v244, v182, v183\nv_max3_u32 v243, v243, v184, v185\n"
    "v_max3_u32 v244, v244, v186, v187\nv_max3_u32 v243, v243, v188, v189\nv_max3_u32 v244, v244, v190, v191\nv_max_u32 v243, v243, v244\nv_cmp_eq_u32 vcc, -1, v243\n"
    "s_cbranch_vccnz .Lx150\ns_branch .Lx151\n.Lx152:\ns_sub_u32 s80, s80, 1\ns_cbranch_scc1 .Lx153\ns_sleep 2\nglobal_load_dword v192, v39, s[58:59] offset:-2048 sc1\n"
    "global_load_dword v193, v39, s[58:59] offset:-1920 sc1\nglobal_load_dword v194, v39, s[58:59] offset:-1792 sc1\nglobal_load_dword v195, v39, s[58:59] offset:-1664 sc1\n"
    "global_load_dword v196, v39, s[58:59] offset:-1024 sc1\nglobal_load_dword v197, v39, s[58:59] offset:-896 sc1\nglobal_load_dword v198, v39, s[58:59] offset:-768 sc1\n"
    "global_load_dword v199, v39, s[58:59] offset:-640 sc1\nglobal_load_dword v200, v39, s[58:59] offset:0 sc1\nglobal_load_dword v201, v39, s[58:59] offset:128 sc1\n"
    "global_load_dword v202, v39, s[58:59] offset:256 sc1\nglobal_load_dword v203, v39, s[58:59] offset:384 sc1\nglobal_load_dword v204, v39, s[58:59] offset:1024 sc1\n"
    "global_load_dword v205, v39, s[58:59] offset:1152 sc1\nglobal_load_dword v206, v39, s[58:59] offset:1280 sc1\nglobal_load_dword v207, v39, s[58:59] offset:1408 sc1\n"
    "s_waitcnt vmcnt(0)\nv_max3_u32 v244, v192, v193, v194\nv_max3_u32 v243, v195, v196, v197\nv_max3_u32 v244, v244, v198, v199\nv_max3_u32 v243, v243, v200, v201\n"
    "v_max3_u32 v244, v244, v202, v203\nv_max3_u32 v243, v243, v204, v205\nv_max3_u32 v244, v244, v206, v207\nv_max_u32 v243, v243, v244\nv_cmp_eq_u32 vcc, -1, v243\n"
    "s_cbranch_vccnz .Lx152\ns_branch .Lx153\n.Lx154:\ns_sub_u32 s80, s80, 1\ns_cbranch_scc1 .Lx155\ns_sleep 2\nglobal_load_dword v144, v39, s[60:61] offset:-2048 sc1\n"
    "global_load_dword v145, v39, s[60:61] offset:-1920 sc1\nglobal_load_dword v146, v39, s[60:61] offset:-1792 sc1\nglobal_load_dword v147, v39, s[60:61] offset:-1664 sc1\n"
    "global_load_dword v148, v39, s[60:61] offset:-1024 sc1\nglobal_load_dword v149, v39, s[60:61] offset:-896 sc1\nglobal_load_dword v150, v39, s[60:61] offset:-768 sc1\n"
    "global_load_dword v151, v39, s[60:61] offset:-640 sc1\nglobal_load_dword v152, v39, s[60:61] offset:0 sc1\nglobal_load_dword v153, v39, s[60:61] offset:128 sc1\ns_nop 0\n"
    "s_cbranch_execz .Lhop\nglobal_load_dword v154, v39, s[60:61] offset:256 sc1\nglobal_load_dword v155, v39, s[60:61] offset:384 sc1\n"
    "global_load_dword v156, v39, s[60:61] offset:1024 sc1\nglobal_load_dword v157, v39, s[60:61] offset:1152 sc1\nglobal_load_dword v158, v39, s[60:61] offset:1280 sc1\n"
    "global_load_dword v159, v39, s[60:61] offset:1408 sc1\ns_waitcnt vmcnt(0)\nv_max3_u32 v244, v144, v145, v146\nv_max3_u32 v243, v147, v148, v149\n"
    "v_max3_u32 v244, v244, v150, v151\nv_max3_u32 v243, v243, v152, v153\nv_max3_u32 v244, v244, v154, v155\nv_max3_u32 v243, v243, v156, v157\n"
    "v_max3_u32 v244, v244, v158, v159\nv_max_u32 v243, v243, v244\nv_cmp_eq_u32 vcc, -1, v243\ns_cbranch_vccnz .Lx154\ns_branch .Lx155\n.Lx156:\ns_sub_u32 s80, s80, 1\n"
    "s_cbranch_scc1 .Lx157\ns_sleep 2\nglobal_load_dword v160, v39, s[62:63] offset:-2048 sc1\nglobal_load_dword v161, v39, s[62:63] offset:-1920 sc1\n"
    "global_load_dword v162, v39, s[62:63] offset:-1792 sc1\nglobal_load_dword v163, v39, s[62:63] offset:-1664 sc1\nglobal_load_dword v164, v39, s[62:63] offset:-1024 sc1\n"
    "global_load_dword v165, v39, s[62:63] offset:-896 sc1\nglobal_load_dword v166, v39, s[62:63] offset:-768 sc1\nglobal_load_dword v167, v39, s[62:63] offset:-640 sc1\n"
    "global_load_dword v168, v39, s[62:63] offset:0 sc1\nglobal_load_dword v169, v39, s[62:63] offset:128 sc1\nglobal_load_dword v170, v39, s[62:63] offset:256 sc1\n"
    "global_load_dword v171, v39, s[62:63] offset:384 sc1\nglobal_load_dword v172, v39, s[62:63] offset:1024 sc1\nglobal_load_dword v173, v39, s[62:63] offset:1152 sc1\n"
    "global_load_dword v174, v39, s[62:63] offset:1280 sc1\nglobal_load_dword v175, v39, s[62:63] offset:1408 sc1\ns_waitcnt vmcnt(0)\nv_max3_u32 v244, v160, v161, v162\n"
    "v_max3_u32 v243, v163, v164, v165\nv_max3_u32 v244, v244, v166, v167\nv_max3_u32 v243, v243, v168, v169\nv_max3_u32 v244, v244, v170, v171\n"
    "v_max3_u32 v243, v243, v172, v173\nv_max3_u32 v244, v244, v174, v175\nv_max_u32 v243, v243, v244\nv_cmp_eq_u32 vcc, -1, v243\ns_cbranch_vccnz .Lx156\ns_branch .Lx157\n"
    ".Lx158:\ns_sub_u32 s80, s80, 1\ns_cbranch_scc1 .Lx159\ns_sleep 2\nglobal_load_dword v176, v39, s[64:65] offset:-2048 sc1\n"
    "global_load_dword v177, v39, s[64:65] offset:-1920 sc1\nglobal_load_dword v178, v39, s[64:65] offset:-1792 sc1\nglobal_load_dword v179, v39, s[64:65] offset:-1664 sc1\n"
    "global_load_dword v180, v39, s[64:65] offset:-1024 sc1\nglobal_load_dword v181, v39, s[64:65] offset:-896 sc1\nglobal_load_dword v182, v39, s[64:65] offset:-768 sc1\n"
    "global_load_dword v183, v39, s[64:65] offset:-640 sc1\nglobal_load_dword v184, v39, s[64:65] offset:0 sc1\nglobal_load_dword v185, v39, s[64:65] offset:128 sc1\n"
    "global_load_dword v186, v39, s[64:65] offset:256 sc1\nglobal_load_dword v187, v39, s[64:65] offset:384 sc1\nglobal_load_dword v188, v39, s[64:65] offset:1024 sc1\n"
    "global_load_dword v189, v39, s[64:65] offset:1152 sc1\nglobal_load_dword v190, v39, s[64:65] offset:1280 sc1\nglobal_load_dword v191, v39, s[64:65] offset:1408 sc1\n"
    "s_waitcnt vmcnt(0)\nv_max3_u32 v244, v176, v177, v178\nv_max3_u32 v243, v179, v180, v181\nv_max3_u32 v244, v244, v182, v183\nv_max3_u32 v243, v243, v184, v185\n"
    "v_max3_u32 v244, v244, v186, v187\nv_max3_u32 v243, v243, v188, v189\nv_max3_u32 v244, v244, v190, v191\nv_max_u32 v243, v243, v244\nv_cmp_eq_u32 vcc, -1, v243\n"
    "s_cbranch_vccnz .Lx158\ns_branch .Lx159\n.Lx160:\ns_sub_u32 s80, s80, 1\ns_cbranch_scc1 .Lx161\ns_sleep 2\nglobal_load_dword v192, v39, s[66:67] offset:-2048 sc1\n"
    "global_load_dword v193, v39, s[66:67] offset:-1920 sc1\nglobal_load_dword v194, v39, s[66:67] offset:-1792 sc1\nglobal_load_dword v195, v39, s[66:67] offset:-1664 sc1\n"
    "global_load_dword v196, v39, s[66:67] offset:-1024 sc1\nglobal_load_dword v197, v39, s[66:67] offset:-896 sc1\nglobal_load_dword v198, v39, s[66:67] offset:-768 sc1\n"
    "global_load_dword v199, v39, s[66:67] offset:-640 sc1\nglobal_load_dword v200, v39, s[66:67] offset:0 sc1\nglobal_load_dword v201, v39, s[66:67] offset:128 sc1\n"
    "global_load_dword v202, v39, s[66:67] offset:256 sc1\nglobal_load_dword v203, v39, s[66:67] offset:384 sc1\nglobal_load_dword v204, v39, s[66:67] offset:1024 sc1\n"
    "global_load_dword v205, v39, s[66:67] offset:1152 sc1\nglobal_load_dword v206, v39, s[66:67] offset:1280 sc1\nglobal_load_dword v207, v39, s[66:67] offset:1408 sc1\n"
    "s_waitcnt vmcnt(0)\nv_max3_u32 v244, v192, v193, v194\nv_max3_u32 v243, v195, v196, v197\nv_max3_u32 v244, v244, v198, v199\nv_max3_u32 v243, v243, v200, v201\n"
    "v_max3_u32 v244, v244, v202, v203\nv_max3_u32 v243, v243, v204, v205\nv_max3_u32 v244, v244, v206, v207\nv_max_u32 v243, v243, v244\nv_cmp_eq_u32 vcc, -1, v243\n"
    "s_cbranch_vccnz .Lx160\ns_branch .Lx161\n.fill 62, 4, 0xBF800000\ns_cbranch_execz .Lhop\n.fill 255, 4, 0xBF800000\ns_cbranch_execz .Lhop\n.fill 255, 4, 0xBF800000\n"
    "s_cbranch_execz .Lhop\n.fill 255, 4, 0xBF800000\ns_cbranch_execz .Lhop\n.fill 255, 4, 0xBF800000\ns_cbranch_execz .Lhop\n.Lk_all_end:\n"
    :: "{s[0:1]}"(__builtin_amdgcn_kernarg_segment_ptr()), "{s2}"(blockIdx.x), "{v0}"(threadIdx.x), "{s3}"((uint32_t)(uintptr_t)(__attribute__((address_space(3))) char*)smem)
    : "v255", "s99", "vcc", "scc", "memory");
}

extern "C" void kernel_launch(void* const* d_in, const int* in_sizes, int n_in,
                              void* d_out, int out_size, void* d_ws, size_t ws_size,
                              hipStream_t stream) {
  Args a;
  a.x = (const u16*)d_in[0]; a.w = (const u16*)d_in[1]; a.wo = (const u16*)d_in[2];
  a.A_re = (const u16*)d_in[4]; a.A_im = (const u16*)d_in[5]; a.B_re = (const u16*)d_in[6]; a.B_im = (const u16*)d_in[7];
  a.C_re = (const u16*)d_in[8]; a.C_im = (const u16*)d_in[9]; a.log_dt = (const u16*)d_in[10]; a.Dskip = (const u16*)d_in[11];
  a.out = (u16*)d_out; a.ws = (char*)d_ws;
  k_all<<<256, 512, 0, stream>>>(a);
}
```

```cpp
#include <hip/hip_runtime.h>
#include <stdint.h>

typedef unsigned short u16;
constexpr int rnd_l(int v) { int e = 0; while ((v >> e) >= 256) ++e; if (!e) return v; const int h = 1 << (e - 1), q = v >> e, r = v & ((1 << e) - 1); return (q + ((r > h || (r == h && (q & 1))) ? 1 : 0)) << e; }
constexpr int slot_l(int v) { v = rnd_l(v); int e = 0; while ((v >> e) >= 256) ++e; return e * 128 + (v >> e); }
struct Tabs { u16 img[1032]; u16 prd[1024]; };
constexpr Tabs mk_tabs() {
  Tabs t{};
  for (int i = 0; i < 1032; ++i) {
    const int f = 1023 - i, o = f >> 3, a = (o - 74) / 12, m = f - 8 * (74 + 12 * a);
    int v = -1;
    if (f >= 32 && f - 32 <= 525) v = f - 32;
    if (o >= 74 && o < 122 && m < 64) v = a >= 2 ? 16 * (65 + m) - 8 * (a - 2) : (m < 62 ? 8 * (66 + m) + 4 * a : (m == 62 ? 1024 : -1));
    t.img[i] = (u16)(2 * (v < 0 ? 700 : slot_l(v)));
  }
  for (int w = 0; w < 1024; ++w) {
    const int tile = (w >> 9) * 2 + ((w >> 2) & 1), lane = (w >> 3) & 63;
    const int r = (lane >> 4) * 4 + (w & 3), C = (tile & 1) * 16 + (lane & 15);
    const int lr = 8 * C + (r < 4 ? r : (r == 4 ? 258 : 256 * (r - 4))) + 4 * (tile >> 1);
    const bool ok = r <= 12 && lr <= 2048 && rnd_l(lr) == lr;
    t.prd[w] = (u16)(2 * (ok ? slot_l(lr) : 704 + (lane & 63)));
  }
  return t;
}
extern "C" { extern __device__ __attribute__((aligned(16), used)) const Tabs g_tabs = mk_tabs(); }

struct Args { const u16 *x, *w, *wo, *A_re, *A_im, *B_re, *B_im, *C_re, *C_im, *log_dt, *Dskip; u16* out; char* ws; };

__global__ __launch_bounds__(512, 2) __attribute__((aligned(4096))) void k_all(const Args ar) {
  __shared__ __attribute__((aligned(128))) char smem[163840];
  asm volatile(
    ".Lk_all_beg:\ns_load_dwordx16 s[64:79], s[0:1], 0\ns_load_dwordx8 s[80:87], s[0:1], 0x40\ns_load_dwordx2 s[4:5], s[0:1], 0x60\nv_readfirstlane_b32 s3, v0\n"
    "s_lshr_b32 s25, s2, 3\ns_lshr_b32 s3, s3, 6\ns_cmp_lg_u32 s3, 4\ns_cbranch_scc1 .Loth1\ns_lshr_b32 s51, s2, 5\ns_and_b32 s60, s25, 3\nv_bfe_u32 v64, v0, 2, 4\n"
    "s_lshl1_add_u32 s50, s60, s51\nv_lshrrev_b32 v65, 4, v0\nv_mul_u32_u24 v64, 0xc00, v64\ns_and_b32 s50, s50, 7\nv_and_b32 v66, 2, v65\ns_and_b32 s61, s2, 1\n"
    "v_xor_b32 v66, v66, v0\ns_lshl3_add_u32 s51, s61, s51\nv_and_b32 v66, 3, v66\ns_lshl_b32 s51, s51, 6\nv_lshl_add_u32 v64, v66, 4, v64\ns_mul_i32 m0, s50, 12288\n"
    "v_add_u32 v64, s51, v64\ns_lshl_b32 s59, m0, 4\ns_mov_b32 s58, 3\ns_waitcnt lgkmcnt(0)\ns_add_u32 s52, s66, s59\n.Le5:\ns_addc_u32 s53, s67, 0\n"
    "global_load_lds_dwordx4 v64, s[52:53] offset:0\nglobal_load_lds_dwordx4 v64, s[52:53] offset:1024\nglobal_load_lds_dwordx4 v64, s[52:53] offset:2048\n"
    "s_add_u32 m0, m0, 3072\ns_add_u32 s59, s59, 49152\ns_sub_u32 s58, s58, 1\ns_cbranch_scc1 .Le5x\ns_add_u32 s52, s66, s59\ns_branch .Le5\n.Le5x:\n"
    "s_bfe_u32 s61, s2, 0x20001\nv_and_b32 v65, 3, v65\ns_lshl2_add_u32 s61, s61, s60\nv_xor_b32 v65, v65, v0\ns_lshl_b32 s61, s61, 17\nv_and_b32 v65, 7, v65\n"
    "s_add_u32 s48, s64, s61\nv_bfe_u32 v66, v0, 3, 3\ns_addc_u32 s49, s65, 0\nv_lshlrev_b32 v66, 10, v66\ns_lshl_b32 s59, s50, 7\nv_lshl_add_u32 v65, v65, 4, v66\n"
    "s_add_u32 s54, s48, s59\nv_xor_b32 v66, 64, v65\ns_addc_u32 s55, s49, 0\nv_add_u32 v67, 14336, v65\ns_mov_b32 m0, 98304\nv_add_u32 v68, 21504, v66\n"
    "v_add_u32 v66, 7168, v66\n.Le6:\nglobal_load_lds_dwordx4 v65, s[54:55] offset:0\nglobal_load_lds_dwordx4 v66, s[54:55] offset:1024\n"
    "global_load_lds_dwordx4 v67, s[54:55] offset:2048\nglobal_load_lds_dwordx4 v68, s[54:55] offset:3072\ns_add_u32 s54, s54, 32768\ns_addc_u32 s55, s55, 0\n"
    "s_add_u32 m0, m0, 12288\ns_cmp_lt_u32 m0, 147456\ns_cbranch_scc1 .Le6\ns_barrier\ns_branch .Lcom2\n.Lhop:\ns_sub_u32 s89, s89, 1\ns_cbranch_scc1 .Lhpx\n"
    "s_add_u32 s90, s90, 1024\ns_addc_u32 s91, s91, 0\ns_setpc_b64 s[90:91]\n.Lhpx:\ns_mov_b64 exec, -1\ns_setpc_b64 s[94:95]\n"
    ".Loth1:\ns_and_b32 s24, s2, 7\ns_lshl_b32 s44, s3, 2\ns_lshl_b32 s18, s24, 5\ns_add_u32 s18, s18, s25\ns_getpc_b64 s[92:93]\n.Lk_pc:\n"
    "s_sub_u32 s92, s92, .Lk_pc-.Lk_all_beg+4\ns_subb_u32 s93, s93, 0\ns_cmp_gt_u32 s3, 3\ns_cbranch_scc1 .Loth3\nv_and_b32 v65, 63, v0\ns_lshl3_add_u32 s47, s44, s25\n"
    "v_lshl_add_u32 v64, v65, 2, s3\ns_sub_u32 s47, s47, 1984\nv_cmp_gt_u32 vcc, 16, v65\nv_lshl_add_u32 v65, v65, 7, s47\ns_and_b32 s47, s18, 15\n"
    "v_cndmask_b32 v64, v65, v64, vcc\ns_lshl1_add_u32 s47, s47, s3\nv_min_u32 v64, (.Lk_all_end-.Lk_all_beg)/128-8, v64\ns_lshl_b32 s47, s47, 14\nv_lshlrev_b32 v64, 7, v64\n"
    "s_mov_b64 exec, 0x3ffff\nglobal_load_dword v255, v64, s[92:93] offset:1024\ns_mov_b64 exec, -1\ns_and_b32 s48, s18, 0xf0\nv_and_b32 v69, 32, v0\n"
    "s_lshl2_add_u32 s47, s48, s47\nv_and_b32 v71, 31, v0\ns_sub_u32 s47, s47, 28672\nv_lshl_add_u32 v69, v69, 8, s47\nv_mov_b32 v70, 16777216\n"
    "v_lshl_add_u32 v69, v71, 1, v69\n.Loth3:\nv_lshlrev_b32 v64, 1, v0\ns_lshl_b32 s47, s18, 9\nv_and_b32 v64, 0x1fc, v64\ns_lshl_b32 s46, s18, 2\nv_or_b32 v64, s47, v64\n"
    "v_mov_b32 v65, s46\ns_mov_b32 s27, 0\ns_cmp_gt_u32 s3, 3\ns_waitcnt lgkmcnt(0)\ns_cbranch_scc1 .Loth3x\nglobal_load_dword v166, v70, s[4:5] sc1\ns_cmp_eq_u32 s3, 0\n"
    "s_cbranch_scc1 .Loth3x\ns_cmp_eq_u32 s3, 1\ns_cbranch_scc1 .Loth3y\nglobal_load_ushort v168, v69, s[68:69] offset:-4096\n"
    "global_load_ushort v169, v69, s[68:69] offset:-3072\nglobal_load_ushort v170, v69, s[68:69] offset:-2048\nglobal_load_ushort v171, v69, s[68:69] offset:-1024\n"
    "global_load_ushort v172, v69, s[68:69] offset:0\nglobal_load_ushort v173, v69, s[68:69] offset:1024\nglobal_load_ushort v174, v69, s[68:69] offset:2048\n"
    "global_load_ushort v175, v69, s[68:69] offset:3072\ns_branch .Lcom2\n.Loth3x:\nglobal_load_dword v160, v64, s[70:71]\nglobal_load_dword v161, v64, s[72:73]\n"
    "global_load_dword v162, v64, s[78:79]\nglobal_load_dword v163, v64, s[80:81]\nglobal_load_dword v164, v64, s[74:75]\nglobal_load_dword v165, v64, s[76:77]\n"
    "global_load_dword v167, v65, s[82:83]\ns_mov_b32 s27, 4\n.Loth3y:\ns_cmp_gt_u32 s3, 1\ns_cbranch_scc1 .Lcom2\ns_cmp_eq_u32 s3, 0\ns_cselect_b64 exec, 0xff, -1\n"
    "s_cselect_b32 s47, 0, 0x3f803f80\nv_mov_b32 v80, s47\nv_mov_b32 v81, s47\nv_mov_b32 v82, s47\nv_mov_b32 v83, s47\ns_cselect_b32 s47, 17280, 0\n"
    "s_add_u32 s47, s47, 146432\nv_lshl_add_u32 v68, v0, 4, s47\nds_write_b128 v68, v[80:83]\ns_mov_b64 exec, -1\n"
    ".Lcom2:\ns_and_b32 s24, s2, 7\ns_lshr_b32 s25, s2, 3\ns_and_b32 s13, s3, 3\ns_lshl_b32 s43, s24, 5\ns_add_u32 s18, s43, s25\ns_lshr_b32 s43, s24, 1\n"
    "s_lshl_b32 s42, s43, 2\ns_and_b32 s41, s25, 3\ns_add_u32 s19, s42, s41\ns_and_b32 s43, s24, 1\ns_lshl_b32 s42, s43, 3\ns_lshr_b32 s41, s25, 2\ns_add_u32 s20, s42, s41\n"
    "s_lshl_b32 s43, s24, 3\ns_and_b32 s42, s25, 7\ns_add_u32 s21, s43, s42\ns_lshr_b32 s22, s25, 3\ns_lshr_b32 s43, s25, 2\ns_and_b32 s42, s25, 3\ns_lshl_b32 s41, s42, 1\n"
    "s_add_u32 s40, s43, s41\ns_and_b32 s23, s40, 7\ns_lshl_b32 s44, s18, 2\ns_mov_b64 s[6:7], s[86:87]\ns_mov_b64 s[8:9], s[64:65]\ns_mov_b32 s12, 0\n"
    "v_bfe_u32 v251, v0, 4, 2\nv_lshlrev_b32 v250, 2, v251\ns_add_u32 s43, s19, s3\ns_cmp_lt_u32 s43, 1\ns_cselect_b32 s42, 8, 0\nv_and_b32 v249, 7, v0\n"
    "v_add_u32 v248, 1, v249\nv_max_u32 v247, s42, v248\nv_sub_u32 v2, v250, v247\nv_add_u32 v246, 9, v249\nv_sub_u32 v40, v246, v247\nv_add_u32 v3, 1, v2\n"
    "v_add_u32 v4, 2, v2\ns_cbranch_execz .Lhop\nv_add_u32 v5, 3, v2\nv_cmp_lt_u32 s[36:37], v2, v40\nv_cmp_lt_u32 s[38:39], v3, v40\nv_cmp_lt_u32 s[40:41], v4, v40\n"
    "v_cmp_lt_u32 s[42:43], v5, v40\nv_mov_b32 v40, 0xf149f2ca\nv_cndmask_b32 v2, v40, 0, s[36:37]\nv_cndmask_b32 v3, v40, 0, s[38:39]\nv_cndmask_b32 v4, v40, 0, s[40:41]\n"
    "v_cndmask_b32 v5, v40, 0, s[42:43]\nv_and_b32 v251, 15, v0\nv_max_u32 v250, 1, v251\nv_mul_u32_u24 v249, 0x1a0, v250\nv_bfe_u32 v248, v0, 4, 2\ns_mul_i32 s43, s3, 0xd00\n"
    "s_add_u32 s42, s43, 0x18040\nv_add_u32 v6, s42, v249\nv_lshl_add_u32 v6, v248, 4, v6\nv_and_b32 v247, 7, v0\nv_mul_u32_u24 v246, 0x1a0, v247\n"
    "s_add_u32 s41, s43, 0x18d00\nv_add_u32 v7, s41, v246\nv_lshl_add_u32 v7, v248, 4, v7\nv_bfe_u32 v245, v0, 2, 2\nv_lshl_add_u32 v244, v248, 2, v245\n"
    "v_max_u32 v243, 1, v244\nv_mul_u32_u24 v242, 0x1a0, v243\nv_and_b32 v241, 3, v0\ns_add_u32 s40, s43, 0x18080\nv_add_u32 v8, s40, v242\nv_lshl_add_u32 v8, v241, 3, v8\n"
    "v_bfe_u32 v240, v0, 4, 1\ns_lshl_b32 s39, s19, 7\ns_lshl_b32 s38, s3, 4\ns_add_u32 s37, s39, s38\nv_add_u32 v239, s37, v240\nv_lshl_add_u32 v239, v247, 1, v239\n"
    "v_lshrrev_b32 v238, 5, v239\nv_bfe_u32 v237, v0, 3, 1\nv_bfe_u32 v236, v0, 5, 1\nv_and_b32 v235, 31, v239\ns_lshl_b32 s36, s20, 11\nv_lshl_add_u32 v9, v238, 15, s36\n"
    "v_lshl_add_u32 v9, v237, 10, v9\nv_lshl_add_u32 v9, v236, 9, v9\nv_lshl_add_u32 v9, v235, 4, v9\nv_mov_b64 v[40:41], 0\nv_mov_b64 v[42:43], 0\nv_mov_b64 v[44:45], 0\n"
    "v_mov_b64 v[46:47], 0\nv_mov_b64 v[48:49], 0\nv_mov_b64 v[50:51], 0\nv_mov_b64 v[52:53], 0\nv_mov_b64 v[54:55], 0\nv_mov_b32 v56, 163712\ns_add_u32 s16, s84, s44\n"
    "s_addc_u32 s17, s85, 0\ns_waitcnt lgkmcnt(0)\ns_cmp_eq_u32 s3, 4\ns_cbranch_scc1 .Llstb7\ns_barrier\ns_cmp_lt_u32 s3, 4\ns_cbranch_scc1 .Lgset8\n"
    "v_bfe_u32 v251, v0, 5, 1\nv_mul_u32_u24 v250, 0xc00, v251\nv_bfe_u32 v249, v0, 4, 1\nv_bfe_u32 v248, v0, 2, 2\nv_and_b32 v247, 3, v0\ns_lshl_b32 s43, s13, 10\n"
    "s_add_u32 s42, s43, 0xfffffc00\nv_add_u32 v27, s42, v250\nv_lshl_add_u32 v27, v249, 9, v27\nv_lshl_add_u32 v27, v248, 6, v27\nv_lshl_add_u32 v27, v247, 3, v27\n"
    "v_lshl_add_u32 v27, v249, 5, v27\nv_and_b32 v246, 15, v0\nv_bfe_u32 v245, v0, 4, 2\nv_xor_b32 v244, v245, v246\ns_mov_b32 s41, 0x23c00\n"
    "v_lshl_add_u32 v32, v246, 10, s41\nv_lshl_add_u32 v32, v244, 4, v32\ns_lshl_b32 s40, s13, 7\ns_add_u32 s39, s40, 0x27b80\nv_lshl_add_u32 v58, v245, 4, s39\n"
    "s_lshl_b32 s38, s13, 2\ns_add_u32 s37, s38, 0x27f80\nv_mov_b32 v37, s37\nv_mul_u32_u24 v243, 0xd0, v246\ns_lshl_b32 s36, s13, 6\ns_add_u32 s35, s36, 0x17fc0\n"
    "v_add_u32 v38, s35, v243\nv_lshl_add_u32 v38, v245, 3, v38\ns_mov_b32 s34, 0x27f80\nv_lshl_add_u32 v39, v247, 2, s34\ns_add_u32 s44, s23, 0\ns_and_b32 s44, s44, 7\n"
    "s_mul_i32 s45, s44, 12288\ns_lshl_b32 s44, s44, 7\nv_add_u32 v192, s45, v27\nv_xor_b32 v208, s44, v32\nv_xor_b32 v193, 32, v192\nv_xor_b32 v209, 64, v208\n"
    "s_add_u32 s44, s23, 1\ns_and_b32 s44, s44, 7\ns_mul_i32 s45, s44, 12288\ns_lshl_b32 s44, s44, 7\nv_add_u32 v194, s45, v27\nv_xor_b32 v210, s44, v32\n"
    "v_xor_b32 v195, 32, v194\nv_xor_b32 v211, 64, v210\ns_add_u32 s44, s23, 2\ns_and_b32 s44, s44, 7\ns_mul_i32 s45, s44, 12288\ns_lshl_b32 s44, s44, 7\n"
    "v_add_u32 v196, s45, v27\nv_xor_b32 v212, s44, v32\nv_xor_b32 v197, 32, v196\nv_xor_b32 v213, 64, v212\ns_add_u32 s44, s23, 3\ns_and_b32 s44, s44, 7\n"
    "s_mul_i32 s45, s44, 12288\ns_lshl_b32 s44, s44, 7\nv_add_u32 v198, s45, v27\nv_xor_b32 v214, s44, v32\nv_xor_b32 v199, 32, v198\nv_xor_b32 v215, 64, v214\n"
    "s_add_u32 s44, s23, 4\ns_and_b32 s44, s44, 7\ns_mul_i32 s45, s44, 12288\ns_lshl_b32 s44, s44, 7\nv_add_u32 v200, s45, v27\nv_xor_b32 v216, s44, v32\n"
    "v_xor_b32 v201, 32, v200\nv_xor_b32 v217, 64, v216\ns_add_u32 s44, s23, 5\ns_and_b32 s44, s44, 7\ns_mul_i32 s45, s44, 12288\ns_lshl_b32 s44, s44, 7\n"
    "v_add_u32 v202, s45, v27\nv_xor_b32 v218, s44, v32\nv_xor_b32 v203, 32, v202\nv_xor_b32 v219, 64, v218\ns_add_u32 s44, s23, 6\ns_and_b32 s44, s44, 7\n"
    "s_mul_i32 s45, s44, 12288\ns_lshl_b32 s44, s44, 7\nv_add_u32 v204, s45, v27\nv_xor_b32 v220, s44, v32\nv_xor_b32 v205, 32, v204\nv_xor_b32 v221, 64, v220\n"
    "s_add_u32 s44, s23, 7\ns_and_b32 s44, s44, 7\ns_mul_i32 s45, s44, 12288\ns_lshl_b32 s44, s44, 7\nv_add_u32 v206, s45, v27\nv_xor_b32 v222, s44, v32\n"
    "v_xor_b32 v207, 32, v206\nv_xor_b32 v223, 64, v222\ns_mov_b32 s10, 64\ns_waitcnt vmcnt(0)\nv_and_b32 v224, 127, v0\ns_getpc_b64 s[44:45]\n"
    "s_add_u32 s44, s44, g_tabs@rel32@lo+4\ns_addc_u32 s45, s45, g_tabs@rel32@hi+12\nv_lshlrev_b32 v224, 4, v224\nglobal_load_dwordx4 v[28:31], v224, s[44:45] offset:2064\n"
    "global_load_dwordx4 v[10:13], v224, s[44:45]\nglobal_load_dwordx2 v[14:15], v224, s[44:45] offset:16\ns_branch .Lkgp9\n"
    ".Lgset8:\nv_bfe_u32 v251, v0, 5, 1\nv_bfe_u32 v250, v0, 2, 2\nv_bfe_u32 v249, v0, 4, 1\nv_xor_b32 v248, v249, v251\nv_and_b32 v247, 3, v0\nv_lshlrev_b32 v36, 9, v251\n"
    "v_lshl_add_u32 v36, v250, 6, v36\nv_lshl_add_u32 v36, v248, 5, v36\nv_lshl_add_u32 v36, v247, 3, v36\nv_and_b32 v246, 31, v0\nv_bfe_u32 v245, v0, 1, 3\n"
    "v_xor_b32 v244, v251, v245\ns_nop 0\ns_cbranch_execz .Lhop\ns_mul_i32 s43, s13, 0x3000\ns_add_u32 s42, s43, 0x18000\nv_lshl_add_u32 v27, v246, 7, s42\n"
    "v_lshl_add_u32 v27, v244, 4, v27\nv_mul_u32_u24 v243, 0xd0, v246\ns_mul_i32 s41, s13, 0x1a00\ns_add_u32 s40, s41, 0x18d00\nv_add_u32 v37, s40, v243\n"
    "v_lshl_add_u32 v37, v251, 3, v37\ns_mov_b32 s39, 0x27c00\nv_lshl_add_u32 v38, v246, 2, s39\ns_mov_b32 s38, 0x27fe0\nv_lshl_add_u32 v242, v247, 2, s38\n"
    "v_and_b32 v241, 4, v0\nv_lshlrev_b32 v240, 4, v241\nv_sub_u32 v28, v242, v240\nv_mov_b32 v239, 16\nv_cmp_gt_u32 vcc, 1, v241\ns_nop 1\nv_cndmask_b32 v29, 1, v239, vcc\n"
    "s_lshl_b32 s37, s13, 2\ns_add_u32 s36, s37, 0x27fe0\nv_mov_b32 v30, s36\nv_mov_b32 v31, 0x27fe8\ns_lshr_b32 s35, s2, 4\ns_lshl_b32 s34, s35, 7\ns_and_b32 s33, s2, 15\n"
    "s_lshl_b32 s32, s33, 2\ns_add_u32 s31, s34, s32\ns_add_u32 s31, s31, 0x1002000\nv_mov_b32 v32, s31\nv_and_b32 v251, 0x7f, v0\ns_lshl_b32 s43, s18, 11\n"
    "s_add_u32 s42, s43, 0xe00000\nv_lshl_add_u32 v57, v251, 4, s42\ns_mov_b32 s10, 4\nv_mov_b64 v[64:65], 0\nv_mov_b64 v[66:67], 0\nv_mov_b64 v[68:69], 0\n"
    "v_mov_b64 v[70:71], 0\nv_mov_b64 v[72:73], 0\nv_mov_b64 v[74:75], 0\nv_mov_b64 v[76:77], 0\nv_mov_b64 v[78:79], 0\nv_mov_b64 v[80:81], 0\nv_mov_b64 v[82:83], 0\n"
    "v_mov_b64 v[84:85], 0\nv_mov_b64 v[86:87], 0\nv_mov_b64 v[88:89], 0\nv_mov_b64 v[90:91], 0\nv_mov_b64 v[92:93], 0\nv_mov_b64 v[94:95], 0\nv_mov_b64 v[96:97], 0\n"
    "v_mov_b64 v[98:99], 0\nv_mov_b64 v[100:101], 0\nv_mov_b64 v[102:103], 0\nv_mov_b64 v[104:105], 0\nv_mov_b64 v[106:107], 0\nv_mov_b64 v[108:109], 0\n"
    "v_mov_b64 v[110:111], 0\nv_mov_b64 v[112:113], 0\nv_mov_b64 v[114:115], 0\nv_mov_b64 v[116:117], 0\nv_mov_b64 v[118:119], 0\nv_mov_b64 v[120:121], 0\n"
    "v_mov_b64 v[122:123], 0\nv_mov_b64 v[124:125], 0\nv_mov_b64 v[126:127], 0\nv_mov_b64 v[128:129], 0\nv_mov_b64 v[130:131], 0\nv_mov_b64 v[132:133], 0\n"
    "v_mov_b64 v[134:135], 0\ns_waitcnt vmcnt(0)\nv_readfirstlane_b32 s11, v166\ns_mul_i32 s11, s11, 0x9E3779B1\ns_add_u32 s11, s11, 0x7F4A7C15\ns_cmp_lg_u32 s3, 0\n"
    "s_cbranch_scc1 .Lgm14w\n"
    ".Lkgp9:\ns_bitcmp1_b32 s3, 1\ns_cselect_b32 s44, 0, 16\nv_lshlrev_b32 v240, s44, v167\nv_and_b32 v224, 1, v0\nv_and_b32 v240, 0xffff0000, v240\n"
    "v_lshlrev_b32 v224, 4, v224\ns_mov_b32 s45, 0x3fb8aa3b\nv_mul_f32 v241, s45, v240\nv_xor_b32 v224, 16, v224\nv_fma_f32 v242, v240, s45, -v241\nv_rndne_f32 v243, v241\n"
    "v_fmamk_f32 v242, v240, 0x32a5705f, v242\nv_sub_f32 v241, v241, v243\nv_add_f32 v241, v241, v242\nv_exp_f32 v241, v241\nv_cvt_i32_f32 v243, v243\n"
    "v_lshlrev_b32 v160, v224, v160\nv_lshlrev_b32 v161, v224, v161\nv_lshlrev_b32 v162, v224, v162\nv_lshlrev_b32 v163, v224, v163\nv_lshlrev_b32 v164, v224, v164\n"
    "v_lshlrev_b32 v165, v224, v165\nv_ldexp_f32 v241, v241, v243\nv_and_b32 v160, 0xffff0000, v160\nv_and_b32 v161, 0xffff0000, v161\nv_and_b32 v162, 0xffff0000, v162\n"
    "v_and_b32 v163, 0xffff0000, v163\nv_and_b32 v164, 0xffff0000, v164\nv_and_b32 v165, 0xffff0000, v165\nv_cvt_pk_bf16_f32 v241, 0, v241\nv_mul_f32 v164, v241, v164\n"
    "v_mul_f32 v165, v241, v165\nv_mul_f32 v16, v241, v160\nv_mul_f32 v17, v241, v161\nv_mul_f32 v242, v162, v164\nv_mul_f32 v243, v163, v165\nv_mul_f32 v244, v162, v165\n"
    "v_mul_f32 v245, v163, v164\nv_sub_f32 v242, v242, v243\nv_add_f32 v243, v244, v245\nv_mul_f32 v16, s45, v16\nv_mul_f32 v17, 0.15915494, v17\nv_mul_f32 v244, v242, v242\n"
    "v_mul_f32 v245, v243, v243\nv_add_f32 v244, v244, v245\nv_log_f32 v244, v244\nv_max_f32 v246, abs(v242), abs(v243)\nv_min_f32 v247, abs(v242), abs(v243)\n"
    "v_mul_f32 v18, 0.5, v244\nv_frexp_mant_f32 v248, v246\nv_max_f32 v244, 0xc2700000, v18\nv_rcp_f32 v248, v248\nv_frexp_exp_i32_f32 v246, v246\n"
    "v_max_f32_dpp v244, v244, v244 row_ror:1 row_mask:0xf bank_mask:0xf\nv_frexp_exp_i32_f32 v249, v247\nv_frexp_mant_f32 v247, v247\n"
    "v_max_f32_dpp v244, v244, v244 row_ror:2 row_mask:0xf bank_mask:0xf\nv_mul_f32 v248, v247, v248\nv_sub_u32 v246, v249, v246\n"
    "v_max_f32_dpp v244, v244, v244 row_ror:4 row_mask:0xf bank_mask:0xf\nv_ldexp_f32 v246, v248, v246\nv_mov_b32 v248, 0xbc7a590c\n"
    "v_max_f32_dpp v244, v244, v244 row_ror:8 row_mask:0xf bank_mask:0xf\nv_mul_f32 v247, v246, v246\nv_mov_b32 v245, v244\nv_fmac_f32 v248, 0x3b2d2a58, v247\n"
    "v_fmaak_f32 v248, v247, v248, 0x3d29fb3f\nv_permlane16_swap_b32 v244, v245\nv_fmaak_f32 v248, v247, v248, 0xbd97d4d7\nv_max_f32 v244, v244, v245\n"
    "v_fmaak_f32 v248, v247, v248, 0x3dd931b2\nv_mov_b32 v245, v244\nv_fmaak_f32 v248, v247, v248, 0xbe1160e6\nv_fmaak_f32 v248, v247, v248, 0x3e4cb8bf\n"
    "v_permlane32_swap_b32 v244, v245\nv_fmaak_f32 v248, v247, v248, 0xbeaaaa62\nv_max_f32 v244, v244, v245\nv_mul_f32 v247, v247, v248\nv_ceil_f32 v244, v244\n"
    "v_fmac_f32 v246, v246, v247\nv_add_f32 v244, 0xc1600000, v244\nv_cmp_gt_f32 vcc, abs(v243), abs(v242)\nv_sub_f32 v247, 0x3fc90fdb, v246\nv_sub_f32 v18, v18, v244\n"
    "v_cndmask_b32 v246, v246, v247, vcc\nv_cmp_gt_f32 vcc, 0, v242\nv_sub_f32 v247, 0x40490fdb, v246\nv_exp_f32 v24, v244\nv_cndmask_b32 v246, v246, v247, vcc\n"
    "s_brev_b32 s44, -2\nv_max_f32 v246, 0, v246\nv_mul_f32 v244, 0x43000000, v16\nv_bfi_b32 v246, s44, v246, v243\nv_mul_f32 v245, 0x43000000, v17\n"
    "v_mul_f32 v19, 0xbe22f983, v246\nv_mul_f32 v247, 4.0, v16\nv_mul_f32 v248, 4.0, v17\nv_fract_f32 v245, v245\nv_fract_f32 v248, v248\nv_exp_f32 v244, v244\n"
    "v_exp_f32 v247, v247\nv_cos_f32 v20, v245\nv_sin_f32 v21, v245\nv_cos_f32 v22, v248\nv_sin_f32 v23, v248\nv_mul_f32 v20, v244, v20\nv_mul_f32 v21, v244, v21\n"
    "v_mul_f32 v22, v247, v22\nv_mul_f32 v23, v247, v23\nv_and_b32 v251, 15, v0\nv_mov_b32 v250, 0x102\nv_min_u32 v249, 12, v251\ns_mov_b32 s43, 0xfffffc00\n"
    "v_lshl_add_u32 v248, v249, 8, s43\nv_cmp_gt_u32 vcc, 5, v251\ns_nop 1\nv_cndmask_b32 v247, v248, v250, vcc\nv_cmp_gt_u32 vcc, 4, v251\ns_cbranch_execz .Lhop\ns_nop 1\n"
    "v_cndmask_b32 v25, v247, v251, vcc\nv_lshlrev_b32 v26, 3, v251\nv_cvt_f32_u32 v25, v25\nv_cvt_f32_u32 v26, v26\ns_cmp_gt_u32 s3, 3\n"
    "s_cselect_b32 s94, .Lhalo10-.Lgm14, 0\ns_add_u32 s94, s94, .Lgm14-.Lk_all_beg+4\ns_add_u32 s94, s94, s92\ns_addc_u32 s95, s93, 0\n"
    ".Lkgl13:\nv_mov_b32_dpp v240, v18 row_newbcast:0 row_mask:0xf bank_mask:0xf\nv_mov_b32_dpp v241, v19 row_newbcast:0 row_mask:0xf bank_mask:0xf\n"
    "v_mul_f32_dpp v242, v16, v26 row_newbcast:0 row_mask:0xf bank_mask:0xf\nv_mul_f32_dpp v243, v17, v26 row_newbcast:0 row_mask:0xf bank_mask:0xf\n"
    "v_fmac_f32_dpp v240, v16, v25 row_newbcast:0 row_mask:0xf bank_mask:0xf\nv_fmac_f32_dpp v241, -v17, v25 row_newbcast:0 row_mask:0xf bank_mask:0xf\n"
    "v_fract_f32 v243, v243\nv_exp_f32 v242, v242\nv_fract_f32 v241, v241\nv_exp_f32 v240, v240\nv_cos_f32 v244, v243\nv_sin_f32 v245, v243\nv_cos_f32 v246, v241\n"
    "v_sin_f32 v247, v241\nv_mul_f32 v244, v242, v244\nv_mul_f32 v245, v242, v245\nv_mul_f32 v246, v240, v246\nv_mul_f32 v247, v240, v247\n"
    "v_mul_f32_dpp v248, v20, v244 row_newbcast:0 row_mask:0xf bank_mask:0xf\nv_mul_f32_dpp v249, v20, v245 row_newbcast:0 row_mask:0xf bank_mask:0xf\n"
    "v_mul_f32_dpp v250, v22, v246 row_newbcast:0 row_mask:0xf bank_mask:0xf\nv_mul_f32_dpp v251, v22, v247 row_newbcast:0 row_mask:0xf bank_mask:0xf\n"
    "v_fmac_f32_dpp v248, -v21, v245 row_newbcast:0 row_mask:0xf bank_mask:0xf\nv_fmac_f32_dpp v249, v21, v244 row_newbcast:0 row_mask:0xf bank_mask:0xf\n"
    "v_fmac_f32_dpp v250, v23, v247 row_newbcast:0 row_mask:0xf bank_mask:0xf\nv_fmac_f32_dpp v251, -v23, v246 row_newbcast:0 row_mask:0xf bank_mask:0xf\n"
    "v_cvt_pk_f16_f32 v232, v244, v245\nv_cvt_pk_f16_f32 v224, v246, v247\nv_cvt_pk_f16_f32 v236, v248, v249\nv_cvt_pk_f16_f32 v228, v250, v251\n"
    "v_mov_b32_dpp v240, v18 row_newbcast:1 row_mask:0xf bank_mask:0xf\nv_mov_b32_dpp v241, v19 row_newbcast:1 row_mask:0xf bank_mask:0xf\n"
    "v_mul_f32_dpp v242, v16, v26 row_newbcast:1 row_mask:0xf bank_mask:0xf\nv_mul_f32_dpp v243, v17, v26 row_newbcast:1 row_mask:0xf bank_mask:0xf\n"
    "v_fmac_f32_dpp v240, v16, v25 row_newbcast:1 row_mask:0xf bank_mask:0xf\nv_fmac_f32_dpp v241, -v17, v25 row_newbcast:1 row_mask:0xf bank_mask:0xf\n"
    "v_fract_f32 v243, v243\nv_exp_f32 v242, v242\nv_fract_f32 v241, v241\nv_exp_f32 v240, v240\nv_cos_f32 v244, v243\nv_sin_f32 v245, v243\nv_cos_f32 v246, v241\n"
    "v_sin_f32 v247, v241\nv_mul_f32 v244, v242, v244\nv_mul_f32 v245, v242, v245\nv_mul_f32 v246, v240, v246\nv_mul_f32 v247, v240, v247\n"
    "v_mul_f32_dpp v248, v20, v244 row_newbcast:1 row_mask:0xf bank_mask:0xf\nv_mul_f32_dpp v249, v20, v245 row_newbcast:1 row_mask:0xf bank_mask:0xf\n"
    "v_mul_f32_dpp v250, v22, v246 row_newbcast:1 row_mask:0xf bank_mask:0xf\nv_mul_f32_dpp v251, v22, v247 row_newbcast:1 row_mask:0xf bank_mask:0xf\n"
    "v_fmac_f32_dpp v248, -v21, v245 row_newbcast:1 row_mask:0xf bank_mask:0xf\nv_fmac_f32_dpp v249, v21, v244 row_newbcast:1 row_mask:0xf bank_mask:0xf\n"
    "v_fmac_f32_dpp v250, v23, v247 row_newbcast:1 row_mask:0xf bank_mask:0xf\nv_fmac_f32_dpp v251, -v23, v246 row_newbcast:1 row_mask:0xf bank_mask:0xf\n"
    "v_cvt_pk_f16_f32 v233, v244, v245\nv_cvt_pk_f16_f32 v225, v246, v247\nv_cvt_pk_f16_f32 v237, v248, v249\nv_cvt_pk_f16_f32 v229, v250, v251\n"
    "v_mov_b32_dpp v240, v18 row_newbcast:2 row_mask:0xf bank_mask:0xf\nv_mov_b32_dpp v241, v19 row_newbcast:2 row_mask:0xf bank_mask:0xf\n"
    "v_mul_f32_dpp v242, v16, v26 row_newbcast:2 row_mask:0xf bank_mask:0xf\nv_mul_f32_dpp v243, v17, v26 row_newbcast:2 row_mask:0xf bank_mask:0xf\n"
    "v_fmac_f32_dpp v240, v16, v25 row_newbcast:2 row_mask:0xf bank_mask:0xf\nv_fmac_f32_dpp v241, -v17, v25 row_newbcast:2 row_mask:0xf bank_mask:0xf\n"
    "v_fract_f32 v243, v243\nv_exp_f32 v242, v242\nv_fract_f32 v241, v241\nv_exp_f32 v240, v240\nv_cos_f32 v244, v243\nv_sin_f32 v245, v243\nv_cos_f32 v246, v241\n"
    "v_sin_f32 v247, v241\nv_mul_f32 v244, v242, v244\nv_mul_f32 v245, v242, v245\nv_mul_f32 v246, v240, v246\nv_mul_f32 v247, v240, v247\n"
    "v_mul_f32_dpp v248, v20, v244 row_newbcast:2 row_mask:0xf bank_mask:0xf\nv_mul_f32_dpp v249, v20, v245 row_newbcast:2 row_mask:0xf bank_mask:0xf\n"
    "v_mul_f32_dpp v250, v22, v246 row_newbcast:2 row_mask:0xf bank_mask:0xf\nv_mul_f32_dpp v251, v22, v247 row_newbcast:2 row_mask:0xf bank_mask:0xf\n"
    "v_fmac_f32_dpp v248, -v21, v245 row_newbcast:2 row_mask:0xf bank_mask:0xf\nv_fmac_f32_dpp v249, v21, v244 row_newbcast:2 row_mask:0xf bank_mask:0xf\n"
    "v_fmac_f32_dpp v250, v23, v247 row_newbcast:2 row_mask:0xf bank_mask:0xf\nv_fmac_f32_dpp v251, -v23, v246 row_newbcast:2 row_mask:0xf bank_mask:0xf\n"
    "v_cvt_pk_f16_f32 v234, v244, v245\nv_cvt_pk_f16_f32 v226, v246, v247\nv_cvt_pk_f16_f32 v238, v248, v249\nv_cvt_pk_f16_f32 v230, v250, v251\nds_read_b32 v57, v56\n"
    "v_mov_b32_dpp v240, v18 row_newbcast:3 row_mask:0xf bank_mask:0xf\nv_mov_b32_dpp v241, v19 row_newbcast:3 row_mask:0xf bank_mask:0xf\n"
    "v_mul_f32_dpp v242, v16, v26 row_newbcast:3 row_mask:0xf bank_mask:0xf\nv_mul_f32_dpp v243, v17, v26 row_newbcast:3 row_mask:0xf bank_mask:0xf\n"
    "v_fmac_f32_dpp v240, v16, v25 row_newbcast:3 row_mask:0xf bank_mask:0xf\nv_fmac_f32_dpp v241, -v17, v25 row_newbcast:3 row_mask:0xf bank_mask:0xf\n"
    "v_fract_f32 v243, v243\nv_exp_f32 v242, v242\nv_fract_f32 v241, v241\nv_exp_f32 v240, v240\nv_cos_f32 v244, v243\nv_sin_f32 v245, v243\nv_cos_f32 v246, v241\n"
    "v_sin_f32 v247, v241\nv_mul_f32 v244, v242, v244\nv_mul_f32 v245, v242, v245\nv_mul_f32 v246, v240, v246\nv_mul_f32 v247, v240, v247\n"
    "v_mul_f32_dpp v248, v20, v244 row_newbcast:3 row_mask:0xf bank_mask:0xf\nv_mul_f32_dpp v249, v20, v245 row_newbcast:3 row_mask:0xf bank_mask:0xf\n"
    "v_mul_f32_dpp v250, v22, v246 row_newbcast:3 row_mask:0xf bank_mask:0xf\nv_mul_f32_dpp v251, v22, v247 row_newbcast:3 row_mask:0xf bank_mask:0xf\n"
    "v_fmac_f32_dpp v248, -v21, v245 row_newbcast:3 row_mask:0xf bank_mask:0xf\nv_fmac_f32_dpp v249, v21, v244 row_newbcast:3 row_mask:0xf bank_mask:0xf\n"
    "v_fmac_f32_dpp v250, v23, v247 row_newbcast:3 row_mask:0xf bank_mask:0xf\nv_fmac_f32_dpp v251, -v23, v246 row_newbcast:3 row_mask:0xf bank_mask:0xf\n"
    "v_cvt_pk_f16_f32 v235, v244, v245\nv_cvt_pk_f16_f32 v227, v246, v247\nv_cvt_pk_f16_f32 v239, v248, v249\nv_cvt_pk_f16_f32 v231, v250, v251\ns_waitcnt lgkmcnt(0)\n"
    "v_readfirstlane_b32 s44, v57\nv_mov_b32_dpp v16, v16 row_ror:12 row_mask:0xf bank_mask:0xf\nv_mov_b32_dpp v17, v17 row_ror:12 row_mask:0xf bank_mask:0xf\n"
    "v_mfma_f32_16x16x32_f16 v[40:43], v[224:227], v[232:235], v[40:43]\nv_mov_b32_dpp v18, v18 row_ror:12 row_mask:0xf bank_mask:0xf\n"
    "v_mov_b32_dpp v19, v19 row_ror:12 row_mask:0xf bank_mask:0xf\nv_mfma_f32_16x16x32_f16 v[44:47], v[224:227], v[236:239], v[44:47]\n"
    "v_mov_b32_dpp v20, v20 row_ror:12 row_mask:0xf bank_mask:0xf\nv_mov_b32_dpp v21, v21 row_ror:12 row_mask:0xf bank_mask:0xf\n"
    "v_mfma_f32_16x16x32_f16 v[48:51], v[228:231], v[232:235], v[48:51]\nv_mov_b32_dpp v22, v22 row_ror:12 row_mask:0xf bank_mask:0xf\n"
    "v_mov_b32_dpp v23, v23 row_ror:12 row_mask:0xf bank_mask:0xf\nv_mfma_f32_16x16x32_f16 v[52:55], v[228:231], v[236:239], v[52:55]\ns_sub_u32 s27, s27, 1\n"
    "s_cmp_eq_u32 s27, 0\ns_cbranch_scc1 .Lkx15\ns_cmp_lt_i32 s44, s10\ns_cbranch_scc1 .Lkgl13\ns_setpc_b64 s[94:95]\n.Lkx15:\nv_and_b32 v251, 63, v0\n"
    "s_lshl_b32 s43, s13, 12\ns_add_u32 s42, s43, 0x1f600\nv_lshl_add_u32 v33, v251, 4, s42\ns_lshl_b32 s41, s13, 2\ns_add_u32 s40, s41, 0x27d80\nv_mov_b32 v242, s40\n"
    "v_mul_f32 v40, v40, v24\nv_mul_f32 v41, v41, v24\nv_mul_f32 v42, v42, v24\nv_mul_f32 v43, v43, v24\nv_mul_f32 v44, v44, v24\nv_mul_f32 v45, v45, v24\n"
    "v_mul_f32 v46, v46, v24\ns_cbranch_execz .Lhop\nv_mul_f32 v47, v47, v24\nv_mul_f32 v48, v48, v24\nv_mul_f32 v49, v49, v24\nv_mul_f32 v50, v50, v24\n"
    "v_mul_f32 v51, v51, v24\nv_mul_f32 v52, v52, v24\nv_mul_f32 v53, v53, v24\nv_mul_f32 v54, v54, v24\nv_mul_f32 v55, v55, v24\n"
    "v_max3_f32 v240, abs(v40), abs(v41), abs(v42)\nv_max3_f32 v241, abs(v43), abs(v44), abs(v45)\nv_max3_f32 v240, v240, abs(v46), abs(v47)\n"
    "v_max3_f32 v241, v241, abs(v48), abs(v49)\nv_max3_f32 v240, v240, abs(v50), abs(v51)\nv_max3_f32 v241, v241, abs(v52), abs(v53)\n"
    "v_max3_f32 v240, v240, abs(v54), abs(v55)\nv_max_f32 v240, v240, v241\ns_nop 1\nv_max_f32_dpp v240, v240, v240 row_ror:1 row_mask:0xf bank_mask:0xf\ns_nop 1\n"
    "v_max_f32_dpp v240, v240, v240 row_ror:2 row_mask:0xf bank_mask:0xf\ns_nop 1\nv_max_f32_dpp v240, v240, v240 row_ror:4 row_mask:0xf bank_mask:0xf\ns_nop 1\n"
    "v_max_f32_dpp v240, v240, v240 row_ror:8 row_mask:0xf bank_mask:0xf\nv_mov_b32 v241, v240\ns_nop 1\nv_permlane16_swap_b32 v240, v241\nv_max_f32 v240, v240, v241\n"
    "v_mov_b32 v241, v240\ns_nop 1\nv_permlane32_swap_b32 v240, v241\nv_max_f32 v240, v240, v241\ns_mov_b64 exec, 1\nds_write_b32 v242, v240\ns_mov_b64 exec, -1\n"
    "s_setpc_b64 s[94:95]\n"
    ".Lgm14w:\ns_cmp_eq_u32 s3, 1\ns_cbranch_scc1 .Lgm14v\nv_lshl_or_b32 v168, v169, 16, v168\nv_lshl_or_b32 v169, v171, 16, v170\nv_lshl_or_b32 v170, v173, 16, v172\n"
    "v_lshl_or_b32 v171, v175, 16, v174\nglobal_store_dwordx4 v57, v[168:171], s[4:5] sc1\n.Lgm14v:\ns_mul_i32 s88, s13, 9\ns_add_u32 s88, s88, -4\ns_lshl_b32 s90, s88, 10\n"
    "s_add_u32 s90, s92, s90\ns_addc_u32 s91, s93, 0\ns_sub_u32 s89, 9, 1\ns_mov_b64 exec, 0\ns_swappc_b64 s[94:95], s[90:91]\n.Lz161:\nv_mov_b32 v240, 1\n"
    "v_mov_b32 v164, s11\ns_mov_b64 exec, 1\ns_waitcnt vmcnt(0)\nds_write_b32 v30, v240\ns_mov_b64 exec, -1\ns_cmp_lg_u32 s13, 3\ns_cbranch_scc1 .Lgm14\n"
    "s_mov_b32 s86, 0x100000\n.Ls18:\nds_read_b32 v243, v31 offset:0\ns_waitcnt lgkmcnt(0)\nv_cmp_gt_i32 vcc, 1, v243\ns_sub_u32 s86, s86, 1\ns_cbranch_scc1 .Ls19\n"
    "s_cbranch_vccz .Ls19\ns_sleep 1\ns_branch .Ls18\n.Ls19:\ns_mov_b64 exec, 1\nglobal_store_dword v32, v164, s[4:5] sc1\ns_mov_b64 exec, -1\n.Lgm14:\n"
    "s_mov_b32 s81, 0x3b000000\ns_mov_b32 s84, -1\ns_mov_b32 s85, 0\nv_mov_b32 v241, 0x3f803f80\ns_mov_b32 s46, 0\ns_mov_b32 s47, s23\ns_mul_i32 s44, s23, 12288\n"
    "v_add_u32 v160, s46, v27\nv_xor_b32 v161, 32, v160\nv_xor_b32 v162, 64, v160\nv_xor_b32 v163, 0x60, v160\nv_add_u32 v242, s44, v36\n.Lgf16:\nv_mov_b32 v240, 2\n"
    "s_mov_b32 s45, 3\ns_mov_b32 s86, 0x100000\n.Ls20:\nds_read_b32 v243, v56 offset:0\ns_waitcnt lgkmcnt(0)\nv_cmp_gt_i32 vcc, 1, v243\ns_sub_u32 s86, s86, 1\n"
    "s_cbranch_scc1 .Ls21\ns_cbranch_vccz .Ls21\ns_sleep 2\ns_branch .Ls20\n.Ls21:\nds_read_b64_tr_b16 v[208:209], v242 offset:0\n"
    "ds_read_b64_tr_b16 v[210:211], v242 offset:256\nds_read_b64_tr_b16 v[212:213], v242 offset:3072\nds_read_b64_tr_b16 v[214:215], v242 offset:3328\n"
    "v_cmp_gt_i32 vcc, 2, v243\ns_cbranch_vccz .Ls23\ns_mov_b32 s86, 0x100000\n.Ls22:\nds_read_b32 v243, v56 offset:0\ns_waitcnt lgkmcnt(0)\nv_cmp_gt_i32 vcc, 2, v243\n"
    "s_sub_u32 s86, s86, 1\ns_cbranch_scc1 .Ls23\ns_cbranch_vccz .Ls23\ns_sleep 1\ns_branch .Ls22\n.Ls23:\nds_read_b128 v[144:147], v160\n"
    "ds_read_b64_tr_b16 v[216:217], v242 offset:6144\nds_read_b64_tr_b16 v[218:219], v242 offset:6400\nds_read_b128 v[148:151], v161\n"
    "ds_read_b64_tr_b16 v[220:221], v242 offset:9216\nds_read_b64_tr_b16 v[222:223], v242 offset:9472\nds_read_b128 v[152:155], v162\n"
    "ds_read_b64_tr_b16 v[224:225], v242 offset:1024\nds_read_b64_tr_b16 v[226:227], v242 offset:1280\nds_read_b128 v[156:159], v163\n"
    "ds_read_b64_tr_b16 v[228:229], v242 offset:4096\nds_read_b64_tr_b16 v[230:231], v242 offset:4352\n.Lgl17:\ns_waitcnt lgkmcnt(11)\nv_dot2c_f32_bf16 v112, v241, v144\n"
    "v_dot2c_f32_bf16 v113, v144, v144\nv_dot2c_f32_bf16 v114, v241, v145\nv_dot2c_f32_bf16 v115, v145, v145\nv_dot2c_f32_bf16 v116, v241, v146\n"
    "v_dot2c_f32_bf16 v117, v146, v146\nv_dot2c_f32_bf16 v118, v241, v147\nv_dot2c_f32_bf16 v119, v147, v147\ns_waitcnt lgkmcnt(8)\nv_dot2c_f32_bf16 v112, v241, v148\n"
    "v_dot2c_f32_bf16 v113, v148, v148\nv_dot2c_f32_bf16 v114, v241, v149\nv_dot2c_f32_bf16 v115, v149, v149\nv_dot2c_f32_bf16 v116, v241, v150\n"
    "v_dot2c_f32_bf16 v117, v150, v150\nv_dot2c_f32_bf16 v118, v241, v151\nv_dot2c_f32_bf16 v119, v151, v151\ns_waitcnt lgkmcnt(5)\nv_dot2c_f32_bf16 v112, v241, v152\n"
    "v_dot2c_f32_bf16 v113, v152, v152\nv_dot2c_f32_bf16 v114, v241, v153\nv_dot2c_f32_bf16 v115, v153, v153\nv_dot2c_f32_bf16 v116, v241, v154\n"
    "v_dot2c_f32_bf16 v117, v154, v154\nv_dot2c_f32_bf16 v118, v241, v155\nv_dot2c_f32_bf16 v119, v155, v155\ns_waitcnt lgkmcnt(2)\nv_dot2c_f32_bf16 v112, v241, v156\n"
    "v_dot2c_f32_bf16 v113, v156, v156\nv_dot2c_f32_bf16 v114, v241, v157\nv_dot2c_f32_bf16 v115, v157, v157\nv_dot2c_f32_bf16 v116, v241, v158\n"
    "v_dot2c_f32_bf16 v117, v158, v158\nv_dot2c_f32_bf16 v118, v241, v159\nv_dot2c_f32_bf16 v119, v159, v159\n"
    "v_mfma_f32_32x32x16_bf16 v[64:79], v[208:211], v[144:147], v[64:79]\nds_read_b64_tr_b16 v[208:209], v242 offset:7168\nds_read_b64_tr_b16 v[210:211], v242 offset:7424\n"
    "s_mov_b64 exec, 1\nds_write_b32 v30, v240\ns_mov_b64 exec, -1\nv_mfma_f32_32x32x16_bf16 v[64:79], v[212:215], v[148:151], v[64:79]\n"
    "ds_read_b64_tr_b16 v[212:213], v242 offset:10240\nds_read_b64_tr_b16 v[214:215], v242 offset:10496\nds_read_b32 v243, v56\ns_add_u32 s47, s47, 1\n"
    "s_add_u32 s46, s46, 4096\nv_mfma_f32_32x32x16_bf16 v[64:79], v[216:219], v[152:155], v[64:79]\nds_read_b64_tr_b16 v[216:217], v242 offset:2048\n"
    "ds_read_b64_tr_b16 v[218:219], v242 offset:2304\ns_and_b32 s47, s47, 7\ns_cmp_eq_u32 s46, 12288\ns_cselect_b32 s46, 0, s46\n"
    "v_mfma_f32_32x32x16_bf16 v[64:79], v[220:223], v[156:159], v[64:79]\nds_read_b64_tr_b16 v[220:221], v242 offset:5120\nds_read_b64_tr_b16 v[222:223], v242 offset:5376\n"
    "s_mul_i32 s44, s47, 12288\nv_add_u32 v160, s46, v27\nv_xor_b32 v161, 32, v160\nv_mfma_f32_32x32x16_bf16 v[80:95], v[224:227], v[144:147], v[80:95]\n"
    "ds_read_b64_tr_b16 v[224:225], v242 offset:8192\nds_read_b64_tr_b16 v[226:227], v242 offset:8448\ns_waitcnt lgkmcnt(6)\nv_readfirstlane_b32 s49, v243\n"
    "ds_read_b32 v243, v56\ns_nop 0\ns_cbranch_execz .Lhop\nv_mfma_f32_32x32x16_bf16 v[80:95], v[228:231], v[148:151], v[80:95]\n"
    "ds_read_b64_tr_b16 v[228:229], v242 offset:11264\nds_read_b64_tr_b16 v[230:231], v242 offset:11520\nv_add_u32 v242, s44, v36\ns_cmp_lt_i32 s49, s45\n"
    "s_cbranch_scc1 .Lc24\n.Lc25:\nv_mfma_f32_32x32x16_bf16 v[80:95], v[208:211], v[152:155], v[80:95]\nds_read_b64_tr_b16 v[208:209], v242 offset:0\n"
    "ds_read_b64_tr_b16 v[210:211], v242 offset:256\ns_add_u32 s45, s45, 1\nv_xor_b32 v162, 64, v160\nv_xor_b32 v163, 0x60, v160\n"
    "v_mfma_f32_32x32x16_bf16 v[80:95], v[212:215], v[156:159], v[80:95]\nds_read_b64_tr_b16 v[212:213], v242 offset:3072\nds_read_b64_tr_b16 v[214:215], v242 offset:3328\n"
    "s_waitcnt lgkmcnt(6)\nv_readfirstlane_b32 s49, v243\nv_add_u32 v240, 2, v240\nv_mfma_f32_32x32x16_bf16 v[96:111], v[216:219], v[144:147], v[96:111]\n"
    "s_cmp_lt_i32 s49, s45\ns_cbranch_scc1 .Lc27\n.Lc28:\nds_read_b128 v[144:147], v160\nds_read_b64_tr_b16 v[216:217], v242 offset:6144\n"
    "ds_read_b64_tr_b16 v[218:219], v242 offset:6400\nv_mfma_f32_32x32x16_bf16 v[96:111], v[220:223], v[148:151], v[96:111]\nds_read_b128 v[148:151], v161\n"
    "ds_read_b64_tr_b16 v[220:221], v242 offset:9216\nds_read_b64_tr_b16 v[222:223], v242 offset:9472\ns_add_u32 s45, s45, 1\n"
    "v_mfma_f32_32x32x16_bf16 v[96:111], v[224:227], v[152:155], v[96:111]\nds_read_b128 v[152:155], v162\nds_read_b64_tr_b16 v[224:225], v242 offset:1024\n"
    "ds_read_b64_tr_b16 v[226:227], v242 offset:1280\ns_cmp_lt_u32 s45, 17\ns_waitcnt lgkmcnt(13)\nv_mfma_f32_32x32x16_bf16 v[96:111], v[228:231], v[156:159], v[96:111]\n"
    "ds_read_b128 v[156:159], v163\nds_read_b64_tr_b16 v[228:229], v242 offset:4096\nds_read_b64_tr_b16 v[230:231], v242 offset:4352\ns_cbranch_scc1 .Lgl17\n"
    "s_waitcnt lgkmcnt(11)\nv_dot2c_f32_bf16 v112, v241, v144\nv_dot2c_f32_bf16 v113, v144, v144\nv_dot2c_f32_bf16 v114, v241, v145\nv_dot2c_f32_bf16 v115, v145, v145\n"
    "v_dot2c_f32_bf16 v116, v241, v146\nv_dot2c_f32_bf16 v117, v146, v146\nv_dot2c_f32_bf16 v118, v241, v147\nv_dot2c_f32_bf16 v119, v147, v147\ns_waitcnt lgkmcnt(8)\n"
    "v_dot2c_f32_bf16 v112, v241, v148\nv_dot2c_f32_bf16 v113, v148, v148\nv_dot2c_f32_bf16 v114, v241, v149\nv_dot2c_f32_bf16 v115, v149, v149\n"
    "v_dot2c_f32_bf16 v116, v241, v150\nv_dot2c_f32_bf16 v117, v150, v150\nv_dot2c_f32_bf16 v118, v241, v151\nv_dot2c_f32_bf16 v119, v151, v151\ns_waitcnt lgkmcnt(5)\n"
    "v_dot2c_f32_bf16 v112, v241, v152\nv_dot2c_f32_bf16 v113, v152, v152\nv_dot2c_f32_bf16 v114, v241, v153\nv_dot2c_f32_bf16 v115, v153, v153\n"
    "v_dot2c_f32_bf16 v116, v241, v154\nv_dot2c_f32_bf16 v117, v154, v154\nv_dot2c_f32_bf16 v118, v241, v155\nv_dot2c_f32_bf16 v119, v155, v155\ns_waitcnt lgkmcnt(2)\n"
    "v_dot2c_f32_bf16 v112, v241, v156\nv_dot2c_f32_bf16 v113, v156, v156\nv_dot2c_f32_bf16 v114, v241, v157\nv_dot2c_f32_bf16 v115, v157, v157\n"
    "v_dot2c_f32_bf16 v116, v241, v158\nv_dot2c_f32_bf16 v117, v158, v158\nv_dot2c_f32_bf16 v118, v241, v159\nv_dot2c_f32_bf16 v119, v159, v159\n"
    "v_mfma_f32_32x32x16_bf16 v[64:79], v[208:211], v[144:147], v[64:79]\nds_read_b64_tr_b16 v[208:209], v242 offset:7168\nds_read_b64_tr_b16 v[210:211], v242 offset:7424\n"
    "s_mov_b64 exec, 1\nds_write_b32 v30, v240\ns_mov_b64 exec, -1\nv_mfma_f32_32x32x16_bf16 v[64:79], v[212:215], v[148:151], v[64:79]\n"
    "ds_read_b64_tr_b16 v[212:213], v242 offset:10240\nds_read_b64_tr_b16 v[214:215], v242 offset:10496\nv_add_f32 v112, v112, v114\nv_add_f32 v113, v113, v115\n"
    "v_add_f32 v116, v116, v118\nv_add_f32 v117, v117, v119\nv_mfma_f32_32x32x16_bf16 v[64:79], v[216:219], v[152:155], v[64:79]\n"
    "ds_read_b64_tr_b16 v[216:217], v242 offset:2048\nds_read_b64_tr_b16 v[218:219], v242 offset:2304\nv_add_f32 v112, v112, v116\nv_add_f32 v113, v113, v117\n"
    "v_mfma_f32_32x32x16_bf16 v[64:79], v[220:223], v[156:159], v[64:79]\nds_read_b32 v243, v28\nds_read_b32 v124, v38 offset:0\nds_read_b32 v128, v38 offset:128\n"
    "ds_read_b32 v132, v38 offset:256\nds_read_b64_tr_b16 v[220:221], v242 offset:5120\nds_read_b64_tr_b16 v[222:223], v242 offset:5376\nv_permlane32_swap_b32 v112, v113\n"
    "v_add_f32 v138, v112, v113\nv_add_f32 v139, v112, v113\nv_mfma_f32_32x32x16_bf16 v[80:95], v[224:227], v[144:147], v[80:95]\n"
    "ds_read_b64_tr_b16 v[224:225], v242 offset:8192\nds_read_b64_tr_b16 v[226:227], v242 offset:8448\nv_permlane32_swap_b32 v138, v139\nv_mul_f32 v138, s81, v138\n"
    "v_mul_f32 v112, v138, v138\nv_cvt_pk_bf16_f32 v138, -v138, -v138\ns_waitcnt lgkmcnt(12)\nv_mfma_f32_32x32x16_bf16 v[80:95], v[228:231], v[148:151], v[80:95]\n"
    "ds_read_b64_tr_b16 v[228:229], v242 offset:11264\nds_read_b64_tr_b16 v[230:231], v242 offset:11520\nv_cndmask_b32 v120, 0, v138, s[84:85]\n"
    "v_fma_f32 v139, v139, s81, -v112\nv_max_f32 v139, 0, v139\nv_cvt_pk_bf16_f32 v139, 0, v139\nv_mfma_f32_32x32x16_bf16 v[80:95], v[208:211], v[152:155], v[80:95]\n"
    "s_waitcnt lgkmcnt(6)\nv_cmp_gt_i32 vcc, v29, v243\nv_add_f32 v139, 0x3727c5ac, v139\nv_cvt_pk_bf16_f32 v139, 0, v139\nv_sqrt_f32 v139, v139\n"
    "v_mfma_f32_32x32x16_bf16 v[80:95], v[212:215], v[156:159], v[80:95]\nv_cvt_pk_bf16_f32 v139, 0, v139\nv_rcp_f32 v136, v139\nv_mov_b64 v[232:233], 0\n"
    "v_mov_b64 v[234:235], 0\ns_cbranch_vccnz .Lt30\n.Lt31:\nv_mfma_f32_32x32x16_bf16 v[64:79], v[124:127], v[120:123], v[64:79]\nv_mov_b32 v137, v136\n"
    "v_mov_b64 v[236:237], 0\nv_mov_b64 v[238:239], 0\nv_mov_b64 v[244:245], 0\nv_mov_b64 v[246:247], 0\nv_mfma_f32_32x32x16_bf16 v[80:95], v[128:131], v[120:123], v[80:95]\n"
    "v_mov_b64 v[248:249], 0\nv_mov_b64 v[250:251], 0\ns_mov_b32 s44, 0x00ff00ff\ns_mov_b32 s45, 0xff00ff00\ns_mov_b32 s46, s44\ns_mov_b32 s47, s44\ns_waitcnt lgkmcnt(0)\n"
    "v_mfma_f32_32x32x16_bf16 v[96:111], v[216:219], v[144:147], v[96:111]\nv_mul_f32 v64, v64, v136\nv_mul_f32 v65, v65, v136\nv_mul_f32 v66, v66, v136\n"
    "v_mul_f32 v67, v67, v136\nv_cvt_pk_bf16_f32 v64, v64, v65\nv_cvt_pk_bf16_f32 v65, v66, v67\nds_write_b64 v37, v[64:65] offset:0\n"
    "v_mfma_f32_32x32x16_bf16 v[96:111], v[220:223], v[148:151], v[96:111]\nv_mul_f32 v68, v68, v136\nv_mul_f32 v69, v69, v136\nv_mul_f32 v70, v70, v136\n"
    "v_mul_f32 v71, v71, v136\nv_cvt_pk_bf16_f32 v68, v68, v69\nv_cvt_pk_bf16_f32 v69, v70, v71\nds_write_b64 v37, v[68:69] offset:16\n"
    "v_mfma_f32_32x32x16_bf16 v[96:111], v[224:227], v[152:155], v[96:111]\nv_mul_f32 v72, v72, v136\nv_mul_f32 v73, v73, v136\nv_mul_f32 v74, v74, v136\n"
    "v_mul_f32 v75, v75, v136\nv_cvt_pk_bf16_f32 v72, v72, v73\nv_cvt_pk_bf16_f32 v73, v74, v75\nds_write_b64 v37, v[72:73] offset:32\n"
    "v_mfma_f32_32x32x16_bf16 v[96:111], v[228:231], v[156:159], v[96:111]\nv_mul_f32 v76, v76, v136\nv_mul_f32 v77, v77, v136\nv_mul_f32 v78, v78, v136\n"
    "v_mul_f32 v79, v79, v136\nv_cvt_pk_bf16_f32 v76, v76, v77\nv_cvt_pk_bf16_f32 v77, v78, v79\nds_write_b64 v37, v[76:77] offset:48\ns_cbranch_execz .Lhop\n"
    "v_mfma_f32_32x32x16_bf16 v[96:111], v[132:135], v[120:123], v[96:111]\nv_pk_mul_f32 v[80:81], v[80:81], v[136:137]\nv_pk_mul_f32 v[82:83], v[82:83], v[136:137]\n"
    "v_cvt_pk_bf16_f32 v80, v80, v81\nv_cvt_pk_bf16_f32 v81, v82, v83\nds_write_b64 v37, v[80:81] offset:64\nv_pk_mul_f32 v[84:85], v[84:85], v[136:137]\n"
    "v_pk_mul_f32 v[86:87], v[86:87], v[136:137]\nv_cvt_pk_bf16_f32 v84, v84, v85\nv_cvt_pk_bf16_f32 v85, v86, v87\nds_write_b64 v37, v[84:85] offset:80\n"
    "v_pk_mul_f32 v[88:89], v[88:89], v[136:137]\nv_pk_mul_f32 v[90:91], v[90:91], v[136:137]\nv_cvt_pk_bf16_f32 v88, v88, v89\nv_cvt_pk_bf16_f32 v89, v90, v91\n"
    "ds_write_b64 v37, v[88:89] offset:96\nv_pk_mul_f32 v[92:93], v[92:93], v[136:137]\nv_pk_mul_f32 v[94:95], v[94:95], v[136:137]\nv_cvt_pk_bf16_f32 v92, v92, v93\n"
    "v_cvt_pk_bf16_f32 v93, v94, v95\nds_write_b64 v37, v[92:93] offset:112\nv_pk_mul_f32 v[96:97], v[96:97], v[136:137]\nv_pk_mul_f32 v[98:99], v[98:99], v[136:137]\n"
    "v_cvt_pk_bf16_f32 v96, v96, v97\nv_cvt_pk_bf16_f32 v97, v98, v99\nds_write_b64 v37, v[96:97] offset:128\nv_pk_mul_f32 v[100:101], v[100:101], v[136:137]\n"
    "v_pk_mul_f32 v[102:103], v[102:103], v[136:137]\nv_cvt_pk_bf16_f32 v100, v100, v101\nv_cvt_pk_bf16_f32 v101, v102, v103\nds_write_b64 v37, v[100:101] offset:144\n"
    "v_pk_mul_f32 v[104:105], v[104:105], v[136:137]\nv_pk_mul_f32 v[106:107], v[106:107], v[136:137]\nv_cvt_pk_bf16_f32 v104, v104, v105\nv_cvt_pk_bf16_f32 v105, v106, v107\n"
    "ds_write_b64 v37, v[104:105] offset:160\nv_pk_mul_f32 v[108:109], v[108:109], v[136:137]\nv_pk_mul_f32 v[110:111], v[110:111], v[136:137]\n"
    "v_cvt_pk_bf16_f32 v108, v108, v109\nv_cvt_pk_bf16_f32 v109, v110, v111\nds_write_b64 v37, v[108:109] offset:176\ns_branch .Latt12\n.Lc24:\ns_mov_b32 s86, 0x100000\n"
    ".Lc26:\nds_read_b32 v243, v56\ns_waitcnt lgkmcnt(0)\nv_readfirstlane_b32 s49, v243\ns_sub_u32 s86, s86, 1\ns_cbranch_scc1 .Lc25\ns_cmp_ge_i32 s49, s45\n"
    "s_cbranch_scc1 .Lc25\ns_branch .Lc26\n.Lc27:\ns_mov_b32 s86, 0x100000\n.Lc29:\nds_read_b32 v243, v56\ns_waitcnt lgkmcnt(0)\nv_readfirstlane_b32 s49, v243\n"
    "s_sub_u32 s86, s86, 1\ns_cbranch_scc1 .Lc28\ns_cmp_ge_i32 s49, s45\ns_cbranch_scc1 .Lc28\ns_branch .Lc29\n.Lt30:\ns_mov_b32 s86, 0x100000\n.Ls32:\n"
    "ds_read_b32 v243, v28 offset:0\ns_waitcnt lgkmcnt(0)\nv_cmp_gt_i32 vcc, v29, v243\ns_sub_u32 s86, s86, 1\ns_cbranch_scc1 .Ls33\ns_cbranch_vccz .Ls33\ns_branch .Ls32\n"
    ".Ls33:\nds_read_b32 v124, v38 offset:0\nds_read_b32 v128, v38 offset:128\nds_read_b32 v132, v38 offset:256\ns_waitcnt lgkmcnt(0)\ns_branch .Lt31\n"
    ".Llstb7:\nv_mov_b32 v72, 1\nds_write_b32 v56, v72 offset:32\ns_mov_b32 s58, 2\ns_mov_b32 s57, 98304\ns_add_u32 s50, s50, 1\ns_and_b32 s50, s50, 7\n"
    "s_sub_u32 s62, s61, 16384\ns_max_i32 s62, s62, 0\ns_add_u32 s62, s8, s62\ns_addc_u32 s63, s9, 0\nv_and_b32 v251, 63, v0\nv_lshlrev_b32 v71, 4, v251\n"
    "v_bfe_u32 v250, v0, 2, 4\nv_and_b32 v249, 3, v0\ns_mov_b32 s43, 0x1002000\nv_lshl_add_u32 v57, v250, 7, s43\nv_lshl_add_u32 v57, v249, 4, v57\ns_lshr_b32 s42, s2, 4\n"
    "s_lshl_b32 s41, s42, 7\ns_and_b32 s40, s2, 15\ns_lshl_b32 s39, s40, 2\ns_add_u32 s38, s41, s39\ns_add_u32 s38, s38, 0x1004000\nv_mov_b32 v58, s38\n"
    "s_lshl_b32 s37, s19, 17\ns_lshl_b32 s36, s20, 11\ns_add_u32 s35, s37, s36\nv_lshl_add_u32 v79, v251, 4, s35\nv_mov_b32 v80, 0x1000000\ns_lshl_b32 s34, s25, 7\n"
    "s_add_u32 s33, s34, 0xe04000\nv_lshl_add_u32 v38, v250, 15, s33\nv_lshl_add_u32 v38, v249, 12, v38\nv_and_b32 v251, 63, v0\ns_lshl_b32 s43, s18, 13\n"
    "s_add_u32 s42, s43, 0x201000\nv_lshl_add_u32 v39, v251, 4, s42\nv_and_b32 v250, 7, v0\ns_lshl_b32 s41, s2, 7\ns_add_u32 s40, s41, 0x400000\n"
    "v_lshl_add_u32 v33, v250, 4, s40\ns_lshl_b32 s39, s13, 2\ns_add_u32 s38, s39, 0x27f80\nv_mov_b32 v37, s38\nv_mov_b64 v[60:61], -1\nv_mov_b64 v[62:63], -1\n"
    "v_and_b32 v224, 127, v0\ns_getpc_b64 s[44:45]\ns_add_u32 s44, s44, g_tabs@rel32@lo+4\ns_addc_u32 s45, s45, g_tabs@rel32@hi+12\nv_lshlrev_b32 v224, 4, v224\n"
    "global_load_dwordx4 v[28:31], v224, s[44:45] offset:2064\nglobal_load_dwordx4 v[10:13], v224, s[44:45]\nglobal_load_dwordx2 v[14:15], v224, s[44:45] offset:16\n"
    "s_lshl_b32 s43, s22, 17\ns_lshl_b32 s42, s13, 15\ns_add_u32 s44, s43, s42\ns_add_u32 s44, s44, 0xe01000\nv_and_b32 v231, 63, v0\nv_lshlrev_b32 v59, 4, v231\n"
    "s_add_u32 s68, s4, s44\ns_addc_u32 s69, s5, 0\ns_add_u32 s70, s68, 8192\ns_addc_u32 s71, s69, 0\ns_add_u32 s72, s68, 16384\ns_addc_u32 s73, s69, 0\n"
    "s_add_u32 s74, s68, 24576\ns_addc_u32 s75, s69, 0\nv_bfe_u32 v251, v0, 5, 1\nv_and_b32 v250, 31, v0\ns_lshl_b32 s43, s13, 11\ns_add_u32 s42, s43, 0x1d000\n"
    "v_lshl_add_u32 v17, v251, 8, s42\nv_lshl_add_u32 v17, v250, 2, v17\nv_and_b32 v249, 63, v0\nv_lshl_add_u32 v18, v249, 4, s42\ns_mov_b32 s41, 0x1f600\n"
    "v_lshl_add_u32 v19, v249, 4, s41\nv_bfe_u32 v248, v0, 3, 3\nv_and_b32 v247, 7, v0\ns_lshl_b32 s40, s22, 19\ns_lshl_b32 s39, s13, 17\ns_lshl_b32 s38, s21, 7\n"
    "s_add_u32 s37, s12, s40\ns_add_u32 s37, s37, s39\ns_add_u32 s37, s37, s38\ns_add_u32 s37, s37, 0x200000\nv_lshl_add_u32 v20, v248, 13, s37\n"
    "v_lshl_add_u32 v20, v247, 4, v20\ns_add_u32 s36, s12, s40\ns_add_u32 s36, s36, s39\ns_add_u32 s36, s36, s38\ns_nop 0\ns_cbranch_execz .Lhop\n"
    "s_add_u32 s36, s36, 0x210000\nv_lshl_add_u32 v21, v248, 13, s36\nv_lshl_add_u32 v21, v247, 4, v21\ns_lshl_b32 s35, s13, 8\ns_add_u32 s34, s35, 0x27600\n"
    "v_lshl_add_u32 v22, v249, 2, s34\nv_bfe_u32 v251, v0, 5, 1\nv_and_b32 v250, 31, v0\ns_lshl_b32 s43, s13, 11\ns_add_u32 s42, s43, 0x1d400\n"
    "v_lshl_add_u32 v26, v251, 8, s42\nv_lshl_add_u32 v26, v250, 2, v26\ns_lshl_b32 s41, s21, 15\ns_lshl_b32 s40, s22, 8\ns_lshl_b32 s39, s13, 6\ns_add_u32 s38, s41, s40\n"
    "s_add_u32 s38, s38, s39\nv_lshl_add_u32 v16, v250, 10, s38\nv_lshl_add_u32 v16, v251, 4, v16\nv_and_b32 v249, 63, v0\ns_lshr_b32 s37, s13, 1\ns_lshl_b32 s36, s37, 13\n"
    "s_and_b32 s35, s13, 1\ns_lshl_b32 s34, s35, 11\ns_add_u32 s33, s36, s34\ns_add_u32 s33, s33, 0x1f600\nv_lshl_add_u32 v27, v249, 4, s33\nv_and_b32 v251, 1, v0\n"
    "s_lshr_b32 s43, s13, 1\ns_lshl_b32 s42, s43, 3\ns_add_u32 s41, s42, 0x27fc0\nv_lshl_add_u32 v32, v251, 2, s41\ns_add_u32 s40, s42, 0x27d80\nv_mov_b32 v34, s40\n"
    "v_and_b32 v250, 0x7f, v0\ns_mul_i32 s39, s43, 0x2100\ns_add_u32 s38, s39, 0x18000\nv_lshl_add_u32 v35, v250, 4, s38\ns_mul_i32 s37, s43, 0x600\n"
    "s_add_u32 s36, s37, 0x1c200\nv_mov_b32 v36, s36\nv_mov_b64 v[232:233], 0\nv_mov_b64 v[234:235], 0\nv_mov_b64 v[236:237], 0\nv_mov_b64 v[238:239], 0\n"
    "v_mov_b64 v[244:245], 0\nv_mov_b64 v[246:247], 0\nv_mov_b64 v[248:249], 0\nv_mov_b64 v[250:251], 0\n.Ll34:\nds_read_b128 v[74:77], v56 offset:96\n"
    "s_mul_i32 s59, s50, 196608\ns_mul_i32 s56, s50, 12288\ns_add_u32 s52, s66, s59\ns_addc_u32 s53, s67, 0\ns_mov_b32 m0, s56\ns_nop 0\n"
    "global_load_lds_dwordx4 v64, s[52:53] offset:0\nglobal_load_lds_dwordx4 v64, s[52:53] offset:1024\nglobal_load_lds_dwordx4 v64, s[52:53] offset:2048\n"
    "s_add_u32 s56, s56, 3072\ns_mov_b32 m0, s56\ns_add_u32 s52, s52, 49152\ns_addc_u32 s53, s53, 0\nglobal_load_lds_dwordx4 v64, s[52:53] offset:0\n"
    "global_load_lds_dwordx4 v64, s[52:53] offset:1024\nglobal_load_lds_dwordx4 v64, s[52:53] offset:2048\ns_add_u32 s56, s56, 3072\ns_mov_b32 m0, s56\n"
    "s_add_u32 s52, s52, 49152\ns_addc_u32 s53, s53, 0\nglobal_load_lds_dwordx4 v64, s[52:53] offset:0\nglobal_load_lds_dwordx4 v64, s[52:53] offset:1024\n"
    "global_load_lds_dwordx4 v64, s[52:53] offset:2048\ns_add_u32 s56, s56, 3072\ns_add_u32 s59, s58, -1\ns_cmp_lg_u32 s58, 4\ns_cbranch_scc1 .Ll38\ns_mov_b32 s59, s58\n"
    "s_waitcnt vmcnt(23)\n.Ll38:\nv_mov_b32 v70, s59\ns_mov_b64 exec, 1\ns_waitcnt vmcnt(25)\nds_max_i32 v56, v70\ns_mov_b64 exec, -1\ns_mov_b32 m0, s56\n"
    "s_add_u32 s52, s52, 49152\ns_addc_u32 s53, s53, 0\nglobal_load_lds_dwordx4 v64, s[52:53] offset:0\nglobal_load_lds_dwordx4 v64, s[52:53] offset:1024\n"
    "global_load_lds_dwordx4 v64, s[52:53] offset:2048\ns_add_u32 s56, s56, 3072\ns_lshl_b32 s59, s50, 7\ns_add_u32 s54, s48, s59\ns_addc_u32 s55, s49, 0\n"
    "s_add_u32 s57, s57, 4096\ns_cmp_eq_u32 s57, 110592\ns_cselect_b32 s57, 98304, s57\ns_sub_u32 s59, s58, 4\ns_mov_b32 s60, 0x100000\ns_waitcnt lgkmcnt(0)\n.Ll35:\n"
    "v_min3_i32 v74, v74, v75, v76\nv_min_i32 v74, v74, v77\nv_cmp_gt_i32 vcc, s59, v74\ns_cbranch_vccz .Ll36\ns_sub_u32 s60, s60, 1\ns_cbranch_scc1 .Ll36\n"
    "v_mov_b32 v70, s58\ns_mov_b64 exec, 1\ns_waitcnt vmcnt(12)\nds_max_i32 v56, v70\ns_mov_b64 exec, -1\ns_sleep 1\nds_read_b128 v[74:77], v56 offset:96\n"
    "s_waitcnt lgkmcnt(0)\ns_branch .Ll35\n.Ll36:\ns_add_u32 m0, s57, 0\ns_nop 0\nglobal_load_lds_dwordx4 v65, s[54:55] offset:0\n"
    "global_load_lds_dwordx4 v66, s[54:55] offset:1024\nglobal_load_lds_dwordx4 v67, s[54:55] offset:2048\nglobal_load_lds_dwordx4 v68, s[54:55] offset:3072\n"
    "s_add_u32 m0, s57, 12288\ns_add_u32 s54, s54, 32768\ns_addc_u32 s55, s55, 0\nglobal_load_lds_dwordx4 v65, s[54:55] offset:0\n"
    "global_load_lds_dwordx4 v66, s[54:55] offset:1024\nglobal_load_lds_dwordx4 v67, s[54:55] offset:2048\nglobal_load_lds_dwordx4 v68, s[54:55] offset:3072\n"
    "s_add_u32 m0, s57, 24576\ns_add_u32 s54, s54, 32768\ns_addc_u32 s55, s55, 0\nglobal_load_lds_dwordx4 v65, s[54:55] offset:0\n"
    "global_load_lds_dwordx4 v66, s[54:55] offset:1024\nglobal_load_lds_dwordx4 v67, s[54:55] offset:2048\nglobal_load_lds_dwordx4 v68, s[54:55] offset:3072\n"
    "s_add_u32 s59, s58, 0\nv_mov_b32 v70, s59\ns_mov_b64 exec, 1\ns_waitcnt vmcnt(24)\nds_max_i32 v56, v70\ns_mov_b64 exec, -1\ns_add_u32 m0, s57, 36864\n"
    "s_add_u32 s54, s54, 32768\ns_addc_u32 s55, s55, 0\nglobal_load_lds_dwordx4 v65, s[54:55] offset:0\nglobal_load_lds_dwordx4 v66, s[54:55] offset:1024\n"
    "global_load_lds_dwordx4 v67, s[54:55] offset:2048\nglobal_load_lds_dwordx4 v68, s[54:55] offset:3072\ns_cmp_lg_u32 s58, 2\ns_cbranch_scc1 .Ll37\ns_mov_b32 m0, 146432\n"
    "v_xor_b32 v72, 32, v71\nglobal_load_lds_dwordx4 v72, s[62:63] offset:2048\nv_xor_b32 v73, 48, v71\nglobal_load_lds_dwordx4 v73, s[62:63] offset:3072\n"
    "s_mov_b32 m0, 150528\nv_xor_b32 v72, 4160, v71\nglobal_load_lds_dwordx4 v72, s[62:63] offset:0\nv_xor_b32 v73, 4176, v71\n"
    "global_load_lds_dwordx4 v73, s[62:63] offset:1024\nv_xor_b32 v72, 4192, v71\nglobal_load_lds_dwordx4 v72, s[62:63] offset:2048\nv_xor_b32 v73, 4208, v71\n"
    "global_load_lds_dwordx4 v73, s[62:63] offset:3072\ns_mov_b32 m0, 154624\nv_xor_b32 v72, 8320, v71\ns_cbranch_execz .Lhop\nglobal_load_lds_dwordx4 v72, s[62:63] offset:0\n"
    "v_xor_b32 v73, 8336, v71\nglobal_load_lds_dwordx4 v73, s[62:63] offset:1024\ns_add_u32 s59, s58, 1\nv_mov_b32 v70, s59\ns_mov_b64 exec, 1\ns_waitcnt vmcnt(24)\n"
    "ds_max_i32 v56, v70\ns_mov_b64 exec, -1\nv_xor_b32 v72, 8352, v71\nglobal_load_lds_dwordx4 v72, s[62:63] offset:2048\nv_xor_b32 v73, 8368, v71\n"
    "global_load_lds_dwordx4 v73, s[62:63] offset:3072\ns_mov_b32 m0, 158720\nv_xor_b32 v72, 12480, v71\nglobal_load_lds_dwordx4 v72, s[62:63] offset:0\n"
    "v_xor_b32 v73, 12496, v71\nglobal_load_lds_dwordx4 v73, s[62:63] offset:1024\nv_xor_b32 v72, 12512, v71\nglobal_load_lds_dwordx4 v72, s[62:63] offset:2048\n"
    "v_xor_b32 v73, 12528, v71\nglobal_load_lds_dwordx4 v73, s[62:63] offset:3072\n.Ll37:\ns_add_u32 s50, s50, 1\ns_and_b32 s50, s50, 7\ns_add_u32 s58, s58, 2\n"
    "s_cmp_lt_u32 s58, 16\ns_cbranch_scc1 .Ll34\nglobal_load_dword v78, v80, s[4:5] sc1\nglobal_load_dwordx4 v[252:255], v57, s[4:5] sc1\ns_add_u32 s59, s58, -1\n"
    "v_mov_b32 v70, s59\ns_mov_b64 exec, 1\ns_waitcnt vmcnt(18)\nds_max_i32 v56, v70\ns_mov_b64 exec, -1\nglobal_store_dwordx4 v79, v[60:63], s[4:5] offset:0 sc1\n"
    "global_store_dwordx4 v79, v[60:63], s[4:5] offset:1024 sc1\nv_add_u32 v79, 0x8000, v79\nglobal_store_dwordx4 v79, v[60:63], s[4:5] offset:0 sc1\n"
    "global_store_dwordx4 v79, v[60:63], s[4:5] offset:1024 sc1\nv_add_u32 v79, 0x8000, v79\nglobal_store_dwordx4 v79, v[60:63], s[4:5] offset:0 sc1\n"
    "global_store_dwordx4 v79, v[60:63], s[4:5] offset:1024 sc1\nv_add_u32 v79, 0x8000, v79\nglobal_store_dwordx4 v79, v[60:63], s[4:5] offset:0 sc1\n"
    "global_store_dwordx4 v79, v[60:63], s[4:5] offset:1024 sc1\ns_add_u32 s59, s58, 0\nv_mov_b32 v70, s59\ns_mov_b64 exec, 1\ns_waitcnt vmcnt(10)\nds_max_i32 v56, v70\n"
    "s_mov_b64 exec, -1\ns_waitcnt vmcnt(8)\nv_readfirstlane_b32 s11, v78\ns_mul_i32 s11, s11, 0x9E3779B1\ns_add_u32 s11, s11, 0x7F4A7C15\nv_xor_b32 v252, s11, v252\n"
    "v_xor_b32 v253, s11, v253\nv_xor_b32 v254, s11, v254\nv_xor_b32 v255, s11, v255\nv_or3_b32 v78, v252, v253, v254\nv_or_b32 v78, v78, v255\nv_cmp_ne_u32 vcc, 0, v78\n"
    "s_cmp_eq_u64 vcc, 0\ns_cselect_b32 s48, 18, 17\nv_mov_b32 v70, s48\ns_mov_b64 exec, 1\nds_write_b32 v56, v70\ns_mov_b64 exec, -1\n"
    "global_store_dwordx4 v39, v[60:63], s[4:5] offset:-4096 sc1\nglobal_store_dwordx4 v39, v[60:63], s[4:5] offset:-3072 sc1\n"
    "global_store_dwordx4 v39, v[60:63], s[4:5] offset:-2048 sc1\nglobal_store_dwordx4 v39, v[60:63], s[4:5] offset:-1024 sc1\n"
    "global_store_dwordx4 v39, v[60:63], s[4:5] offset:0 sc1\nglobal_store_dwordx4 v39, v[60:63], s[4:5] offset:1024 sc1\n"
    "global_store_dwordx4 v39, v[60:63], s[4:5] offset:2048 sc1\nglobal_store_dwordx4 v39, v[60:63], s[4:5] offset:3072 sc1\nglobal_store_dwordx4 v33, v[60:63], s[4:5] sc1\n"
    "s_branch .Lafl11\n"
    ".Lhalo10:\ns_lshl_b32 s43, s22, 17\ns_lshl_b32 s42, s13, 15\ns_add_u32 s44, s43, s42\ns_add_u32 s44, s44, 0xe01000\nv_and_b32 v231, 63, v0\nv_lshlrev_b32 v59, 4, v231\n"
    "s_add_u32 s68, s4, s44\ns_addc_u32 s69, s5, 0\ns_add_u32 s70, s68, 8192\ns_addc_u32 s71, s69, 0\ns_add_u32 s72, s68, 16384\ns_addc_u32 s73, s69, 0\n"
    "s_add_u32 s74, s68, 24576\ns_addc_u32 s75, s69, 0\nv_bfe_u32 v251, v0, 5, 1\nv_and_b32 v250, 31, v0\ns_lshl_b32 s43, s13, 11\ns_add_u32 s42, s43, 0x1d000\n"
    "v_lshl_add_u32 v17, v251, 8, s42\nv_lshl_add_u32 v17, v250, 2, v17\nv_and_b32 v249, 63, v0\nv_lshl_add_u32 v18, v249, 4, s42\ns_mov_b32 s41, 0x1f600\n"
    "v_lshl_add_u32 v19, v249, 4, s41\nv_bfe_u32 v248, v0, 3, 3\nv_and_b32 v247, 7, v0\ns_lshl_b32 s40, s22, 19\ns_lshl_b32 s39, s13, 17\ns_lshl_b32 s38, s21, 7\n"
    "s_add_u32 s37, s12, s40\ns_add_u32 s37, s37, s39\ns_add_u32 s37, s37, s38\ns_add_u32 s37, s37, 0x200000\nv_lshl_add_u32 v20, v248, 13, s37\n"
    "v_lshl_add_u32 v20, v247, 4, v20\ns_add_u32 s36, s12, s40\ns_add_u32 s36, s36, s39\ns_add_u32 s36, s36, s38\ns_add_u32 s36, s36, 0x210000\n"
    "v_lshl_add_u32 v21, v248, 13, s36\nv_lshl_add_u32 v21, v247, 4, v21\ns_lshl_b32 s35, s13, 8\ns_add_u32 s34, s35, 0x27600\nv_lshl_add_u32 v22, v249, 2, s34\n"
    "v_bfe_u32 v251, v0, 5, 1\nv_and_b32 v250, 31, v0\ns_lshl_b32 s43, s13, 11\ns_add_u32 s42, s43, 0x1d400\nv_lshl_add_u32 v26, v251, 8, s42\n"
    "v_lshl_add_u32 v26, v250, 2, v26\ns_lshl_b32 s41, s21, 15\ns_lshl_b32 s40, s22, 8\ns_lshl_b32 s39, s13, 6\ns_add_u32 s38, s41, s40\ns_add_u32 s38, s38, s39\n"
    "v_lshl_add_u32 v16, v250, 10, s38\nv_lshl_add_u32 v16, v251, 4, v16\nv_and_b32 v249, 63, v0\ns_lshr_b32 s37, s13, 1\ns_lshl_b32 s36, s37, 13\ns_and_b32 s35, s13, 1\n"
    "s_lshl_b32 s34, s35, 11\ns_add_u32 s33, s36, s34\ns_add_u32 s33, s33, 0x1f600\nv_lshl_add_u32 v27, v249, 4, s33\nv_and_b32 v251, 1, v0\ns_lshr_b32 s43, s13, 1\n"
    "s_lshl_b32 s42, s43, 3\ns_add_u32 s41, s42, 0x27fc0\nv_lshl_add_u32 v32, v251, 2, s41\ns_add_u32 s40, s42, 0x27d80\nv_mov_b32 v34, s40\nv_and_b32 v250, 0x7f, v0\n"
    "s_mul_i32 s39, s43, 0x2100\ns_add_u32 s38, s39, 0x18000\nv_lshl_add_u32 v35, v250, 4, s38\ns_mul_i32 s37, s43, 0x600\ns_add_u32 s36, s37, 0x1c200\nv_mov_b32 v36, s36\n"
    "v_mov_b64 v[232:233], 0\nv_mov_b64 v[234:235], 0\nv_mov_b64 v[236:237], 0\nv_mov_b64 v[238:239], 0\nv_mov_b64 v[244:245], 0\nv_mov_b64 v[246:247], 0\n"
    "v_mov_b64 v[248:249], 0\nv_mov_b64 v[250:251], 0\ns_mov_b32 s48, 0\ns_cmp_lt_i32 s48, 5\ns_cbranch_scc1 .Lc40\n.Lc41:\nds_read_b128 v[64:67], v208\n"
    "ds_read_b128 v[68:71], v209\ns_cbranch_execz .Lhop\nds_read_b128 v[72:75], v210\nds_read_b128 v[76:79], v211\nds_read_b64_tr_b16 v[128:129], v192 offset:0\n"
    "ds_read_b64_tr_b16 v[130:131], v192 offset:256\nds_read_b64_tr_b16 v[132:133], v193 offset:0\nds_read_b64_tr_b16 v[134:135], v193 offset:256\n"
    "ds_read_b64_tr_b16 v[136:137], v192 offset:6144\nds_read_b64_tr_b16 v[138:139], v192 offset:6400\nds_read_b64_tr_b16 v[140:141], v193 offset:6144\n"
    "ds_read_b64_tr_b16 v[142:143], v193 offset:6400\ns_waitcnt lgkmcnt(4)\nv_mfma_f32_16x16x32_bf16 v[144:147], v[128:131], v[64:67], 0\n"
    "v_mfma_f32_16x16x32_bf16 v[148:151], v[132:135], v[64:67], 0\ns_waitcnt lgkmcnt(0)\nv_mfma_f32_16x16x32_bf16 v[144:147], v[136:139], v[68:71], v[144:147]\n"
    "v_mfma_f32_16x16x32_bf16 v[148:151], v[140:143], v[68:71], v[148:151]\ns_cmp_lt_i32 s48, 3\ns_cbranch_scc1 .Lc43\n.Lc44:\nds_read_b128 v[80:83], v212\n"
    "ds_read_b128 v[84:87], v213\nds_read_b128 v[88:91], v214\nds_read_b128 v[92:95], v215\nds_read_b64_tr_b16 v[128:129], v194 offset:0\n"
    "ds_read_b64_tr_b16 v[130:131], v194 offset:256\nds_read_b64_tr_b16 v[132:133], v195 offset:0\nds_read_b64_tr_b16 v[134:135], v195 offset:256\n"
    "ds_read_b64_tr_b16 v[136:137], v194 offset:6144\nds_read_b64_tr_b16 v[138:139], v194 offset:6400\nds_read_b64_tr_b16 v[140:141], v195 offset:6144\n"
    "ds_read_b64_tr_b16 v[142:143], v195 offset:6400\ns_waitcnt lgkmcnt(4)\nv_mfma_f32_16x16x32_bf16 v[144:147], v[128:131], v[72:75], v[144:147]\n"
    "v_mfma_f32_16x16x32_bf16 v[148:151], v[132:135], v[72:75], v[148:151]\ns_waitcnt lgkmcnt(0)\nv_mfma_f32_16x16x32_bf16 v[144:147], v[136:139], v[76:79], v[144:147]\n"
    "v_mfma_f32_16x16x32_bf16 v[148:151], v[140:143], v[76:79], v[148:151]\ns_cmp_lt_i32 s48, 5\ns_cbranch_scc1 .Lc46\n.Lc47:\nds_read_b128 v[96:99], v216\n"
    "ds_read_b128 v[100:103], v217\nds_read_b128 v[104:107], v218\nds_read_b128 v[108:111], v219\nds_read_b64_tr_b16 v[128:129], v196 offset:0\n"
    "ds_read_b64_tr_b16 v[130:131], v196 offset:256\nds_read_b64_tr_b16 v[132:133], v197 offset:0\nds_read_b64_tr_b16 v[134:135], v197 offset:256\n"
    "ds_read_b64_tr_b16 v[136:137], v196 offset:6144\nds_read_b64_tr_b16 v[138:139], v196 offset:6400\nds_read_b64_tr_b16 v[140:141], v197 offset:6144\n"
    "ds_read_b64_tr_b16 v[142:143], v197 offset:6400\nv_mfma_f32_16x16x32_bf16 v[152:155], v[64:67], v[64:67], 0\n"
    "v_mfma_f32_16x16x32_bf16 v[152:155], v[68:71], v[68:71], v[152:155]\nv_mfma_f32_16x16x32_bf16 v[152:155], v[72:75], v[72:75], v[152:155]\n"
    "v_mfma_f32_16x16x32_bf16 v[152:155], v[76:79], v[76:79], v[152:155]\ns_waitcnt lgkmcnt(4)\nv_mfma_f32_16x16x32_bf16 v[144:147], v[128:131], v[80:83], v[144:147]\n"
    "v_mfma_f32_16x16x32_bf16 v[148:151], v[132:135], v[80:83], v[148:151]\ns_waitcnt lgkmcnt(0)\nv_mfma_f32_16x16x32_bf16 v[144:147], v[136:139], v[84:87], v[144:147]\n"
    "v_mfma_f32_16x16x32_bf16 v[148:151], v[140:143], v[84:87], v[148:151]\ns_cmp_lt_i32 s48, 7\ns_cbranch_scc1 .Lc49\n.Lc50:\nds_read_b128 v[112:115], v220\n"
    "ds_read_b128 v[116:119], v221\nds_read_b128 v[120:123], v222\nds_read_b128 v[124:127], v223\nds_read_b64_tr_b16 v[128:129], v198 offset:0\n"
    "ds_read_b64_tr_b16 v[130:131], v198 offset:256\nds_read_b64_tr_b16 v[132:133], v199 offset:0\nds_read_b64_tr_b16 v[134:135], v199 offset:256\n"
    "ds_read_b64_tr_b16 v[136:137], v198 offset:6144\nds_read_b64_tr_b16 v[138:139], v198 offset:6400\nds_read_b64_tr_b16 v[140:141], v199 offset:6144\n"
    "ds_read_b64_tr_b16 v[142:143], v199 offset:6400\nv_mfma_f32_16x16x32_bf16 v[152:155], v[80:83], v[80:83], v[152:155]\n"
    "v_mfma_f32_16x16x32_bf16 v[152:155], v[84:87], v[84:87], v[152:155]\nv_mfma_f32_16x16x32_bf16 v[152:155], v[88:91], v[88:91], v[152:155]\n"
    "v_mfma_f32_16x16x32_bf16 v[152:155], v[92:95], v[92:95], v[152:155]\ns_waitcnt lgkmcnt(4)\nv_mfma_f32_16x16x32_bf16 v[144:147], v[128:131], v[88:91], v[144:147]\n"
    "v_mfma_f32_16x16x32_bf16 v[148:151], v[132:135], v[88:91], v[148:151]\ns_waitcnt lgkmcnt(0)\nv_mfma_f32_16x16x32_bf16 v[144:147], v[136:139], v[92:95], v[144:147]\n"
    "v_mfma_f32_16x16x32_bf16 v[148:151], v[140:143], v[92:95], v[148:151]\ns_cmp_lt_i32 s48, 9\ns_cbranch_scc1 .Lc52\n.Lc53:\nds_read_b64_tr_b16 v[128:129], v200 offset:0\n"
    "ds_read_b64_tr_b16 v[130:131], v200 offset:256\nds_read_b64_tr_b16 v[132:133], v201 offset:0\nds_read_b64_tr_b16 v[134:135], v201 offset:256\n"
    "ds_read_b64_tr_b16 v[136:137], v200 offset:6144\nds_read_b64_tr_b16 v[138:139], v200 offset:6400\nds_read_b64_tr_b16 v[140:141], v201 offset:6144\n"
    "ds_read_b64_tr_b16 v[142:143], v201 offset:6400\nv_mfma_f32_16x16x32_bf16 v[152:155], v[96:99], v[96:99], v[152:155]\n"
    "v_mfma_f32_16x16x32_bf16 v[152:155], v[100:103], v[100:103], v[152:155]\nv_mfma_f32_16x16x32_bf16 v[152:155], v[104:107], v[104:107], v[152:155]\n"
    "v_mfma_f32_16x16x32_bf16 v[152:155], v[108:111], v[108:111], v[152:155]\ns_waitcnt lgkmcnt(4)\nv_mfma_f32_16x16x32_bf16 v[144:147], v[128:131], v[96:99], v[144:147]\n"
    "v_mfma_f32_16x16x32_bf16 v[148:151], v[132:135], v[96:99], v[148:151]\ns_waitcnt lgkmcnt(0)\nv_mfma_f32_16x16x32_bf16 v[144:147], v[136:139], v[100:103], v[144:147]\n"
    "v_mfma_f32_16x16x32_bf16 v[148:151], v[140:143], v[100:103], v[148:151]\ns_cmp_lt_i32 s48, 11\ns_cbranch_scc1 .Lc55\n.Lc56:\n"
    "ds_read_b64_tr_b16 v[128:129], v202 offset:0\nds_read_b64_tr_b16 v[130:131], v202 offset:256\nds_read_b64_tr_b16 v[132:133], v203 offset:0\n"
    "ds_read_b64_tr_b16 v[134:135], v203 offset:256\nds_read_b64_tr_b16 v[136:137], v202 offset:6144\nds_read_b64_tr_b16 v[138:139], v202 offset:6400\n"
    "ds_read_b64_tr_b16 v[140:141], v203 offset:6144\nds_read_b64_tr_b16 v[142:143], v203 offset:6400\n"
    "v_mfma_f32_16x16x32_bf16 v[152:155], v[112:115], v[112:115], v[152:155]\nv_mfma_f32_16x16x32_bf16 v[152:155], v[116:119], v[116:119], v[152:155]\n"
    "v_mfma_f32_16x16x32_bf16 v[152:155], v[120:123], v[120:123], v[152:155]\nv_mfma_f32_16x16x32_bf16 v[152:155], v[124:127], v[124:127], v[152:155]\ns_waitcnt lgkmcnt(4)\n"
    "v_mfma_f32_16x16x32_bf16 v[144:147], v[128:131], v[104:107], v[144:147]\nv_mfma_f32_16x16x32_bf16 v[148:151], v[132:135], v[104:107], v[148:151]\ns_waitcnt lgkmcnt(0)\n"
    "v_mfma_f32_16x16x32_bf16 v[144:147], v[136:139], v[108:111], v[144:147]\nv_mfma_f32_16x16x32_bf16 v[148:151], v[140:143], v[108:111], v[148:151]\ns_cmp_lt_i32 s48, 13\n"
    "s_cbranch_scc1 .Lc58\n.Lc59:\nds_read_b64_tr_b16 v[128:129], v204 offset:0\nds_read_b64_tr_b16 v[130:131], v204 offset:256\nds_read_b64_tr_b16 v[132:133], v205 offset:0\n"
    "ds_read_b64_tr_b16 v[134:135], v205 offset:256\nds_read_b64_tr_b16 v[136:137], v204 offset:6144\nds_read_b64_tr_b16 v[138:139], v204 offset:6400\n"
    "ds_read_b64_tr_b16 v[140:141], v205 offset:6144\nds_read_b64_tr_b16 v[142:143], v205 offset:6400\ns_waitcnt lgkmcnt(4)\n"
    "v_mfma_f32_16x16x32_bf16 v[144:147], v[128:131], v[112:115], v[144:147]\nv_mfma_f32_16x16x32_bf16 v[148:151], v[132:135], v[112:115], v[148:151]\ns_waitcnt lgkmcnt(0)\n"
    "v_mfma_f32_16x16x32_bf16 v[144:147], v[136:139], v[116:119], v[144:147]\nv_mfma_f32_16x16x32_bf16 v[148:151], v[140:143], v[116:119], v[148:151]\ns_nop 0\n"
    "s_cbranch_execz .Lhop\ns_mov_b32 s44, 0xaaaaaaaa\ns_mov_b32 s45, s44\ns_mov_b32 s46, 0xcccccccc\ns_mov_b32 s47, s46\nv_cndmask_b32 v156, v152, v153, s[44:45]\n"
    "v_cndmask_b32 v157, v154, v155, s[44:45]\ns_mov_b32 s44, 0x00f0000f\ns_mov_b32 s45, 0xf0000f00\nv_cndmask_b32 v156, v156, v157, s[46:47]\ns_mov_b64 s[46:47], 0xffff\n"
    "v_cndmask_b32 v156, 0, v156, s[44:45]\nv_cndmask_b32 v157, 0, v153, s[46:47]\ns_mov_b32 s44, 0x3b000000\ns_nop 0\nv_permlane32_swap_b32 v156, v157\n"
    "v_add_f32 v158, v156, v157\nv_add_f32 v159, v156, v157\ns_nop 1\nv_permlane16_swap_b32 v158, v159\nv_add_f32 v156, v158, v159\nv_add_f32 v157, v158, v159\ns_nop 1\n"
    "v_permlane32_swap_b32 v156, v157\nv_mul_f32 v157, s44, v157\nv_mul_f32 v158, v157, v157\nv_fma_f32 v158, v156, s44, -v158\nv_max_f32 v158, 0, v158\n"
    "v_cvt_pk_bf16_f32 v158, 0, v158\nv_add_f32 v158, 0x3727c5ac, v158\nv_cvt_pk_bf16_f32 v158, 0, v158\nv_sqrt_f32 v158, v158\nv_cvt_pk_bf16_f32 v157, 0, -v157\n"
    "v_cvt_pk_bf16_f32 v158, 0, v158\nv_rcp_f32 v158, v158\nv_mov_b32 v159, 1\ns_mov_b32 s46, 0xffff0000\ns_mov_b32 s44, 0x00020002\ns_mov_b32 s45, s44\ns_cmp_lt_i32 s48, 15\n"
    "s_cbranch_scc1 .Lc61\n.Lc62:\ns_setprio 3\nds_read_b64_tr_b16 v[128:129], v206 offset:0\nds_read_b64_tr_b16 v[130:131], v206 offset:256\n"
    "ds_read_b64_tr_b16 v[132:133], v207 offset:0\nds_read_b64_tr_b16 v[134:135], v207 offset:256\nds_read_b64_tr_b16 v[136:137], v206 offset:6144\n"
    "ds_read_b64_tr_b16 v[138:139], v206 offset:6400\nds_read_b64_tr_b16 v[140:141], v207 offset:6144\nds_read_b64_tr_b16 v[142:143], v207 offset:6400\ns_waitcnt lgkmcnt(4)\n"
    "v_mfma_f32_16x16x32_bf16 v[144:147], v[128:131], v[120:123], v[144:147]\nv_mfma_f32_16x16x32_bf16 v[148:151], v[132:135], v[120:123], v[148:151]\ns_waitcnt lgkmcnt(0)\n"
    "v_mfma_f32_16x16x32_bf16 v[144:147], v[136:139], v[124:127], v[144:147]\nv_mfma_f32_16x16x32_bf16 v[148:151], v[140:143], v[124:127], v[148:151]\ns_nop 6\n"
    "v_and_b32 v160, s46, v144\nv_and_b32 v161, s46, v145\nv_and_b32 v162, s46, v146\nv_and_b32 v163, s46, v147\nv_and_b32 v164, s46, v148\nv_and_b32 v165, s46, v149\n"
    "v_and_b32 v166, s46, v150\nv_and_b32 v167, s46, v151\nv_sub_f32 v168, v144, v160\nv_sub_f32 v169, v145, v161\nv_sub_f32 v170, v146, v162\nv_sub_f32 v171, v147, v163\n"
    "v_sub_f32 v172, v148, v164\nv_sub_f32 v173, v149, v165\nv_sub_f32 v174, v150, v166\nv_sub_f32 v175, v151, v167\nv_cvt_pk_bf16_f32 v160, v160, v168\n"
    "v_cvt_pk_bf16_f32 v161, v161, v169\nv_cvt_pk_bf16_f32 v162, v162, v170\nv_cvt_pk_bf16_f32 v163, v163, v171\nv_cvt_pk_bf16_f32 v164, v164, v172\n"
    "v_cvt_pk_bf16_f32 v165, v165, v173\nv_cvt_pk_bf16_f32 v166, v166, v174\nv_cvt_pk_bf16_f32 v167, v167, v175\ns_mov_b64 exec, s[44:45]\nds_write_b128 v58, v[160:163]\n"
    "ds_write_b128 v58, v[164:167] offset:64\nds_write_b32 v37, v159 offset:32\ns_mov_b64 exec, -1\ns_setprio 0\nds_read_b32 v156, v56\n"
    "v_fmac_f32_dpp v144, v144, v157 row_newbcast:1 row_mask:0xf bank_mask:0xf\nv_fmac_f32_dpp v145, v145, v157 row_newbcast:1 row_mask:0xf bank_mask:0xf\n"
    "v_fmac_f32_dpp v146, v146, v157 row_newbcast:1 row_mask:0xf bank_mask:0xf\nv_fmac_f32_dpp v147, v147, v157 row_newbcast:1 row_mask:0xf bank_mask:0xf\n"
    "v_fmac_f32_dpp v148, v148, v157 row_newbcast:1 row_mask:0xf bank_mask:0xf\nv_fmac_f32_dpp v149, v149, v157 row_newbcast:1 row_mask:0xf bank_mask:0xf\n"
    "v_fmac_f32_dpp v150, v150, v157 row_newbcast:1 row_mask:0xf bank_mask:0xf\nv_fmac_f32_dpp v151, v151, v157 row_newbcast:1 row_mask:0xf bank_mask:0xf\n"
    "v_mul_f32 v144, v144, v158\nv_mul_f32 v145, v145, v158\nv_mul_f32 v146, v146, v158\nv_mul_f32 v147, v147, v158\nv_mul_f32 v148, v148, v158\nv_mul_f32 v149, v149, v158\n"
    "s_waitcnt lgkmcnt(0)\nv_readfirstlane_b32 s48, v156\nv_mul_f32 v150, v150, v158\nv_mul_f32 v151, v151, v158\nv_cvt_pk_bf16_f32 v60, v144, v145\n"
    "v_cvt_pk_bf16_f32 v61, v146, v147\nv_cvt_pk_bf16_f32 v62, v148, v149\nv_cvt_pk_bf16_f32 v63, v150, v151\ns_cmp_lt_i32 s48, 17\ns_cbranch_scc1 .Lc64\n.Lc65:\n"
    ".Lafl11:\ns_mov_b32 s44, 0x00ff00ff\ns_mov_b32 s45, 0xff00ff00\ns_mov_b32 s46, s44\ns_mov_b32 s47, s44\ns_sub_u32 s26, s48, 17\ns_cmp_eq_u32 s26, 0\n"
    "s_cbranch_scc1 .La67\nglobal_load_dwordx4 v[64:67], v59, s[68:69] offset:-4096 sc1\nglobal_load_dwordx4 v[68:71], v59, s[68:69] offset:-3072 sc1\n"
    "global_load_dwordx4 v[72:75], v59, s[68:69] offset:-2048 sc1\nglobal_load_dwordx4 v[76:79], v59, s[68:69] offset:-1024 sc1\n"
    "global_load_dwordx4 v[80:83], v59, s[68:69] offset:0 sc1\nglobal_load_dwordx4 v[84:87], v59, s[68:69] offset:1024 sc1\n"
    "global_load_dwordx4 v[88:91], v59, s[68:69] offset:2048 sc1\nglobal_load_dwordx4 v[92:95], v59, s[68:69] offset:3072 sc1\n"
    "global_load_dwordx4 v[96:99], v59, s[70:71] offset:-4096 sc1\nglobal_load_dwordx4 v[100:103], v59, s[70:71] offset:-3072 sc1\n"
    "global_load_dwordx4 v[104:107], v59, s[70:71] offset:-2048 sc1\nglobal_load_dwordx4 v[108:111], v59, s[70:71] offset:-1024 sc1\n"
    "global_load_dwordx4 v[112:115], v59, s[70:71] offset:0 sc1\nglobal_load_dwordx4 v[116:119], v59, s[70:71] offset:1024 sc1\n"
    "global_load_dwordx4 v[120:123], v59, s[70:71] offset:2048 sc1\nglobal_load_dwordx4 v[124:127], v59, s[70:71] offset:3072 sc1\n.La69:\ns_cmp_eq_u32 s13, 1\n"
    "s_cbranch_scc1 .La71\ns_cmp_eq_u32 s13, 0\ns_cbranch_scc1 .La70\ns_mov_b32 s86, 0x100000\n.Ls72:\nds_read_b32 v224, v39 offset:96\ns_waitcnt lgkmcnt(0)\n"
    "v_cmp_gt_i32 vcc, 16, v224\ns_sub_u32 s86, s86, 1\ns_cbranch_scc1 .Ls73\ns_cbranch_vccz .Ls73\ns_sleep 1\ns_branch .Ls72\n.Ls73:\n"
    "ds_write2_b64 v38, v[60:61], v[62:63] offset1:4\ns_branch .La71\n.Lc40:\ns_mov_b32 s86, 0x100000\n.Lc42:\nds_read_b32 v156, v56\ns_waitcnt lgkmcnt(0)\n"
    "v_readfirstlane_b32 s48, v156\ns_sub_u32 s86, s86, 1\ns_cbranch_scc1 .Lc41\ns_cmp_ge_i32 s48, 5\ns_cbranch_scc1 .Lc41\ns_sleep 1\ns_branch .Lc42\n.Lc43:\n"
    "s_mov_b32 s86, 0x100000\n.Lc45:\nds_read_b32 v176, v56\ns_waitcnt lgkmcnt(0)\nv_readfirstlane_b32 s48, v176\ns_sub_u32 s86, s86, 1\ns_cbranch_scc1 .Lc44\n"
    "s_cmp_ge_i32 s48, 3\ns_cbranch_scc1 .Lc44\ns_sleep 1\ns_branch .Lc45\n.Lc46:\ns_mov_b32 s86, 0x100000\n.Lc48:\nds_read_b32 v176, v56\ns_waitcnt lgkmcnt(0)\n"
    "v_readfirstlane_b32 s48, v176\ns_cbranch_execz .Lhop\ns_sub_u32 s86, s86, 1\ns_cbranch_scc1 .Lc47\ns_cmp_ge_i32 s48, 5\ns_cbranch_scc1 .Lc47\ns_sleep 1\ns_branch .Lc48\n"
    ".Lc49:\ns_mov_b32 s86, 0x100000\n.Lc51:\nds_read_b32 v176, v56\ns_waitcnt lgkmcnt(0)\nv_readfirstlane_b32 s48, v176\ns_sub_u32 s86, s86, 1\ns_cbranch_scc1 .Lc50\n"
    "s_cmp_ge_i32 s48, 7\ns_cbranch_scc1 .Lc50\ns_sleep 1\ns_branch .Lc51\n.Lc52:\ns_mov_b32 s86, 0x100000\n.Lc54:\nds_read_b32 v176, v56\ns_waitcnt lgkmcnt(0)\n"
    "v_readfirstlane_b32 s48, v176\ns_sub_u32 s86, s86, 1\ns_cbranch_scc1 .Lc53\ns_cmp_ge_i32 s48, 9\ns_cbranch_scc1 .Lc53\ns_sleep 1\ns_branch .Lc54\n.Lc55:\n"
    "s_mov_b32 s86, 0x100000\n.Lc57:\nds_read_b32 v176, v56\ns_waitcnt lgkmcnt(0)\nv_readfirstlane_b32 s48, v176\ns_sub_u32 s86, s86, 1\ns_cbranch_scc1 .Lc56\n"
    "s_cmp_ge_i32 s48, 11\ns_cbranch_scc1 .Lc56\ns_sleep 1\ns_branch .Lc57\n.Lc58:\ns_mov_b32 s86, 0x100000\n.Lc60:\nds_read_b32 v176, v56\ns_waitcnt lgkmcnt(0)\n"
    "v_readfirstlane_b32 s48, v176\ns_sub_u32 s86, s86, 1\ns_cbranch_scc1 .Lc59\ns_cmp_ge_i32 s48, 13\ns_cbranch_scc1 .Lc59\ns_sleep 1\ns_branch .Lc60\n.Lc61:\n"
    "s_mov_b32 s86, 0x100000\n.Lc63:\nds_read_b32 v176, v56\ns_waitcnt lgkmcnt(0)\nv_readfirstlane_b32 s48, v176\ns_sub_u32 s86, s86, 1\ns_cbranch_scc1 .Lc62\n"
    "s_cmp_ge_i32 s48, 15\ns_cbranch_scc1 .Lc62\ns_branch .Lc63\n.Lc64:\ns_mov_b32 s86, 0x100000\n.Lc66:\nds_read_b32 v156, v56\ns_waitcnt lgkmcnt(0)\n"
    "v_readfirstlane_b32 s48, v156\ns_sub_u32 s86, s86, 1\ns_cbranch_scc1 .Lc65\ns_cmp_ge_i32 s48, 17\ns_cbranch_scc1 .Lc65\ns_sleep 1\ns_branch .Lc66\n.La70:\n"
    "v_mov_b32 v224, s11\ns_mov_b64 exec, 1\ns_waitcnt vmcnt(16)\nglobal_store_dword v58, v224, s[4:5] sc1\ns_mov_b64 exec, -1\ns_cmp_eq_u32 s26, 0\ns_cbranch_scc1 .La71\n"
    "global_load_dword v255, v38, s[4:5] sc1\n.La71:\n"
    ".Latt12:\ns_setprio 1\ns_waitcnt lgkmcnt(0)\ns_barrier\nds_read_b128 v[192:195], v6\nds_read_b128 v[196:199], v6 offset:208\ns_mov_b64 exec, s[44:45]\n"
    "ds_read_b128 v[232:235], v7\nds_read_b128 v[236:239], v7 offset:208\ns_mov_b64 exec, -1\nds_read_b64_tr_b16 v[200:201], v8 offset:0\n"
    "ds_read_b64_tr_b16 v[202:203], v8 offset:32\nds_read_b64_tr_b16 v[204:205], v8 offset:208\nds_read_b64_tr_b16 v[206:207], v8 offset:240\ns_mov_b32 s48, 0x3eb8aa3b\n"
    "s_waitcnt lgkmcnt(5)\nv_mfma_f32_16x16x32_bf16 v[208:211], v[192:195], v[232:235], v[2:5]\ns_waitcnt lgkmcnt(4)\n"
    "v_mfma_f32_16x16x32_bf16 v[212:215], v[196:199], v[236:239], v[2:5]\ns_nop 5\nv_max3_f32 v216, v208, v209, v210\ns_nop 0\nv_max3_f32 v217, v212, v213, v214\n"
    "v_max_f32 v216, v216, v211\nv_max_f32 v217, v217, v215\ns_nop 1\nv_permlane16_swap_b32 v216, v217\nv_max_f32 v218, v216, v217\nv_max_f32 v217, v216, v217\ns_nop 1\n"
    "v_permlane32_swap_b32 v218, v217\nv_max_f32 v218, v218, v217\nv_mul_f32 v216, 0xbeb8aa3b, v218\nv_mul_f32 v217, 0xbeb8aa3b, v218\ns_nop 1\n"
    "v_permlane16_swap_b32 v216, v217\nv_fma_f32 v208, v208, s48, v216\nv_fma_f32 v209, v209, s48, v216\nv_fma_f32 v210, v210, s48, v216\nv_fma_f32 v211, v211, s48, v216\n"
    "v_fma_f32 v212, v212, s48, v217\nv_fma_f32 v213, v213, s48, v217\nv_fma_f32 v214, v214, s48, v217\nv_fma_f32 v215, v215, s48, v217\nv_exp_f32 v208, v208\n"
    "v_exp_f32 v209, v209\nv_exp_f32 v210, v210\nv_exp_f32 v211, v211\nv_exp_f32 v212, v212\nv_exp_f32 v213, v213\nv_exp_f32 v214, v214\nv_exp_f32 v215, v215\n"
    "s_mov_b64 exec, s[46:47]\nv_cvt_pk_bf16_f32 v244, v208, v209\nv_cvt_pk_bf16_f32 v245, v210, v211\nv_cvt_pk_bf16_f32 v248, v212, v213\nv_cvt_pk_bf16_f32 v249, v214, v215\n"
    "s_not_b64 exec, exec\nv_cvt_pk_bf16_f32 v246, v208, v209\nv_cvt_pk_bf16_f32 v247, v210, v211\nv_cvt_pk_bf16_f32 v250, v212, v213\nv_cvt_pk_bf16_f32 v251, v214, v215\n"
    "s_mov_b64 exec, -1\nv_add_f32 v208, v208, v209\nv_add_f32 v209, v210, v211\ns_waitcnt lgkmcnt(0)\nv_mfma_f32_16x16x32_bf16 v[192:195], v[200:203], v[244:247], 0\n"
    "v_add_f32 v212, v212, v213\nv_add_f32 v214, v214, v215\nv_mfma_f32_16x16x32_bf16 v[196:199], v[204:207], v[248:251], 0\nv_add_f32 v216, v208, v209\n"
    "v_add_f32 v217, v212, v214\ns_nop 1\nv_permlane16_swap_b32 v216, v217\nv_add_f32 v218, v216, v217\nv_add_f32 v217, v216, v217\ns_nop 1\nv_permlane32_swap_b32 v218, v217\n"
    "v_add_f32 v218, v218, v217\nv_rcp_f32 v216, v218\nv_rcp_f32 v217, v218\ns_nop 1\nv_permlane16_swap_b32 v216, v217\n"
    "v_pk_mul_f32 v[192:193], v[192:193], v[216:217] op_sel:[0,0] op_sel_hi:[1,0]\nv_pk_mul_f32 v[194:195], v[194:195], v[216:217] op_sel:[0,0] op_sel_hi:[1,0]\n"
    "v_pk_mul_f32 v[196:197], v[196:197], v[216:217] op_sel:[0,1] op_sel_hi:[1,1]\nv_pk_mul_f32 v[198:199], v[198:199], v[216:217] op_sel:[0,1] op_sel_hi:[1,1]\n"
    "v_cvt_pk_bf16_f32 v200, v192, v193\nv_cvt_pk_bf16_f32 v201, v194, v195\nv_cvt_pk_bf16_f32 v202, v196, v197\nv_cvt_pk_bf16_f32 v203, v198, v199\ns_nop 0\n"
    "v_permlane16_swap_b32 v200, v202\nv_permlane16_swap_b32 v201, v203\nglobal_store_dwordx4 v9, v[200:203], s[4:5] sc1\ns_setprio 0\ns_cmp_gt_u32 s3, 3\n"
    "s_cbranch_scc1 .Lhb75\n"
    "s_or_b32 s44, s12, s13\ns_cbranch_scc1 .Lb74\ns_cmp_eq_u32 s27, 0\ns_cbranch_scc1 .Lb83\ns_mov_b32 s10, 64\ns_call_b64 s[94:95], .Lkgl13\n.Lb83:\n"
    "ds_write_b128 v33, v[40:43] offset:0\nds_write_b128 v33, v[44:47] offset:1024\nds_write_b128 v33, v[48:51] offset:2048\nds_write_b128 v33, v[52:55] offset:3072\n.Lb74:\n"
    "s_lshl_b32 s43, s21, 15\ns_add_u32 s42, s12, s43\ns_add_u32 s42, s42, 0x1000\nv_lshl_add_u32 v60, v0, 4, s42\ns_mov_b32 s41, 0x1f600\nv_lshl_add_u32 v57, v0, 4, s41\n"
    "v_and_b32 v239, 3, v0\ns_mov_b32 s40, 0x27f80\nv_lshl_add_u32 v58, v239, 2, s40\ns_cbranch_execz .Lhop\nv_and_b32 v238, 0x7f, v0\ns_lshr_b32 s39, s13, 1\n"
    "s_mul_i32 s38, s39, 0x2100\ns_add_u32 s37, s38, 0x18000\nv_lshl_add_u32 v59, v238, 4, s37\nv_and_b32 v237, 31, v0\ns_mov_b32 s36, 0x27600\n"
    "v_lshl_add_u32 v39, v237, 2, s36\ns_lshl_b32 s35, s22, 13\ns_lshl_b32 s34, s21, 7\ns_add_u32 s33, s12, s35\ns_add_u32 s33, s33, s34\ns_add_u32 s33, s33, 0x400000\n"
    "v_lshl_add_u32 v56, v237, 2, s33\nv_bfe_u32 v239, v0, 2, 4\nv_and_b32 v238, 3, v0\ns_mov_b32 s43, 0x1004000\nv_lshl_add_u32 v240, v239, 7, s43\n"
    "v_lshl_add_u32 v240, v238, 4, v240\nv_add_u32 v61, 8192, v60\nv_add_u32 v62, 16384, v60\nv_add_u32 v63, 24576, v60\ns_mov_b32 s80, 0x10000\ns_cmp_lg_u32 s12, 0\n"
    "s_cbranch_scc1 .Lgb76\ns_waitcnt lgkmcnt(0)\ns_barrier\nglobal_load_dwordx4 v[252:255], v240, s[4:5] sc1\ns_add_u32 s43, s12, 0x400000\nv_lshl_add_u32 v16, v0, 4, s43\n"
    "s_lshl_b32 s42, s18, 13\ns_add_u32 s41, s12, s42\ns_add_u32 s41, s41, 0x200000\nv_lshl_add_u32 v17, v0, 4, s41\nv_lshrrev_b32 v127, 3, v0\n"
    "v_mul_u32_u24 v126, 0x50, v127\nv_and_b32 v125, 7, v0\nv_add_u32 v18, 0x1d550, v126\nv_lshl_add_u32 v18, v125, 3, v18\nv_bfe_u32 v124, v0, 4, 3\n"
    "v_xor_b32 v123, v0, v124\ns_mov_b32 s40, 0x19400\nv_lshl_add_u32 v19, v123, 3, s40\nv_bfe_u32 v122, v0, 4, 1\nv_xor_b32 v121, v0, v122\ns_mov_b32 s39, 0x17c00\n"
    "v_lshl_add_u32 v20, v121, 3, s39\ns_lshl_b32 s38, s18, 7\ns_add_u32 s37, s38, 0x600000\nv_lshl_add_u32 v120, v127, 15, s37\nv_lshl_add_u32 v120, v125, 4, v120\n"
    "v_lshrrev_b32 v119, 4, v0\nv_and_b32 v118, 15, v0\ns_lshl_b32 s36, s24, 18\ns_lshl_b32 s35, s25, 8\ns_add_u32 s34, s36, s35\ns_add_u32 s34, s34, 0x1200000\n"
    "v_lshl_add_u32 v117, v119, 13, s34\nv_lshl_add_u32 v117, v118, 4, v117\ns_cmp_lt_u32 s12, 1\ns_cselect_b64 vcc, -1, 0\nv_cndmask_b32 v21, v117, v120, vcc\n"
    "v_lshrrev_b32 v127, 8, v0\nv_mul_u32_u24 v126, 0x1950, v127\nv_and_b32 v125, 0xff, v0\nv_add_u32 v124, 0x1d000, v126\nv_lshl_add_u32 v124, v125, 4, v124\n"
    "v_mul_u32_u24 v123, 0x3180, v127\ns_mov_b32 s43, 0xfffffab0\nv_lshl_add_u32 v122, v125, 4, s43\ns_mov_b32 s42, 0x810\nv_lshl_add_u32 v121, v125, 4, s42\n"
    "v_mov_b32 v120, 0x1f80\nv_cmp_gt_u32 vcc, 0xa1, v125\ns_nop 1\nv_cndmask_b32 v119, v120, v121, vcc\nv_cmp_gt_u32 vcc, 0x7b, v125\ns_nop 1\n"
    "v_cndmask_b32 v118, v119, v122, vcc\nv_add_u32 v117, 0x20300, v123\nv_add_u32 v117, v117, v118\nv_cmp_gt_u32 vcc, 0x55, v125\ns_nop 1\n"
    "v_cndmask_b32 v22, v117, v124, vcc\nv_and_b32 v116, 31, v0\nv_sub_u32 v115, 0x3ff, v116\nv_bfe_u32 v114, v0, 5, 1\nv_lshl_add_u32 v113, v114, 3, v115\n"
    "v_and_b32 v112, 3, v113\nv_mul_u32_u24 v111, 0x840, v112\nv_lshrrev_b32 v110, 2, v113\ns_lshr_b32 s41, s13, 1\ns_mul_i32 s40, s41, 0x2100\ns_add_u32 s39, s40, 0x17800\n"
    "v_add_u32 v23, s39, v111\nv_lshl_add_u32 v23, v110, 3, v23\ns_lshl_b32 s38, s13, 2\ns_add_u32 s37, s38, 0x27f80\nv_mov_b32 v25, s37\ns_lshr_b32 s36, s12, 23\n"
    "s_add_u32 s35, s36, 4\nv_mov_b32 v26, s35\nv_mul_u32_u24 v109, 0x50, v116\ns_mul_i32 s34, s41, 0x1950\ns_and_b32 s33, s13, 1\ns_mul_i32 s32, s33, 0xa00\n"
    "s_add_u32 s31, s34, s32\ns_add_u32 s31, s31, 0x1d000\nv_add_u32 v27, s31, v109\nv_lshl_add_u32 v27, v114, 4, v27\nv_and_b32 v127, 31, v0\nv_bfe_u32 v126, v0, 5, 1\n"
    "v_bfe_u32 v125, v0, 1, 3\nv_xor_b32 v124, v126, v125\ns_lshr_b32 s43, s13, 1\ns_mul_i32 s42, s43, 0x2800\ns_and_b32 s41, s13, 1\ns_lshl_b32 s40, s41, 11\n"
    "s_add_u32 s39, s42, s40\ns_add_u32 s39, s39, 0x19400\nv_lshl_add_u32 v28, v127, 6, s39\nv_lshl_add_u32 v28, v124, 3, v28\nv_xor_b32 v29, 16, v28\nv_xor_b32 v30, 32, v28\n"
    "v_xor_b32 v31, 48, v28\n.Lg80:\ns_waitcnt vmcnt(0)\nv_xor_b32 v252, s11, v252\nv_xor_b32 v253, s11, v253\nv_xor_b32 v254, s11, v254\nv_xor_b32 v255, s11, v255\n"
    "v_or3_b32 v224, v252, v253, v254\nv_or_b32 v224, v224, v255\nv_cmp_ne_u32 vcc, 0, v224\ns_cbranch_vccz .Lg81\ns_sub_u32 s80, s80, 1\ns_cbranch_scc1 .Lg81\ns_sleep 2\n"
    "global_load_dwordx4 v[252:255], v240, s[4:5] sc1\ns_branch .Lg80\n.Lg81:\ns_mov_b32 s80, 0x10000\ns_sleep 2\ns_branch .Lgst77\n.Lgb76:\ns_barrier\ns_or_b32 s44, s2, s13\n"
    "s_cbranch_scc1 .Lg82\nv_mov_b32 v224, 16777216\nv_mov_b32 v225, s11\ns_mov_b64 exec, 1\nglobal_store_dword v224, v225, s[4:5] sc1\ns_mov_b64 exec, -1\n.Lg82:\n"
    "v_alignbit_b32 v228, v11, v10, 16\nv_alignbit_b32 v229, v12, v11, 16\nv_alignbit_b32 v230, v13, v12, 16\nv_alignbit_b32 v231, v14, v13, 16\nds_write_b128 v59, v[10:13]\n"
    "v_mov_b32 v232, v11\nv_mov_b32 v233, v12\nv_mov_b32 v234, v13\nv_mov_b32 v235, v14\nds_write_b128 v59, v[228:231] offset:2112\nv_alignbit_b32 v236, v12, v11, 16\n"
    "v_alignbit_b32 v237, v13, v12, 16\nv_alignbit_b32 v238, v14, v13, 16\nv_alignbit_b32 v239, v15, v14, 16\nds_write_b128 v59, v[232:235] offset:4224\n"
    "ds_write_b128 v59, v[236:239] offset:6336\ns_add_u32 s43, s12, 0x400000\ns_nop 0\ns_cbranch_execz .Lhop\nv_lshl_add_u32 v16, v0, 4, s43\ns_lshl_b32 s42, s18, 13\n"
    "s_add_u32 s41, s12, s42\ns_add_u32 s41, s41, 0x200000\nv_lshl_add_u32 v17, v0, 4, s41\nv_lshrrev_b32 v127, 3, v0\nv_and_b32 v126, 7, v0\ns_lshl_b32 s40, s18, 7\n"
    "s_add_u32 s39, s40, 0x600000\nv_lshl_add_u32 v125, v127, 15, s39\nv_lshl_add_u32 v125, v126, 4, v125\nv_lshrrev_b32 v124, 4, v0\nv_and_b32 v123, 15, v0\n"
    "s_lshl_b32 s38, s24, 18\ns_lshl_b32 s37, s25, 8\ns_add_u32 s36, s38, s37\ns_add_u32 s36, s36, 0x1200000\nv_lshl_add_u32 v122, v124, 13, s36\n"
    "v_lshl_add_u32 v122, v123, 4, v122\ns_cmp_lt_u32 s12, 1\ns_cselect_b64 vcc, -1, 0\nv_cndmask_b32 v21, v122, v125, vcc\ns_lshr_b32 s35, s12, 23\ns_add_u32 s34, s35, 4\n"
    "v_mov_b32 v26, s34\ns_waitcnt vmcnt(0)\ns_sleep 14\n.Lgst77:\nglobal_load_dwordx4 v[208:211], v60, s[4:5] offset:-4096 sc1\ns_nop 4\n"
    "global_load_dwordx4 v[212:215], v60, s[4:5] offset:0 sc1\ns_nop 4\nglobal_load_dwordx4 v[216:219], v61, s[4:5] offset:-4096 sc1\n"
    "global_load_dwordx4 v[220:223], v61, s[4:5] offset:0 sc1\ns_nop 4\nglobal_load_dwordx4 v[224:227], v62, s[4:5] offset:-4096 sc1\n"
    "global_load_dwordx4 v[228:231], v62, s[4:5] offset:0 sc1\ns_nop 4\nglobal_load_dwordx4 v[232:235], v63, s[4:5] offset:-4096 sc1\n"
    "global_load_dwordx4 v[236:239], v63, s[4:5] offset:0 sc1\ns_nop 4\ns_cmp_lg_u32 s12, 0\ns_cbranch_scc1 .Lb83x\ns_mov_b32 s86, 0x100000\n.Ls84:\n"
    "ds_read_b32 v244, v58 offset:64\ns_waitcnt lgkmcnt(0)\nv_cmp_gt_i32 vcc, 3, v244\ns_sub_u32 s86, s86, 1\ns_cbranch_scc1 .Ls85\ns_cbranch_vccz .Ls85\ns_sleep 1\n"
    "s_branch .Ls84\n.Ls85:\n.Lb83x:\ns_setprio 3\ns_waitcnt vmcnt(7)\n.Lr86:\nds_write_b128 v57, v[208:211] offset:0\nv_max3_u32 v244, v208, v209, 0\n"
    "v_max3_u32 v244, v210, v211, v244\nv_cmp_eq_u32 vcc, -1, v244\ns_waitcnt lgkmcnt(0)\ns_cbranch_vccnz .Lw87\n.Lw87x:\ns_barrier\ns_waitcnt vmcnt(6)\n.Lr88:\n"
    "ds_write_b128 v57, v[212:215] offset:4096\nv_max3_u32 v244, v212, v213, 0\nv_max3_u32 v244, v214, v215, v244\nv_cmp_eq_u32 vcc, -1, v244\ns_waitcnt lgkmcnt(0)\n"
    "s_cbranch_vccnz .Lw89\n.Lw89x:\ns_barrier\ns_waitcnt vmcnt(5)\n.Lr90:\nds_write_b128 v57, v[216:219] offset:8192\nv_max3_u32 v244, v216, v217, 0\n"
    "v_max3_u32 v244, v218, v219, v244\ns_waitcnt vmcnt(4)\nds_write_b128 v57, v[220:223] offset:12288\nv_max3_u32 v244, v220, v221, v244\nv_max3_u32 v244, v222, v223, v244\n"
    "v_cmp_eq_u32 vcc, -1, v244\ns_waitcnt lgkmcnt(0)\ns_cbranch_vccnz .Lw91\n.Lw91x:\ns_barrier\ns_waitcnt vmcnt(3)\n.Lr92:\nds_write_b128 v57, v[224:227] offset:16384\n"
    "v_max3_u32 v244, v224, v225, 0\nv_max3_u32 v244, v226, v227, v244\ns_waitcnt vmcnt(2)\nds_write_b128 v57, v[228:231] offset:20480\nv_max3_u32 v244, v228, v229, v244\n"
    "v_max3_u32 v244, v230, v231, v244\nv_cmp_eq_u32 vcc, -1, v244\ns_waitcnt lgkmcnt(0)\ns_cbranch_vccnz .Lw93\n.Lw93x:\ns_barrier\ns_waitcnt vmcnt(1)\n.Lr94:\n"
    "ds_write_b128 v57, v[232:235] offset:24576\nv_max3_u32 v244, v232, v233, 0\nv_max3_u32 v244, v234, v235, v244\ns_waitcnt vmcnt(0)\n"
    "ds_write_b128 v57, v[236:239] offset:28672\nv_max3_u32 v244, v236, v237, v244\nv_max3_u32 v244, v238, v239, v244\nv_cmp_eq_u32 vcc, -1, v244\ns_waitcnt lgkmcnt(0)\n"
    "s_cbranch_vccnz .Lw95\n.Lw95x:\ns_barrier\ns_setprio 0\n"
    "s_or_b32 s44, s12, s13\ns_cbranch_scc1 .Lg96\nv_and_b32 v239, 63, v0\ns_lshl_b32 s43, s18, 13\ns_add_u32 s42, s43, 0x601000\nv_lshl_add_u32 v40, v239, 4, s42\n"
    "s_lshl_b32 s41, s18, 10\ns_add_u32 s40, s41, 0xd00000\nv_lshl_add_u32 v41, v239, 4, s40\nv_mov_b64 v[52:53], -1\nv_mov_b64 v[54:55], -1\n"
    "global_store_dwordx4 v40, v[52:55], s[4:5] offset:-4096 sc1\nglobal_store_dwordx4 v40, v[52:55], s[4:5] offset:-3072 sc1\n"
    "global_store_dwordx4 v40, v[52:55], s[4:5] offset:-2048 sc1\nglobal_store_dwordx4 v40, v[52:55], s[4:5] offset:-1024 sc1\n"
    "global_store_dwordx4 v40, v[52:55], s[4:5] offset:0 sc1\nglobal_store_dwordx4 v40, v[52:55], s[4:5] offset:1024 sc1\n"
    "global_store_dwordx4 v40, v[52:55], s[4:5] offset:2048 sc1\nglobal_store_dwordx4 v40, v[52:55], s[4:5] offset:3072 sc1\nglobal_store_dwordx4 v41, v[52:55], s[4:5] sc1\n"
    ".Lg96:\ns_mov_b32 s45, 0x3c000000\ns_cmp_lg_u32 s13, 0\ns_waitcnt vmcnt(0)\ns_cbranch_scc1 .Lg100\ns_setprio 3\ns_mov_b32 exec_hi, 0\ns_barrier\n"
    "ds_read2_b32 v[44:45], v39 offset0:0 offset1:32\nds_read2_b32 v[46:47], v39 offset0:64 offset1:96\nds_read2_b32 v[48:49], v39 offset0:128 offset1:160\n"
    "ds_read2_b32 v[50:51], v39 offset0:192 offset1:224\ns_waitcnt lgkmcnt(0)\nv_pk_add_f32 v[44:45], v[44:45], v[46:47]\nv_pk_add_f32 v[48:49], v[48:49], v[50:51]\n"
    "v_pk_add_f32 v[40:41], v[44:45], v[48:49]\nv_mul_f32 v40, s45, v40\nv_mul_f32 v42, v40, v40\nv_fma_f32 v41, v41, s45, -v42\nv_max_f32 v41, 0, v41\nv_sqrt_f32 v41, v41\n"
    "s_nop 0\nv_cvt_pk_bf16_f32 v40, v40, v41\nglobal_store_dword v56, v40, s[4:5] sc1\ns_mov_b32 exec_hi, -1\ns_setprio 0\ns_branch .Lg99\n.Lg100:\ns_barrier\ns_sleep 3\n"
    ".Lg99:\nds_read_b128 v[10:13], v59\nds_read_b32 v14, v59 offset:4236\nds_read_u16 v15, v59 offset:6350\nds_read_b64 v[64:65], v23 offset:2016\n"
    "ds_read_b64 v[66:67], v23 offset:2024\nds_read_b64 v[68:69], v23 offset:1984\nds_read_b64 v[70:71], v23 offset:1992\nds_read_b64 v[72:73], v23 offset:1952\n"
    "ds_read_b64 v[74:75], v23 offset:1960\nds_read_b64 v[76:77], v23 offset:1920\nds_read_b64 v[78:79], v23 offset:1928\nds_read_b64 v[80:81], v23 offset:1888\n"
    "ds_read_b64 v[82:83], v23 offset:1896\nds_read_b64 v[84:85], v23 offset:1856\nds_read_b64 v[86:87], v23 offset:1864\nds_read_b64 v[88:89], v23 offset:1824\n"
    "ds_read_b64 v[90:91], v23 offset:1832\ns_nop 0\ns_cbranch_execz .Lhop\nds_read_b64 v[92:93], v23 offset:1792\nds_read_b64 v[94:95], v23 offset:1800\n"
    "ds_read_b64 v[96:97], v23 offset:1760\nds_read_b64 v[98:99], v23 offset:1768\nds_read_b64 v[100:101], v23 offset:1728\nds_read_b64 v[102:103], v23 offset:1736\n"
    "ds_read_b64 v[104:105], v23 offset:1696\nds_read_b64 v[106:107], v23 offset:1704\nds_read_b64 v[108:109], v23 offset:1664\nds_read_b64 v[110:111], v23 offset:1672\n"
    "ds_read_b64 v[112:113], v23 offset:1632\nds_read_b64 v[114:115], v23 offset:1640\nds_read_b64 v[116:117], v23 offset:1600\nds_read_b64 v[118:119], v23 offset:1608\n"
    "ds_read_b64 v[120:121], v23 offset:1568\nds_read_b64 v[122:123], v23 offset:1576\nds_read_b64 v[124:125], v23 offset:1536\nds_read_b64 v[126:127], v23 offset:1544\n"
    "ds_read_b64 v[128:129], v23 offset:1504\nds_read_b64 v[130:131], v23 offset:1512\nds_read_b64 v[132:133], v23 offset:1472\nds_read_b64 v[134:135], v23 offset:1480\n"
    "ds_read_b64 v[136:137], v23 offset:1440\nds_read_b64 v[138:139], v23 offset:1448\nds_read_b64 v[140:141], v23 offset:1408\nds_read_b64 v[142:143], v23 offset:1416\n"
    "ds_read_b64 v[144:145], v23 offset:1376\nds_read_b64 v[146:147], v23 offset:1384\nds_read_b64 v[148:149], v23 offset:1344\nds_read_b64 v[150:151], v23 offset:1352\n"
    "ds_read_b64 v[152:153], v23 offset:1312\nds_read_b64 v[154:155], v23 offset:1320\nds_read_b64 v[156:157], v23 offset:1280\nds_read_b64 v[158:159], v23 offset:1288\n"
    "ds_read_b64 v[160:161], v23 offset:1248\nds_read_b64 v[162:163], v23 offset:1256\nds_read_b64 v[164:165], v23 offset:1216\nds_read_b64 v[166:167], v23 offset:1224\n"
    "ds_read_b64 v[168:169], v23 offset:1184\nds_read_b64 v[170:171], v23 offset:1192\nds_read_b64 v[172:173], v23 offset:1152\nds_read_b64 v[174:175], v23 offset:1160\n"
    "ds_read_b64 v[176:177], v23 offset:1120\nds_read_b64 v[178:179], v23 offset:1128\nds_read_b64 v[180:181], v23 offset:1088\nds_read_b64 v[182:183], v23 offset:1096\n"
    "ds_read_b64 v[184:185], v23 offset:1056\nds_read_b64 v[186:187], v23 offset:1064\nds_read_b64 v[188:189], v23 offset:1024\nds_read_b64 v[190:191], v23 offset:1032\n"
    "ds_read_b64 v[192:193], v23 offset:992\nds_read_b64 v[194:195], v23 offset:1000\nds_read_b64 v[196:197], v23 offset:960\nds_read_b64 v[198:199], v23 offset:968\n"
    "ds_read_b64 v[200:201], v23 offset:928\nds_read_b64 v[202:203], v23 offset:936\ns_branch .Lln79\n"
    ".Lhb75:\ns_cmp_lg_u32 s12, 0\ns_cbranch_scc1 .Lh102\ns_setprio 1\ns_cmp_eq_u32 s13, 0\ns_cbranch_scc1 .Lh102x\nds_write_b128 v33, v[40:43] offset:0\n"
    "ds_write_b128 v33, v[44:47] offset:1024\nds_write_b128 v33, v[48:51] offset:2048\nds_write_b128 v33, v[52:55] offset:3072\n.Lh102x:\ns_cmp_eq_u32 s26, 0\n"
    "s_waitcnt lgkmcnt(0)\ns_barrier\ns_cbranch_scc1 .Lh103\n.Lh104:\nds_read_b64 v[48:49], v34\nds_read_b128 v[192:195], v27 offset:0\n"
    "ds_read_b128 v[196:199], v27 offset:1024\nds_read_b128 v[200:203], v27 offset:4096\nds_read_b128 v[204:207], v27 offset:5120\nv_mov_b32 v52, 0\n"
    "ds_write_b16 v36, v52 offset:1400\ns_waitcnt vmcnt(17)\nglobal_load_dwordx4 v[240:243], v16, s[8:9] offset:0\n"
    "v_add_u32_sdwa v224, v36, v28 dst_sel:DWORD dst_unused:UNUSED_PAD src0_sel:DWORD src1_sel:WORD_0\n"
    "v_add_u32_sdwa v225, v36, v28 dst_sel:DWORD dst_unused:UNUSED_PAD src0_sel:DWORD src1_sel:WORD_1\n"
    "v_add_u32_sdwa v226, v36, v29 dst_sel:DWORD dst_unused:UNUSED_PAD src0_sel:DWORD src1_sel:WORD_0\n"
    "v_add_u32_sdwa v227, v36, v29 dst_sel:DWORD dst_unused:UNUSED_PAD src0_sel:DWORD src1_sel:WORD_1\nglobal_load_dwordx4 v[244:247], v16, s[8:9] offset:32\n"
    "v_add_u32_sdwa v228, v36, v30 dst_sel:DWORD dst_unused:UNUSED_PAD src0_sel:DWORD src1_sel:WORD_0\n"
    "v_add_u32_sdwa v229, v36, v30 dst_sel:DWORD dst_unused:UNUSED_PAD src0_sel:DWORD src1_sel:WORD_1\n"
    "v_add_u32_sdwa v230, v36, v31 dst_sel:DWORD dst_unused:UNUSED_PAD src0_sel:DWORD src1_sel:WORD_0\n"
    "v_add_u32_sdwa v231, v36, v31 dst_sel:DWORD dst_unused:UNUSED_PAD src0_sel:DWORD src1_sel:WORD_1\ns_waitcnt lgkmcnt(5)\nv_mov_b32 v50, 3\n"
    "global_load_dwordx4 v[128:131], v59, s[72:73] offset:-4096 sc1\nv_add_f32 v48, v48, v49\nv_lshrrev_b32 v48, 23, v48\nv_sub_u32 v48, 0x10c, v48\nv_min_u32 v48, 0xfc, v48\n"
    "global_load_dwordx4 v[132:135], v59, s[72:73] offset:-3072 sc1\nv_sub_u32 v49, 0xfe, v48\nv_lshlrev_b32 v48, 23, v48\nv_lshlrev_b32 v49, 23, v49\ns_waitcnt lgkmcnt(1)\n"
    "v_pk_add_f32 v[40:41], v[192:193], v[200:201]\nv_pk_add_f32 v[42:43], v[194:195], v[202:203]\nglobal_load_dwordx4 v[136:139], v59, s[72:73] offset:-2048 sc1\n"
    "v_pk_add_f32 v[44:45], v[196:197], v[204:205]\nv_pk_add_f32 v[46:47], v[198:199], v[206:207]\nv_pk_mul_f32 v[40:41], v[40:41], v[48:49] op_sel_hi:[1,0]\n"
    "v_pk_mul_f32 v[42:43], v[42:43], v[48:49] op_sel_hi:[1,0]\nglobal_load_dwordx4 v[140:143], v59, s[72:73] offset:-1024 sc1\n"
    "v_pk_mul_f32 v[44:45], v[44:45], v[48:49] op_sel_hi:[1,0]\nv_pk_mul_f32 v[46:47], v[46:47], v[48:49] op_sel_hi:[1,0]\nv_cvt_pk_f16_f32 v40, v40, v41\n"
    "v_cvt_pk_f16_f32 v41, v42, v43\nv_cvt_pk_f16_f32 v42, v44, v45\nglobal_load_dwordx4 v[144:147], v59, s[72:73] offset:0 sc1\nv_cvt_pk_f16_f32 v43, v46, v47\n"
    "ds_write_b16 v224, v40\nds_write_b16_d16_hi v225, v40\nds_write_b16 v226, v41\nglobal_load_dwordx4 v[148:151], v59, s[72:73] offset:1024 sc1\n"
    "ds_write_b16_d16_hi v227, v41\nds_write_b16 v228, v42\nds_write_b16_d16_hi v229, v42\nds_write_b16 v230, v43\nds_write_b16_d16_hi v231, v43\n"
    "global_load_dwordx4 v[152:155], v59, s[72:73] offset:2048 sc1\ns_mov_b64 exec, 1\nds_write_b32 v37, v50 offset:64\nds_write_b32 v34, v49 offset:32\ns_mov_b64 exec, -1\n"
    "v_add_u32_sdwa v232, v36, v10 dst_sel:DWORD dst_unused:UNUSED_PAD src0_sel:DWORD src1_sel:WORD_0\n"
    "v_add_u32_sdwa v233, v36, v10 dst_sel:DWORD dst_unused:UNUSED_PAD src0_sel:DWORD src1_sel:WORD_1\n"
    "v_add_u32_sdwa v234, v36, v11 dst_sel:DWORD dst_unused:UNUSED_PAD src0_sel:DWORD src1_sel:WORD_0\n"
    "v_add_u32_sdwa v235, v36, v11 dst_sel:DWORD dst_unused:UNUSED_PAD src0_sel:DWORD src1_sel:WORD_1\nglobal_load_dwordx4 v[156:159], v59, s[72:73] offset:3072 sc1\n"
    "v_add_u32_sdwa v236, v36, v12 dst_sel:DWORD dst_unused:UNUSED_PAD src0_sel:DWORD src1_sel:WORD_0\n"
    "v_add_u32_sdwa v237, v36, v12 dst_sel:DWORD dst_unused:UNUSED_PAD src0_sel:DWORD src1_sel:WORD_1\n"
    "v_add_u32_sdwa v238, v36, v13 dst_sel:DWORD dst_unused:UNUSED_PAD src0_sel:DWORD src1_sel:WORD_0\n"
    "v_add_u32_sdwa v239, v36, v13 dst_sel:DWORD dst_unused:UNUSED_PAD src0_sel:DWORD src1_sel:WORD_1\ns_cbranch_execz .Lhop\n"
    "global_load_dwordx4 v[160:163], v59, s[74:75] offset:-4096 sc1\nv_add_u32_sdwa v248, v36, v14 dst_sel:DWORD dst_unused:UNUSED_PAD src0_sel:DWORD src1_sel:WORD_0\n"
    "v_add_u32_sdwa v249, v36, v14 dst_sel:DWORD dst_unused:UNUSED_PAD src0_sel:DWORD src1_sel:WORD_1\n"
    "v_add_u32_sdwa v250, v36, v15 dst_sel:DWORD dst_unused:UNUSED_PAD src0_sel:DWORD src1_sel:WORD_0\ns_mov_b32 s86, 0x100000\n.Ls111:\nds_read_b32 v48, v32 offset:0\n"
    "s_waitcnt lgkmcnt(0)\nv_cmp_gt_i32 vcc, 3, v48\ns_sub_u32 s86, s86, 1\ns_cbranch_scc1 .Ls112\ns_cbranch_vccz .Ls112\ns_branch .Ls111\n.Ls112:\nds_read_u16 v200, v232\n"
    "global_load_dwordx4 v[164:167], v59, s[74:75] offset:-3072 sc1\nds_read_u16 v201, v233\nds_read_u16 v202, v234\nds_read_u16 v203, v235\nds_read_u16 v204, v236\n"
    "global_load_dwordx4 v[168:171], v59, s[74:75] offset:-2048 sc1\nds_read_u16 v205, v237\nds_read_u16 v206, v238\nds_read_u16 v207, v239\nds_read_u16 v208, v248\n"
    "ds_read_u16 v209, v249\nglobal_load_dwordx4 v[172:175], v59, s[74:75] offset:-1024 sc1\nds_read_u16 v210, v250\ns_waitcnt lgkmcnt(0)\nv_lshl_or_b32 v192, v201, 16, v200\n"
    "v_lshl_or_b32 v193, v203, 16, v202\nv_lshl_or_b32 v194, v205, 16, v204\nglobal_load_dwordx4 v[176:179], v59, s[74:75] offset:0 sc1\nv_lshl_or_b32 v195, v207, 16, v206\n"
    "v_lshl_or_b32 v196, v209, 16, v208\nv_mov_b32 v197, v210\nv_alignbit_b32 v212, v193, v192, 16\nv_alignbit_b32 v213, v194, v193, 16\n"
    "global_load_dwordx4 v[180:183], v59, s[74:75] offset:1024 sc1\nv_alignbit_b32 v214, v195, v194, 16\nv_alignbit_b32 v215, v196, v195, 16\nds_write_b128 v35, v[192:195]\n"
    "v_mov_b32 v216, v193\nglobal_load_dwordx4 v[184:187], v59, s[74:75] offset:2048 sc1\nv_mov_b32 v217, v194\nv_mov_b32 v218, v195\nv_mov_b32 v219, v196\n"
    "ds_write_b128 v35, v[212:215] offset:2112\nv_alignbit_b32 v220, v194, v193, 16\nglobal_load_dwordx4 v[188:191], v59, s[74:75] offset:3072 sc1\n"
    "v_alignbit_b32 v221, v195, v194, 16\nv_alignbit_b32 v222, v196, v195, 16\nv_alignbit_b32 v223, v197, v196, 16\nds_write_b128 v35, v[216:219] offset:4224\n"
    "ds_write_b128 v35, v[220:223] offset:6336\ns_waitcnt vmcnt(16)\nv_permlane32_swap_b32 v240, v242\nv_permlane32_swap_b32 v241, v243\nv_permlane32_swap_b32 v244, v246\n"
    "v_permlane32_swap_b32 v245, v247\nv_and_b32 v239, 63, v0\nv_add_u32 v238, 3, v239\nv_lshrrev_b32 v237, 2, v238\nv_mul_u32_u24 v236, 0x50, v237\nv_and_b32 v235, 3, v238\n"
    "s_lshr_b32 s43, s13, 1\ns_mul_i32 s42, s43, 0x1950\ns_add_u32 s41, s42, 0x1d500\nv_add_u32 v10, s41, v236\nv_lshl_add_u32 v10, v235, 4, v10\nv_add_u32 v234, 2, v239\n"
    "v_lshrrev_b32 v233, 2, v234\nv_mul_u32_u24 v232, 0x50, v233\nv_and_b32 v231, 3, v234\ns_add_u32 s40, s42, 0x1d508\nv_add_u32 v11, s40, v232\n"
    "v_lshl_add_u32 v11, v231, 4, v11\nv_bfe_u32 v230, v0, 2, 4\nv_mul_u32_u24 v229, 0x50, v230\nv_and_b32 v228, 3, v0\ns_add_u32 s39, s42, 0x1d550\nv_add_u32 v12, s39, v229\n"
    "v_lshl_add_u32 v12, v228, 4, v12\ns_and_b32 s38, s13, 1\nv_add_u32 v227, 1, v239\nv_lshrrev_b32 v226, 5, v227\nv_mul_u32_u24 v225, 0x240, v226\nv_and_b32 v224, 31, v227\n"
    "s_mul_i32 s37, s43, 0x3180\ns_add_u32 s36, s37, 0x20540\nv_add_u32 v223, s36, v225\nv_lshl_add_u32 v223, v224, 4, v223\nv_lshrrev_b32 v222, 6, v227\n"
    "v_mul_u32_u24 v221, 0x480, v222\nv_and_b32 v220, 63, v227\ns_add_u32 s35, s37, 0x22280\nv_add_u32 v219, s35, v221\nv_lshl_add_u32 v219, v220, 4, v219\n"
    "s_cmp_lt_u32 s38, 1\ns_cselect_b64 vcc, -1, 0\nv_cndmask_b32 v13, v219, v223, vcc\nv_lshrrev_b32 v218, 5, v234\nv_mul_u32_u24 v217, 0x240, v218\n"
    "v_and_b32 v216, 31, v234\nv_add_u32 v215, s36, v217\nv_lshl_add_u32 v215, v216, 4, v215\ns_add_u32 s34, s37, 0x22b80\nv_lshl_add_u32 v214, v239, 4, s34\n"
    "s_cmp_lt_u32 s38, 1\ns_cselect_b64 vcc, -1, 0\nv_cndmask_b32 v14, v214, v215, vcc\ns_and_b32 s43, s13, 1\nv_and_b32 v239, 63, v0\nv_add_u32 v238, 1, v239\n"
    "v_lshrrev_b32 v237, 5, v238\nv_mul_u32_u24 v236, 0x240, v237\nv_and_b32 v235, 31, v238\ns_lshr_b32 s42, s13, 1\ns_mul_i32 s41, s42, 0x3180\ns_add_u32 s40, s41, 0x20540\n"
    "v_add_u32 v234, s40, v236\nv_lshl_add_u32 v234, v235, 4, v234\nv_lshrrev_b32 v233, 6, v238\nv_mul_u32_u24 v232, 0x480, v233\nv_and_b32 v231, 63, v238\n"
    "s_add_u32 s39, s41, 0x22280\nv_add_u32 v230, s39, v232\nv_lshl_add_u32 v230, v231, 4, v230\ns_cmp_lt_u32 s43, 1\ns_cselect_b64 vcc, -1, 0\n"
    "v_cndmask_b32 v229, v230, v234, vcc\ns_mov_b32 s38, 0xfffffb40\ns_cmp_lt_u32 s43, 1\ns_cselect_b32 s37, s38, 0x1f00\ns_add_u32 s36, s41, s37\n"
    "s_add_u32 s36, s36, 0x20300\nv_mov_b32 v228, s36\nv_cmp_gt_u32 vcc, 64, v238\ns_nop 1\nv_cndmask_b32 v15, v228, v229, vcc\nv_add_u32 v227, 2, v239\n"
    "v_lshrrev_b32 v226, 5, v227\nv_mul_u32_u24 v225, 0x240, v226\nv_and_b32 v224, 31, v227\nv_add_u32 v223, s40, v225\nv_lshl_add_u32 v223, v224, 4, v223\n"
    "s_add_u32 s35, s41, 0x1fe40\nv_mov_b32 v222, s35\nv_cmp_gt_u32 vcc, 64, v227\ns_nop 1\nv_cndmask_b32 v35, v222, v223, vcc\ns_lshl_b32 s34, s18, 13\n"
    "s_lshl_b32 s33, s13, 11\ns_add_u32 s32, s34, s33\ns_add_u32 s32, s32, 0x800000\nv_lshl_add_u32 v37, v239, 4, s32\nv_and_b32 v239, 7, v0\ns_lshl_b32 s43, s2, 7\n"
    "s_add_u32 s42, s43, 0xc00000\nv_lshl_add_u32 v38, v239, 4, s42\ns_branch .Lhwo78\n.Lh102:\ns_barrier\nv_bfe_u32 v223, v0, 5, 1\nv_and_b32 v222, 31, v0\n"
    "s_lshl_b32 s43, s21, 15\ns_lshl_b32 s42, s22, 13\ns_lshl_b32 s41, s13, 11\ns_add_u32 s40, s43, s42\ns_add_u32 s40, s40, s41\ns_add_u32 s40, s40, 0x600000\n"
    "v_lshl_add_u32 v16, v223, 8, s40\nv_lshl_add_u32 v16, v222, 2, v16\ns_nop 0\ns_cbranch_execz .Lhop\nglobal_load_dword v240, v16, s[4:5] offset:0 sc1\n"
    "global_load_dword v241, v16, s[4:5] offset:128 sc1\nglobal_load_dword v242, v16, s[4:5] offset:512 sc1\nglobal_load_dword v243, v16, s[4:5] offset:640 sc1\n"
    "global_load_dword v244, v16, s[4:5] offset:1024 sc1\nglobal_load_dword v245, v16, s[4:5] offset:1152 sc1\nglobal_load_dword v246, v16, s[4:5] offset:1536 sc1\n"
    "global_load_dword v247, v16, s[4:5] offset:1664 sc1\nglobal_load_dwordx4 v[128:131], v59, s[72:73] offset:-4096 sc1\nv_bfe_u32 v223, v0, 5, 1\nv_and_b32 v222, 31, v0\n"
    "global_load_dwordx4 v[132:135], v59, s[72:73] offset:-3072 sc1\ns_lshl_b32 s43, s13, 11\ns_add_u32 s42, s43, 0x1d000\n"
    "global_load_dwordx4 v[136:139], v59, s[72:73] offset:-2048 sc1\nv_lshl_add_u32 v17, v223, 8, s42\nv_lshl_add_u32 v17, v222, 2, v17\n"
    "global_load_dwordx4 v[140:143], v59, s[72:73] offset:-1024 sc1\nv_and_b32 v221, 63, v0\nv_lshl_add_u32 v18, v221, 4, s42\ns_mov_b32 s41, 0x1f600\n"
    "global_load_dwordx4 v[144:147], v59, s[72:73] offset:0 sc1\nv_lshl_add_u32 v19, v221, 4, s41\nv_bfe_u32 v220, v0, 3, 3\n"
    "global_load_dwordx4 v[148:151], v59, s[72:73] offset:1024 sc1\nv_and_b32 v219, 7, v0\ns_lshl_b32 s40, s22, 19\n"
    "global_load_dwordx4 v[152:155], v59, s[72:73] offset:2048 sc1\ns_lshl_b32 s39, s13, 17\ns_lshl_b32 s38, s21, 7\n"
    "global_load_dwordx4 v[156:159], v59, s[72:73] offset:3072 sc1\ns_add_u32 s37, s12, s40\ns_add_u32 s37, s37, s39\ns_add_u32 s37, s37, s38\n"
    "global_load_dwordx4 v[160:163], v59, s[74:75] offset:-4096 sc1\ns_add_u32 s37, s37, 0x200000\nv_lshl_add_u32 v20, v220, 13, s37\n"
    "global_load_dwordx4 v[164:167], v59, s[74:75] offset:-3072 sc1\nv_lshl_add_u32 v20, v219, 4, v20\ns_add_u32 s36, s12, s40\n"
    "global_load_dwordx4 v[168:171], v59, s[74:75] offset:-2048 sc1\ns_add_u32 s36, s36, s39\ns_add_u32 s36, s36, s38\n"
    "global_load_dwordx4 v[172:175], v59, s[74:75] offset:-1024 sc1\ns_add_u32 s36, s36, 0x210000\nv_lshl_add_u32 v21, v220, 13, s36\nv_lshl_add_u32 v21, v219, 4, v21\n"
    "global_load_dwordx4 v[176:179], v59, s[74:75] offset:0 sc1\ns_lshl_b32 s35, s13, 8\ns_add_u32 s34, s35, 0x27600\n"
    "global_load_dwordx4 v[180:183], v59, s[74:75] offset:1024 sc1\nv_lshl_add_u32 v22, v221, 2, s34\nv_bfe_u32 v223, v0, 5, 1\n"
    "global_load_dwordx4 v[184:187], v59, s[74:75] offset:2048 sc1\nv_and_b32 v222, 31, v0\ns_lshl_b32 s43, s13, 11\n"
    "global_load_dwordx4 v[188:191], v59, s[74:75] offset:3072 sc1\ns_add_u32 s42, s43, 0x1d400\nv_lshl_add_u32 v26, v223, 8, s42\nv_lshl_add_u32 v26, v222, 2, v26\n"
    "s_mov_b32 s80, 0x10000\ns_waitcnt vmcnt(16)\nv_max3_u32 v232, v240, v241, v242\nv_max3_u32 v233, v243, v244, v245\nv_max3_u32 v232, v232, v246, v247\n"
    "v_max_u32 v232, v232, v233\nv_cmp_eq_u32 vcc, -1, v232\ns_cbranch_vccnz .Lw110\n"
    ".Lhwo78:\nv_lshlrev_b32 v192, 16, v240\nv_and_b32 v193, 0xffff0000, v240\nv_lshlrev_b32 v194, 16, v241\nv_and_b32 v195, 0xffff0000, v241\nv_lshlrev_b32 v196, 16, v242\n"
    "v_and_b32 v197, 0xffff0000, v242\nv_lshlrev_b32 v198, 16, v243\nv_and_b32 v199, 0xffff0000, v243\nv_lshlrev_b32 v200, 16, v244\nv_and_b32 v201, 0xffff0000, v244\n"
    "v_lshlrev_b32 v202, 16, v245\nv_and_b32 v203, 0xffff0000, v245\nv_lshlrev_b32 v204, 16, v246\nv_and_b32 v205, 0xffff0000, v246\nv_lshlrev_b32 v206, 16, v247\n"
    "v_and_b32 v207, 0xffff0000, v247\ns_mov_b32 s48, 0x3f803f80\ns_waitcnt lgkmcnt(0)\ns_setprio 2\ns_barrier\nds_read_b128 v[208:211], v19 offset:0\n"
    "ds_read_b128 v[212:215], v19 offset:1024\nds_read_b128 v[216:219], v19 offset:2048\nds_read_b128 v[220:223], v19 offset:3072\ns_waitcnt lgkmcnt(3)\n"
    "v_mfma_f32_32x32x16_bf16 v[192:207], v[64:67], v[208:211], v[192:207]\ns_barrier\nds_read_b128 v[208:211], v19 offset:4096\ns_waitcnt lgkmcnt(3)\n"
    "v_mfma_f32_32x32x16_bf16 v[192:207], v[68:71], v[212:215], v[192:207]\nds_read_b128 v[212:215], v19 offset:5120\ns_waitcnt lgkmcnt(3)\n"
    "v_mfma_f32_32x32x16_bf16 v[192:207], v[72:75], v[216:219], v[192:207]\nds_read_b128 v[216:219], v19 offset:6144\ns_waitcnt lgkmcnt(3)\n"
    "v_mfma_f32_32x32x16_bf16 v[192:207], v[76:79], v[220:223], v[192:207]\nds_read_b128 v[220:223], v19 offset:7168\ns_waitcnt lgkmcnt(3)\n"
    "v_mfma_f32_32x32x16_bf16 v[192:207], v[80:83], v[208:211], v[192:207]\ns_barrier\nds_read_b128 v[208:211], v19 offset:8192\ns_waitcnt lgkmcnt(3)\n"
    "v_mfma_f32_32x32x16_bf16 v[192:207], v[84:87], v[212:215], v[192:207]\nds_read_b128 v[212:215], v19 offset:9216\ns_waitcnt lgkmcnt(3)\n"
    "v_mfma_f32_32x32x16_bf16 v[192:207], v[88:91], v[216:219], v[192:207]\nds_read_b128 v[216:219], v19 offset:10240\ns_waitcnt lgkmcnt(3)\n"
    "v_mfma_f32_32x32x16_bf16 v[192:207], v[92:95], v[220:223], v[192:207]\nds_read_b128 v[220:223], v19 offset:11264\ns_waitcnt lgkmcnt(3)\n"
    "v_mfma_f32_32x32x16_bf16 v[192:207], v[96:99], v[208:211], v[192:207]\nds_read_b128 v[208:211], v19 offset:12288\ns_waitcnt lgkmcnt(3)\n"
    "v_mfma_f32_32x32x16_bf16 v[192:207], v[100:103], v[212:215], v[192:207]\nds_read_b128 v[212:215], v19 offset:13312\ns_waitcnt lgkmcnt(3)\n"
    "v_mfma_f32_32x32x16_bf16 v[192:207], v[104:107], v[216:219], v[192:207]\nds_read_b128 v[216:219], v19 offset:14336\ns_waitcnt lgkmcnt(3)\n"
    "v_mfma_f32_32x32x16_bf16 v[192:207], v[108:111], v[220:223], v[192:207]\nds_read_b128 v[220:223], v19 offset:15360\ns_waitcnt lgkmcnt(3)\n"
    "v_mfma_f32_32x32x16_bf16 v[192:207], v[112:115], v[208:211], v[192:207]\ns_barrier\nds_read_b128 v[208:211], v19 offset:16384\ns_waitcnt lgkmcnt(3)\n"
    "v_mfma_f32_32x32x16_bf16 v[192:207], v[116:119], v[212:215], v[192:207]\nds_read_b128 v[212:215], v19 offset:17408\ns_waitcnt lgkmcnt(3)\n"
    "v_mfma_f32_32x32x16_bf16 v[192:207], v[120:123], v[216:219], v[192:207]\nds_read_b128 v[216:219], v19 offset:18432\ns_waitcnt lgkmcnt(3)\n"
    "v_mfma_f32_32x32x16_bf16 v[192:207], v[124:127], v[220:223], v[192:207]\nds_read_b128 v[220:223], v19 offset:19456\ns_waitcnt vmcnt(8) lgkmcnt(3)\n"
    "v_mfma_f32_32x32x16_bf16 v[192:207], v[128:131], v[208:211], v[192:207]\nds_read_b128 v[208:211], v19 offset:20480\ns_waitcnt lgkmcnt(3)\n"
    "v_mfma_f32_32x32x16_bf16 v[192:207], v[132:135], v[212:215], v[192:207]\nds_read_b128 v[212:215], v19 offset:21504\ns_waitcnt lgkmcnt(3)\n"
    "v_mfma_f32_32x32x16_bf16 v[192:207], v[136:139], v[216:219], v[192:207]\nds_read_b128 v[216:219], v19 offset:22528\ns_waitcnt lgkmcnt(3)\n"
    "v_mfma_f32_32x32x16_bf16 v[192:207], v[140:143], v[220:223], v[192:207]\ns_nop 0\ns_cbranch_execz .Lhop\nds_read_b128 v[220:223], v19 offset:23552\ns_waitcnt lgkmcnt(3)\n"
    "v_mfma_f32_32x32x16_bf16 v[192:207], v[144:147], v[208:211], v[192:207]\ns_barrier\nds_read_b128 v[208:211], v19 offset:24576\ns_waitcnt lgkmcnt(3)\n"
    "v_mfma_f32_32x32x16_bf16 v[192:207], v[148:151], v[212:215], v[192:207]\nds_read_b128 v[212:215], v19 offset:25600\ns_waitcnt lgkmcnt(3)\n"
    "v_mfma_f32_32x32x16_bf16 v[192:207], v[152:155], v[216:219], v[192:207]\nds_read_b128 v[216:219], v19 offset:26624\ns_waitcnt lgkmcnt(3)\n"
    "v_mfma_f32_32x32x16_bf16 v[192:207], v[156:159], v[220:223], v[192:207]\nds_read_b128 v[220:223], v19 offset:27648\ns_waitcnt vmcnt(0) lgkmcnt(3)\n"
    "v_mfma_f32_32x32x16_bf16 v[192:207], v[160:163], v[208:211], v[192:207]\nds_read_b128 v[208:211], v19 offset:28672\ns_waitcnt lgkmcnt(3)\n"
    "v_mfma_f32_32x32x16_bf16 v[192:207], v[164:167], v[212:215], v[192:207]\nds_read_b128 v[212:215], v19 offset:29696\ns_waitcnt lgkmcnt(3)\n"
    "v_mfma_f32_32x32x16_bf16 v[192:207], v[168:171], v[216:219], v[192:207]\nds_read_b128 v[216:219], v19 offset:30720\ns_waitcnt lgkmcnt(3)\n"
    "v_mfma_f32_32x32x16_bf16 v[192:207], v[172:175], v[220:223], v[192:207]\nds_read_b128 v[220:223], v19 offset:31744\ns_waitcnt lgkmcnt(3)\n"
    "v_mfma_f32_32x32x16_bf16 v[192:207], v[176:179], v[208:211], v[192:207]\ns_waitcnt lgkmcnt(2)\nv_mfma_f32_32x32x16_bf16 v[192:207], v[180:183], v[212:215], v[192:207]\n"
    "s_waitcnt lgkmcnt(1)\nv_mfma_f32_32x32x16_bf16 v[192:207], v[184:187], v[216:219], v[192:207]\ns_waitcnt lgkmcnt(0)\n"
    "v_mfma_f32_32x32x16_bf16 v[192:207], v[188:191], v[220:223], v[192:207]\ns_nop 11\nv_cvt_pk_bf16_f32 v208, v192, v193\nv_cvt_pk_bf16_f32 v209, v194, v195\n"
    "v_cvt_pk_bf16_f32 v210, v196, v197\nv_cvt_pk_bf16_f32 v211, v198, v199\nv_cvt_pk_bf16_f32 v212, v200, v201\nv_cvt_pk_bf16_f32 v213, v202, v203\n"
    "v_cvt_pk_bf16_f32 v214, v204, v205\nv_cvt_pk_bf16_f32 v215, v206, v207\nv_dot2_f32_bf16 v236, v208, s48, 0\nv_dot2_f32_bf16 v237, v208, v208, 0\n"
    "v_dot2_f32_bf16 v238, v209, s48, 0\nv_dot2_f32_bf16 v239, v209, v209, 0\nv_dot2_f32_bf16 v236, v210, s48, v236\nv_dot2_f32_bf16 v237, v210, v210, v237\n"
    "v_dot2_f32_bf16 v238, v211, s48, v238\nv_dot2_f32_bf16 v239, v211, v211, v239\nv_dot2_f32_bf16 v236, v212, s48, v236\nv_dot2_f32_bf16 v237, v212, v212, v237\n"
    "v_dot2_f32_bf16 v238, v213, s48, v238\nv_dot2_f32_bf16 v239, v213, v213, v239\nv_dot2_f32_bf16 v236, v214, s48, v236\nv_dot2_f32_bf16 v237, v214, v214, v237\n"
    "v_dot2_f32_bf16 v238, v215, s48, v238\nv_dot2_f32_bf16 v239, v215, v215, v239\nds_write2_b32 v17, v208, v209 offset1:32\ns_nop 1\n"
    "v_pk_add_f32 v[236:237], v[236:237], v[238:239]\ns_nop 1\nv_permlane32_swap_b32 v236, v237\nv_add_f32 v236, v236, v237\nds_write_b32 v22, v236\n"
    "ds_write2_b32 v17, v210, v211 offset0:128 offset1:160\nds_read_b128 v[216:219], v18\nds_write2_b32 v26, v212, v213 offset1:32\ns_waitcnt lgkmcnt(3)\ns_barrier\n"
    "ds_write2_b32 v26, v214, v215 offset0:128 offset1:160\nds_read_b128 v[220:223], v18 offset:1024\ns_setprio 0\ns_waitcnt lgkmcnt(3)\n"
    "global_store_dwordx4 v20, v[216:219], s[4:5] sc1\ns_waitcnt lgkmcnt(0)\nglobal_store_dwordx4 v21, v[220:223], s[4:5] sc1\ns_add_u32 s43, s12, 0x400000\n"
    "v_lshl_add_u32 v16, v0, 4, s43\ns_lshl_b32 s42, s18, 13\ns_add_u32 s41, s12, s42\ns_add_u32 s41, s41, 0x200000\nv_lshl_add_u32 v17, v0, 4, s41\n"
    "v_lshrrev_b32 v239, 3, v0\nv_mul_u32_u24 v238, 0x50, v239\nv_and_b32 v237, 7, v0\nv_add_u32 v18, 0x1d550, v238\nv_lshl_add_u32 v18, v237, 3, v18\n"
    "v_bfe_u32 v236, v0, 4, 3\nv_xor_b32 v235, v0, v236\ns_mov_b32 s40, 0x19400\nv_lshl_add_u32 v19, v235, 3, s40\nv_bfe_u32 v234, v0, 4, 1\nv_xor_b32 v233, v0, v234\n"
    "s_mov_b32 s39, 0x17c00\nv_lshl_add_u32 v20, v233, 3, s39\ns_lshl_b32 s38, s18, 7\ns_add_u32 s37, s38, 0x600000\nv_lshl_add_u32 v232, v239, 15, s37\n"
    "v_lshl_add_u32 v232, v237, 4, v232\nv_lshrrev_b32 v231, 4, v0\nv_and_b32 v230, 15, v0\ns_lshl_b32 s36, s24, 18\ns_lshl_b32 s35, s25, 8\ns_add_u32 s34, s36, s35\n"
    "s_add_u32 s34, s34, 0x1200000\nv_lshl_add_u32 v229, v231, 13, s34\nv_lshl_add_u32 v229, v230, 4, v229\ns_cmp_lt_u32 s12, 1\ns_cselect_b64 vcc, -1, 0\n"
    "v_cndmask_b32 v21, v229, v232, vcc\nv_lshrrev_b32 v239, 8, v0\nv_mul_u32_u24 v238, 0x1950, v239\nv_and_b32 v237, 0xff, v0\nv_add_u32 v236, 0x1d000, v238\n"
    "v_lshl_add_u32 v236, v237, 4, v236\nv_mul_u32_u24 v235, 0x3180, v239\ns_mov_b32 s43, 0xfffffab0\nv_lshl_add_u32 v234, v237, 4, s43\ns_mov_b32 s42, 0x810\n"
    "v_lshl_add_u32 v233, v237, 4, s42\nv_mov_b32 v232, 0x1f80\nv_cmp_gt_u32 vcc, 0xa1, v237\ns_nop 1\nv_cndmask_b32 v231, v232, v233, vcc\nv_cmp_gt_u32 vcc, 0x7b, v237\n"
    "s_nop 1\nv_cndmask_b32 v230, v231, v234, vcc\nv_add_u32 v229, 0x20300, v235\nv_add_u32 v229, v229, v230\nv_cmp_gt_u32 vcc, 0x55, v237\ns_nop 1\n"
    "v_cndmask_b32 v22, v229, v236, vcc\nv_and_b32 v228, 31, v0\nv_sub_u32 v227, 0x3ff, v228\nv_bfe_u32 v226, v0, 5, 1\nv_lshl_add_u32 v225, v226, 3, v227\n"
    "v_and_b32 v224, 3, v225\nv_mul_u32_u24 v223, 0x840, v224\nv_lshrrev_b32 v222, 2, v225\ns_lshr_b32 s41, s13, 1\ns_mul_i32 s40, s41, 0x2100\ns_add_u32 s39, s40, 0x17800\n"
    "v_add_u32 v23, s39, v223\nv_lshl_add_u32 v23, v222, 3, v23\ns_lshl_b32 s38, s13, 2\ns_add_u32 s37, s38, 0x27f80\nv_mov_b32 v25, s37\ns_lshr_b32 s36, s12, 23\n"
    "s_add_u32 s35, s36, 4\nv_mov_b32 v26, s35\ns_and_b32 s34, s13, 1\nv_and_b32 v221, 1, v0\ns_mov_b32 s33, 0x50\nv_lshl_add_u32 v220, v221, 2, s33\nv_mov_b32 v219, 48\n"
    "s_cbranch_execz .Lhop\ns_cmp_lt_u32 s34, 1\ns_cselect_b64 vcc, -1, 0\nv_cndmask_b32 v218, v219, v220, vcc\ns_lshl_b32 s32, s41, 3\ns_add_u32 s31, s32, 0x27f80\n"
    "v_add_u32 v24, s31, v218\nv_add_u32 v239, 0xffffff86, v0\nv_lshrrev_b32 v238, 3, v239\nv_mul_u32_u24 v237, 0x50, v238\nv_and_b32 v236, 7, v239\n"
    "v_add_u32 v30, 0x1d004, v237\nv_lshl_add_u32 v30, v236, 3, v30\nv_add_u32 v235, 0xffffff87, v0\nv_lshrrev_b32 v234, 3, v235\nv_mul_u32_u24 v233, 0x50, v234\n"
    "v_and_b32 v232, 7, v235\nv_add_u32 v31, 0x1d000, v233\nv_lshl_add_u32 v31, v232, 3, v31\ns_and_b32 s43, s13, 1\nv_bfe_u32 v231, v0, 4, 1\n"
    "v_mul_u32_u24 v230, 0x480, v231\nv_mov_b32 v229, 0\ns_cmp_lt_u32 s43, 1\ns_cselect_b64 vcc, -1, 0\nv_cndmask_b32 v228, v229, v230, vcc\nv_bfe_u32 v227, v0, 1, 1\n"
    "v_mul_u32_u24 v226, 0x240, v227\nv_bfe_u32 v225, v0, 5, 1\nv_bfe_u32 v224, v0, 2, 2\nv_and_b32 v223, 1, v0\ns_lshr_b32 s42, s13, 1\ns_mul_i32 s41, s42, 0x3180\n"
    "s_add_u32 s40, s41, 0x20300\nv_add_u32 v27, s40, v228\nv_add_u32 v27, v27, v226\nv_lshl_add_u32 v27, v225, 7, v27\nv_lshl_add_u32 v27, v224, 4, v27\n"
    "v_lshl_add_u32 v27, v223, 3, v27\nv_and_b32 v222, 3, v0\ns_add_u32 s39, s41, 0x22280\nv_lshl_add_u32 v29, v225, 8, s39\nv_lshl_add_u32 v29, v224, 5, v29\n"
    "v_lshl_add_u32 v29, v222, 3, v29\nv_add_u32 v28, s39, v230\nv_lshl_add_u32 v28, v225, 8, v28\nv_lshl_add_u32 v28, v224, 5, v28\nv_lshl_add_u32 v28, v222, 3, v28\n"
    "v_and_b32 v221, 31, v0\nv_bfe_u32 v220, v0, 3, 1\nv_xor_b32 v219, v225, v220\ns_mul_i32 s38, s42, 0x2800\ns_add_u32 s37, s38, 0x18000\nv_lshl_add_u32 v32, v221, 4, s37\n"
    "v_lshl_add_u32 v32, v219, 3, v32\nv_xor_b32 v218, v225, v224\ns_add_u32 s36, s38, 0x18c00\nv_lshl_add_u32 v33, v221, 5, s36\nv_lshl_add_u32 v33, v218, 3, v33\n"
    "v_bfe_u32 v217, v0, 4, 2\nv_xor_b32 v216, v0, v217\ns_mov_b32 s35, 0x18400\nv_lshl_add_u32 v34, v216, 3, s35\ns_add_u32 s54, s4, 2097152\ns_addc_u32 s55, s5, 0\n"
    "s_add_u32 s56, s4, 10485760\ns_addc_u32 s57, s5, 0\nv_mov_b64 v[252:253], -1\nv_mov_b64 v[254:255], -1\nv_xor_b32 v36, 16, v33\ns_bitcmp0_b32 s13, 0\n"
    "s_cselect_b32 s44, 256, 0\nv_add_u32 v232, s44, v23\nds_read_b64 v[128:129], v23 offset:896\nds_read_b64 v[130:131], v23 offset:904\n"
    "ds_read_b64 v[132:133], v23 offset:864\nds_read_b64 v[134:135], v23 offset:872\nds_read_b64 v[136:137], v23 offset:832\nds_read_b64 v[138:139], v23 offset:840\n"
    "ds_read_b64 v[140:141], v23 offset:800\nds_read_b64 v[142:143], v23 offset:808\nds_read_b64 v[144:145], v23 offset:704\nds_read_b64 v[146:147], v23 offset:712\n"
    "ds_read_b64 v[148:149], v23 offset:672\nds_read_b64 v[150:151], v23 offset:680\nds_read_b64 v[152:153], v23 offset:640\nds_read_b64 v[154:155], v23 offset:648\n"
    "ds_read_b64 v[156:157], v23 offset:608\nds_read_b64 v[158:159], v23 offset:616\nds_read_b64 v[160:161], v232 offset:512\nds_read_b64 v[162:163], v232 offset:520\n"
    "ds_read_b64 v[164:165], v232 offset:480\nds_read_b64 v[166:167], v232 offset:488\nds_read_b64 v[168:169], v232 offset:448\nds_read_b64 v[170:171], v232 offset:456\n"
    "ds_read_b64 v[172:173], v232 offset:416\nds_read_b64 v[174:175], v232 offset:424\nds_read_b64 v[176:177], v232 offset:320\nds_read_b64 v[178:179], v232 offset:328\n"
    "ds_read_b64 v[180:181], v232 offset:288\nds_read_b64 v[182:183], v232 offset:296\nds_read_b64 v[184:185], v232 offset:256\nds_read_b64 v[186:187], v232 offset:264\n"
    "ds_read_b64 v[188:189], v232 offset:224\nds_read_b64 v[190:191], v232 offset:232\n"
    ".Lln79:\ns_add_u32 s44, s25, 0\ns_and_b32 s44, s44, 3\ns_lshl_b32 s44, s44, 13\ns_add_u32 s68, s4, s44\ns_addc_u32 s69, s5, 0\ns_add_u32 s44, s25, 1\n"
    "s_and_b32 s44, s44, 3\ns_lshl_b32 s44, s44, 13\ns_add_u32 s70, s4, s44\ns_addc_u32 s71, s5, 0\ns_add_u32 s44, s25, 2\ns_and_b32 s44, s44, 3\ns_lshl_b32 s44, s44, 13\n"
    "s_add_u32 s72, s4, s44\ns_addc_u32 s73, s5, 0\ns_add_u32 s44, s25, 3\ns_and_b32 s44, s44, 3\ns_lshl_b32 s44, s44, 13\ns_add_u32 s74, s4, s44\ns_addc_u32 s75, s5, 0\n"
    "s_waitcnt vmcnt(0)\ns_sleep 5\ns_cmp_lg_u32 s12, 0\ns_waitcnt lgkmcnt(0)\ns_barrier\nglobal_load_dwordx4 v[240:243], v17, s[4:5] sc1\n"
    "global_load_dwordx4 v[208:211], v16, s[68:69] sc1\nglobal_load_dwordx4 v[212:215], v16, s[70:71] sc1\nglobal_load_dwordx4 v[216:219], v16, s[72:73] sc1\n"
    "global_load_dwordx4 v[220:223], v16, s[74:75] sc1\nv_mov_b64 v[236:237], 0\nv_mov_b64 v[238:239], 0\ns_cbranch_scc1 .Ln115\ns_load_dword s17, s[16:17], 0\n"
    "v_mov_b32 v232, 115200\nds_read_u16 v233, v232 offset:1024\nds_read_u16 v234, v232 offset:2560\nds_read_b32 v235, v232 offset:48032\nds_read_b32 v232, v232 offset:48040\n"
    "s_waitcnt lgkmcnt(0)\nv_fma_mix_f32 v233, v233, -v235, 0 op_sel_hi:[1,0,0]\nv_fma_mix_f32 v234, v234, -v232, 0 op_sel_hi:[1,0,0]\ns_lshl_b32 s16, s17, 16\n"
    "s_and_b32 s17, s17, 0xffff0000\nv_readfirstlane_b32 s14, v235\nv_readfirstlane_b32 s15, v232\nv_readfirstlane_b32 s28, v233\nv_readfirstlane_b32 s29, v234\n"
    "s_lshr_b32 s26, s14, 16\ns_lshr_b32 s27, s15, 16\n.Ln115:\nds_write_b128 v22, v[236:239]\ns_mov_b32 s44, 0x3f80\ns_mov_b32 s46, 0x3e800000\ns_mov_b32 s48, 0x3727c5ac\n"
    "s_mov_b32 s80, 0x10000\n.Ln117:\ns_waitcnt vmcnt(4)\nv_lshlrev_b32 v56, 16, v240\nv_lshlrev_b32 v57, 16, v241\nv_lshlrev_b32 v58, 16, v242\ns_cbranch_execz .Lhop\n"
    "v_lshlrev_b32 v59, 16, v243\nv_and_b32 v60, 0xffff0000, v240\nv_and_b32 v61, 0xffff0000, v241\nv_and_b32 v62, 0xffff0000, v242\nv_and_b32 v63, 0xffff0000, v243\n"
    "v_max3_u32 v232, v240, v241, v242\nv_max_u32 v232, v232, v243\nv_cmp_eq_u32 s[50:51], -1, v232\ns_waitcnt vmcnt(3)\nv_dot2_f32_bf16 v224, v208, s44, 0\n"
    "v_dot2_f32_bf16 v228, v208, v208, 0\nv_dot2_f32_bf16 v225, v209, s44, 0\nv_dot2_f32_bf16 v229, v209, v209, 0\nv_dot2_f32_bf16 v226, v210, s44, 0\n"
    "v_dot2_f32_bf16 v230, v210, v210, 0\nv_dot2_f32_bf16 v227, v211, s44, 0\nv_dot2_f32_bf16 v231, v211, v211, 0\ns_waitcnt vmcnt(2)\nv_dot2_f32_bf16 v224, v212, s44, v224\n"
    "v_dot2_f32_bf16 v228, v212, v212, v228\nv_dot2_f32_bf16 v225, v213, s44, v225\nv_dot2_f32_bf16 v229, v213, v213, v229\nv_dot2_f32_bf16 v226, v214, s44, v226\n"
    "v_dot2_f32_bf16 v230, v214, v214, v230\nv_dot2_f32_bf16 v227, v215, s44, v227\nv_dot2_f32_bf16 v231, v215, v215, v231\ns_waitcnt vmcnt(1)\n"
    "v_dot2_f32_bf16 v224, v216, s44, v224\nv_dot2_f32_bf16 v228, v216, v216, v228\nv_dot2_f32_bf16 v225, v217, s44, v225\nv_dot2_f32_bf16 v229, v217, v217, v229\n"
    "v_dot2_f32_bf16 v226, v218, s44, v226\nv_dot2_f32_bf16 v230, v218, v218, v230\nv_dot2_f32_bf16 v227, v219, s44, v227\nv_dot2_f32_bf16 v231, v219, v219, v231\n"
    "s_waitcnt vmcnt(0)\nv_dot2_f32_bf16 v224, v220, s44, v224\nv_dot2_f32_bf16 v228, v220, v220, v228\nv_dot2_f32_bf16 v225, v221, s44, v225\n"
    "v_dot2_f32_bf16 v229, v221, v221, v229\nv_dot2_f32_bf16 v226, v222, s44, v226\nv_dot2_f32_bf16 v230, v222, v222, v230\nv_dot2_f32_bf16 v227, v223, s44, v227\n"
    "v_dot2_f32_bf16 v231, v223, v223, v231\nv_pk_mul_f32 v[224:225], v[224:225], s[46:47] op_sel_hi:[1,0]\ns_nop 0\n"
    "v_pk_mul_f32 v[226:227], v[226:227], s[46:47] op_sel_hi:[1,0]\nv_cmp_u_f32 vcc, v228, v229\nv_cmp_u_f32 s[52:53], v230, v231\n"
    "v_pk_fma_f32 v[232:233], v[224:225], v[224:225], s[48:49] op_sel_hi:[1,1,0] neg_lo:[0,0,1] neg_hi:[0,0,1]\n"
    "v_pk_fma_f32 v[234:235], v[226:227], v[226:227], s[48:49] op_sel_hi:[1,1,0] neg_lo:[0,0,1] neg_hi:[0,0,1]\n"
    "v_pk_fma_f32 v[228:229], v[228:229], s[46:47], v[232:233] op_sel_hi:[1,0,1] neg_lo:[0,0,1] neg_hi:[0,0,1]\n"
    "v_pk_fma_f32 v[230:231], v[230:231], s[46:47], v[234:235] op_sel_hi:[1,0,1] neg_lo:[0,0,1] neg_hi:[0,0,1]\nv_max_f32 v228, s48, v228\nv_max_f32 v229, s48, v229\n"
    "v_max_f32 v230, s48, v230\nv_max_f32 v231, s48, v231\nv_rsq_f32 v228, v228\nv_rsq_f32 v229, v229\nv_rsq_f32 v230, v230\nv_rsq_f32 v231, v231\n"
    "v_pk_add_f32 v[244:245], v[56:57], v[224:225] neg_lo:[0,1] neg_hi:[0,1]\nv_pk_add_f32 v[246:247], v[58:59], v[226:227] neg_lo:[0,1] neg_hi:[0,1]\n"
    "v_pk_add_f32 v[248:249], v[60:61], v[224:225] neg_lo:[0,1] neg_hi:[0,1]\nv_pk_add_f32 v[250:251], v[62:63], v[226:227] neg_lo:[0,1] neg_hi:[0,1]\n"
    "s_or_b64 vcc, vcc, s[50:51]\nv_pk_mul_f32 v[244:245], v[244:245], v[228:229]\nv_pk_mul_f32 v[246:247], v[246:247], v[230:231]\n"
    "v_pk_mul_f32 v[248:249], v[248:249], v[228:229]\nv_pk_mul_f32 v[250:251], v[250:251], v[230:231]\ns_or_b64 vcc, vcc, s[52:53]\nv_cvt_pk_f16_f32 v232, v244, v245\n"
    "v_cvt_pk_f16_f32 v233, v246, v247\nv_cvt_pk_f16_f32 v234, v248, v249\nds_write_b64 v18, v[232:233]\nv_cvt_pk_f16_f32 v235, v250, v251\n"
    "ds_write_b64 v18, v[234:235] offset:6480\ns_cbranch_scc1 .Llw120\n.Llx121:\ns_cmp_gt_u32 s3, 3\ns_cbranch_scc1 .Lhcv118\n"
    "s_waitcnt lgkmcnt(0)\ns_barrier\nds_read_b128 v[220:223], v27 offset:1392\nds_read_b128 v[224:227], v27 offset:1360\nds_read_b128 v[228:231], v27 offset:1312\n"
    "v_fmac_f32 v56, s16, v244\nv_fmac_f32 v57, s16, v245\nv_fmac_f32 v58, s16, v246\nv_fmac_f32 v59, s16, v247\nv_fmac_f32 v60, s17, v248\nv_fmac_f32 v61, s17, v249\n"
    "v_fmac_f32 v62, s17, v250\nv_fmac_f32 v63, s17, v251\ns_waitcnt lgkmcnt(2)\nv_mfma_f32_32x32x16_f16 v[204:219], v[64:67], v[220:223], 0\n"
    "ds_read_b128 v[232:235], v27 offset:1280\nds_read_b128 v[236:239], v27 offset:1232\ns_waitcnt lgkmcnt(3)\n"
    "v_mfma_f32_32x32x16_f16 v[204:219], v[68:71], v[224:227], v[204:219]\nds_read_b128 v[240:243], v27 offset:1200\nds_read_b128 v[220:223], v27 offset:1152\n"
    "s_waitcnt lgkmcnt(4)\nv_mfma_f32_32x32x16_f16 v[204:219], v[72:75], v[228:231], v[204:219]\nds_read_b128 v[224:227], v27 offset:1120\n"
    "ds_read_b128 v[228:231], v27 offset:1072\ns_waitcnt lgkmcnt(5)\nv_mfma_f32_32x32x16_f16 v[204:219], v[76:79], v[232:235], v[204:219]\n"
    "ds_read_b128 v[232:235], v27 offset:1040\ns_waitcnt lgkmcnt(5)\nv_mfma_f32_32x32x16_f16 v[204:219], v[80:83], v[236:239], v[204:219]\n"
    "ds_read_b128 v[236:239], v27 offset:992\ns_waitcnt lgkmcnt(5)\nv_mfma_f32_32x32x16_f16 v[204:219], v[84:87], v[240:243], v[204:219]\n"
    "ds_read_b128 v[240:243], v27 offset:960\ns_waitcnt lgkmcnt(5)\nv_mfma_f32_32x32x16_f16 v[204:219], v[88:91], v[220:223], v[204:219]\n"
    "ds_read_b128 v[220:223], v27 offset:912\ns_waitcnt lgkmcnt(5)\nv_mfma_f32_32x32x16_f16 v[204:219], v[92:95], v[224:227], v[204:219]\n"
    "ds_read_b128 v[224:227], v27 offset:880\ns_waitcnt lgkmcnt(5)\nv_mfma_f32_32x32x16_f16 v[204:219], v[96:99], v[228:231], v[204:219]\n"
    "ds_read_b128 v[228:231], v27 offset:832\ns_waitcnt lgkmcnt(5)\nv_mfma_f32_32x32x16_f16 v[204:219], v[100:103], v[232:235], v[204:219]\n"
    "ds_read_b128 v[232:235], v27 offset:800\ns_waitcnt lgkmcnt(5)\nv_mfma_f32_32x32x16_f16 v[204:219], v[104:107], v[236:239], v[204:219]\n"
    "ds_read_b128 v[236:239], v27 offset:752\ns_waitcnt lgkmcnt(5)\nv_mfma_f32_32x32x16_f16 v[204:219], v[108:111], v[240:243], v[204:219]\n"
    "ds_read_b128 v[240:243], v27 offset:720\ns_waitcnt lgkmcnt(5)\nv_mfma_f32_32x32x16_f16 v[204:219], v[112:115], v[220:223], v[204:219]\n"
    "ds_read_b128 v[220:223], v27 offset:672\ns_waitcnt lgkmcnt(5)\nv_mfma_f32_32x32x16_f16 v[204:219], v[116:119], v[224:227], v[204:219]\n"
    "ds_read_b128 v[224:227], v27 offset:640\ns_waitcnt lgkmcnt(5)\nv_mfma_f32_32x32x16_f16 v[204:219], v[120:123], v[228:231], v[204:219]\n"
    "ds_read_b128 v[228:231], v27 offset:592\ns_waitcnt lgkmcnt(5)\nv_mfma_f32_32x32x16_f16 v[204:219], v[124:127], v[232:235], v[204:219]\n"
    "ds_read_b128 v[232:235], v27 offset:560\ns_waitcnt lgkmcnt(5)\nv_mfma_f32_32x32x16_f16 v[204:219], v[128:131], v[236:239], v[204:219]\n"
    "ds_read_b128 v[236:239], v27 offset:512\ns_waitcnt lgkmcnt(5)\nv_mfma_f32_32x32x16_f16 v[204:219], v[132:135], v[240:243], v[204:219]\n"
    "ds_read_b128 v[240:243], v27 offset:480\ns_waitcnt lgkmcnt(5)\nv_mfma_f32_32x32x16_f16 v[204:219], v[136:139], v[220:223], v[204:219]\ns_nop 0\ns_cbranch_execz .Lhop\n"
    "ds_read_b128 v[220:223], v27 offset:432\ns_waitcnt lgkmcnt(5)\nv_mfma_f32_32x32x16_f16 v[204:219], v[140:143], v[224:227], v[204:219]\n"
    "ds_read_b128 v[224:227], v27 offset:400\ns_waitcnt lgkmcnt(5)\nv_mfma_f32_32x32x16_f16 v[204:219], v[144:147], v[228:231], v[204:219]\n"
    "ds_read_b128 v[228:231], v27 offset:352\ns_waitcnt lgkmcnt(5)\nv_mfma_f32_32x32x16_f16 v[204:219], v[148:151], v[232:235], v[204:219]\n"
    "ds_read_b128 v[232:235], v27 offset:320\ns_waitcnt lgkmcnt(5)\nv_mfma_f32_32x32x16_f16 v[204:219], v[152:155], v[236:239], v[204:219]\n"
    "ds_read_b128 v[236:239], v27 offset:272\ns_waitcnt lgkmcnt(5)\nv_mfma_f32_32x32x16_f16 v[204:219], v[156:159], v[240:243], v[204:219]\n"
    "ds_read_b128 v[240:243], v27 offset:240\ns_waitcnt lgkmcnt(5)\nv_mfma_f32_32x32x16_f16 v[204:219], v[160:163], v[220:223], v[204:219]\n"
    "ds_read_b128 v[220:223], v27 offset:192\ns_waitcnt lgkmcnt(5)\nv_mfma_f32_32x32x16_f16 v[204:219], v[164:167], v[224:227], v[204:219]\n"
    "ds_read_b128 v[224:227], v27 offset:160\ns_waitcnt lgkmcnt(5)\nv_mfma_f32_32x32x16_f16 v[204:219], v[168:171], v[228:231], v[204:219]\n"
    "ds_read_b128 v[228:231], v27 offset:112\ns_waitcnt lgkmcnt(5)\nv_mfma_f32_32x32x16_f16 v[204:219], v[172:175], v[232:235], v[204:219]\n"
    "ds_read_b128 v[232:235], v27 offset:80\ns_waitcnt lgkmcnt(5)\nv_mfma_f32_32x32x16_f16 v[204:219], v[176:179], v[236:239], v[204:219]\n"
    "ds_read_b128 v[236:239], v27 offset:32\ns_waitcnt lgkmcnt(5)\nv_mfma_f32_32x32x16_f16 v[204:219], v[180:183], v[240:243], v[204:219]\ns_waitcnt lgkmcnt(4)\n"
    "v_mfma_f32_32x32x16_f16 v[204:219], v[184:187], v[220:223], v[204:219]\ns_waitcnt lgkmcnt(3)\nv_mfma_f32_32x32x16_f16 v[204:219], v[188:191], v[224:227], v[204:219]\n"
    "s_waitcnt lgkmcnt(2)\nv_mfma_f32_32x32x16_f16 v[204:219], v[192:195], v[228:231], v[204:219]\ns_waitcnt lgkmcnt(1)\n"
    "v_mfma_f32_32x32x16_f16 v[204:219], v[196:199], v[232:235], v[204:219]\ns_waitcnt lgkmcnt(0)\nv_mfma_f32_32x32x16_f16 v[204:219], v[200:203], v[236:239], v[204:219]\n"
    "s_cmp_lt_u32 s3, 2\ns_nop 10\nv_cvt_pk_bf16_f32 v204, v204, v205\nv_cvt_pk_bf16_f32 v205, v206, v207\nds_write_b64 v28, v[204:205] offset:0\n"
    "v_cvt_pk_bf16_f32 v208, v208, v209\nv_cvt_pk_bf16_f32 v209, v210, v211\nds_write_b64 v29, v[208:209] offset:0\nv_cvt_pk_bf16_f32 v212, v212, v213\n"
    "v_cvt_pk_bf16_f32 v213, v214, v215\nds_write_b64 v30, v[212:213] offset:0\nv_cvt_pk_bf16_f32 v216, v216, v217\nv_cvt_pk_bf16_f32 v217, v218, v219\n"
    "ds_write_b64 v31, v[216:217] offset:0\ns_waitcnt lgkmcnt(0)\ns_barrier\nds_read_b64 v[208:209], v19\nds_read_b64 v[210:211], v19 offset:10240\ns_cbranch_scc1 .Le122\n"
    "ds_read_b64 v[212:213], v20\nds_read_b64 v[214:215], v20 offset:10240\ns_waitcnt lgkmcnt(0)\nv_dot2c_f32_bf16 v56, s26, v212\nv_dot2c_f32_bf16 v57, s14, v212\n"
    "v_dot2c_f32_bf16 v58, s26, v213\nv_dot2c_f32_bf16 v59, s14, v213\nv_dot2c_f32_bf16 v60, s27, v214\nv_dot2c_f32_bf16 v61, s15, v214\nv_dot2c_f32_bf16 v62, s27, v215\n"
    "v_dot2c_f32_bf16 v63, s15, v215\n.Le122:\ns_waitcnt lgkmcnt(0)\nv_dot2c_f32_bf16 v56, s26, v208\nv_dot2c_f32_bf16 v57, s14, v208\nv_dot2c_f32_bf16 v58, s26, v209\n"
    "v_dot2c_f32_bf16 v59, s14, v209\nv_dot2c_f32_bf16 v60, s27, v210\nv_dot2c_f32_bf16 v61, s15, v210\nv_dot2c_f32_bf16 v62, s27, v211\nv_dot2c_f32_bf16 v63, s15, v211\n"
    "v_cvt_pk_bf16_f32 v40, v56, v60\nv_cvt_pk_bf16_f32 v41, v57, v61\nv_cvt_pk_bf16_f32 v42, v58, v62\nv_cvt_pk_bf16_f32 v43, v59, v63\n"
    "global_store_dwordx4 v21, v[40:43], s[4:5] sc1\ns_cmp_eq_u32 s12, 0\ns_cbranch_scc1 .Lqg124\n"
    ".Lfin123:\ns_lshl_b32 s43, s18, 13\ns_add_u32 s42, s43, 0x1200000\nv_lshl_add_u32 v16, v0, 4, s42\nv_lshrrev_b32 v239, 3, v0\nv_and_b32 v238, 7, v0\n"
    "s_lshl_b32 s41, s25, 16\ns_lshl_b32 s40, s24, 7\ns_add_u32 s39, s41, s40\nv_lshl_add_u32 v17, v239, 10, s39\nv_lshl_add_u32 v17, v238, 4, v17\ns_lshl_b32 s38, s3, 4\n"
    "v_lshl_add_u32 v18, v0, 4, s38\nv_mul_u32_u24 v237, 0x410, v238\nv_lshl_add_u32 v19, v239, 2, v237\ns_mov_b32 s80, 0x10000\ns_waitcnt vmcnt(0)\ns_sleep 4\n.Lf126:\n"
    "global_load_dwordx4 v[44:47], v16, s[4:5] sc1\ns_waitcnt vmcnt(0)\nds_write_b128 v18, v[44:47]\nv_max3_u32 v48, v44, v45, v46\nv_max_u32 v48, v48, v47\n"
    "v_cmp_eq_u32 vcc, -1, v48\ns_waitcnt lgkmcnt(0)\ns_cbranch_vccnz .Lf127\n.Lf127x:\ns_barrier\nds_read2st64_b32 v[44:45], v19 offset1:1\n"
    "ds_read2st64_b32 v[46:47], v19 offset0:2 offset1:3\ns_waitcnt lgkmcnt(0)\nglobal_store_dwordx4 v17, v[44:47], s[6:7] sc1\ns_endpgm\n.Lf127:\ns_sub_u32 s80, s80, 1\n"
    "s_cbranch_scc0 .Lf126\ns_branch .Lf127x\n"
    ".Lhcv118:\ns_setprio 0\ns_bitcmp1_b32 s13, 0\ns_cbranch_scc1 .Lk129\ns_cmp_lg_u32 s12, 0\ns_waitcnt lgkmcnt(0)\ns_barrier\nds_read_b64 v[192:193], v11 offset:0\n"
    "ds_read_b128 v[194:197], v10 offset:0\nds_read_b64 v[198:199], v12 offset:0\nds_read_b64 v[200:201], v11 offset:1280\nds_read_b128 v[202:205], v10 offset:1280\n"
    "ds_read_b64 v[206:207], v12 offset:1280\nds_read_b64 v[208:209], v11 offset:2560\nds_read_b128 v[210:213], v10 offset:2560\nds_read_b64 v[214:215], v12 offset:2560\n"
    "s_cbranch_scc1 .Lz132\nglobal_store_dwordx4 v37, v[252:255], s[4:5] offset:0 sc1\nglobal_store_dwordx4 v37, v[252:255], s[4:5] offset:1024 sc1\n"
    "global_store_dwordx4 v37, v[252:255], s[54:55] offset:0 sc1\nglobal_store_dwordx4 v37, v[252:255], s[54:55] offset:1024 sc1\n"
    "global_store_dwordx4 v37, v[252:255], s[56:57] offset:0 sc1\nglobal_store_dwordx4 v37, v[252:255], s[56:57] offset:1024 sc1\ns_cmp_lg_u32 s13, 2\ns_cbranch_scc1 .Lz132\n"
    "global_store_dwordx4 v38, v[252:255], s[4:5] sc1\n.Lz132:\ns_waitcnt lgkmcnt(6)\nv_alignbit_b32 v216, v194, v193, 16\nv_alignbit_b32 v217, v195, v194, 16\n"
    "v_alignbit_b32 v218, v196, v195, 16\nv_alignbit_b32 v219, v197, v196, 16\nv_alignbit_b32 v220, v198, v197, 16\nv_pk_add_f16 v224, v193, v216\n"
    "v_pk_add_f16 v225, v194, v217\nv_pk_add_f16 v226, v195, v218\nv_pk_add_f16 v227, v196, v219\nv_pk_add_f16 v228, v197, v220\nv_pk_add_f16 v236, v216, v225\n"
    "v_pk_add_f16 v237, v217, v226\nv_pk_add_f16 v238, v218, v227\nv_pk_add_f16 v239, v219, v228\ns_cbranch_execz .Lhop\nds_write_b64 v13, v[236:237] offset:4040\n"
    "v_pk_add_f16 v232, v224, v195\nv_pk_add_f16 v233, v225, v196\nv_pk_add_f16 v234, v226, v197\nv_pk_add_f16 v235, v227, v198\nds_write_b64 v14, v[238:239] offset:4032\n"
    "v_pk_add_f16 v232, v232, v225\nv_pk_add_f16 v233, v233, v226\nv_pk_add_f16 v234, v234, v227\nv_pk_add_f16 v235, v235, v228\nds_write_b128 v13, v[232:235] offset:0\n"
    "s_mov_b64 exec, 1\nds_write_b32 v25, v26 offset:48\ns_mov_b64 exec, -1\ns_waitcnt lgkmcnt(6)\nv_alignbit_b32 v216, v202, v201, 16\nv_alignbit_b32 v217, v203, v202, 16\n"
    "v_alignbit_b32 v218, v204, v203, 16\nv_alignbit_b32 v219, v205, v204, 16\nv_alignbit_b32 v220, v206, v205, 16\nv_pk_add_f16 v224, v201, v216\n"
    "v_pk_add_f16 v225, v202, v217\nv_pk_add_f16 v226, v203, v218\nv_pk_add_f16 v227, v204, v219\nv_pk_add_f16 v228, v205, v220\nv_pk_add_f16 v236, v216, v225\n"
    "v_pk_add_f16 v237, v217, v226\nv_pk_add_f16 v238, v218, v227\nv_pk_add_f16 v239, v219, v228\nds_write_b64 v13, v[236:237] offset:5192\nv_pk_add_f16 v232, v224, v203\n"
    "v_pk_add_f16 v233, v225, v204\nv_pk_add_f16 v234, v226, v205\nv_pk_add_f16 v235, v227, v206\nds_write_b64 v14, v[238:239] offset:5184\nv_pk_add_f16 v232, v232, v225\n"
    "v_pk_add_f16 v233, v233, v226\nv_pk_add_f16 v234, v234, v227\nv_pk_add_f16 v235, v235, v228\nds_write_b128 v13, v[232:235] offset:1152\ns_waitcnt lgkmcnt(6)\n"
    "v_alignbit_b32 v216, v210, v209, 16\nv_alignbit_b32 v217, v211, v210, 16\nv_alignbit_b32 v218, v212, v211, 16\nv_alignbit_b32 v219, v213, v212, 16\n"
    "v_alignbit_b32 v220, v214, v213, 16\nv_pk_add_f16 v224, v209, v216\nv_pk_add_f16 v225, v210, v217\nv_pk_add_f16 v226, v211, v218\nv_pk_add_f16 v227, v212, v219\n"
    "v_pk_add_f16 v228, v213, v220\nv_pk_add_f16 v236, v216, v225\nv_pk_add_f16 v237, v217, v226\nv_pk_add_f16 v238, v218, v227\nv_pk_add_f16 v239, v219, v228\n"
    "ds_write_b64 v15, v[236:237] offset:6344\nv_pk_add_f16 v232, v224, v211\nv_pk_add_f16 v233, v225, v212\nv_pk_add_f16 v234, v226, v213\nv_pk_add_f16 v235, v227, v214\n"
    "ds_write_b64 v35, v[238:239] offset:6336\nv_pk_add_f16 v232, v232, v225\nv_pk_add_f16 v233, v233, v226\nv_pk_add_f16 v234, v234, v227\nv_pk_add_f16 v235, v235, v228\n"
    "ds_write_b128 v15, v[232:235] offset:2304\ns_setprio 0\nds_read_b64_tr_b16 v[40:41], v27 offset:1984\nds_read_b64_tr_b16 v[42:43], v27 offset:2048\n"
    "ds_read_b64_tr_b16 v[44:45], v27 offset:1728\nds_read_b64_tr_b16 v[46:47], v27 offset:1792\nds_read_b64_tr_b16 v[48:49], v27 offset:1408\n"
    "ds_read_b64_tr_b16 v[50:51], v27 offset:1472\nds_read_b64_tr_b16 v[52:53], v27 offset:1152\nds_read_b64_tr_b16 v[54:55], v27 offset:1216\n"
    "ds_read_b64_tr_b16 v[240:241], v27 offset:832\nds_read_b64_tr_b16 v[242:243], v27 offset:896\ns_waitcnt lgkmcnt(8)\n"
    "v_mfma_f32_32x32x16_f16 v[192:207], v[40:43], v[128:131], 0\nds_read_b64_tr_b16 v[40:41], v27 offset:576\nds_read_b64_tr_b16 v[42:43], v27 offset:640\n"
    "s_waitcnt lgkmcnt(8)\nv_mfma_f32_32x32x16_f16 v[192:207], v[44:47], v[132:135], v[192:207]\nds_read_b64_tr_b16 v[44:45], v27 offset:6016\n"
    "ds_read_b64_tr_b16 v[46:47], v27 offset:6080\nv_fmac_f32 v56, s16, v244\nv_fmac_f32 v57, s16, v245\ns_waitcnt lgkmcnt(8)\n"
    "v_mfma_f32_32x32x16_f16 v[192:207], v[48:51], v[136:139], v[192:207]\nds_read_b64_tr_b16 v[48:49], v27 offset:5760\nds_read_b64_tr_b16 v[50:51], v27 offset:5824\n"
    "v_fmac_f32 v58, s16, v246\nv_fmac_f32 v59, s16, v247\ns_waitcnt lgkmcnt(8)\nv_mfma_f32_32x32x16_f16 v[192:207], v[52:55], v[140:143], v[192:207]\n"
    "ds_read_b64_tr_b16 v[52:53], v27 offset:5440\nds_read_b64_tr_b16 v[54:55], v27 offset:5504\nv_fmac_f32 v60, s17, v248\nv_fmac_f32 v61, s17, v249\ns_waitcnt lgkmcnt(8)\n"
    "v_mfma_f32_32x32x16_f16 v[192:207], v[240:243], v[160:163], v[192:207]\nds_read_b64_tr_b16 v[240:241], v27 offset:5184\nds_read_b64_tr_b16 v[242:243], v27 offset:5248\n"
    "v_fmac_f32 v62, s17, v250\nv_fmac_f32 v63, s17, v251\ns_waitcnt lgkmcnt(8)\nv_mfma_f32_32x32x16_f16 v[192:207], v[40:43], v[164:167], v[192:207]\n"
    "ds_read_b64_tr_b16 v[40:41], v27 offset:4864\nds_read_b64_tr_b16 v[42:43], v27 offset:4928\ns_waitcnt lgkmcnt(8)\n"
    "v_mfma_f32_32x32x16_f16 v[192:207], v[44:47], v[144:147], v[192:207]\nds_read_b64_tr_b16 v[44:45], v27 offset:4608\nds_read_b64_tr_b16 v[46:47], v27 offset:4672\n"
    "s_waitcnt lgkmcnt(8)\nv_mfma_f32_32x32x16_f16 v[192:207], v[48:51], v[148:151], v[192:207]\nds_read_b32 v244, v30\nds_read_b64 v[246:247], v31\n"
    "ds_read_b32 v245, v30 offset:6480\nds_read_b64 v[248:249], v31 offset:6480\ns_waitcnt lgkmcnt(10)\nv_mfma_f32_32x32x16_f16 v[192:207], v[52:55], v[152:155], v[192:207]\n"
    "s_waitcnt lgkmcnt(8)\nv_mfma_f32_32x32x16_f16 v[192:207], v[240:243], v[156:159], v[192:207]\ns_waitcnt lgkmcnt(6)\n"
    "v_mfma_f32_32x32x16_f16 v[192:207], v[40:43], v[176:179], v[192:207]\ns_waitcnt lgkmcnt(0)\nv_fma_mix_f32 v56, v244, s28, v56 op_sel:[1,0,0] op_sel_hi:[1,0,0]\n"
    "v_fma_mix_f32 v57, v246, s28, v57 op_sel:[0,0,0] op_sel_hi:[1,0,0]\nv_fma_mix_f32 v58, v246, s28, v58 op_sel:[1,0,0] op_sel_hi:[1,0,0]\ns_waitcnt lgkmcnt(4)\n"
    "v_mfma_f32_32x32x16_f16 v[192:207], v[44:47], v[180:183], v[192:207]\nv_fma_mix_f32 v59, v247, s28, v59 op_sel:[0,0,0] op_sel_hi:[1,0,0]\n"
    "v_fma_mix_f32 v60, v245, s29, v60 op_sel:[1,0,0] op_sel_hi:[1,0,0]\nv_fma_mix_f32 v61, v248, s29, v61 op_sel:[0,0,0] op_sel_hi:[1,0,0]\n"
    "v_fma_mix_f32 v62, v248, s29, v62 op_sel:[1,0,0] op_sel_hi:[1,0,0]\nv_fma_mix_f32 v63, v249, s29, v63 op_sel:[0,0,0] op_sel_hi:[1,0,0]\ns_nop 6\n"
    "v_cvt_pk_bf16_f32 v192, v192, v193\nv_cvt_pk_bf16_f32 v193, v194, v195\nds_write_b64 v32, v[192:193] offset:1024\nv_cvt_pk_bf16_f32 v196, v196, v197\n"
    "s_cbranch_execz .Lhop\nv_cvt_pk_bf16_f32 v197, v198, v199\nds_write_b64 v32, v[196:197] offset:1536\nv_cvt_pk_bf16_f32 v200, v200, v201\n"
    "v_cvt_pk_bf16_f32 v201, v202, v203\nds_write_b64 v32, v[200:201] offset:2048\nv_cvt_pk_bf16_f32 v204, v204, v205\nv_cvt_pk_bf16_f32 v205, v206, v207\n"
    "ds_write_b64 v32, v[204:205] offset:2560\ns_branch .Lk130\n.Lk129:\ns_cmp_lg_u32 s12, 0\ns_waitcnt lgkmcnt(0)\ns_barrier\nds_read_b64 v[192:193], v11 offset:0\n"
    "ds_read_b128 v[194:197], v10 offset:0\nds_read_b64 v[198:199], v12 offset:0\nds_read_b64 v[200:201], v11 offset:1280\nds_read_b128 v[202:205], v10 offset:1280\n"
    "ds_read_b64 v[206:207], v12 offset:1280\ns_cbranch_scc1 .Lz133\nglobal_store_dwordx4 v37, v[252:255], s[4:5] offset:0 sc1\n"
    "global_store_dwordx4 v37, v[252:255], s[4:5] offset:1024 sc1\nglobal_store_dwordx4 v37, v[252:255], s[54:55] offset:0 sc1\n"
    "global_store_dwordx4 v37, v[252:255], s[54:55] offset:1024 sc1\nglobal_store_dwordx4 v37, v[252:255], s[56:57] offset:0 sc1\n"
    "global_store_dwordx4 v37, v[252:255], s[56:57] offset:1024 sc1\ns_cmp_lg_u32 s13, 2\ns_cbranch_scc1 .Lz133\nglobal_store_dwordx4 v38, v[252:255], s[4:5] sc1\n.Lz133:\n"
    "s_waitcnt lgkmcnt(3)\nv_alignbit_b32 v216, v193, v192, 16\nv_alignbit_b32 v217, v194, v193, 16\nv_alignbit_b32 v218, v195, v194, 16\nv_alignbit_b32 v219, v196, v195, 16\n"
    "v_alignbit_b32 v220, v197, v196, 16\nv_alignbit_b32 v221, v198, v197, 16\nv_alignbit_b32 v222, v199, v198, 16\nv_pk_add_f16 v224, v192, v216\n"
    "v_pk_add_f16 v225, v193, v217\nv_pk_add_f16 v226, v194, v218\nv_pk_add_f16 v227, v195, v219\nv_pk_add_f16 v228, v196, v220\nv_pk_add_f16 v229, v197, v221\n"
    "v_pk_add_f16 v230, v198, v222\nv_pk_add_f16 v223, v226, v227\nv_pk_add_f16 v231, v228, v229\nv_pk_add_f16 v232, v224, v196\nv_pk_add_f16 v233, v225, v197\n"
    "v_pk_add_f16 v234, v226, v198\nv_pk_add_f16 v235, v227, v199\nv_pk_add_f16 v208, v225, v223\nv_pk_add_f16 v209, v223, v228\nv_pk_add_f16 v210, v227, v231\n"
    "v_pk_add_f16 v211, v231, v230\nv_pk_add_f16 v236, v208, v216\nv_pk_add_f16 v237, v209, v217\nv_pk_add_f16 v238, v210, v218\nv_pk_add_f16 v239, v211, v219\n"
    "ds_write_b128 v14, v[236:239] offset:0\nv_pk_add_f16 v232, v232, v208\nv_pk_add_f16 v233, v233, v209\nv_pk_add_f16 v234, v234, v210\nv_pk_add_f16 v235, v235, v211\n"
    "ds_write_b128 v13, v[232:235] offset:0\ns_waitcnt lgkmcnt(2)\nv_alignbit_b32 v216, v201, v200, 16\nv_alignbit_b32 v217, v202, v201, 16\n"
    "v_alignbit_b32 v218, v203, v202, 16\nv_alignbit_b32 v219, v204, v203, 16\nv_alignbit_b32 v220, v205, v204, 16\nv_alignbit_b32 v221, v206, v205, 16\n"
    "v_alignbit_b32 v222, v207, v206, 16\nv_pk_add_f16 v224, v200, v216\nv_pk_add_f16 v225, v201, v217\nv_pk_add_f16 v226, v202, v218\nv_pk_add_f16 v227, v203, v219\n"
    "v_pk_add_f16 v228, v204, v220\nv_pk_add_f16 v229, v205, v221\nv_pk_add_f16 v230, v206, v222\nv_pk_add_f16 v223, v226, v227\nv_pk_add_f16 v231, v228, v229\n"
    "v_pk_add_f16 v232, v224, v204\nv_pk_add_f16 v233, v225, v205\nv_pk_add_f16 v234, v226, v206\nv_pk_add_f16 v235, v227, v207\nv_pk_add_f16 v208, v225, v223\n"
    "v_pk_add_f16 v209, v223, v228\nv_pk_add_f16 v210, v227, v231\nv_pk_add_f16 v211, v231, v230\nv_pk_add_f16 v236, v208, v216\nv_pk_add_f16 v237, v209, v217\n"
    "v_pk_add_f16 v238, v210, v218\nv_pk_add_f16 v239, v211, v219\nds_write_b128 v14, v[236:239] offset:1152\nv_pk_add_f16 v232, v232, v208\nv_pk_add_f16 v233, v233, v209\n"
    "v_pk_add_f16 v234, v234, v210\nv_pk_add_f16 v235, v235, v211\nds_write_b128 v15, v[232:235] offset:1152\nv_fmac_f32 v56, s16, v244\nv_fmac_f32 v57, s16, v245\n"
    "v_fmac_f32 v58, s16, v246\nv_fmac_f32 v59, s16, v247\nv_fmac_f32 v60, s17, v248\nv_fmac_f32 v61, s17, v249\nv_fmac_f32 v62, s17, v250\nv_fmac_f32 v63, s17, v251\n"
    "ds_read_b32 v244, v30\nds_read_b64 v[246:247], v31\nds_read_b32 v245, v30 offset:6480\nds_read_b64 v[248:249], v31 offset:6480\ns_setprio 0\nds_read_b32 v39, v24\n"
    "ds_read_b64_tr_b16 v[40:41], v28 offset:512\nds_read_b64_tr_b16 v[42:43], v28 offset:640\nds_read_b64_tr_b16 v[44:45], v28 offset:0\n"
    "ds_read_b64_tr_b16 v[46:47], v28 offset:128\nds_read_b64_tr_b16 v[48:49], v29 offset:512\nds_read_b64_tr_b16 v[50:51], v29 offset:640\n"
    "ds_read_b64_tr_b16 v[52:53], v29 offset:0\nds_read_b64_tr_b16 v[54:55], v29 offset:128\nds_read_b64_tr_b16 v[240:241], v28 offset:2816\n"
    "ds_read_b64_tr_b16 v[242:243], v28 offset:2944\ns_waitcnt lgkmcnt(8)\nv_mfma_f32_32x32x16_f16 v[224:239], v[40:43], v[160:163], 0\nv_cmp_gt_i32 vcc, v26, v39\n"
    "ds_read_b64_tr_b16 v[40:41], v28 offset:2304\nds_read_b64_tr_b16 v[42:43], v28 offset:2432\ns_waitcnt lgkmcnt(8)\n"
    "v_mfma_f32_32x32x16_f16 v[224:239], v[44:47], v[164:167], v[224:239]\nds_read_b64_tr_b16 v[44:45], v29 offset:2816\nds_read_b64_tr_b16 v[46:47], v29 offset:2944\n"
    "s_waitcnt lgkmcnt(8)\nv_mfma_f32_32x32x16_f16 v[208:223], v[48:51], v[168:171], 0\nds_read_b64_tr_b16 v[48:49], v29 offset:2304\n"
    "ds_read_b64_tr_b16 v[50:51], v29 offset:2432\ns_waitcnt lgkmcnt(8)\nv_mfma_f32_32x32x16_f16 v[208:223], v[52:55], v[172:175], v[208:223]\ns_cbranch_vccnz .Lw134\n"
    ".Lr135:\nds_read_b64_tr_b16 v[52:53], v27 offset:832\ns_nop 0\ns_cbranch_execz .Lhop\nds_read_b64_tr_b16 v[54:55], v27 offset:896\ns_waitcnt lgkmcnt(8)\n"
    "v_mfma_f32_32x32x16_f16 v[224:239], v[240:243], v[176:179], v[224:239]\nds_read_b64_tr_b16 v[240:241], v27 offset:576\nds_read_b64_tr_b16 v[242:243], v27 offset:640\n"
    "v_fma_mix_f32 v56, v244, s28, v56 op_sel:[1,0,0] op_sel_hi:[1,0,0]\nv_fma_mix_f32 v57, v246, s28, v57 op_sel:[0,0,0] op_sel_hi:[1,0,0]\ns_waitcnt lgkmcnt(8)\n"
    "v_mfma_f32_32x32x16_f16 v[224:239], v[40:43], v[180:183], v[224:239]\nds_read_b64_tr_b16 v[40:41], v27 offset:256\nds_read_b64_tr_b16 v[42:43], v27 offset:320\n"
    "v_fma_mix_f32 v58, v246, s28, v58 op_sel:[1,0,0] op_sel_hi:[1,0,0]\nv_fma_mix_f32 v59, v247, s28, v59 op_sel:[0,0,0] op_sel_hi:[1,0,0]\ns_waitcnt lgkmcnt(8)\n"
    "v_mfma_f32_32x32x16_f16 v[208:223], v[44:47], v[184:187], v[208:223]\nds_read_b64_tr_b16 v[44:45], v27 offset:0\nds_read_b64_tr_b16 v[46:47], v27 offset:64\n"
    "v_fma_mix_f32 v60, v245, s29, v60 op_sel:[1,0,0] op_sel_hi:[1,0,0]\nv_fma_mix_f32 v61, v248, s29, v61 op_sel:[0,0,0] op_sel_hi:[1,0,0]\ns_waitcnt lgkmcnt(8)\n"
    "v_mfma_f32_32x32x16_f16 v[208:223], v[48:51], v[188:191], v[208:223]\nds_read_b64_tr_b16 v[48:49], v27 offset:4864\nds_read_b64_tr_b16 v[50:51], v27 offset:4928\n"
    "v_fma_mix_f32 v62, v248, s29, v62 op_sel:[1,0,0] op_sel_hi:[1,0,0]\nv_fma_mix_f32 v63, v249, s29, v63 op_sel:[0,0,0] op_sel_hi:[1,0,0]\ns_waitcnt lgkmcnt(8)\n"
    "v_mfma_f32_32x32x16_f16 v[192:207], v[52:55], v[128:131], 0\nds_read_b64_tr_b16 v[52:53], v27 offset:4608\nds_read_b64_tr_b16 v[54:55], v27 offset:4672\n"
    "s_waitcnt lgkmcnt(8)\nv_mfma_f32_32x32x16_f16 v[192:207], v[240:243], v[132:135], v[192:207]\nds_read_b64_tr_b16 v[240:241], v27 offset:4288\n"
    "ds_read_b64_tr_b16 v[242:243], v27 offset:4352\nv_cvt_pk_bf16_f32 v224, v224, v225\nv_cvt_pk_bf16_f32 v225, v226, v227\nds_write_b64 v33, v[224:225] offset:0\n"
    "s_waitcnt lgkmcnt(9)\nv_mfma_f32_32x32x16_f16 v[192:207], v[40:43], v[136:139], v[192:207]\nds_read_b64_tr_b16 v[40:41], v27 offset:4032\n"
    "ds_read_b64_tr_b16 v[42:43], v27 offset:4096\nv_cvt_pk_bf16_f32 v228, v228, v229\nv_cvt_pk_bf16_f32 v229, v230, v231\nds_write_b64 v36, v[228:229] offset:0\n"
    "s_waitcnt lgkmcnt(10)\nv_mfma_f32_32x32x16_f16 v[192:207], v[44:47], v[140:143], v[192:207]\nv_add_f32 v232, v232, v216\nv_add_f32 v233, v233, v217\n"
    "v_add_f32 v234, v234, v218\nv_add_f32 v235, v235, v219\ns_waitcnt lgkmcnt(8)\nv_mfma_f32_32x32x16_f16 v[192:207], v[48:51], v[144:147], v[192:207]\n"
    "v_add_f32 v236, v236, v220\nv_add_f32 v237, v237, v221\nv_add_f32 v238, v238, v222\nv_add_f32 v239, v239, v223\ns_waitcnt lgkmcnt(6)\n"
    "v_mfma_f32_32x32x16_f16 v[192:207], v[52:55], v[148:151], v[192:207]\nv_cvt_pk_bf16_f32 v232, v232, v233\nv_cvt_pk_bf16_f32 v233, v234, v235\n"
    "ds_write_b64 v33, v[232:233] offset:1024\ns_waitcnt lgkmcnt(5)\nv_mfma_f32_32x32x16_f16 v[192:207], v[240:243], v[152:155], v[192:207]\n"
    "v_cvt_pk_bf16_f32 v236, v236, v237\nv_cvt_pk_bf16_f32 v237, v238, v239\nds_write_b64 v36, v[236:237] offset:1024\ns_waitcnt lgkmcnt(3)\n"
    "v_mfma_f32_32x32x16_f16 v[192:207], v[40:43], v[156:159], v[192:207]\ns_nop 11\nv_cvt_pk_bf16_f32 v192, v192, v193\nv_cvt_pk_bf16_f32 v193, v194, v195\n"
    "ds_write_b64 v32, v[192:193] offset:0\nv_cvt_pk_bf16_f32 v196, v196, v197\nv_cvt_pk_bf16_f32 v197, v198, v199\nds_write_b64 v32, v[196:197] offset:512\n.Lk130:\n"
    "s_setprio 1\ns_waitcnt vmcnt(0) lgkmcnt(0)\ns_barrier\nds_read_b64 v[208:209], v19\nds_read_b64 v[210:211], v19 offset:10240\nds_read_b64 v[212:213], v20\n"
    "ds_read_b64 v[214:215], v20 offset:10240\nds_read_b64 v[216:217], v34\nds_read_b64 v[218:219], v34 offset:10240\ns_cmp_lg_u32 s12, 0\ns_waitcnt lgkmcnt(4)\n"
    "v_dot2c_f32_bf16 v56, s26, v208\nv_dot2c_f32_bf16 v57, s14, v208\nv_dot2c_f32_bf16 v58, s26, v209\nv_dot2c_f32_bf16 v59, s14, v209\nv_dot2c_f32_bf16 v60, s27, v210\n"
    "v_dot2c_f32_bf16 v61, s15, v210\nv_dot2c_f32_bf16 v62, s27, v211\nv_dot2c_f32_bf16 v63, s15, v211\ns_waitcnt lgkmcnt(2)\nv_dot2c_f32_bf16 v56, s26, v212\n"
    "v_dot2c_f32_bf16 v57, s14, v212\nv_dot2c_f32_bf16 v58, s26, v213\nv_dot2c_f32_bf16 v59, s14, v213\nv_dot2c_f32_bf16 v60, s27, v214\nv_dot2c_f32_bf16 v61, s15, v214\n"
    "v_dot2c_f32_bf16 v62, s27, v215\nv_dot2c_f32_bf16 v63, s15, v215\ns_waitcnt lgkmcnt(0)\nv_dot2c_f32_bf16 v56, s26, v216\nv_dot2c_f32_bf16 v57, s14, v216\n"
    "v_dot2c_f32_bf16 v58, s26, v217\nv_dot2c_f32_bf16 v59, s14, v217\nv_dot2c_f32_bf16 v60, s27, v218\nv_dot2c_f32_bf16 v61, s15, v218\nv_dot2c_f32_bf16 v62, s27, v219\n"
    "v_dot2c_f32_bf16 v63, s15, v219\nv_cvt_pk_bf16_f32 v40, v56, v60\nv_cvt_pk_bf16_f32 v41, v57, v61\nv_cvt_pk_bf16_f32 v42, v58, v62\nv_cvt_pk_bf16_f32 v43, v59, v63\n"
    "global_store_dwordx4 v21, v[40:43], s[4:5] sc1\ns_setprio 0\ns_cbranch_scc1 .Lfin123\n"
    "s_mov_b32 s12, 8388608\nv_add_u32 v9, 8388608, v9\ns_lshl_b32 s43, s22, 17\ns_lshl_b32 s42, s13, 15\ns_add_u32 s44, s43, s42\ns_add_u32 s44, s44, 0xe01000\n"
    "v_and_b32 v231, 63, v0\nv_lshlrev_b32 v59, 4, v231\ns_add_u32 s68, s4, s44\ns_addc_u32 s69, s5, 0\ns_add_u32 s70, s68, 8192\ns_addc_u32 s71, s69, 0\n"
    "s_add_u32 s72, s68, 16384\ns_addc_u32 s73, s69, 0\ns_add_u32 s74, s68, 24576\ns_addc_u32 s75, s69, 0\ns_lshl_b32 s43, s19, 2\ns_max_u32 s42, s43, 1\n"
    "s_lshl_b32 s41, s42, 15\ns_lshl_b32 s40, s13, 13\ns_add_u32 s46, s41, s40\ns_add_u32 s46, s46, 0x5f9000\nv_bfe_u32 v251, v0, 4, 2\nv_and_b32 v250, 15, v0\n"
    "v_lshl_add_u32 v16, v251, 9, 64\nv_lshl_add_u32 v16, v250, 2, v16\nv_and_b32 v249, 63, v0\ns_lshl_b32 s39, s13, 12\ns_add_u32 s38, s39, 0x23000\n"
    "v_lshl_add_u32 v17, v249, 4, s38\ns_lshl_b32 s37, s13, 10\ns_add_u32 s36, s37, 0x23000\nv_lshl_add_u32 v21, v249, 4, s36\ns_lshl_b32 s35, s13, 7\n"
    "s_add_u32 s34, s35, 0x27d80\nv_lshl_add_u32 v25, v250, 3, s34\nv_and_b32 v251, 15, v0\ns_mov_b32 s43, 0x27d80\nv_lshl_add_u32 v26, v251, 3, s43\n"
    "v_bfe_u32 v250, v0, 5, 1\nv_mul_u32_u24 v249, 0xc00, v250\nv_bfe_u32 v248, v0, 4, 1\nv_bfe_u32 v247, v0, 2, 2\nv_and_b32 v246, 3, v0\ns_mul_i32 s42, s13, 0x6000\n"
    "s_add_u32 s41, s42, 0x400\nv_add_u32 v27, s41, v249\nv_lshl_add_u32 v27, v248, 9, v27\ns_cbranch_execz .Lhop\nv_lshl_add_u32 v27, v247, 6, v27\n"
    "v_lshl_add_u32 v27, v246, 3, v27\nv_lshl_add_u32 v27, v248, 5, v27\nv_add_u32 v245, s41, v249\nv_lshl_add_u32 v245, v248, 9, v245\nv_lshl_add_u32 v245, v247, 6, v245\n"
    "v_lshl_add_u32 v245, v246, 3, v245\nv_lshl_add_u32 v245, v248, 5, v245\nv_xor_b32 v28, 32, v245\nv_bfe_u32 v244, v0, 4, 2\ns_lshr_b32 s40, s13, 1\n"
    "s_lshl_b32 s39, s40, 7\ns_and_b32 s38, s13, 1\ns_lshl_b32 s37, s38, 6\ns_add_u32 s36, s39, s37\ns_add_u32 s36, s36, 0x27c80\nv_lshl_add_u32 v29, v244, 4, s36\n"
    "v_mul_u32_u24 v243, 0xd0, v251\ns_lshl_b32 s35, s40, 6\ns_lshl_b32 s34, s38, 5\ns_add_u32 s33, s35, s34\ns_add_u32 s33, s33, 0x18040\nv_add_u32 v30, s33, v243\n"
    "v_lshl_add_u32 v30, v244, 3, v30\ns_lshl_b32 s43, s13, 2\ns_add_u32 s42, s43, 0x27f80\nv_mov_b32 v31, s42\nv_and_b32 v251, 3, v0\ns_mov_b32 s41, 0x27f80\n"
    "v_lshl_add_u32 v32, v251, 2, s41\nds_read_b128 v[40:43], v29\nds_read_b64_tr_b16 v[128:129], v27 offset:0\nds_read_b64_tr_b16 v[130:131], v27 offset:256\n"
    "ds_read_b64_tr_b16 v[132:133], v27 offset:6144\nds_read_b64_tr_b16 v[134:135], v27 offset:6400\nds_read_b64_tr_b16 v[136:137], v27 offset:12288\n"
    "ds_read_b64_tr_b16 v[138:139], v27 offset:12544\nds_read_b64_tr_b16 v[140:141], v27 offset:18432\nds_read_b64_tr_b16 v[142:143], v27 offset:18688\n"
    "ds_read_b64_tr_b16 v[144:145], v28 offset:0\nds_read_b64_tr_b16 v[146:147], v28 offset:256\nds_read_b64_tr_b16 v[148:149], v28 offset:6144\n"
    "ds_read_b64_tr_b16 v[150:151], v28 offset:6400\nds_read_b64_tr_b16 v[152:153], v28 offset:12288\nds_read_b64_tr_b16 v[154:155], v28 offset:12544\n"
    "ds_read_b64_tr_b16 v[156:157], v28 offset:18432\nds_read_b64_tr_b16 v[158:159], v28 offset:18688\nds_read_b64_tr_b16 v[160:161], v27 offset:1024\n"
    "ds_read_b64_tr_b16 v[162:163], v27 offset:1280\nds_read_b64_tr_b16 v[164:165], v27 offset:7168\nds_read_b64_tr_b16 v[166:167], v27 offset:7424\n"
    "ds_read_b64_tr_b16 v[168:169], v27 offset:13312\nds_read_b64_tr_b16 v[170:171], v27 offset:13568\nds_read_b64_tr_b16 v[172:173], v27 offset:19456\n"
    "ds_read_b64_tr_b16 v[174:175], v27 offset:19712\nds_read_b64_tr_b16 v[176:177], v28 offset:1024\nds_read_b64_tr_b16 v[178:179], v28 offset:1280\n"
    "ds_read_b64_tr_b16 v[180:181], v28 offset:7168\nds_read_b64_tr_b16 v[182:183], v28 offset:7424\nds_read_b64_tr_b16 v[184:185], v28 offset:13312\n"
    "ds_read_b64_tr_b16 v[186:187], v28 offset:13568\nds_read_b64_tr_b16 v[188:189], v28 offset:19456\nds_read_b64_tr_b16 v[190:191], v28 offset:19712\n"
    "s_add_u32 s44, s4, s46\ns_addc_u32 s45, s5, 0\ns_mov_b32 s46, 0x3f803f80\ns_mov_b32 s47, 0x3b000000\ns_mov_b32 s80, 0x10000\nv_mov_b32 v33, 3\nv_mov_b64 v[232:233], 0\n"
    "v_mov_b64 v[234:235], 0\nv_mov_b64 v[236:237], 0\nv_mov_b64 v[238:239], 0\nv_mov_b64 v[244:245], 0\nv_mov_b64 v[246:247], 0\nv_mov_b64 v[248:249], 0\n"
    "v_mov_b64 v[250:251], 0\ns_waitcnt vmcnt(0) lgkmcnt(0)\nv_lshlrev_b32 v44, 16, v40\nv_lshlrev_b32 v45, 16, v41\nv_lshlrev_b32 v46, 16, v42\nv_lshlrev_b32 v47, 16, v43\n"
    "v_and_b32 v40, 0xffff0000, v40\nv_and_b32 v41, 0xffff0000, v41\nv_and_b32 v42, 0xffff0000, v42\nv_and_b32 v43, 0xffff0000, v43\nv_add_f32 v40, v40, v44\n"
    "v_add_f32 v41, v41, v45\nv_add_f32 v42, v42, v46\nv_add_f32 v43, v43, v47\ns_sleep 7\ns_setprio 3\ns_barrier\n.Lq138:\n"
    "global_load_dword v192, v16, s[44:45] offset:-4096 sc1\nglobal_load_dword v193, v16, s[44:45] offset:-3968 sc1\nglobal_load_dword v194, v16, s[44:45] offset:-3840 sc1\n"
    "global_load_dword v195, v16, s[44:45] offset:-3712 sc1\nglobal_load_dword v196, v16, s[44:45] offset:-2048 sc1\nglobal_load_dword v197, v16, s[44:45] offset:-1920 sc1\n"
    "global_load_dword v198, v16, s[44:45] offset:-1792 sc1\nglobal_load_dword v199, v16, s[44:45] offset:-1664 sc1\nglobal_load_dword v200, v16, s[44:45] offset:0 sc1\n"
    "global_load_dword v201, v16, s[44:45] offset:128 sc1\nglobal_load_dword v202, v16, s[44:45] offset:256 sc1\nglobal_load_dword v203, v16, s[44:45] offset:384 sc1\n"
    "global_load_dword v204, v16, s[44:45] offset:2048 sc1\nglobal_load_dword v205, v16, s[44:45] offset:2176 sc1\nglobal_load_dword v206, v16, s[44:45] offset:2304 sc1\n"
    "global_load_dword v207, v16, s[44:45] offset:2432 sc1\ns_setprio 3\ns_waitcnt vmcnt(0)\nv_mfma_f32_16x16x32_bf16 v[208:211], v[128:131], v[192:195], 0\n"
    "v_mfma_f32_16x16x32_bf16 v[212:215], v[144:147], v[192:195], 0\nv_mfma_f32_16x16x32_bf16 v[216:219], v[160:163], v[192:195], 0\n"
    "v_mfma_f32_16x16x32_bf16 v[220:223], v[176:179], v[192:195], 0\nv_mfma_f32_16x16x32_bf16 v[208:211], v[132:135], v[196:199], v[208:211]\n"
    "v_mfma_f32_16x16x32_bf16 v[212:215], v[148:151], v[196:199], v[212:215]\nv_mfma_f32_16x16x32_bf16 v[216:219], v[164:167], v[196:199], v[216:219]\n"
    "v_mfma_f32_16x16x32_bf16 v[220:223], v[180:183], v[196:199], v[220:223]\nv_mfma_f32_16x16x32_bf16 v[208:211], v[136:139], v[200:203], v[208:211]\n"
    "v_mfma_f32_16x16x32_bf16 v[212:215], v[152:155], v[200:203], v[212:215]\nv_mfma_f32_16x16x32_bf16 v[216:219], v[168:171], v[200:203], v[216:219]\n"
    "v_mfma_f32_16x16x32_bf16 v[220:223], v[184:187], v[200:203], v[220:223]\nv_mfma_f32_16x16x32_bf16 v[208:211], v[140:143], v[204:207], v[208:211]\n"
    "v_mfma_f32_16x16x32_bf16 v[212:215], v[156:159], v[204:207], v[212:215]\nv_mfma_f32_16x16x32_bf16 v[216:219], v[172:175], v[204:207], v[216:219]\n"
    "v_mfma_f32_16x16x32_bf16 v[220:223], v[188:191], v[204:207], v[220:223]\nv_dot2_f32_bf16 v48, v192, s46, 0\nv_dot2_f32_bf16 v49, v192, v192, 0\n"
    "v_dot2_f32_bf16 v50, v193, s46, 0\nv_dot2_f32_bf16 v51, v193, v193, 0\nv_dot2_f32_bf16 v48, v194, s46, v48\nv_dot2_f32_bf16 v49, v194, v194, v49\n"
    "v_dot2_f32_bf16 v50, v195, s46, v50\nv_dot2_f32_bf16 v51, v195, v195, v51\nv_dot2_f32_bf16 v48, v196, s46, v48\nv_dot2_f32_bf16 v49, v196, v196, v49\n"
    "v_dot2_f32_bf16 v50, v197, s46, v50\nv_dot2_f32_bf16 v51, v197, v197, v51\nv_dot2_f32_bf16 v48, v198, s46, v48\nv_dot2_f32_bf16 v49, v198, v198, v49\n"
    "v_dot2_f32_bf16 v50, v199, s46, v50\nv_dot2_f32_bf16 v51, v199, v199, v51\nv_dot2_f32_bf16 v48, v200, s46, v48\nv_dot2_f32_bf16 v49, v200, v200, v49\n"
    "v_dot2_f32_bf16 v50, v201, s46, v50\nv_dot2_f32_bf16 v51, v201, v201, v51\ns_cbranch_execz .Lhop\nv_dot2_f32_bf16 v48, v202, s46, v48\n"
    "v_dot2_f32_bf16 v49, v202, v202, v49\nv_dot2_f32_bf16 v50, v203, s46, v50\nv_dot2_f32_bf16 v51, v203, v203, v51\nv_dot2_f32_bf16 v48, v204, s46, v48\n"
    "v_dot2_f32_bf16 v49, v204, v204, v49\nv_dot2_f32_bf16 v50, v205, s46, v50\nv_dot2_f32_bf16 v51, v205, v205, v51\nv_dot2_f32_bf16 v48, v206, s46, v48\n"
    "v_dot2_f32_bf16 v49, v206, v206, v49\nv_dot2_f32_bf16 v50, v207, s46, v50\nv_dot2_f32_bf16 v51, v207, v207, v51\ns_setprio 0\nds_write_b128 v17, v[208:211] offset:0\n"
    "ds_write_b128 v17, v[212:215] offset:1024\nds_write_b128 v17, v[216:219] offset:2048\nds_write_b128 v17, v[220:223] offset:3072\nv_add_f32 v48, v48, v50\n"
    "v_add_f32 v49, v49, v51\nv_cmp_u_f32 vcc, v48, v49\ns_nop 0\nv_permlane16_swap_b32 v48, v49\nv_add_f32 v50, v48, v49\nv_add_f32 v49, v48, v49\ns_nop 1\n"
    "v_permlane32_swap_b32 v50, v49\nv_add_f32 v50, v50, v49\nv_mov_b32 v48, v50\nv_mov_b32 v49, v50\ns_nop 1\nv_permlane16_swap_b32 v48, v49\ns_cbranch_vccnz .Lq139\n"
    ".Lq139x:\ns_mov_b64 exec, 0xffff\nds_write_b64 v25, v[48:49]\ns_mov_b64 exec, 1\nds_write_b32 v31, v33 offset:16\ns_mov_b64 exec, -1\ns_mov_b32 s86, 0x100000\n.Ls140:\n"
    "ds_read_b32 v48, v32 offset:16\ns_waitcnt lgkmcnt(0)\nv_cmp_gt_i32 vcc, 3, v48\ns_sub_u32 s86, s86, 1\ns_cbranch_scc1 .Ls141\ns_cbranch_vccz .Ls141\ns_sleep 1\n"
    "s_branch .Ls140\n.Ls141:\nds_read_b64 v[48:49], v26 offset:0\nds_read_b64 v[50:51], v26 offset:128\nds_read_b64 v[52:53], v26 offset:256\n"
    "ds_read_b64 v[54:55], v26 offset:384\nds_read_b128 v[208:211], v21 offset:0\nds_read_b128 v[212:215], v21 offset:4096\nds_read_b128 v[216:219], v21 offset:8192\n"
    "ds_read_b128 v[220:223], v21 offset:12288\ns_waitcnt lgkmcnt(4)\nv_add_f32 v48, v48, v50\nv_add_f32 v49, v49, v51\nv_add_f32 v52, v52, v54\nv_add_f32 v53, v53, v55\n"
    "v_add_f32 v48, v48, v52\nv_add_f32 v49, v49, v53\nv_mul_f32 v48, s47, v48\nv_mul_f32 v50, v48, v48\nv_fma_f32 v49, v49, s47, -v50\nv_max_f32 v49, 0, v49\n"
    "v_cvt_pk_bf16_f32 v49, 0, v49\nv_add_f32 v49, 0x3727c5ac, v49\nv_cvt_pk_bf16_f32 v49, 0, v49\nv_sqrt_f32 v49, v49\nv_cvt_pk_bf16_f32 v48, 0, -v48\n"
    "v_cvt_pk_bf16_f32 v49, 0, v49\nv_rcp_f32 v49, v49\ns_waitcnt lgkmcnt(0)\nv_add_f32 v208, v208, v216\nv_add_f32 v209, v209, v217\nv_add_f32 v210, v210, v218\n"
    "v_add_f32 v211, v211, v219\nv_add_f32 v212, v212, v220\nv_add_f32 v213, v213, v221\nv_add_f32 v214, v214, v222\nv_add_f32 v215, v215, v223\nv_fmac_f32 v208, v48, v40\n"
    "v_fmac_f32 v209, v48, v41\nv_fmac_f32 v210, v48, v42\nv_fmac_f32 v211, v48, v43\nv_add_f32 v208, v208, v212\nv_add_f32 v209, v209, v213\nv_add_f32 v210, v210, v214\n"
    "v_add_f32 v211, v211, v215\nv_mul_f32 v208, v208, v49\nv_mul_f32 v209, v209, v49\nv_mul_f32 v210, v210, v49\nv_mul_f32 v211, v211, v49\n"
    "v_cvt_pk_bf16_f32 v44, v208, v209\nv_cvt_pk_bf16_f32 v45, v210, v211\nds_write_b64 v30, v[44:45]\ns_setprio 0\ns_mov_b32 s44, 0x00ff00ff\ns_mov_b32 s45, 0xff00ff00\n"
    "s_mov_b32 s46, s44\ns_mov_b32 s47, s44\ns_branch .Latt12\n"
    ".Lqg124:\ns_mov_b32 s12, 8388608\nv_add_u32 v9, 8388608, v9\nv_bfe_u32 v239, v0, 5, 1\nv_and_b32 v238, 31, v0\nv_lshlrev_b32 v39, 9, v239\n"
    "v_lshl_add_u32 v39, v238, 2, v39\nv_and_b32 v237, 63, v0\ns_lshl_b32 s43, s24, 15\ns_and_b32 s42, s25, 3\ns_lshl_b32 s41, s42, 13\ns_lshl_b32 s40, s13, 11\n"
    "s_add_u32 s39, s43, s41\ns_add_u32 s39, s39, s40\ns_add_u32 s39, s39, 0xd00000\nv_lshl_add_u32 v56, v237, 2, s39\ns_lshl_b32 s38, s19, 17\ns_lshl_b32 s37, s13, 15\n"
    "s_add_u32 s82, s38, s37\ns_add_u32 s82, s82, 0x600800\ns_lshl_b32 s36, s23, 8\ns_add_u32 s35, s43, s41\ns_add_u32 s35, s35, s40\ns_add_u32 s35, s35, s36\n"
    "s_add_u32 s35, s35, 0xd00000\nv_lshl_add_u32 v57, v237, 2, s35\ns_add_u32 s82, s4, s82\ns_addc_u32 s83, s5, 0\ns_add_u32 s86, s23, 0\ns_and_b32 s86, s86, 7\n"
    "s_lshl_b32 s44, s86, 12\ns_add_u32 s52, s82, s44\ns_addc_u32 s53, s83, 0\ns_add_u32 s86, s23, 1\ns_and_b32 s86, s86, 7\ns_lshl_b32 s45, s86, 12\ns_add_u32 s54, s82, s45\n"
    "s_addc_u32 s55, s83, 0\ns_add_u32 s86, s23, 2\ns_and_b32 s86, s86, 7\ns_lshl_b32 s46, s86, 12\ns_add_u32 s56, s82, s46\ns_addc_u32 s57, s83, 0\ns_add_u32 s86, s23, 3\n"
    "s_and_b32 s86, s86, 7\ns_lshl_b32 s47, s86, 12\ns_add_u32 s58, s82, s47\ns_addc_u32 s59, s83, 0\ns_add_u32 s86, s23, 4\ns_and_b32 s86, s86, 7\ns_lshl_b32 s48, s86, 12\n"
    "s_add_u32 s60, s82, s48\ns_addc_u32 s61, s83, 0\ns_add_u32 s86, s23, 5\ns_and_b32 s86, s86, 7\ns_lshl_b32 s49, s86, 12\ns_add_u32 s62, s82, s49\ns_addc_u32 s63, s83, 0\n"
    "s_add_u32 s86, s23, 6\ns_and_b32 s86, s86, 7\ns_lshl_b32 s50, s86, 12\ns_add_u32 s64, s82, s50\ns_addc_u32 s65, s83, 0\ns_add_u32 s86, s23, 7\ns_and_b32 s86, s86, 7\n"
    "s_lshl_b32 s51, s86, 12\ns_add_u32 s66, s82, s51\ns_addc_u32 s67, s83, 0\ns_mov_b32 s81, 0x3b000000\ns_mov_b32 s84, -1\ns_mov_b32 s85, 0\nv_mov_b32 v241, 0x3f803f80\n"
    "v_mov_b64 v[120:121], 0\nv_mov_b64 v[122:123], 0\nv_mov_b64 v[124:125], 0\nv_mov_b64 v[126:127], 0\nv_mov_b64 v[128:129], 0\nv_mov_b64 v[130:131], 0\n"
    "v_mov_b64 v[132:133], 0\nv_mov_b64 v[134:135], 0\nv_mad_u32_u24 v242, s44, 3, v36\ns_mov_b32 s80, 0x10000\nds_read_b64_tr_b16 v[208:209], v242 offset:0\n"
    "ds_read_b64_tr_b16 v[210:211], v242 offset:256\nds_read_b64_tr_b16 v[212:213], v242 offset:3072\nds_read_b64_tr_b16 v[214:215], v242 offset:3328\n"
    "ds_read_b64_tr_b16 v[216:217], v242 offset:6144\nds_read_b64_tr_b16 v[218:219], v242 offset:6400\nds_read_b64_tr_b16 v[220:221], v242 offset:9216\n"
    "ds_read_b64_tr_b16 v[222:223], v242 offset:9472\nds_read_b64_tr_b16 v[224:225], v242 offset:1024\ns_nop 0\ns_cbranch_execz .Lhop\n"
    "ds_read_b64_tr_b16 v[226:227], v242 offset:1280\nds_read_b64_tr_b16 v[228:229], v242 offset:4096\nds_read_b64_tr_b16 v[230:231], v242 offset:4352\n"
    "ds_read_b32 v124, v38 offset:0\nds_read_b32 v128, v38 offset:128\nds_read_b32 v132, v38 offset:256\ns_waitcnt vmcnt(0) lgkmcnt(0)\ns_barrier\n"
    "global_load_dword v144, v39, s[52:53] offset:-2048 sc1\nglobal_load_dword v145, v39, s[52:53] offset:-1920 sc1\nglobal_load_dword v146, v39, s[52:53] offset:-1792 sc1\n"
    "global_load_dword v147, v39, s[52:53] offset:-1664 sc1\nglobal_load_dword v148, v39, s[52:53] offset:-1024 sc1\nglobal_load_dword v149, v39, s[52:53] offset:-896 sc1\n"
    "global_load_dword v150, v39, s[52:53] offset:-768 sc1\nglobal_load_dword v151, v39, s[52:53] offset:-640 sc1\nglobal_load_dword v152, v39, s[52:53] offset:0 sc1\n"
    "global_load_dword v153, v39, s[52:53] offset:128 sc1\nglobal_load_dword v154, v39, s[52:53] offset:256 sc1\nglobal_load_dword v155, v39, s[52:53] offset:384 sc1\n"
    "global_load_dword v156, v39, s[52:53] offset:1024 sc1\nglobal_load_dword v157, v39, s[52:53] offset:1152 sc1\nglobal_load_dword v158, v39, s[52:53] offset:1280 sc1\n"
    "global_load_dword v159, v39, s[52:53] offset:1408 sc1\nglobal_load_dword v160, v39, s[54:55] offset:-2048 sc1\nglobal_load_dword v161, v39, s[54:55] offset:-1920 sc1\n"
    "global_load_dword v162, v39, s[54:55] offset:-1792 sc1\nglobal_load_dword v163, v39, s[54:55] offset:-1664 sc1\nglobal_load_dword v164, v39, s[54:55] offset:-1024 sc1\n"
    "global_load_dword v165, v39, s[54:55] offset:-896 sc1\nglobal_load_dword v166, v39, s[54:55] offset:-768 sc1\nglobal_load_dword v167, v39, s[54:55] offset:-640 sc1\n"
    "global_load_dword v168, v39, s[54:55] offset:0 sc1\nglobal_load_dword v169, v39, s[54:55] offset:128 sc1\nglobal_load_dword v170, v39, s[54:55] offset:256 sc1\n"
    "global_load_dword v171, v39, s[54:55] offset:384 sc1\nglobal_load_dword v172, v39, s[54:55] offset:1024 sc1\nglobal_load_dword v173, v39, s[54:55] offset:1152 sc1\n"
    "global_load_dword v174, v39, s[54:55] offset:1280 sc1\nglobal_load_dword v175, v39, s[54:55] offset:1408 sc1\nglobal_load_dword v176, v39, s[56:57] offset:-2048 sc1\n"
    "global_load_dword v177, v39, s[56:57] offset:-1920 sc1\nglobal_load_dword v178, v39, s[56:57] offset:-1792 sc1\nglobal_load_dword v179, v39, s[56:57] offset:-1664 sc1\n"
    "global_load_dword v180, v39, s[56:57] offset:-1024 sc1\nglobal_load_dword v181, v39, s[56:57] offset:-896 sc1\nglobal_load_dword v182, v39, s[56:57] offset:-768 sc1\n"
    "global_load_dword v183, v39, s[56:57] offset:-640 sc1\nglobal_load_dword v184, v39, s[56:57] offset:0 sc1\nglobal_load_dword v185, v39, s[56:57] offset:128 sc1\n"
    "global_load_dword v186, v39, s[56:57] offset:256 sc1\nglobal_load_dword v187, v39, s[56:57] offset:384 sc1\nglobal_load_dword v188, v39, s[56:57] offset:1024 sc1\n"
    "global_load_dword v189, v39, s[56:57] offset:1152 sc1\nglobal_load_dword v190, v39, s[56:57] offset:1280 sc1\nglobal_load_dword v191, v39, s[56:57] offset:1408 sc1\n"
    "s_waitcnt vmcnt(32)\n.Ld142:\nv_dot2_f32_bf16 v112, v144, v241, 0\nv_dot2_f32_bf16 v113, v144, v144, 0\nv_dot2_f32_bf16 v114, v145, v241, 0\n"
    "v_dot2_f32_bf16 v115, v145, v145, 0\nv_dot2_f32_bf16 v116, v146, v241, 0\nv_dot2_f32_bf16 v117, v146, v146, 0\nv_dot2_f32_bf16 v118, v147, v241, 0\n"
    "v_dot2_f32_bf16 v119, v147, v147, 0\nv_dot2_f32_bf16 v112, v148, v241, v112\nv_dot2_f32_bf16 v113, v148, v148, v113\nv_dot2_f32_bf16 v114, v149, v241, v114\n"
    "v_dot2_f32_bf16 v115, v149, v149, v115\nv_dot2_f32_bf16 v116, v150, v241, v116\nv_dot2_f32_bf16 v117, v150, v150, v117\nv_dot2_f32_bf16 v118, v151, v241, v118\n"
    "v_dot2_f32_bf16 v119, v151, v151, v119\nv_dot2_f32_bf16 v112, v152, v241, v112\nv_dot2_f32_bf16 v113, v152, v152, v113\nv_dot2_f32_bf16 v114, v153, v241, v114\n"
    "v_dot2_f32_bf16 v115, v153, v153, v115\nv_dot2_f32_bf16 v116, v154, v241, v116\nv_dot2_f32_bf16 v117, v154, v154, v117\nv_dot2_f32_bf16 v118, v155, v241, v118\n"
    "v_dot2_f32_bf16 v119, v155, v155, v119\nv_dot2_f32_bf16 v112, v156, v241, v112\nv_dot2_f32_bf16 v113, v156, v156, v113\nv_dot2_f32_bf16 v114, v157, v241, v114\n"
    "v_dot2_f32_bf16 v115, v157, v157, v115\nv_dot2_f32_bf16 v116, v158, v241, v116\nv_dot2_f32_bf16 v117, v158, v158, v117\nv_dot2_f32_bf16 v118, v159, v241, v118\n"
    "v_dot2_f32_bf16 v119, v159, v159, v119\nv_pk_add_f32 v[112:113], v[112:113], v[114:115]\ns_nop 1\nv_pk_add_f32 v[116:117], v[116:117], v[118:119]\n"
    "v_pk_add_f32 v[112:113], v[112:113], v[116:117]\nv_cmp_u_f32 vcc, v112, v113\ns_nop 0\nv_permlane32_swap_b32 v112, v113\nv_add_f32 v112, v112, v113\n"
    "s_cbranch_vccnz .Ld143\n.Ld144:\nglobal_store_dword v57, v112, s[4:5] sc1\nv_mfma_f32_32x32x16_bf16 v[64:79], v[208:211], v[144:147], 0\n"
    "ds_read_b64_tr_b16 v[208:209], v242 offset:7168\nds_read_b64_tr_b16 v[210:211], v242 offset:7424\nglobal_load_dword v192, v39, s[58:59] offset:-2048 sc1\n"
    "global_load_dword v193, v39, s[58:59] offset:-1920 sc1\nv_mfma_f32_32x32x16_bf16 v[64:79], v[212:215], v[148:151], v[64:79]\n"
    "ds_read_b64_tr_b16 v[212:213], v242 offset:10240\nds_read_b64_tr_b16 v[214:215], v242 offset:10496\nglobal_load_dword v194, v39, s[58:59] offset:-1792 sc1\n"
    "global_load_dword v195, v39, s[58:59] offset:-1664 sc1\nv_mfma_f32_32x32x16_bf16 v[64:79], v[216:219], v[152:155], v[64:79]\n"
    "ds_read_b64_tr_b16 v[216:217], v242 offset:2048\nds_read_b64_tr_b16 v[218:219], v242 offset:2304\nglobal_load_dword v196, v39, s[58:59] offset:-1024 sc1\n"
    "global_load_dword v197, v39, s[58:59] offset:-896 sc1\ns_waitcnt lgkmcnt(6)\nv_mfma_f32_32x32x16_bf16 v[64:79], v[220:223], v[156:159], v[64:79]\n"
    "ds_read_b64_tr_b16 v[220:221], v242 offset:5120\nds_read_b64_tr_b16 v[222:223], v242 offset:5376\nglobal_load_dword v198, v39, s[58:59] offset:-768 sc1\n"
    "global_load_dword v199, v39, s[58:59] offset:-640 sc1\nv_mfma_f32_32x32x16_bf16 v[80:95], v[224:227], v[144:147], 0\nds_read_b64_tr_b16 v[224:225], v242 offset:8192\n"
    "ds_read_b64_tr_b16 v[226:227], v242 offset:8448\nglobal_load_dword v200, v39, s[58:59] offset:0 sc1\nglobal_load_dword v201, v39, s[58:59] offset:128 sc1\n"
    "v_mad_u32_u24 v240, s45, 3, v36\nv_mfma_f32_32x32x16_bf16 v[80:95], v[228:231], v[148:151], v[80:95]\nds_read_b64_tr_b16 v[228:229], v242 offset:11264\n"
    "ds_read_b64_tr_b16 v[230:231], v242 offset:11520\nglobal_load_dword v202, v39, s[58:59] offset:256 sc1\nglobal_load_dword v203, v39, s[58:59] offset:384 sc1\n"
    "s_waitcnt lgkmcnt(6)\nv_mfma_f32_32x32x16_bf16 v[80:95], v[208:211], v[152:155], v[80:95]\ns_cbranch_execz .Lhop\nds_read_b64_tr_b16 v[208:209], v240 offset:0\n"
    "ds_read_b64_tr_b16 v[210:211], v240 offset:256\nglobal_load_dword v204, v39, s[58:59] offset:1024 sc1\nglobal_load_dword v205, v39, s[58:59] offset:1152 sc1\n"
    "v_mfma_f32_32x32x16_bf16 v[80:95], v[212:215], v[156:159], v[80:95]\nds_read_b64_tr_b16 v[212:213], v240 offset:3072\nds_read_b64_tr_b16 v[214:215], v240 offset:3328\n"
    "global_load_dword v206, v39, s[58:59] offset:1280 sc1\nglobal_load_dword v207, v39, s[58:59] offset:1408 sc1\n"
    "v_mfma_f32_32x32x16_bf16 v[96:111], v[216:219], v[144:147], 0\nds_read_b64_tr_b16 v[216:217], v240 offset:6144\nds_read_b64_tr_b16 v[218:219], v240 offset:6400\n"
    "s_waitcnt vmcnt(33)\nv_max3_u32 v244, v160, v161, v162\nv_max3_u32 v243, v163, v164, v165\ns_waitcnt lgkmcnt(6)\n"
    "v_mfma_f32_32x32x16_bf16 v[96:111], v[220:223], v[148:151], v[96:111]\nds_read_b64_tr_b16 v[220:221], v240 offset:9216\nds_read_b64_tr_b16 v[222:223], v240 offset:9472\n"
    "v_max3_u32 v244, v244, v166, v167\nv_max3_u32 v243, v243, v168, v169\nv_max3_u32 v244, v244, v170, v171\n"
    "v_mfma_f32_32x32x16_bf16 v[96:111], v[224:227], v[152:155], v[96:111]\nds_read_b64_tr_b16 v[224:225], v240 offset:1024\nds_read_b64_tr_b16 v[226:227], v240 offset:1280\n"
    "v_max3_u32 v243, v243, v172, v173\nv_max3_u32 v244, v244, v174, v175\nv_max_u32 v243, v243, v244\nv_cmp_eq_u32 vcc, -1, v243\n"
    "v_mfma_f32_32x32x16_bf16 v[96:111], v[228:231], v[156:159], v[96:111]\nds_read_b64_tr_b16 v[228:229], v240 offset:4096\nds_read_b64_tr_b16 v[230:231], v240 offset:4352\n"
    "s_cbranch_vccnz .Lx147\n.Lx148:\ns_waitcnt lgkmcnt(6)\nv_mfma_f32_32x32x16_bf16 v[64:79], v[208:211], v[160:163], v[64:79]\n"
    "ds_read_b64_tr_b16 v[208:209], v240 offset:7168\nds_read_b64_tr_b16 v[210:211], v240 offset:7424\nglobal_load_dword v144, v39, s[60:61] offset:-2048 sc1\n"
    "global_load_dword v145, v39, s[60:61] offset:-1920 sc1\nv_mfma_f32_32x32x16_bf16 v[64:79], v[212:215], v[164:167], v[64:79]\n"
    "ds_read_b64_tr_b16 v[212:213], v240 offset:10240\nds_read_b64_tr_b16 v[214:215], v240 offset:10496\nglobal_load_dword v146, v39, s[60:61] offset:-1792 sc1\n"
    "global_load_dword v147, v39, s[60:61] offset:-1664 sc1\nv_mfma_f32_32x32x16_bf16 v[64:79], v[216:219], v[168:171], v[64:79]\n"
    "ds_read_b64_tr_b16 v[216:217], v240 offset:2048\nds_read_b64_tr_b16 v[218:219], v240 offset:2304\nglobal_load_dword v148, v39, s[60:61] offset:-1024 sc1\n"
    "global_load_dword v149, v39, s[60:61] offset:-896 sc1\ns_waitcnt lgkmcnt(6)\nv_mfma_f32_32x32x16_bf16 v[64:79], v[220:223], v[172:175], v[64:79]\n"
    "ds_read_b64_tr_b16 v[220:221], v240 offset:5120\nds_read_b64_tr_b16 v[222:223], v240 offset:5376\nglobal_load_dword v150, v39, s[60:61] offset:-768 sc1\n"
    "global_load_dword v151, v39, s[60:61] offset:-640 sc1\nv_mfma_f32_32x32x16_bf16 v[80:95], v[224:227], v[160:163], v[80:95]\n"
    "ds_read_b64_tr_b16 v[224:225], v240 offset:8192\nds_read_b64_tr_b16 v[226:227], v240 offset:8448\nglobal_load_dword v152, v39, s[60:61] offset:0 sc1\n"
    "global_load_dword v153, v39, s[60:61] offset:128 sc1\nv_mad_u32_u24 v242, s46, 3, v36\nv_mfma_f32_32x32x16_bf16 v[80:95], v[228:231], v[164:167], v[80:95]\n"
    "ds_read_b64_tr_b16 v[228:229], v240 offset:11264\nds_read_b64_tr_b16 v[230:231], v240 offset:11520\nglobal_load_dword v154, v39, s[60:61] offset:256 sc1\n"
    "global_load_dword v155, v39, s[60:61] offset:384 sc1\ns_waitcnt lgkmcnt(6)\nv_mfma_f32_32x32x16_bf16 v[80:95], v[208:211], v[168:171], v[80:95]\n"
    "ds_read_b64_tr_b16 v[208:209], v242 offset:0\nds_read_b64_tr_b16 v[210:211], v242 offset:256\nglobal_load_dword v156, v39, s[60:61] offset:1024 sc1\n"
    "global_load_dword v157, v39, s[60:61] offset:1152 sc1\nv_mfma_f32_32x32x16_bf16 v[80:95], v[212:215], v[172:175], v[80:95]\n"
    "ds_read_b64_tr_b16 v[212:213], v242 offset:3072\nds_read_b64_tr_b16 v[214:215], v242 offset:3328\nglobal_load_dword v158, v39, s[60:61] offset:1280 sc1\n"
    "global_load_dword v159, v39, s[60:61] offset:1408 sc1\nv_mfma_f32_32x32x16_bf16 v[96:111], v[216:219], v[160:163], v[96:111]\n"
    "ds_read_b64_tr_b16 v[216:217], v242 offset:6144\nds_read_b64_tr_b16 v[218:219], v242 offset:6400\ns_waitcnt vmcnt(33)\nv_max3_u32 v244, v176, v177, v178\n"
    "v_max3_u32 v243, v179, v180, v181\ns_waitcnt lgkmcnt(6)\nv_mfma_f32_32x32x16_bf16 v[96:111], v[220:223], v[164:167], v[96:111]\n"
    "ds_read_b64_tr_b16 v[220:221], v242 offset:9216\nds_read_b64_tr_b16 v[222:223], v242 offset:9472\nv_max3_u32 v244, v244, v182, v183\nv_max3_u32 v243, v243, v184, v185\n"
    "v_max3_u32 v244, v244, v186, v187\nv_mfma_f32_32x32x16_bf16 v[96:111], v[224:227], v[168:171], v[96:111]\nds_read_b64_tr_b16 v[224:225], v242 offset:1024\n"
    "ds_read_b64_tr_b16 v[226:227], v242 offset:1280\nv_max3_u32 v243, v243, v188, v189\nv_max3_u32 v244, v244, v190, v191\nv_max_u32 v243, v243, v244\n"
    "v_cmp_eq_u32 vcc, -1, v243\nv_mfma_f32_32x32x16_bf16 v[96:111], v[228:231], v[172:175], v[96:111]\nds_read_b64_tr_b16 v[228:229], v242 offset:4096\n"
    "ds_read_b64_tr_b16 v[230:231], v242 offset:4352\ns_cbranch_vccnz .Lx149\n.Lx150:\ns_waitcnt lgkmcnt(6)\n"
    "v_mfma_f32_32x32x16_bf16 v[64:79], v[208:211], v[176:179], v[64:79]\nds_read_b64_tr_b16 v[208:209], v242 offset:7168\nds_read_b64_tr_b16 v[210:211], v242 offset:7424\n"
    "global_load_dword v160, v39, s[62:63] offset:-2048 sc1\nglobal_load_dword v161, v39, s[62:63] offset:-1920 sc1\n"
    "v_mfma_f32_32x32x16_bf16 v[64:79], v[212:215], v[180:183], v[64:79]\nds_read_b64_tr_b16 v[212:213], v242 offset:10240\nds_read_b64_tr_b16 v[214:215], v242 offset:10496\n"
    "global_load_dword v162, v39, s[62:63] offset:-1792 sc1\nglobal_load_dword v163, v39, s[62:63] offset:-1664 sc1\n"
    "v_mfma_f32_32x32x16_bf16 v[64:79], v[216:219], v[184:187], v[64:79]\nds_read_b64_tr_b16 v[216:217], v242 offset:2048\nds_read_b64_tr_b16 v[218:219], v242 offset:2304\n"
    "global_load_dword v164, v39, s[62:63] offset:-1024 sc1\nglobal_load_dword v165, v39, s[62:63] offset:-896 sc1\ns_waitcnt lgkmcnt(6)\n"
    "v_mfma_f32_32x32x16_bf16 v[64:79], v[220:223], v[188:191], v[64:79]\nds_read_b64_tr_b16 v[220:221], v242 offset:5120\nds_read_b64_tr_b16 v[222:223], v242 offset:5376\n"
    "global_load_dword v166, v39, s[62:63] offset:-768 sc1\nglobal_load_dword v167, v39, s[62:63] offset:-640 sc1\n"
    "v_mfma_f32_32x32x16_bf16 v[80:95], v[224:227], v[176:179], v[80:95]\nds_read_b64_tr_b16 v[224:225], v242 offset:8192\nds_read_b64_tr_b16 v[226:227], v242 offset:8448\n"
    "global_load_dword v168, v39, s[62:63] offset:0 sc1\nglobal_load_dword v169, v39, s[62:63] offset:128 sc1\nv_mad_u32_u24 v240, s47, 3, v36\n"
    "v_mfma_f32_32x32x16_bf16 v[80:95], v[228:231], v[180:183], v[80:95]\nds_read_b64_tr_b16 v[228:229], v242 offset:11264\nds_read_b64_tr_b16 v[230:231], v242 offset:11520\n"
    "global_load_dword v170, v39, s[62:63] offset:256 sc1\nglobal_load_dword v171, v39, s[62:63] offset:384 sc1\ns_waitcnt lgkmcnt(6)\ns_nop 0\ns_cbranch_execz .Lhop\n"
    "v_mfma_f32_32x32x16_bf16 v[80:95], v[208:211], v[184:187], v[80:95]\nds_read_b64_tr_b16 v[208:209], v240 offset:0\nds_read_b64_tr_b16 v[210:211], v240 offset:256\n"
    "global_load_dword v172, v39, s[62:63] offset:1024 sc1\nglobal_load_dword v173, v39, s[62:63] offset:1152 sc1\n"
    "v_mfma_f32_32x32x16_bf16 v[80:95], v[212:215], v[188:191], v[80:95]\nds_read_b64_tr_b16 v[212:213], v240 offset:3072\nds_read_b64_tr_b16 v[214:215], v240 offset:3328\n"
    "global_load_dword v174, v39, s[62:63] offset:1280 sc1\nglobal_load_dword v175, v39, s[62:63] offset:1408 sc1\n"
    "v_mfma_f32_32x32x16_bf16 v[96:111], v[216:219], v[176:179], v[96:111]\nds_read_b64_tr_b16 v[216:217], v240 offset:6144\nds_read_b64_tr_b16 v[218:219], v240 offset:6400\n"
    "s_waitcnt vmcnt(32)\nv_max3_u32 v244, v192, v193, v194\nv_max3_u32 v243, v195, v196, v197\ns_waitcnt lgkmcnt(6)\n"
    "v_mfma_f32_32x32x16_bf16 v[96:111], v[220:223], v[180:183], v[96:111]\nds_read_b64_tr_b16 v[220:221], v240 offset:9216\nds_read_b64_tr_b16 v[222:223], v240 offset:9472\n"
    "v_max3_u32 v244, v244, v198, v199\nv_max3_u32 v243, v243, v200, v201\nv_max3_u32 v244, v244, v202, v203\n"
    "v_mfma_f32_32x32x16_bf16 v[96:111], v[224:227], v[184:187], v[96:111]\nds_read_b64_tr_b16 v[224:225], v240 offset:1024\nds_read_b64_tr_b16 v[226:227], v240 offset:1280\n"
    "v_max3_u32 v243, v243, v204, v205\nv_max3_u32 v244, v244, v206, v207\nv_max_u32 v243, v243, v244\nv_cmp_eq_u32 vcc, -1, v243\n"
    "v_mfma_f32_32x32x16_bf16 v[96:111], v[228:231], v[188:191], v[96:111]\nds_read_b64_tr_b16 v[228:229], v240 offset:4096\nds_read_b64_tr_b16 v[230:231], v240 offset:4352\n"
    "s_cbranch_vccnz .Lx151\n.Lx152:\ns_waitcnt lgkmcnt(6)\nv_mfma_f32_32x32x16_bf16 v[64:79], v[208:211], v[192:195], v[64:79]\n"
    "ds_read_b64_tr_b16 v[208:209], v240 offset:7168\nds_read_b64_tr_b16 v[210:211], v240 offset:7424\nglobal_load_dword v176, v39, s[64:65] offset:-2048 sc1\n"
    "global_load_dword v177, v39, s[64:65] offset:-1920 sc1\nv_mfma_f32_32x32x16_bf16 v[64:79], v[212:215], v[196:199], v[64:79]\n"
    "ds_read_b64_tr_b16 v[212:213], v240 offset:10240\nds_read_b64_tr_b16 v[214:215], v240 offset:10496\nglobal_load_dword v178, v39, s[64:65] offset:-1792 sc1\n"
    "global_load_dword v179, v39, s[64:65] offset:-1664 sc1\nv_mfma_f32_32x32x16_bf16 v[64:79], v[216:219], v[200:203], v[64:79]\n"
    "ds_read_b64_tr_b16 v[216:217], v240 offset:2048\nds_read_b64_tr_b16 v[218:219], v240 offset:2304\nglobal_load_dword v180, v39, s[64:65] offset:-1024 sc1\n"
    "global_load_dword v181, v39, s[64:65] offset:-896 sc1\ns_waitcnt lgkmcnt(6)\nv_mfma_f32_32x32x16_bf16 v[64:79], v[220:223], v[204:207], v[64:79]\n"
    "ds_read_b64_tr_b16 v[220:221], v240 offset:5120\nds_read_b64_tr_b16 v[222:223], v240 offset:5376\nglobal_load_dword v182, v39, s[64:65] offset:-768 sc1\n"
    "global_load_dword v183, v39, s[64:65] offset:-640 sc1\nv_mfma_f32_32x32x16_bf16 v[80:95], v[224:227], v[192:195], v[80:95]\n"
    "ds_read_b64_tr_b16 v[224:225], v240 offset:8192\nds_read_b64_tr_b16 v[226:227], v240 offset:8448\nglobal_load_dword v184, v39, s[64:65] offset:0 sc1\n"
    "global_load_dword v185, v39, s[64:65] offset:128 sc1\nv_mad_u32_u24 v242, s48, 3, v36\nv_mfma_f32_32x32x16_bf16 v[80:95], v[228:231], v[196:199], v[80:95]\n"
    "ds_read_b64_tr_b16 v[228:229], v240 offset:11264\nds_read_b64_tr_b16 v[230:231], v240 offset:11520\nglobal_load_dword v186, v39, s[64:65] offset:256 sc1\n"
    "global_load_dword v187, v39, s[64:65] offset:384 sc1\ns_waitcnt lgkmcnt(6)\nv_mfma_f32_32x32x16_bf16 v[80:95], v[208:211], v[200:203], v[80:95]\n"
    "ds_read_b64_tr_b16 v[208:209], v242 offset:0\nds_read_b64_tr_b16 v[210:211], v242 offset:256\nglobal_load_dword v188, v39, s[64:65] offset:1024 sc1\n"
    "global_load_dword v189, v39, s[64:65] offset:1152 sc1\nv_mfma_f32_32x32x16_bf16 v[80:95], v[212:215], v[204:207], v[80:95]\n"
    "ds_read_b64_tr_b16 v[212:213], v242 offset:3072\nds_read_b64_tr_b16 v[214:215], v242 offset:3328\nglobal_load_dword v190, v39, s[64:65] offset:1280 sc1\n"
    "global_load_dword v191, v39, s[64:65] offset:1408 sc1\nv_mfma_f32_32x32x16_bf16 v[96:111], v[216:219], v[192:195], v[96:111]\n"
    "ds_read_b64_tr_b16 v[216:217], v242 offset:6144\nds_read_b64_tr_b16 v[218:219], v242 offset:6400\ns_waitcnt vmcnt(32)\nv_max3_u32 v244, v144, v145, v146\n"
    "v_max3_u32 v243, v147, v148, v149\ns_waitcnt lgkmcnt(6)\nv_mfma_f32_32x32x16_bf16 v[96:111], v[220:223], v[196:199], v[96:111]\n"
    "ds_read_b64_tr_b16 v[220:221], v242 offset:9216\nds_read_b64_tr_b16 v[222:223], v242 offset:9472\nv_max3_u32 v244, v244, v150, v151\nv_max3_u32 v243, v243, v152, v153\n"
    "v_max3_u32 v244, v244, v154, v155\nv_mfma_f32_32x32x16_bf16 v[96:111], v[224:227], v[200:203], v[96:111]\nds_read_b64_tr_b16 v[224:225], v242 offset:1024\n"
    "ds_read_b64_tr_b16 v[226:227], v242 offset:1280\nv_max3_u32 v243, v243, v156, v157\nv_max3_u32 v244, v244, v158, v159\nv_max_u32 v243, v243, v244\n"
    "v_cmp_eq_u32 vcc, -1, v243\nv_mfma_f32_32x32x16_bf16 v[96:111], v[228:231], v[204:207], v[96:111]\nds_read_b64_tr_b16 v[228:229], v242 offset:4096\n"
    "ds_read_b64_tr_b16 v[230:231], v242 offset:4352\ns_cbranch_vccnz .Lx153\n.Lx154:\ns_waitcnt lgkmcnt(6)\n"
    "v_mfma_f32_32x32x16_bf16 v[64:79], v[208:211], v[144:147], v[64:79]\nds_read_b64_tr_b16 v[208:209], v242 offset:7168\nds_read_b64_tr_b16 v[210:211], v242 offset:7424\n"
    "global_load_dword v192, v39, s[66:67] offset:-2048 sc1\nglobal_load_dword v193, v39, s[66:67] offset:-1920 sc1\n"
    "v_mfma_f32_32x32x16_bf16 v[64:79], v[212:215], v[148:151], v[64:79]\nds_read_b64_tr_b16 v[212:213], v242 offset:10240\nds_read_b64_tr_b16 v[214:215], v242 offset:10496\n"
    "global_load_dword v194, v39, s[66:67] offset:-1792 sc1\nglobal_load_dword v195, v39, s[66:67] offset:-1664 sc1\n"
    "v_mfma_f32_32x32x16_bf16 v[64:79], v[216:219], v[152:155], v[64:79]\nds_read_b64_tr_b16 v[216:217], v242 offset:2048\nds_read_b64_tr_b16 v[218:219], v242 offset:2304\n"
    "global_load_dword v196, v39, s[66:67] offset:-1024 sc1\nglobal_load_dword v197, v39, s[66:67] offset:-896 sc1\ns_waitcnt lgkmcnt(6)\n"
    "v_mfma_f32_32x32x16_bf16 v[64:79], v[220:223], v[156:159], v[64:79]\nds_read_b64_tr_b16 v[220:221], v242 offset:5120\nds_read_b64_tr_b16 v[222:223], v242 offset:5376\n"
    "global_load_dword v198, v39, s[66:67] offset:-768 sc1\nglobal_load_dword v199, v39, s[66:67] offset:-640 sc1\n"
    "v_mfma_f32_32x32x16_bf16 v[80:95], v[224:227], v[144:147], v[80:95]\nds_read_b64_tr_b16 v[224:225], v242 offset:8192\nds_read_b64_tr_b16 v[226:227], v242 offset:8448\n"
    "global_load_dword v200, v39, s[66:67] offset:0 sc1\nglobal_load_dword v201, v39, s[66:67] offset:128 sc1\nv_mad_u32_u24 v240, s49, 3, v36\n"
    "v_mfma_f32_32x32x16_bf16 v[80:95], v[228:231], v[148:151], v[80:95]\nds_read_b64_tr_b16 v[228:229], v242 offset:11264\nds_read_b64_tr_b16 v[230:231], v242 offset:11520\n"
    "global_load_dword v202, v39, s[66:67] offset:256 sc1\nglobal_load_dword v203, v39, s[66:67] offset:384 sc1\ns_cbranch_execz .Lhop\ns_waitcnt lgkmcnt(6)\n"
    "v_mfma_f32_32x32x16_bf16 v[80:95], v[208:211], v[152:155], v[80:95]\nds_read_b64_tr_b16 v[208:209], v240 offset:0\nds_read_b64_tr_b16 v[210:211], v240 offset:256\n"
    "global_load_dword v204, v39, s[66:67] offset:1024 sc1\nglobal_load_dword v205, v39, s[66:67] offset:1152 sc1\n"
    "v_mfma_f32_32x32x16_bf16 v[80:95], v[212:215], v[156:159], v[80:95]\nds_read_b64_tr_b16 v[212:213], v240 offset:3072\nds_read_b64_tr_b16 v[214:215], v240 offset:3328\n"
    "global_load_dword v206, v39, s[66:67] offset:1280 sc1\nglobal_load_dword v207, v39, s[66:67] offset:1408 sc1\n"
    "v_mfma_f32_32x32x16_bf16 v[96:111], v[216:219], v[144:147], v[96:111]\nds_read_b64_tr_b16 v[216:217], v240 offset:6144\nds_read_b64_tr_b16 v[218:219], v240 offset:6400\n"
    "s_waitcnt vmcnt(32)\nv_max3_u32 v244, v160, v161, v162\nv_max3_u32 v243, v163, v164, v165\ns_waitcnt lgkmcnt(6)\n"
    "v_mfma_f32_32x32x16_bf16 v[96:111], v[220:223], v[148:151], v[96:111]\nds_read_b64_tr_b16 v[220:221], v240 offset:9216\nds_read_b64_tr_b16 v[222:223], v240 offset:9472\n"
    "v_max3_u32 v244, v244, v166, v167\nv_max3_u32 v243, v243, v168, v169\nv_max3_u32 v244, v244, v170, v171\n"
    "v_mfma_f32_32x32x16_bf16 v[96:111], v[224:227], v[152:155], v[96:111]\nds_read_b64_tr_b16 v[224:225], v240 offset:1024\nds_read_b64_tr_b16 v[226:227], v240 offset:1280\n"
    "v_max3_u32 v243, v243, v172, v173\nv_max3_u32 v244, v244, v174, v175\nv_max_u32 v243, v243, v244\nv_cmp_eq_u32 vcc, -1, v243\n"
    "v_mfma_f32_32x32x16_bf16 v[96:111], v[228:231], v[156:159], v[96:111]\nds_read_b64_tr_b16 v[228:229], v240 offset:4096\nds_read_b64_tr_b16 v[230:231], v240 offset:4352\n"
    "s_cbranch_vccnz .Lx155\n.Lx156:\nglobal_load_dword v112, v56, s[4:5] offset:0 sc1\nglobal_load_dword v113, v56, s[4:5] offset:256 sc1\ns_waitcnt lgkmcnt(6)\n"
    "v_mfma_f32_32x32x16_bf16 v[64:79], v[208:211], v[160:163], v[64:79]\nds_read_b64_tr_b16 v[208:209], v240 offset:7168\nds_read_b64_tr_b16 v[210:211], v240 offset:7424\n"
    "global_load_dword v114, v56, s[4:5] offset:512 sc1\nglobal_load_dword v115, v56, s[4:5] offset:768 sc1\n"
    "v_mfma_f32_32x32x16_bf16 v[64:79], v[212:215], v[164:167], v[64:79]\nds_read_b64_tr_b16 v[212:213], v240 offset:10240\nds_read_b64_tr_b16 v[214:215], v240 offset:10496\n"
    "global_load_dword v116, v56, s[4:5] offset:1024 sc1\nglobal_load_dword v117, v56, s[4:5] offset:1280 sc1\n"
    "v_mfma_f32_32x32x16_bf16 v[64:79], v[216:219], v[168:171], v[64:79]\nds_read_b64_tr_b16 v[216:217], v240 offset:2048\nds_read_b64_tr_b16 v[218:219], v240 offset:2304\n"
    "v_mad_u32_u24 v242, s50, 3, v36\nglobal_load_dword v118, v56, s[4:5] offset:1536 sc1\nglobal_load_dword v119, v56, s[4:5] offset:1792 sc1\ns_waitcnt lgkmcnt(6)\n"
    "v_mfma_f32_32x32x16_bf16 v[64:79], v[220:223], v[172:175], v[64:79]\nds_read_b64_tr_b16 v[220:221], v240 offset:5120\nds_read_b64_tr_b16 v[222:223], v240 offset:5376\n"
    "s_waitcnt vmcnt(24)\nv_max3_u32 v244, v176, v177, v178\nv_max3_u32 v243, v179, v180, v181\nv_mfma_f32_32x32x16_bf16 v[80:95], v[224:227], v[160:163], v[80:95]\n"
    "ds_read_b64_tr_b16 v[224:225], v240 offset:8192\nds_read_b64_tr_b16 v[226:227], v240 offset:8448\nv_max3_u32 v244, v244, v182, v183\nv_max3_u32 v243, v243, v184, v185\n"
    "v_max3_u32 v244, v244, v186, v187\nv_mfma_f32_32x32x16_bf16 v[80:95], v[228:231], v[164:167], v[80:95]\nds_read_b64_tr_b16 v[228:229], v240 offset:11264\n"
    "ds_read_b64_tr_b16 v[230:231], v240 offset:11520\nv_max3_u32 v243, v243, v188, v189\nv_max3_u32 v244, v244, v190, v191\nv_max_u32 v243, v243, v244\n"
    "v_cmp_eq_u32 vcc, -1, v243\ns_waitcnt lgkmcnt(6)\nv_mfma_f32_32x32x16_bf16 v[80:95], v[208:211], v[168:171], v[80:95]\nds_read_b64_tr_b16 v[208:209], v242 offset:0\n"
    "ds_read_b64_tr_b16 v[210:211], v242 offset:256\ns_cbranch_vccnz .Lx157\n.Lx158:\nv_mfma_f32_32x32x16_bf16 v[80:95], v[212:215], v[172:175], v[80:95]\n"
    "ds_read_b64_tr_b16 v[212:213], v242 offset:3072\nds_read_b64_tr_b16 v[214:215], v242 offset:3328\nv_mad_u32_u24 v240, s51, 3, v36\n"
    "v_mfma_f32_32x32x16_bf16 v[96:111], v[216:219], v[160:163], v[96:111]\nds_read_b64_tr_b16 v[216:217], v242 offset:6144\nds_read_b64_tr_b16 v[218:219], v242 offset:6400\n"
    "s_waitcnt vmcnt(8)\nv_max3_u32 v244, v192, v193, v194\nv_max3_u32 v243, v195, v196, v197\ns_waitcnt lgkmcnt(6)\n"
    "v_mfma_f32_32x32x16_bf16 v[96:111], v[220:223], v[164:167], v[96:111]\nds_read_b64_tr_b16 v[220:221], v242 offset:9216\nds_read_b64_tr_b16 v[222:223], v242 offset:9472\n"
    "v_max3_u32 v244, v244, v198, v199\nv_max3_u32 v243, v243, v200, v201\nv_max3_u32 v244, v244, v202, v203\n"
    "v_mfma_f32_32x32x16_bf16 v[96:111], v[224:227], v[168:171], v[96:111]\nds_read_b64_tr_b16 v[224:225], v240 offset:0\nds_read_b64_tr_b16 v[226:227], v240 offset:256\n"
    "v_max3_u32 v243, v243, v204, v205\nv_max3_u32 v244, v244, v206, v207\nv_max_u32 v243, v243, v244\nv_cmp_eq_u32 vcc, -1, v243\n"
    "v_mfma_f32_32x32x16_bf16 v[96:111], v[228:231], v[172:175], v[96:111]\nds_read_b64_tr_b16 v[228:229], v240 offset:3072\nds_read_b64_tr_b16 v[230:231], v240 offset:3328\n"
    "s_cbranch_vccnz .Lx159\n.Lx160:\ns_waitcnt lgkmcnt(6)\nv_mfma_f32_32x32x16_bf16 v[64:79], v[208:211], v[176:179], v[64:79]\n"
    "ds_read_b64_tr_b16 v[208:209], v240 offset:6144\nds_read_b64_tr_b16 v[210:211], v240 offset:6400\nv_mfma_f32_32x32x16_bf16 v[64:79], v[212:215], v[180:183], v[64:79]\n"
    "ds_read_b64_tr_b16 v[212:213], v240 offset:9216\nds_read_b64_tr_b16 v[214:215], v240 offset:9472\ns_waitcnt vmcnt(0)\nv_add_f32 v138, v112, v113\n"
    "v_add_f32 v139, v114, v115\nv_add_f32 v244, v116, v117\nv_mfma_f32_32x32x16_bf16 v[64:79], v[216:219], v[184:187], v[64:79]\n"
    "ds_read_b64_tr_b16 v[216:217], v242 offset:1024\nds_read_b64_tr_b16 v[218:219], v242 offset:1280\nv_add_f32 v243, v118, v119\nv_add_f32 v138, v138, v139\n"
    "v_add_f32 v244, v244, v243\nv_add_f32 v139, v138, v244\ns_waitcnt lgkmcnt(6)\nv_mfma_f32_32x32x16_bf16 v[64:79], v[220:223], v[188:191], v[64:79]\n"
    "ds_read_b64_tr_b16 v[220:221], v242 offset:4096\nds_read_b64_tr_b16 v[222:223], v242 offset:4352\nv_add_f32 v138, v138, v244\nv_cmp_u_f32 vcc, v138, v138\n"
    "v_mfma_f32_32x32x16_bf16 v[64:79], v[224:227], v[192:195], v[64:79]\nds_read_b64_tr_b16 v[224:225], v242 offset:7168\nds_read_b64_tr_b16 v[226:227], v242 offset:7424\n"
    "s_cbranch_vccnz .Lp145\n.Lp146:\nv_permlane32_swap_b32 v138, v139\nv_mul_f32 v138, s81, v138\nv_mul_f32 v112, v138, v138\n"
    "v_mfma_f32_32x32x16_bf16 v[64:79], v[228:231], v[196:199], v[64:79]\nds_read_b64_tr_b16 v[228:229], v242 offset:10240\nds_read_b64_tr_b16 v[230:231], v242 offset:10496\n"
    "v_cvt_pk_bf16_f32 v138, -v138, -v138\nv_fma_f32 v139, v139, s81, -v112\nv_cndmask_b32 v120, 0, v138, s[84:85]\ns_waitcnt lgkmcnt(6)\ns_cbranch_execz .Lhop\n"
    "v_mfma_f32_32x32x16_bf16 v[64:79], v[208:211], v[200:203], v[64:79]\nds_read_b64_tr_b16 v[208:209], v240 offset:1024\nds_read_b64_tr_b16 v[210:211], v240 offset:1280\n"
    "v_max_f32 v139, 0, v139\nv_cvt_pk_bf16_f32 v139, 0, v139\nv_add_f32 v139, 0x3727c5ac, v139\nv_mfma_f32_32x32x16_bf16 v[64:79], v[212:215], v[204:207], v[64:79]\n"
    "ds_read_b64_tr_b16 v[212:213], v240 offset:4096\nds_read_b64_tr_b16 v[214:215], v240 offset:4352\nv_mfma_f32_32x32x16_bf16 v[64:79], v[124:127], v[120:123], v[64:79]\n"
    "v_cvt_pk_bf16_f32 v139, 0, v139\nv_sqrt_f32 v139, v139\nv_mov_b64 v[232:233], 0\nv_mov_b64 v[234:235], 0\nv_cvt_pk_bf16_f32 v139, 0, v139\nv_rcp_f32 v136, v139\n"
    "v_mfma_f32_32x32x16_bf16 v[80:95], v[128:131], v[120:123], v[80:95]\nv_mov_b32 v137, v136\nv_mov_b64 v[236:237], 0\nv_mov_b64 v[238:239], 0\nv_mov_b64 v[244:245], 0\n"
    "v_mov_b64 v[246:247], 0\nv_mfma_f32_32x32x16_bf16 v[80:95], v[216:219], v[176:179], v[80:95]\nds_read_b64_tr_b16 v[216:217], v240 offset:7168\n"
    "ds_read_b64_tr_b16 v[218:219], v240 offset:7424\nv_mul_f32 v64, v64, v136\nv_mul_f32 v65, v65, v136\nv_mul_f32 v66, v66, v136\ns_waitcnt lgkmcnt(6)\n"
    "v_mfma_f32_32x32x16_bf16 v[80:95], v[220:223], v[180:183], v[80:95]\nds_read_b64_tr_b16 v[220:221], v240 offset:10240\nds_read_b64_tr_b16 v[222:223], v240 offset:10496\n"
    "v_mul_f32 v67, v67, v136\nv_cvt_pk_bf16_f32 v64, v64, v65\nv_cvt_pk_bf16_f32 v65, v66, v67\nv_mfma_f32_32x32x16_bf16 v[80:95], v[224:227], v[184:187], v[80:95]\n"
    "ds_read_b64_tr_b16 v[224:225], v242 offset:2048\nds_read_b64_tr_b16 v[226:227], v242 offset:2304\nds_write_b64 v37, v[64:65] offset:0\nv_mul_f32 v68, v68, v136\n"
    "v_mul_f32 v69, v69, v136\nv_mfma_f32_32x32x16_bf16 v[80:95], v[228:231], v[188:191], v[80:95]\nds_read_b64_tr_b16 v[228:229], v242 offset:5120\n"
    "ds_read_b64_tr_b16 v[230:231], v242 offset:5376\nv_mul_f32 v70, v70, v136\nv_mul_f32 v71, v71, v136\nv_cvt_pk_bf16_f32 v68, v68, v69\ns_waitcnt lgkmcnt(7)\n"
    "v_mfma_f32_32x32x16_bf16 v[80:95], v[208:211], v[192:195], v[80:95]\nds_read_b64_tr_b16 v[208:209], v242 offset:8192\nds_read_b64_tr_b16 v[210:211], v242 offset:8448\n"
    "v_cvt_pk_bf16_f32 v69, v70, v71\nds_write_b64 v37, v[68:69] offset:16\nv_mul_f32 v72, v72, v136\nv_mfma_f32_32x32x16_bf16 v[80:95], v[212:215], v[196:199], v[80:95]\n"
    "ds_read_b64_tr_b16 v[212:213], v242 offset:11264\nds_read_b64_tr_b16 v[214:215], v242 offset:11520\nv_mul_f32 v73, v73, v136\nv_mul_f32 v74, v74, v136\n"
    "v_mul_f32 v75, v75, v136\nv_mfma_f32_32x32x16_bf16 v[80:95], v[216:219], v[200:203], v[80:95]\nds_read_b64_tr_b16 v[216:217], v240 offset:2048\n"
    "ds_read_b64_tr_b16 v[218:219], v240 offset:2304\nv_cvt_pk_bf16_f32 v72, v72, v73\nv_cvt_pk_bf16_f32 v73, v74, v75\nds_write_b64 v37, v[72:73] offset:32\n"
    "s_waitcnt lgkmcnt(8)\nv_mfma_f32_32x32x16_bf16 v[80:95], v[220:223], v[204:207], v[80:95]\nds_read_b64_tr_b16 v[220:221], v240 offset:5120\n"
    "ds_read_b64_tr_b16 v[222:223], v240 offset:5376\nv_mul_f32 v76, v76, v136\nv_mul_f32 v77, v77, v136\nv_mul_f32 v78, v78, v136\n"
    "v_mfma_f32_32x32x16_bf16 v[96:111], v[224:227], v[176:179], v[96:111]\nds_read_b64_tr_b16 v[224:225], v240 offset:8192\nds_read_b64_tr_b16 v[226:227], v240 offset:8448\n"
    "v_mul_f32 v79, v79, v136\nv_cvt_pk_bf16_f32 v76, v76, v77\nv_cvt_pk_bf16_f32 v77, v78, v79\nv_mfma_f32_32x32x16_bf16 v[96:111], v[132:135], v[120:123], v[96:111]\n"
    "ds_write_b64 v37, v[76:77] offset:48\nv_mfma_f32_32x32x16_bf16 v[96:111], v[228:231], v[180:183], v[96:111]\nds_read_b64_tr_b16 v[228:229], v240 offset:11264\n"
    "ds_read_b64_tr_b16 v[230:231], v240 offset:11520\nv_mul_f32 v80, v80, v136\nv_mul_f32 v81, v81, v136\nv_mul_f32 v82, v82, v136\nv_mul_f32 v83, v83, v136\n"
    "v_cvt_pk_bf16_f32 v80, v80, v81\ns_waitcnt lgkmcnt(8)\nv_mfma_f32_32x32x16_bf16 v[96:111], v[208:211], v[184:187], v[96:111]\nv_cvt_pk_bf16_f32 v81, v82, v83\n"
    "ds_write_b64 v37, v[80:81] offset:64\nv_mul_f32 v84, v84, v136\nv_mul_f32 v85, v85, v136\nv_mul_f32 v86, v86, v136\n"
    "v_mfma_f32_32x32x16_bf16 v[96:111], v[212:215], v[188:191], v[96:111]\nv_mul_f32 v87, v87, v136\nv_cvt_pk_bf16_f32 v84, v84, v85\nv_cvt_pk_bf16_f32 v85, v86, v87\n"
    "ds_write_b64 v37, v[84:85] offset:80\nv_mul_f32 v88, v88, v136\nv_mfma_f32_32x32x16_bf16 v[96:111], v[216:219], v[192:195], v[96:111]\nv_mul_f32 v89, v89, v136\n"
    "v_mul_f32 v90, v90, v136\nv_mul_f32 v91, v91, v136\nv_cvt_pk_bf16_f32 v88, v88, v89\nv_cvt_pk_bf16_f32 v89, v90, v91\ns_waitcnt lgkmcnt(2)\n"
    "v_mfma_f32_32x32x16_bf16 v[96:111], v[220:223], v[196:199], v[96:111]\nds_write_b64 v37, v[88:89] offset:96\nv_mul_f32 v92, v92, v136\nv_mul_f32 v93, v93, v136\n"
    "v_mul_f32 v94, v94, v136\nv_mul_f32 v95, v95, v136\nv_mfma_f32_32x32x16_bf16 v[96:111], v[224:227], v[200:203], v[96:111]\nv_cvt_pk_bf16_f32 v92, v92, v93\n"
    "v_cvt_pk_bf16_f32 v93, v94, v95\nds_write_b64 v37, v[92:93] offset:112\nv_mfma_f32_32x32x16_bf16 v[96:111], v[228:231], v[204:207], v[96:111]\nv_mov_b64 v[248:249], 0\n"
    "v_mov_b64 v[250:251], 0\ns_mov_b32 s44, 0x00ff00ff\ns_mov_b32 s45, 0xff00ff00\ns_mov_b32 s46, s44\ns_mov_b32 s47, s44\ns_nop 5\n"
    "v_pk_mul_f32 v[96:97], v[96:97], v[136:137]\nv_pk_mul_f32 v[98:99], v[98:99], v[136:137]\nv_cvt_pk_bf16_f32 v96, v96, v97\nv_cvt_pk_bf16_f32 v97, v98, v99\n"
    "ds_write_b64 v37, v[96:97] offset:128\nv_pk_mul_f32 v[100:101], v[100:101], v[136:137]\nv_pk_mul_f32 v[102:103], v[102:103], v[136:137]\n"
    "v_cvt_pk_bf16_f32 v100, v100, v101\nv_cvt_pk_bf16_f32 v101, v102, v103\nds_write_b64 v37, v[100:101] offset:144\nv_pk_mul_f32 v[104:105], v[104:105], v[136:137]\n"
    "v_pk_mul_f32 v[106:107], v[106:107], v[136:137]\nv_cvt_pk_bf16_f32 v104, v104, v105\nv_cvt_pk_bf16_f32 v105, v106, v107\nds_write_b64 v37, v[104:105] offset:160\n"
    "v_pk_mul_f32 v[108:109], v[108:109], v[136:137]\nv_pk_mul_f32 v[110:111], v[110:111], v[136:137]\nv_cvt_pk_bf16_f32 v108, v108, v109\nv_cvt_pk_bf16_f32 v109, v110, v111\n"
    "ds_write_b64 v37, v[108:109] offset:176\ns_branch .Latt12\n"
    ".La67:\ns_waitcnt vmcnt(0)\ns_branch .La69\n.Lw87:\ns_sub_u32 s80, s80, 1\ns_cbranch_scc1 .Lw87x\ns_mov_b64 exec, vcc\n"
    "global_load_dwordx4 v[208:211], v60, s[4:5] offset:-4096 sc1\ns_mov_b64 exec, -1\ns_waitcnt vmcnt(0)\ns_branch .Lr86\n.Lw89:\ns_sub_u32 s80, s80, 1\n"
    "s_cbranch_scc1 .Lw89x\ns_cbranch_execz .Lhop\ns_mov_b64 exec, vcc\nglobal_load_dwordx4 v[212:215], v60, s[4:5] offset:0 sc1\ns_mov_b64 exec, -1\ns_waitcnt vmcnt(0)\n"
    "s_branch .Lr88\n.Lw91:\ns_sub_u32 s80, s80, 1\ns_cbranch_scc1 .Lw91x\ns_mov_b64 exec, vcc\nglobal_load_dwordx4 v[216:219], v61, s[4:5] offset:-4096 sc1\n"
    "global_load_dwordx4 v[220:223], v61, s[4:5] offset:0 sc1\ns_mov_b64 exec, -1\ns_waitcnt vmcnt(0)\ns_branch .Lr90\n.Lw93:\ns_sub_u32 s80, s80, 1\ns_cbranch_scc1 .Lw93x\n"
    "s_mov_b64 exec, vcc\nglobal_load_dwordx4 v[224:227], v62, s[4:5] offset:-4096 sc1\nglobal_load_dwordx4 v[228:231], v62, s[4:5] offset:0 sc1\ns_mov_b64 exec, -1\n"
    "s_waitcnt vmcnt(0)\ns_branch .Lr92\n.Lw95:\ns_sub_u32 s80, s80, 1\ns_cbranch_scc1 .Lw95x\ns_mov_b64 exec, vcc\n"
    "global_load_dwordx4 v[232:235], v63, s[4:5] offset:-4096 sc1\nglobal_load_dwordx4 v[236:239], v63, s[4:5] offset:0 sc1\ns_mov_b64 exec, -1\ns_waitcnt vmcnt(0)\n"
    "s_branch .Lr94\n.Lh103:\ns_cmp_lg_u32 s13, 0\ns_cbranch_scc1 .Lh106\ns_mov_b32 s80, 0x10000\nv_mov_b32 v225, 18\n.Lh105:\n"
    "global_load_dwordx4 v[252:255], v57, s[4:5] sc1\ns_waitcnt vmcnt(0)\nv_xor_b32 v252, s11, v252\nv_xor_b32 v253, s11, v253\nv_xor_b32 v254, s11, v254\n"
    "v_xor_b32 v255, s11, v255\nv_or3_b32 v226, v252, v253, v254\nv_or_b32 v226, v226, v255\nv_cmp_ne_u32 vcc, 0, v226\ns_cbranch_vccz .Lh107\ns_sub_u32 s80, s80, 1\n"
    "s_cbranch_scc1 .Lh107\ns_sleep 4\ns_branch .Lh105\n.Lh107:\ns_mov_b64 exec, 1\nds_write_b32 v56, v225\ns_mov_b64 exec, -1\n.Lh106:\ns_mov_b32 s86, 0x100000\n.Ls108:\n"
    "ds_read_b32 v226, v56 offset:0\ns_waitcnt lgkmcnt(0)\nv_cmp_gt_i32 vcc, 18, v226\ns_sub_u32 s86, s86, 1\ns_cbranch_scc1 .Ls109\ns_cbranch_vccz .Ls109\ns_sleep 1\n"
    "s_branch .Ls108\n.Ls109:\nglobal_load_dwordx4 v[64:67], v59, s[68:69] offset:-4096 sc1\nglobal_load_dwordx4 v[68:71], v59, s[68:69] offset:-3072 sc1\n"
    "global_load_dwordx4 v[72:75], v59, s[68:69] offset:-2048 sc1\nglobal_load_dwordx4 v[76:79], v59, s[68:69] offset:-1024 sc1\n"
    "global_load_dwordx4 v[80:83], v59, s[68:69] offset:0 sc1\nglobal_load_dwordx4 v[84:87], v59, s[68:69] offset:1024 sc1\n"
    "global_load_dwordx4 v[88:91], v59, s[68:69] offset:2048 sc1\nglobal_load_dwordx4 v[92:95], v59, s[68:69] offset:3072 sc1\n"
    "global_load_dwordx4 v[96:99], v59, s[70:71] offset:-4096 sc1\nglobal_load_dwordx4 v[100:103], v59, s[70:71] offset:-3072 sc1\n"
    "global_load_dwordx4 v[104:107], v59, s[70:71] offset:-2048 sc1\nglobal_load_dwordx4 v[108:111], v59, s[70:71] offset:-1024 sc1\n"
    "global_load_dwordx4 v[112:115], v59, s[70:71] offset:0 sc1\nglobal_load_dwordx4 v[116:119], v59, s[70:71] offset:1024 sc1\n"
    "global_load_dwordx4 v[120:123], v59, s[70:71] offset:2048 sc1\nglobal_load_dwordx4 v[124:127], v59, s[70:71] offset:3072 sc1\ns_branch .Lh104\n.Lw110:\n"
    "s_sub_u32 s80, s80, 1\ns_cbranch_scc1 .Lhwo78\nglobal_load_dword v240, v16, s[4:5] offset:0 sc1\nglobal_load_dword v241, v16, s[4:5] offset:128 sc1\n"
    "global_load_dword v242, v16, s[4:5] offset:512 sc1\nglobal_load_dword v243, v16, s[4:5] offset:640 sc1\nglobal_load_dword v244, v16, s[4:5] offset:1024 sc1\n"
    "global_load_dword v245, v16, s[4:5] offset:1152 sc1\nglobal_load_dword v246, v16, s[4:5] offset:1536 sc1\nglobal_load_dword v247, v16, s[4:5] offset:1664 sc1\n"
    "s_waitcnt vmcnt(0)\nv_max3_u32 v232, v240, v241, v242\nv_max3_u32 v233, v243, v244, v245\nv_max3_u32 v232, v232, v246, v247\nv_max_u32 v232, v232, v233\n"
    "v_cmp_eq_u32 vcc, -1, v232\ns_cbranch_vccnz .Lw110\ns_branch .Lhwo78\n.Llw120:\nv_max3_u32 v232, v208, v209, v210\nv_max3_u32 v232, v232, v211, v212\n"
    "v_max3_u32 v232, v232, v213, v214\nv_max3_u32 v232, v232, v215, v216\nv_max3_u32 v232, v232, v217, v218\nv_max3_u32 v232, v232, v219, v220\n"
    "v_max3_u32 v232, v232, v221, v222\nv_max_u32 v232, v232, v223\nv_cmp_eq_u32 vcc, -1, v232\ns_or_b64 vcc, vcc, s[50:51]\ns_cbranch_scc0 .Llx121\ns_sub_u32 s80, s80, 1\n"
    "s_cbranch_scc1 .Llx121\ns_mov_b64 exec, vcc\nglobal_load_dwordx4 v[240:243], v17, s[4:5] sc1\nglobal_load_dwordx4 v[208:211], v16, s[68:69] sc1\n"
    "global_load_dwordx4 v[212:215], v16, s[70:71] sc1\nglobal_load_dwordx4 v[216:219], v16, s[72:73] sc1\nglobal_load_dwordx4 v[220:223], v16, s[74:75] sc1\n"
    "s_mov_b64 exec, -1\ns_branch .Ln117\n.Lw134:\ns_mov_b32 s86, 0x100000\n.Ls136:\nds_read_b32 v39, v24 offset:0\ns_waitcnt lgkmcnt(0)\nv_cmp_gt_i32 vcc, v26, v39\n"
    "s_sub_u32 s86, s86, 1\ns_cbranch_scc1 .Ls137\ns_cbranch_vccz .Ls137\ns_sleep 1\ns_branch .Ls136\n.Ls137:\ns_branch .Lr135\n.Lq139:\nv_max3_u32 v52, v192, v193, v194\n"
    "v_max3_u32 v52, v52, v195, v196\nv_max3_u32 v52, v52, v197, v198\nv_max3_u32 v52, v52, v199, v200\nv_max3_u32 v52, v52, v201, v202\nv_max3_u32 v52, v52, v203, v204\n"
    "v_max3_u32 v52, v52, v205, v206\nv_max_u32 v52, v52, v207\nv_cmp_eq_u32 vcc, -1, v52\ns_cbranch_vccz .Lq139x\ns_sub_u32 s80, s80, 1\ns_cbranch_scc1 .Lq139x\ns_sleep 2\n"
    "s_waitcnt lgkmcnt(0)\ns_branch .Lq138\n.Ld143:\nv_max3_u32 v244, v144, v145, v146\nv_max3_u32 v243, v147, v148, v149\nv_max3_u32 v244, v244, v150, v151\n"
    "v_max3_u32 v243, v243, v152, v153\nv_max3_u32 v244, v244, v154, v155\nv_max3_u32 v243, v243, v156, v157\nv_max3_u32 v244, v244, v158, v159\nv_max_u32 v243, v243, v244\n"
    "v_cmp_eq_u32 vcc, -1, v243\ns_cbranch_vccz .Ld144\ns_sub_u32 s80, s80, 1\ns_cbranch_scc1 .Ld144\ns_sleep 2\nglobal_load_dword v144, v39, s[52:53] offset:-2048 sc1\n"
    "global_load_dword v145, v39, s[52:53] offset:-1920 sc1\nglobal_load_dword v146, v39, s[52:53] offset:-1792 sc1\nglobal_load_dword v147, v39, s[52:53] offset:-1664 sc1\n"
    "global_load_dword v148, v39, s[52:53] offset:-1024 sc1\nglobal_load_dword v149, v39, s[52:53] offset:-896 sc1\nglobal_load_dword v150, v39, s[52:53] offset:-768 sc1\n"
    "global_load_dword v151, v39, s[52:53] offset:-640 sc1\nglobal_load_dword v152, v39, s[52:53] offset:0 sc1\nglobal_load_dword v153, v39, s[52:53] offset:128 sc1\n"
    "global_load_dword v154, v39, s[52:53] offset:256 sc1\nglobal_load_dword v155, v39, s[52:53] offset:384 sc1\nglobal_load_dword v156, v39, s[52:53] offset:1024 sc1\n"
    "global_load_dword v157, v39, s[52:53] offset:1152 sc1\nglobal_load_dword v158, v39, s[52:53] offset:1280 sc1\nglobal_load_dword v159, v39, s[52:53] offset:1408 sc1\n"
    "s_waitcnt vmcnt(0)\ns_branch .Ld142\ns_cbranch_execz .Lhop\n.Lp145:\nv_max3_u32 v243, v112, v113, v114\nv_max3_u32 v243, v243, v115, v116\n"
    "v_max3_u32 v243, v243, v117, v118\nv_max_u32 v243, v243, v119\nv_cmp_eq_u32 vcc, -1, v243\ns_cbranch_vccz .Lp146\ns_sub_u32 s80, s80, 1\ns_cbranch_scc1 .Lp146\n"
    "global_load_dword v112, v56, s[4:5] offset:0 sc1\nglobal_load_dword v113, v56, s[4:5] offset:256 sc1\nglobal_load_dword v114, v56, s[4:5] offset:512 sc1\n"
    "global_load_dword v115, v56, s[4:5] offset:768 sc1\nglobal_load_dword v116, v56, s[4:5] offset:1024 sc1\nglobal_load_dword v117, v56, s[4:5] offset:1280 sc1\n"
    "global_load_dword v118, v56, s[4:5] offset:1536 sc1\nglobal_load_dword v119, v56, s[4:5] offset:1792 sc1\ns_waitcnt vmcnt(0)\nv_add_f32 v138, v112, v113\n"
    "v_add_f32 v139, v114, v115\nv_add_f32 v244, v116, v117\nv_add_f32 v243, v118, v119\nv_add_f32 v138, v138, v139\nv_add_f32 v244, v244, v243\nv_add_f32 v139, v138, v244\n"
    "v_add_f32 v138, v138, v244\nv_cmp_u_f32 vcc, v138, v138\ns_nop 0\ns_branch .Lp145\n.Lx147:\ns_sub_u32 s80, s80, 1\ns_cbranch_scc1 .Lx148\ns_sleep 2\n"
    "global_load_dword v160, v39, s[54:55] offset:-2048 sc1\nglobal_load_dword v161, v39, s[54:55] offset:-1920 sc1\nglobal_load_dword v162, v39, s[54:55] offset:-1792 sc1\n"
    "global_load_dword v163, v39, s[54:55] offset:-1664 sc1\nglobal_load_dword v164, v39, s[54:55] offset:-1024 sc1\nglobal_load_dword v165, v39, s[54:55] offset:-896 sc1\n"
    "global_load_dword v166, v39, s[54:55] offset:-768 sc1\nglobal_load_dword v167, v39, s[54:55] offset:-640 sc1\nglobal_load_dword v168, v39, s[54:55] offset:0 sc1\n"
    "global_load_dword v169, v39, s[54:55] offset:128 sc1\nglobal_load_dword v170, v39, s[54:55] offset:256 sc1\nglobal_load_dword v171, v39, s[54:55] offset:384 sc1\n"
    "global_load_dword v172, v39, s[54:55] offset:1024 sc1\nglobal_load_dword v173, v39, s[54:55] offset:1152 sc1\nglobal_load_dword v174, v39, s[54:55] offset:1280 sc1\n"
    "global_load_dword v175, v39, s[54:55] offset:1408 sc1\ns_waitcnt vmcnt(0)\nv_max3_u32 v244, v160, v161, v162\nv_max3_u32 v243, v163, v164, v165\n"
    "v_max3_u32 v244, v244, v166, v167\nv_max3_u32 v243, v243, v168, v169\nv_max3_u32 v244, v244, v170, v171\nv_max3_u32 v243, v243, v172, v173\n"
    "v_max3_u32 v244, v244, v174, v175\nv_max_u32 v243, v243, v244\nv_cmp_eq_u32 vcc, -1, v243\ns_cbranch_vccnz .Lx147\ns_branch .Lx148\n.Lx149:\ns_sub_u32 s80, s80, 1\n"
    "s_cbranch_scc1 .Lx150\ns_sleep 2\nglobal_load_dword v176, v39, s[56:57] offset:-2048 sc1\nglobal_load_dword v177, v39, s[56:57] offset:-1920 sc1\n"
    "global_load_dword v178, v39, s[56:57] offset:-1792 sc1\nglobal_load_dword v179, v39, s[56:57] offset:-1664 sc1\nglobal_load_dword v180, v39, s[56:57] offset:-1024 sc1\n"
    "global_load_dword v181, v39, s[56:57] offset:-896 sc1\nglobal_load_dword v182, v39, s[56:57] offset:-768 sc1\nglobal_load_dword v183, v39, s[56:57] offset:-640 sc1\n"
    "global_load_dword v184, v39, s[56:57] offset:0 sc1\nglobal_load_dword v185, v39, s[56:57] offset:128 sc1\nglobal_load_dword v186, v39, s[56:57] offset:256 sc1\n"
    "global_load_dword v187, v39, s[56:57] offset:384 sc1\nglobal_load_dword v188, v39, s[56:57] offset:1024 sc1\nglobal_load_dword v189, v39, s[56:57] offset:1152 sc1\n"
    "global_load_dword v190, v39, s[56:57] offset:1280 sc1\nglobal_load_dword v191, v39, s[56:57] offset:1408 sc1\ns_waitcnt vmcnt(0)\nv_max3_u32 v244, v176, v177, v178\n"
    "v_max3_u32 v243, v179, v180, v181\nv_max3_u32 v244, v244, v182, v183\nv_max3_u32 v243, v243, v184, v185\nv_max3_u32 v244, v244, v186, v187\n"
    "v_max3_u32 v243, v243, v188, v189\nv_max3_u32 v244, v244, v190, v191\nv_max_u32 v243, v243, v244\nv_cmp_eq_u32 vcc, -1, v243\ns_cbranch_vccnz .Lx149\ns_branch .Lx150\n"
    ".Lx151:\ns_sub_u32 s80, s80, 1\ns_cbranch_scc1 .Lx152\ns_sleep 2\nglobal_load_dword v192, v39, s[58:59] offset:-2048 sc1\n"
    "global_load_dword v193, v39, s[58:59] offset:-1920 sc1\nglobal_load_dword v194, v39, s[58:59] offset:-1792 sc1\nglobal_load_dword v195, v39, s[58:59] offset:-1664 sc1\n"
    "global_load_dword v196, v39, s[58:59] offset:-1024 sc1\nglobal_load_dword v197, v39, s[58:59] offset:-896 sc1\nglobal_load_dword v198, v39, s[58:59] offset:-768 sc1\n"
    "global_load_dword v199, v39, s[58:59] offset:-640 sc1\nglobal_load_dword v200, v39, s[58:59] offset:0 sc1\nglobal_load_dword v201, v39, s[58:59] offset:128 sc1\n"
    "global_load_dword v202, v39, s[58:59] offset:256 sc1\nglobal_load_dword v203, v39, s[58:59] offset:384 sc1\nglobal_load_dword v204, v39, s[58:59] offset:1024 sc1\n"
    "global_load_dword v205, v39, s[58:59] offset:1152 sc1\nglobal_load_dword v206, v39, s[58:59] offset:1280 sc1\nglobal_load_dword v207, v39, s[58:59] offset:1408 sc1\n"
    "s_waitcnt vmcnt(0)\nv_max3_u32 v244, v192, v193, v194\nv_max3_u32 v243, v195, v196, v197\nv_max3_u32 v244, v244, v198, v199\nv_max3_u32 v243, v243, v200, v201\n"
    "v_max3_u32 v244, v244, v202, v203\nv_max3_u32 v243, v243, v204, v205\nv_max3_u32 v244, v244, v206, v207\nv_max_u32 v243, v243, v244\nv_cmp_eq_u32 vcc, -1, v243\n"
    "s_cbranch_vccnz .Lx151\ns_branch .Lx152\n.Lx153:\ns_sub_u32 s80, s80, 1\ns_cbranch_scc1 .Lx154\ns_sleep 2\nglobal_load_dword v144, v39, s[60:61] offset:-2048 sc1\n"
    "global_load_dword v145, v39, s[60:61] offset:-1920 sc1\nglobal_load_dword v146, v39, s[60:61] offset:-1792 sc1\nglobal_load_dword v147, v39, s[60:61] offset:-1664 sc1\n"
    "global_load_dword v148, v39, s[60:61] offset:-1024 sc1\nglobal_load_dword v149, v39, s[60:61] offset:-896 sc1\nglobal_load_dword v150, v39, s[60:61] offset:-768 sc1\n"
    "global_load_dword v151, v39, s[60:61] offset:-640 sc1\nglobal_load_dword v152, v39, s[60:61] offset:0 sc1\nglobal_load_dword v153, v39, s[60:61] offset:128 sc1\n"
    "global_load_dword v154, v39, s[60:61] offset:256 sc1\nglobal_load_dword v155, v39, s[60:61] offset:384 sc1\nglobal_load_dword v156, v39, s[60:61] offset:1024 sc1\n"
    "global_load_dword v157, v39, s[60:61] offset:1152 sc1\nglobal_load_dword v158, v39, s[60:61] offset:1280 sc1\nglobal_load_dword v159, v39, s[60:61] offset:1408 sc1\n"
    "s_waitcnt vmcnt(0)\nv_max3_u32 v244, v144, v145, v146\nv_max3_u32 v243, v147, v148, v149\nv_max3_u32 v244, v244, v150, v151\nv_max3_u32 v243, v243, v152, v153\n"
    "v_max3_u32 v244, v244, v154, v155\nv_max3_u32 v243, v243, v156, v157\nv_max3_u32 v244, v244, v158, v159\nv_max_u32 v243, v243, v244\nv_cmp_eq_u32 vcc, -1, v243\n"
    "s_cbranch_vccnz .Lx153\ns_branch .Lx154\ns_cbranch_execz .Lhop\n.Lx155:\ns_sub_u32 s80, s80, 1\ns_cbranch_scc1 .Lx156\ns_sleep 2\n"
    "global_load_dword v160, v39, s[62:63] offset:-2048 sc1\nglobal_load_dword v161, v39, s[62:63] offset:-1920 sc1\nglobal_load_dword v162, v39, s[62:63] offset:-1792 sc1\n"
    "global_load_dword v163, v39, s[62:63] offset:-1664 sc1\nglobal_load_dword v164, v39, s[62:63] offset:-1024 sc1\nglobal_load_dword v165, v39, s[62:63] offset:-896 sc1\n"
    "global_load_dword v166, v39, s[62:63] offset:-768 sc1\nglobal_load_dword v167, v39, s[62:63] offset:-640 sc1\nglobal_load_dword v168, v39, s[62:63] offset:0 sc1\n"
    "global_load_dword v169, v39, s[62:63] offset:128 sc1\nglobal_load_dword v170, v39, s[62:63] offset:256 sc1\nglobal_load_dword v171, v39, s[62:63] offset:384 sc1\n"
    "global_load_dword v172, v39, s[62:63] offset:1024 sc1\nglobal_load_dword v173, v39, s[62:63] offset:1152 sc1\nglobal_load_dword v174, v39, s[62:63] offset:1280 sc1\n"
    "global_load_dword v175, v39, s[62:63] offset:1408 sc1\ns_waitcnt vmcnt(0)\nv_max3_u32 v244, v160, v161, v162\nv_max3_u32 v243, v163, v164, v165\n"
    "v_max3_u32 v244, v244, v166, v167\nv_max3_u32 v243, v243, v168, v169\nv_max3_u32 v244, v244, v170, v171\nv_max3_u32 v243, v243, v172, v173\n"
    "v_max3_u32 v244, v244, v174, v175\nv_max_u32 v243, v243, v244\nv_cmp_eq_u32 vcc, -1, v243\ns_cbranch_vccnz .Lx155\ns_branch .Lx156\n.Lx157:\ns_sub_u32 s80, s80, 1\n"
    "s_cbranch_scc1 .Lx158\ns_sleep 2\nglobal_load_dword v176, v39, s[64:65] offset:-2048 sc1\nglobal_load_dword v177, v39, s[64:65] offset:-1920 sc1\n"
    "global_load_dword v178, v39, s[64:65] offset:-1792 sc1\nglobal_load_dword v179, v39, s[64:65] offset:-1664 sc1\nglobal_load_dword v180, v39, s[64:65] offset:-1024 sc1\n"
    "global_load_dword v181, v39, s[64:65] offset:-896 sc1\nglobal_load_dword v182, v39, s[64:65] offset:-768 sc1\nglobal_load_dword v183, v39, s[64:65] offset:-640 sc1\n"
    "global_load_dword v184, v39, s[64:65] offset:0 sc1\nglobal_load_dword v185, v39, s[64:65] offset:128 sc1\nglobal_load_dword v186, v39, s[64:65] offset:256 sc1\n"
    "global_load_dword v187, v39, s[64:65] offset:384 sc1\nglobal_load_dword v188, v39, s[64:65] offset:1024 sc1\nglobal_load_dword v189, v39, s[64:65] offset:1152 sc1\n"
    "global_load_dword v190, v39, s[64:65] offset:1280 sc1\nglobal_load_dword v191, v39, s[64:65] offset:1408 sc1\ns_waitcnt vmcnt(0)\nv_max3_u32 v244, v176, v177, v178\n"
    "v_max3_u32 v243, v179, v180, v181\nv_max3_u32 v244, v244, v182, v183\nv_max3_u32 v243, v243, v184, v185\nv_max3_u32 v244, v244, v186, v187\n"
    "v_max3_u32 v243, v243, v188, v189\nv_max3_u32 v244, v244, v190, v191\nv_max_u32 v243, v243, v244\nv_cmp_eq_u32 vcc, -1, v243\ns_cbranch_vccnz .Lx157\ns_branch .Lx158\n"
    ".Lx159:\ns_sub_u32 s80, s80, 1\ns_cbranch_scc1 .Lx160\ns_sleep 2\nglobal_load_dword v192, v39, s[66:67] offset:-2048 sc1\n"
    "global_load_dword v193, v39, s[66:67] offset:-1920 sc1\nglobal_load_dword v194, v39, s[66:67] offset:-1792 sc1\nglobal_load_dword v195, v39, s[66:67] offset:-1664 sc1\n"
    "global_load_dword v196, v39, s[66:67] offset:-1024 sc1\nglobal_load_dword v197, v39, s[66:67] offset:-896 sc1\nglobal_load_dword v198, v39, s[66:67] offset:-768 sc1\n"
    "global_load_dword v199, v39, s[66:67] offset:-640 sc1\nglobal_load_dword v200, v39, s[66:67] offset:0 sc1\nglobal_load_dword v201, v39, s[66:67] offset:128 sc1\n"
    "global_load_dword v202, v39, s[66:67] offset:256 sc1\nglobal_load_dword v203, v39, s[66:67] offset:384 sc1\nglobal_load_dword v204, v39, s[66:67] offset:1024 sc1\n"
    "global_load_dword v205, v39, s[66:67] offset:1152 sc1\nglobal_load_dword v206, v39, s[66:67] offset:1280 sc1\nglobal_load_dword v207, v39, s[66:67] offset:1408 sc1\n"
    "s_waitcnt vmcnt(0)\nv_max3_u32 v244, v192, v193, v194\nv_max3_u32 v243, v195, v196, v197\nv_max3_u32 v244, v244, v198, v199\nv_max3_u32 v243, v243, v200, v201\n"
    "v_max3_u32 v244, v244, v202, v203\nv_max3_u32 v243, v243, v204, v205\nv_max3_u32 v244, v244, v206, v207\nv_max_u32 v243, v243, v244\nv_cmp_eq_u32 vcc, -1, v243\n"
    "s_cbranch_vccnz .Lx159\ns_branch .Lx160\n.fill 93, 4, 0xBF800000\ns_cbranch_execz .Lhop\n.fill 255, 4, 0xBF800000\ns_cbranch_execz .Lhop\n.fill 255, 4, 0xBF800000\n"
    "s_cbranch_execz .Lhop\n.fill 255, 4, 0xBF800000\ns_cbranch_execz .Lhop\n.fill 255, 4, 0xBF800000\ns_cbranch_execz .Lhop\n.Lk_all_end:\n"
    :: "{s[0:1]}"(__builtin_amdgcn_kernarg_segment_ptr()), "{s2}"(blockIdx.x), "{v0}"(threadIdx.x), "{s3}"((uint32_t)(uintptr_t)(__attribute__((address_space(3))) char*)smem)
    : "v255", "s99", "vcc", "scc", "memory");
}

extern "C" void kernel_launch(void* const* d_in, const int* in_sizes, int n_in,
                              void* d_out, int out_size, void* d_ws, size_t ws_size,
                              hipStream_t stream) {
  Args a;
  a.x = (const u16*)d_in[0]; a.w = (const u16*)d_in[1]; a.wo = (const u16*)d_in[2];
  a.A_re = (const u16*)d_in[4]; a.A_im = (const u16*)d_in[5]; a.B_re = (const u16*)d_in[6]; a.B_im = (const u16*)d_in[7];
  a.C_re = (const u16*)d_in[8]; a.C_im = (const u16*)d_in[9]; a.log_dt = (const u16*)d_in[10]; a.Dskip = (const u16*)d_in[11];
  a.out = (u16*)d_out; a.ws = (char*)d_ws;
  k_all<<<256, 512, 0, stream>>>(a);
}
```

```cpp
#include <hip/hip_runtime.h>
#include <stdint.h>

typedef unsigned short u16;
constexpr int rnd_l(int v) { int e = 0; while ((v >> e) >= 256) ++e; if (!e) return v; const int h = 1 << (e - 1), q = v >> e, r = v & ((1 << e) - 1); return (q + ((r > h || (r == h && (q & 1))) ? 1 : 0)) << e; }
constexpr int slot_l(int v) { v = rnd_l(v); int e = 0; while ((v >> e) >= 256) ++e; return e * 128 + (v >> e); }
struct Tabs { u16 img[1032]; u16 prd[1024]; };
constexpr Tabs mk_tabs() {
  Tabs t{};
  for (int i = 0; i < 1032; ++i) {
    const int f = 1023 - i, o = f >> 3, a = (o - 74) / 12, m = f - 8 * (74 + 12 * a);
    int v = -1;
    if (f >= 32 && f - 32 <= 525) v = f - 32;
    if (o >= 74 && o < 122 && m < 64) v = a >= 2 ? 16 * (65 + m) - 8 * (a - 2) : (m < 62 ? 8 * (66 + m) + 4 * a : (m == 62 ? 1024 : -1));
    t.img[i] = (u16)(2 * (v < 0 ? 700 : slot_l(v)));
  }
  for (int w = 0; w < 1024; ++w) {
    const int tile = (w >> 9) * 2 + ((w >> 2) & 1), lane = (w >> 3) & 63;
    const int r = (lane >> 4) * 4 + (w & 3), C = (tile & 1) * 16 + (lane & 15);
    const int lr = 8 * C + (r < 4 ? r : (r == 4 ? 258 : 256 * (r - 4))) + 4 * (tile >> 1);
    const bool ok = r <= 12 && lr <= 2048 && rnd_l(lr) == lr;
    t.prd[w] = (u16)(2 * (ok ? slot_l(lr) : 704 + (lane & 63)));
  }
  return t;
}
extern "C" { extern __device__ __attribute__((aligned(16), used)) const Tabs g_tabs = mk_tabs(); }

struct Args { const u16 *x, *w, *wo, *A_re, *A_im, *B_re, *B_im, *C_re, *C_im, *log_dt, *Dskip; u16* out; char* ws; };

__global__ __launch_bounds__(512, 2) __attribute__((aligned(4096))) void k_all(const Args ar) {
  __shared__ __attribute__((aligned(128))) char smem[163840];
  asm volatile(
    ".Lk_all_beg:\ns_load_dwordx16 s[64:79], s[0:1], 0\ns_load_dwordx8 s[80:87], s[0:1], 0x40\ns_load_dwordx2 s[4:5], s[0:1], 0x60\nv_readfirstlane_b32 s3, v0\n"
    "s_lshr_b32 s25, s2, 3\ns_lshr_b32 s3, s3, 6\ns_cmp_lg_u32 s3, 4\ns_cbranch_scc1 .Loth1\ns_lshr_b32 s51, s2, 5\ns_and_b32 s60, s25, 3\nv_bfe_u32 v64, v0, 2, 4\n"
    "s_lshl1_add_u32 s50, s60, s51\nv_lshrrev_b32 v65, 4, v0\nv_mul_u32_u24 v64, 0xc00, v64\ns_and_b32 s50, s50, 7\nv_and_b32 v66, 2, v65\ns_and_b32 s61, s2, 1\n"
    "v_xor_b32 v66, v66, v0\ns_lshl3_add_u32 s51, s61, s51\nv_and_b32 v66, 3, v66\ns_lshl_b32 s51, s51, 6\nv_lshl_add_u32 v64, v66, 4, v64\ns_mul_i32 m0, s50, 12288\n"
    "v_add_u32 v64, s51, v64\ns_lshl_b32 s59, m0, 4\ns_mov_b32 s58, 3\ns_waitcnt lgkmcnt(0)\ns_add_u32 s52, s66, s59\n.Le5:\ns_addc_u32 s53, s67, 0\n"
    "global_load_lds_dwordx4 v64, s[52:53] offset:0\nglobal_load_lds_dwordx4 v64, s[52:53] offset:1024\nglobal_load_lds_dwordx4 v64, s[52:53] offset:2048\n"
    "s_add_u32 m0, m0, 3072\ns_add_u32 s59, s59, 49152\ns_sub_u32 s58, s58, 1\ns_cbranch_scc1 .Le5x\ns_add_u32 s52, s66, s59\ns_branch .Le5\n.Le5x:\n"
    "s_bfe_u32 s61, s2, 0x20001\nv_and_b32 v65, 3, v65\ns_lshl2_add_u32 s61, s61, s60\nv_xor_b32 v65, v65, v0\ns_lshl_b32 s61, s61, 17\nv_and_b32 v65, 7, v65\n"
    "s_add_u32 s48, s64, s61\nv_bfe_u32 v66, v0, 3, 3\ns_addc_u32 s49, s65, 0\nv_lshlrev_b32 v66, 10, v66\ns_lshl_b32 s59, s50, 7\nv_lshl_add_u32 v65, v65, 4, v66\n"
    "s_add_u32 s54, s48, s59\nv_xor_b32 v66, 64, v65\ns_addc_u32 s55, s49, 0\nv_add_u32 v67, 14336, v65\ns_mov_b32 m0, 98304\nv_add_u32 v68, 21504, v66\n"
    "v_add_u32 v66, 7168, v66\n.Le6:\nglobal_load_lds_dwordx4 v65, s[54:55] offset:0\nglobal_load_lds_dwordx4 v66, s[54:55] offset:1024\n"
    "global_load_lds_dwordx4 v67, s[54:55] offset:2048\nglobal_load_lds_dwordx4 v68, s[54:55] offset:3072\ns_add_u32 s54, s54, 32768\ns_addc_u32 s55, s55, 0\n"
    "s_add_u32 m0, m0, 12288\ns_cmp_lt_u32 m0, 147456\ns_cbranch_scc1 .Le6\ns_barrier\ns_branch .Lcom2\n.Lhop:\ns_sub_u32 s89, s89, 1\ns_cbranch_scc1 .Lhpx\n"
    "s_add_u32 s90, s90, 1024\ns_addc_u32 s91, s91, 0\ns_setpc_b64 s[90:91]\n.Lhpx:\ns_mov_b64 exec, -1\ns_setpc_b64 s[94:95]\n"
    ".Loth1:\ns_and_b32 s24, s2, 7\ns_lshl_b32 s44, s3, 2\ns_lshl_b32 s18, s24, 5\ns_add_u32 s18, s18, s25\ns_getpc_b64 s[92:93]\n.Lk_pc:\n"
    "s_sub_u32 s92, s92, .Lk_pc-.Lk_all_beg+4\ns_subb_u32 s93, s93, 0\nv_mov_b32 v70, 16777216\ns_cmp_gt_u32 s3, 3\ns_cbranch_scc1 .Loth3\nv_and_b32 v65, 63, v0\n"
    "s_lshl3_add_u32 s47, s44, s25\nv_lshl_add_u32 v64, v65, 2, s3\ns_sub_u32 s47, s47, 1984\nv_cmp_gt_u32 vcc, 16, v65\nv_lshl_add_u32 v65, v65, 7, s47\n"
    "s_lshl_b32 s47, s18, 9\nv_cndmask_b32 v64, v65, v64, vcc\ns_lshl_b32 s46, s18, 2\nv_min_u32 v64, (.Lk_all_end-.Lk_all_beg)/128-8, v64\nv_lshlrev_b32 v64, 7, v64\n"
    "s_mov_b64 exec, 0x3ffff\nglobal_load_dword v255, v64, s[92:93] offset:1024\ns_mov_b64 exec, -1\nv_lshlrev_b32 v64, 1, v0\nv_and_b32 v64, 0x1fc, v64\nv_mov_b32 v65, s46\n"
    "v_or_b32 v64, s47, v64\ns_waitcnt lgkmcnt(0)\nglobal_load_dword v160, v64, s[70:71]\nglobal_load_dword v161, v64, s[72:73]\nglobal_load_dword v162, v64, s[78:79]\n"
    "global_load_dword v163, v64, s[80:81]\nglobal_load_dword v164, v64, s[74:75]\nglobal_load_dword v165, v64, s[76:77]\nglobal_load_dword v167, v65, s[82:83]\n"
    "global_load_dword v166, v70, s[4:5] sc1\ns_branch .Lcom2\n.Loth3:\ns_cmp_eq_u32 s3, 5\ns_cbranch_scc1 .Loth4\ns_and_b32 s47, s18, 15\ns_lshl1_add_u32 s47, s47, s3\n"
    "s_lshl_b32 s47, s47, 14\ns_and_b32 s48, s18, 0xf0\nv_and_b32 v69, 32, v0\ns_lshl2_add_u32 s47, s48, s47\nv_and_b32 v71, 31, v0\ns_sub_u32 s47, s47, 94208\n"
    "v_lshl_add_u32 v69, v69, 8, s47\nv_lshl_add_u32 v69, v71, 1, v69\ns_waitcnt lgkmcnt(0)\nglobal_load_ushort v168, v69, s[68:69] offset:-4096\n"
    "global_load_ushort v169, v69, s[68:69] offset:-3072\nglobal_load_ushort v170, v69, s[68:69] offset:-2048\nglobal_load_ushort v171, v69, s[68:69] offset:-1024\n"
    "global_load_ushort v172, v69, s[68:69] offset:0\nglobal_load_ushort v173, v69, s[68:69] offset:1024\nglobal_load_ushort v174, v69, s[68:69] offset:2048\n"
    "global_load_ushort v175, v69, s[68:69] offset:3072\nglobal_load_dword v166, v70, s[4:5] sc1\ns_branch .Lcom2\n.Loth4:\nv_lshlrev_b32 v68, 4, v0\n"
    "v_mov_b32 v80, 0x3f803f80\nv_mov_b32 v81, 0x3f803f80\nv_mov_b32 v82, 0x3f803f80\nv_mov_b32 v83, 0x3f803f80\nv_add_u32 v68, 142336, v68\nv_mov_b64 v[84:85], 0\n"
    "v_mov_b64 v[86:87], 0\nds_write_b128 v68, v[80:83]\ns_mov_b64 exec, 0xff\nds_write_b128 v68, v[84:87] offset:16256\ns_mov_b64 exec, -1\ns_waitcnt lgkmcnt(0)\n"
    ".Lcom2:\ns_and_b32 s24, s2, 7\ns_lshr_b32 s25, s2, 3\ns_and_b32 s13, s3, 3\ns_lshl_b32 s43, s24, 5\ns_add_u32 s18, s43, s25\ns_lshr_b32 s42, s24, 1\n"
    "s_lshl_b32 s41, s42, 2\ns_and_b32 s40, s25, 3\ns_add_u32 s19, s41, s40\ns_and_b32 s39, s24, 1\ns_lshl_b32 s38, s39, 3\ns_lshr_b32 s37, s25, 2\ns_add_u32 s20, s38, s37\n"
    "s_lshl_b32 s36, s24, 3\ns_and_b32 s35, s25, 7\ns_add_u32 s21, s36, s35\ns_lshr_b32 s22, s25, 3\ns_lshl_b32 s34, s40, 1\ns_add_u32 s33, s37, s34\ns_and_b32 s23, s33, 7\n"
    "s_lshl_b32 s44, s18, 2\ns_mov_b64 s[6:7], s[86:87]\ns_mov_b64 s[8:9], s[64:65]\ns_mov_b32 s12, 0\nv_bfe_u32 v251, v0, 4, 2\nv_lshlrev_b32 v250, 2, v251\n"
    "s_add_u32 s43, s19, s3\ns_cmp_lt_u32 s43, 1\ns_cselect_b32 s42, 8, 0\nv_and_b32 v249, 7, v0\nv_add_u32 v248, 1, v249\nv_max_u32 v247, s42, v248\n"
    "v_sub_u32 v2, v250, v247\nv_add_u32 v246, 9, v249\nv_sub_u32 v40, v246, v247\nv_add_u32 v3, 1, v2\nv_add_u32 v4, 2, v2\nv_add_u32 v5, 3, v2\n"
    "v_cmp_lt_u32 s[36:37], v2, v40\ns_cbranch_execz .Lhop\nv_cmp_lt_u32 s[38:39], v3, v40\nv_cmp_lt_u32 s[40:41], v4, v40\nv_cmp_lt_u32 s[42:43], v5, v40\n"
    "v_mov_b32 v40, 0xf149f2ca\nv_cndmask_b32 v2, v40, 0, s[36:37]\nv_cndmask_b32 v3, v40, 0, s[38:39]\nv_cndmask_b32 v4, v40, 0, s[40:41]\n"
    "v_cndmask_b32 v5, v40, 0, s[42:43]\nv_and_b32 v251, 15, v0\nv_max_u32 v250, 1, v251\nv_mul_u32_u24 v249, 0x1a0, v250\nv_bfe_u32 v248, v0, 4, 2\ns_mul_i32 s43, s3, 0xd00\n"
    "s_add_u32 s42, s43, 0x18040\nv_add_u32 v6, s42, v249\nv_lshl_add_u32 v6, v248, 4, v6\nv_and_b32 v247, 7, v0\nv_mul_u32_u24 v246, 0x1a0, v247\n"
    "s_add_u32 s41, s43, 0x18d00\nv_add_u32 v7, s41, v246\nv_lshl_add_u32 v7, v248, 4, v7\nv_bfe_u32 v245, v0, 2, 2\nv_lshl_add_u32 v244, v248, 2, v245\n"
    "v_max_u32 v243, 1, v244\nv_mul_u32_u24 v242, 0x1a0, v243\nv_and_b32 v241, 3, v0\ns_add_u32 s40, s43, 0x18080\nv_add_u32 v8, s40, v242\nv_lshl_add_u32 v8, v241, 3, v8\n"
    "v_bfe_u32 v240, v0, 4, 1\ns_lshl_b32 s39, s19, 7\ns_lshl_b32 s38, s3, 4\ns_add_u32 s37, s39, s38\nv_add_u32 v239, s37, v240\nv_lshl_add_u32 v239, v247, 1, v239\n"
    "v_lshrrev_b32 v238, 5, v239\nv_bfe_u32 v237, v0, 3, 1\nv_bfe_u32 v236, v0, 5, 1\nv_and_b32 v235, 31, v239\ns_lshl_b32 s36, s20, 11\nv_lshl_add_u32 v9, v238, 15, s36\n"
    "v_lshl_add_u32 v9, v237, 10, v9\nv_lshl_add_u32 v9, v236, 9, v9\nv_lshl_add_u32 v9, v235, 4, v9\nv_mov_b32 v56, 163712\ns_add_u32 s16, s84, s44\ns_addc_u32 s17, s85, 0\n"
    "s_cmp_eq_u32 s3, 4\ns_cbranch_scc1 .Llstb7\ns_barrier\ns_cmp_gt_u32 s3, 3\ns_cbranch_scc1 .Lhalo10\n"
    "v_bfe_u32 v251, v0, 5, 1\nv_bfe_u32 v250, v0, 2, 2\nv_bfe_u32 v249, v0, 4, 1\nv_xor_b32 v248, v249, v251\nv_and_b32 v247, 3, v0\nv_lshlrev_b32 v36, 9, v251\n"
    "v_lshl_add_u32 v36, v250, 6, v36\nv_lshl_add_u32 v36, v248, 5, v36\nv_lshl_add_u32 v36, v247, 3, v36\nv_and_b32 v246, 31, v0\nv_bfe_u32 v245, v0, 1, 3\n"
    "v_xor_b32 v244, v251, v245\ns_mul_i32 s43, s13, 0x3000\ns_add_u32 s42, s43, 0x18000\nv_lshl_add_u32 v27, v246, 7, s42\nv_lshl_add_u32 v27, v244, 4, v27\n"
    "v_mul_u32_u24 v243, 0xd0, v246\ns_mul_i32 s41, s13, 0x1a00\ns_add_u32 s40, s41, 0x18d00\nv_add_u32 v37, s40, v243\nv_lshl_add_u32 v37, v251, 3, v37\n"
    "s_mov_b32 s39, 0x27c00\nv_lshl_add_u32 v38, v246, 2, s39\ns_mov_b32 s38, 0x27fe0\nv_lshl_add_u32 v242, v247, 2, s38\nv_and_b32 v241, 4, v0\nv_lshlrev_b32 v240, 4, v241\n"
    "v_sub_u32 v28, v242, v240\nv_mov_b32 v239, 16\nv_cmp_gt_u32 vcc, 1, v241\ns_nop 1\nv_cndmask_b32 v29, 1, v239, vcc\ns_lshl_b32 s37, s13, 2\ns_add_u32 s36, s37, 0x27fe0\n"
    "v_mov_b32 v238, s36\nv_and_b32 v237, 63, v0\ns_mov_b32 s35, 0x27e00\nv_lshl_add_u32 v236, v237, 2, s35\nv_cmp_gt_u32 vcc, 1, v237\ns_nop 1\n"
    "v_cndmask_b32 v30, v236, v238, vcc\ns_and_b32 s34, s23, 7\ns_mul_i32 s33, s34, 0x3000\nv_lshl_add_u32 v176, v251, 9, s33\nv_lshl_add_u32 v176, v250, 6, v176\n"
    "v_lshl_add_u32 v176, v248, 5, v176\nv_lshl_add_u32 v176, v247, 3, v176\ns_add_u32 s32, s23, 1\ns_and_b32 s31, s32, 7\ns_mul_i32 s30, s31, 0x3000\n"
    "v_lshl_add_u32 v177, v251, 9, s30\nv_lshl_add_u32 v177, v250, 6, v177\nv_lshl_add_u32 v177, v248, 5, v177\nv_lshl_add_u32 v177, v247, 3, v177\nv_bfe_u32 v251, v0, 5, 1\n"
    "v_bfe_u32 v250, v0, 2, 2\nv_bfe_u32 v249, v0, 4, 1\nv_xor_b32 v248, v249, v251\nv_and_b32 v247, 3, v0\ns_add_u32 s43, s23, 2\ns_and_b32 s42, s43, 7\n"
    "s_mul_i32 s41, s42, 0x3000\nv_lshl_add_u32 v178, v251, 9, s41\nv_lshl_add_u32 v178, v250, 6, v178\nv_lshl_add_u32 v178, v248, 5, v178\n"
    "v_lshl_add_u32 v178, v247, 3, v178\ns_add_u32 s40, s23, 3\ns_and_b32 s39, s40, 7\ns_mul_i32 s38, s39, 0x3000\nv_lshl_add_u32 v179, v251, 9, s38\n"
    "v_lshl_add_u32 v179, v250, 6, v179\nv_lshl_add_u32 v179, v248, 5, v179\nv_lshl_add_u32 v179, v247, 3, v179\ns_add_u32 s37, s23, 4\ns_and_b32 s36, s37, 7\n"
    "s_mul_i32 s35, s36, 0x3000\nv_lshl_add_u32 v180, v251, 9, s35\nv_lshl_add_u32 v180, v250, 6, v180\nv_lshl_add_u32 v180, v248, 5, v180\n"
    "v_lshl_add_u32 v180, v247, 3, v180\ns_add_u32 s34, s23, 5\ns_and_b32 s33, s34, 7\ns_mul_i32 s32, s33, 0x3000\nv_lshl_add_u32 v181, v251, 9, s32\n"
    "v_lshl_add_u32 v181, v250, 6, v181\nv_lshl_add_u32 v181, v248, 5, v181\nv_lshl_add_u32 v181, v247, 3, v181\nv_bfe_u32 v251, v0, 5, 1\nv_bfe_u32 v250, v0, 2, 2\n"
    "v_bfe_u32 v249, v0, 4, 1\nv_xor_b32 v248, v249, v251\nv_and_b32 v247, 3, v0\ns_add_u32 s43, s23, 6\ns_and_b32 s42, s43, 7\ns_mul_i32 s41, s42, 0x3000\n"
    "v_lshl_add_u32 v182, v251, 9, s41\nv_lshl_add_u32 v182, v250, 6, v182\nv_lshl_add_u32 v182, v248, 5, v182\nv_lshl_add_u32 v182, v247, 3, v182\ns_add_u32 s40, s23, 7\n"
    "s_and_b32 s39, s40, 7\ns_mul_i32 s38, s39, 0x3000\nv_lshl_add_u32 v183, v251, 9, s38\nv_lshl_add_u32 v183, v250, 6, v183\nv_lshl_add_u32 v183, v248, 5, v183\ns_nop 0\n"
    "s_cbranch_execz .Lhop\nv_lshl_add_u32 v183, v247, 3, v183\ns_mul_i32 s37, s13, 0xc00\ns_add_u32 s36, s37, 0x1f600\nv_lshl_add_u32 v33, v0, 4, s36\n"
    "s_lshl_b32 s35, s13, 2\ns_add_u32 s34, s35, 0x27d80\nv_mov_b32 v34, s34\nv_bfe_u32 v246, v0, 2, 4\ns_mov_b32 s33, 0x1006000\nv_lshl_add_u32 v35, v246, 7, s33\n"
    "v_lshl_add_u32 v35, v247, 4, v35\ns_lshr_b32 s32, s21, 2\ns_lshl_b32 s31, s32, 7\ns_add_u32 s30, s31, 0x1004000\nv_lshl_add_u32 v39, v247, 4, s30\n"
    "v_and_b32 v251, 15, v0\nv_mov_b32 v250, 0x102\nv_min_u32 v249, 12, v251\ns_mov_b32 s43, 0xfffffc00\nv_lshl_add_u32 v248, v249, 8, s43\nv_cmp_gt_u32 vcc, 5, v251\n"
    "s_nop 1\nv_cndmask_b32 v247, v248, v250, vcc\nv_cmp_gt_u32 vcc, 4, v251\ns_nop 1\nv_cndmask_b32 v25, v247, v251, vcc\nv_lshlrev_b32 v26, 3, v251\nv_cvt_f32_u32 v25, v25\n"
    "v_cvt_f32_u32 v26, v26\ns_mov_b32 s10, 4\ns_mov_b32 s27, 4\ns_add_u32 s94, s92, .Lgm14-.Lk_all_beg+4\ns_addc_u32 s95, s93, 0\nv_mov_b64 v[40:41], 0\n"
    "v_mov_b64 v[42:43], 0\nv_mov_b64 v[44:45], 0\nv_mov_b64 v[46:47], 0\nv_mov_b64 v[48:49], 0\nv_mov_b64 v[50:51], 0\nv_mov_b64 v[52:53], 0\nv_mov_b64 v[54:55], 0\n"
    "v_mov_b64 v[64:65], 0\nv_mov_b64 v[66:67], 0\nv_mov_b64 v[68:69], 0\nv_mov_b64 v[70:71], 0\nv_mov_b64 v[72:73], 0\nv_mov_b64 v[74:75], 0\nv_mov_b64 v[76:77], 0\n"
    "v_mov_b64 v[78:79], 0\nv_mov_b64 v[80:81], 0\nv_mov_b64 v[82:83], 0\nv_mov_b64 v[84:85], 0\nv_mov_b64 v[86:87], 0\nv_mov_b64 v[88:89], 0\nv_mov_b64 v[90:91], 0\n"
    "v_mov_b64 v[92:93], 0\nv_mov_b64 v[94:95], 0\nv_mov_b64 v[96:97], 0\nv_mov_b64 v[98:99], 0\nv_mov_b64 v[100:101], 0\nv_mov_b64 v[102:103], 0\nv_mov_b64 v[104:105], 0\n"
    "v_mov_b64 v[106:107], 0\nv_mov_b64 v[108:109], 0\nv_mov_b64 v[110:111], 0\nv_mov_b64 v[112:113], 0\nv_mov_b64 v[114:115], 0\nv_mov_b64 v[116:117], 0\n"
    "v_mov_b64 v[118:119], 0\nv_mov_b64 v[120:121], 0\nv_mov_b64 v[122:123], 0\nv_mov_b64 v[124:125], 0\nv_mov_b64 v[126:127], 0\nv_mov_b64 v[128:129], 0\n"
    "v_mov_b64 v[130:131], 0\nv_mov_b64 v[132:133], 0\nv_mov_b64 v[134:135], 0\ns_waitcnt vmcnt(1)\n"
    ".Lkgp9:\ns_bitcmp1_b32 s3, 1\ns_cselect_b32 s44, 0, 16\nv_lshlrev_b32 v240, s44, v167\nv_and_b32 v224, 1, v0\nv_and_b32 v240, 0xffff0000, v240\n"
    "v_lshlrev_b32 v224, 4, v224\ns_mov_b32 s45, 0x3fb8aa3b\nv_mul_f32 v241, s45, v240\nv_xor_b32 v224, 16, v224\nv_fma_f32 v242, v240, s45, -v241\nv_rndne_f32 v243, v241\n"
    "v_fmamk_f32 v242, v240, 0x32a5705f, v242\nv_sub_f32 v241, v241, v243\nv_add_f32 v241, v241, v242\nv_exp_f32 v241, v241\nv_cvt_i32_f32 v243, v243\n"
    "v_lshlrev_b32 v160, v224, v160\nv_lshlrev_b32 v161, v224, v161\nv_lshlrev_b32 v162, v224, v162\nv_lshlrev_b32 v163, v224, v163\nv_lshlrev_b32 v164, v224, v164\n"
    "v_lshlrev_b32 v165, v224, v165\nv_ldexp_f32 v241, v241, v243\nv_and_b32 v160, 0xffff0000, v160\nv_and_b32 v161, 0xffff0000, v161\nv_and_b32 v162, 0xffff0000, v162\n"
    "v_and_b32 v163, 0xffff0000, v163\nv_and_b32 v164, 0xffff0000, v164\nv_and_b32 v165, 0xffff0000, v165\nv_cvt_pk_bf16_f32 v241, 0, v241\nv_mul_f32 v164, v241, v164\n"
    "v_mul_f32 v165, v241, v165\nv_mul_f32 v16, v241, v160\nv_mul_f32 v17, v241, v161\nv_mul_f32 v242, v162, v164\nv_mul_f32 v243, v163, v165\nv_mul_f32 v244, v162, v165\n"
    "v_mul_f32 v245, v163, v164\nv_sub_f32 v242, v242, v243\nv_add_f32 v243, v244, v245\nv_mul_f32 v16, s45, v16\nv_mul_f32 v17, 0.15915494, v17\nv_mul_f32 v244, v242, v242\n"
    "v_mul_f32 v245, v243, v243\nv_add_f32 v244, v244, v245\nv_log_f32 v244, v244\nv_max_f32 v246, abs(v242), abs(v243)\nv_min_f32 v247, abs(v242), abs(v243)\n"
    "v_mul_f32 v18, 0.5, v244\nv_frexp_mant_f32 v248, v246\nv_max_f32 v244, 0xc2700000, v18\nv_rcp_f32 v248, v248\nv_frexp_exp_i32_f32 v246, v246\n"
    "v_max_f32_dpp v244, v244, v244 row_ror:1 row_mask:0xf bank_mask:0xf\nv_frexp_exp_i32_f32 v249, v247\nv_frexp_mant_f32 v247, v247\n"
    "v_max_f32_dpp v244, v244, v244 row_ror:2 row_mask:0xf bank_mask:0xf\nv_mul_f32 v248, v247, v248\nv_sub_u32 v246, v249, v246\n"
    "v_max_f32_dpp v244, v244, v244 row_ror:4 row_mask:0xf bank_mask:0xf\nv_ldexp_f32 v246, v248, v246\nv_mov_b32 v248, 0xbc7a590c\n"
    "v_max_f32_dpp v244, v244, v244 row_ror:8 row_mask:0xf bank_mask:0xf\nv_mul_f32 v247, v246, v246\nv_mov_b32 v245, v244\nv_fmac_f32 v248, 0x3b2d2a58, v247\n"
    "v_fmaak_f32 v248, v247, v248, 0x3d29fb3f\nv_permlane16_swap_b32 v244, v245\nv_fmaak_f32 v248, v247, v248, 0xbd97d4d7\nv_max_f32 v244, v244, v245\n"
    "v_fmaak_f32 v248, v247, v248, 0x3dd931b2\nv_mov_b32 v245, v244\nv_fmaak_f32 v248, v247, v248, 0xbe1160e6\nv_fmaak_f32 v248, v247, v248, 0x3e4cb8bf\n"
    "v_permlane32_swap_b32 v244, v245\nv_fmaak_f32 v248, v247, v248, 0xbeaaaa62\nv_max_f32 v244, v244, v245\nv_mul_f32 v247, v247, v248\nv_ceil_f32 v244, v244\n"
    "v_fmac_f32 v246, v246, v247\nv_add_f32 v244, 0xc1600000, v244\nv_cmp_gt_f32 vcc, abs(v243), abs(v242)\nv_sub_f32 v247, 0x3fc90fdb, v246\nv_sub_f32 v18, v18, v244\n"
    "v_cndmask_b32 v246, v246, v247, vcc\nv_cmp_gt_f32 vcc, 0, v242\nv_sub_f32 v247, 0x40490fdb, v246\nv_exp_f32 v24, v244\nv_cndmask_b32 v246, v246, v247, vcc\n"
    "s_brev_b32 s44, -2\nv_max_f32 v246, 0, v246\nv_mul_f32 v244, 0x43000000, v16\nv_bfi_b32 v246, s44, v246, v243\nv_mul_f32 v245, 0x43000000, v17\n"
    "v_mul_f32 v19, 0xbe22f983, v246\nv_mul_f32 v247, 4.0, v16\nv_mul_f32 v248, 4.0, v17\nv_fract_f32 v245, v245\nv_fract_f32 v248, v248\nv_exp_f32 v244, v244\n"
    "v_exp_f32 v247, v247\nv_cos_f32 v20, v245\nv_sin_f32 v21, v245\nv_cos_f32 v22, v248\nv_sin_f32 v23, v248\nv_mul_f32 v20, v244, v20\nv_mul_f32 v21, v244, v21\n"
    "v_mul_f32 v22, v247, v22\nv_mul_f32 v23, v247, v23\n"
    ".Lkgl13:\nv_mov_b32_dpp v240, v18 row_newbcast:0 row_mask:0xf bank_mask:0xf\nv_mov_b32_dpp v241, v19 row_newbcast:0 row_mask:0xf bank_mask:0xf\n"
    "v_mul_f32_dpp v242, v16, v26 row_newbcast:0 row_mask:0xf bank_mask:0xf\nv_mul_f32_dpp v243, v17, v26 row_newbcast:0 row_mask:0xf bank_mask:0xf\n"
    "v_fmac_f32_dpp v240, v16, v25 row_newbcast:0 row_mask:0xf bank_mask:0xf\nv_fmac_f32_dpp v241, -v17, v25 row_newbcast:0 row_mask:0xf bank_mask:0xf\n"
    "v_fract_f32 v243, v243\nv_exp_f32 v242, v242\nv_fract_f32 v241, v241\nv_exp_f32 v240, v240\nv_cos_f32 v244, v243\ns_cbranch_execz .Lhop\nv_sin_f32 v245, v243\n"
    "v_cos_f32 v246, v241\nv_sin_f32 v247, v241\nv_mul_f32 v244, v242, v244\nv_mul_f32 v245, v242, v245\nv_mul_f32 v246, v240, v246\nv_mul_f32 v247, v240, v247\n"
    "v_mul_f32_dpp v248, v20, v244 row_newbcast:0 row_mask:0xf bank_mask:0xf\nv_mul_f32_dpp v249, v20, v245 row_newbcast:0 row_mask:0xf bank_mask:0xf\n"
    "v_mul_f32_dpp v250, v22, v246 row_newbcast:0 row_mask:0xf bank_mask:0xf\nv_mul_f32_dpp v251, v22, v247 row_newbcast:0 row_mask:0xf bank_mask:0xf\n"
    "v_fmac_f32_dpp v248, -v21, v245 row_newbcast:0 row_mask:0xf bank_mask:0xf\nv_fmac_f32_dpp v249, v21, v244 row_newbcast:0 row_mask:0xf bank_mask:0xf\n"
    "v_fmac_f32_dpp v250, v23, v247 row_newbcast:0 row_mask:0xf bank_mask:0xf\nv_fmac_f32_dpp v251, -v23, v246 row_newbcast:0 row_mask:0xf bank_mask:0xf\n"
    "v_cvt_pk_f16_f32 v232, v244, v245\nv_cvt_pk_f16_f32 v224, v246, v247\nv_cvt_pk_f16_f32 v236, v248, v249\nv_cvt_pk_f16_f32 v228, v250, v251\n"
    "v_mov_b32_dpp v240, v18 row_newbcast:1 row_mask:0xf bank_mask:0xf\nv_mov_b32_dpp v241, v19 row_newbcast:1 row_mask:0xf bank_mask:0xf\n"
    "v_mul_f32_dpp v242, v16, v26 row_newbcast:1 row_mask:0xf bank_mask:0xf\nv_mul_f32_dpp v243, v17, v26 row_newbcast:1 row_mask:0xf bank_mask:0xf\n"
    "v_fmac_f32_dpp v240, v16, v25 row_newbcast:1 row_mask:0xf bank_mask:0xf\nv_fmac_f32_dpp v241, -v17, v25 row_newbcast:1 row_mask:0xf bank_mask:0xf\n"
    "v_fract_f32 v243, v243\nv_exp_f32 v242, v242\nv_fract_f32 v241, v241\nv_exp_f32 v240, v240\nv_cos_f32 v244, v243\nv_sin_f32 v245, v243\nv_cos_f32 v246, v241\n"
    "v_sin_f32 v247, v241\nv_mul_f32 v244, v242, v244\nv_mul_f32 v245, v242, v245\nv_mul_f32 v246, v240, v246\nv_mul_f32 v247, v240, v247\n"
    "v_mul_f32_dpp v248, v20, v244 row_newbcast:1 row_mask:0xf bank_mask:0xf\nv_mul_f32_dpp v249, v20, v245 row_newbcast:1 row_mask:0xf bank_mask:0xf\n"
    "v_mul_f32_dpp v250, v22, v246 row_newbcast:1 row_mask:0xf bank_mask:0xf\nv_mul_f32_dpp v251, v22, v247 row_newbcast:1 row_mask:0xf bank_mask:0xf\n"
    "v_fmac_f32_dpp v248, -v21, v245 row_newbcast:1 row_mask:0xf bank_mask:0xf\nv_fmac_f32_dpp v249, v21, v244 row_newbcast:1 row_mask:0xf bank_mask:0xf\n"
    "v_fmac_f32_dpp v250, v23, v247 row_newbcast:1 row_mask:0xf bank_mask:0xf\nv_fmac_f32_dpp v251, -v23, v246 row_newbcast:1 row_mask:0xf bank_mask:0xf\n"
    "v_cvt_pk_f16_f32 v233, v244, v245\nv_cvt_pk_f16_f32 v225, v246, v247\nv_cvt_pk_f16_f32 v237, v248, v249\nv_cvt_pk_f16_f32 v229, v250, v251\n"
    "v_mov_b32_dpp v240, v18 row_newbcast:2 row_mask:0xf bank_mask:0xf\nv_mov_b32_dpp v241, v19 row_newbcast:2 row_mask:0xf bank_mask:0xf\n"
    "v_mul_f32_dpp v242, v16, v26 row_newbcast:2 row_mask:0xf bank_mask:0xf\nv_mul_f32_dpp v243, v17, v26 row_newbcast:2 row_mask:0xf bank_mask:0xf\n"
    "v_fmac_f32_dpp v240, v16, v25 row_newbcast:2 row_mask:0xf bank_mask:0xf\nv_fmac_f32_dpp v241, -v17, v25 row_newbcast:2 row_mask:0xf bank_mask:0xf\n"
    "v_fract_f32 v243, v243\nv_exp_f32 v242, v242\nv_fract_f32 v241, v241\nv_exp_f32 v240, v240\nv_cos_f32 v244, v243\nv_sin_f32 v245, v243\nv_cos_f32 v246, v241\n"
    "v_sin_f32 v247, v241\nv_mul_f32 v244, v242, v244\nv_mul_f32 v245, v242, v245\nv_mul_f32 v246, v240, v246\nv_mul_f32 v247, v240, v247\n"
    "v_mul_f32_dpp v248, v20, v244 row_newbcast:2 row_mask:0xf bank_mask:0xf\nv_mul_f32_dpp v249, v20, v245 row_newbcast:2 row_mask:0xf bank_mask:0xf\n"
    "v_mul_f32_dpp v250, v22, v246 row_newbcast:2 row_mask:0xf bank_mask:0xf\nv_mul_f32_dpp v251, v22, v247 row_newbcast:2 row_mask:0xf bank_mask:0xf\n"
    "v_fmac_f32_dpp v248, -v21, v245 row_newbcast:2 row_mask:0xf bank_mask:0xf\nv_fmac_f32_dpp v249, v21, v244 row_newbcast:2 row_mask:0xf bank_mask:0xf\n"
    "v_fmac_f32_dpp v250, v23, v247 row_newbcast:2 row_mask:0xf bank_mask:0xf\nv_fmac_f32_dpp v251, -v23, v246 row_newbcast:2 row_mask:0xf bank_mask:0xf\n"
    "v_cvt_pk_f16_f32 v234, v244, v245\nv_cvt_pk_f16_f32 v226, v246, v247\nv_cvt_pk_f16_f32 v238, v248, v249\nv_cvt_pk_f16_f32 v230, v250, v251\nds_read_b32 v57, v56\n"
    "v_mov_b32_dpp v240, v18 row_newbcast:3 row_mask:0xf bank_mask:0xf\nv_mov_b32_dpp v241, v19 row_newbcast:3 row_mask:0xf bank_mask:0xf\n"
    "v_mul_f32_dpp v242, v16, v26 row_newbcast:3 row_mask:0xf bank_mask:0xf\nv_mul_f32_dpp v243, v17, v26 row_newbcast:3 row_mask:0xf bank_mask:0xf\n"
    "v_fmac_f32_dpp v240, v16, v25 row_newbcast:3 row_mask:0xf bank_mask:0xf\nv_fmac_f32_dpp v241, -v17, v25 row_newbcast:3 row_mask:0xf bank_mask:0xf\n"
    "v_fract_f32 v243, v243\nv_exp_f32 v242, v242\nv_fract_f32 v241, v241\nv_exp_f32 v240, v240\nv_cos_f32 v244, v243\nv_sin_f32 v245, v243\nv_cos_f32 v246, v241\n"
    "v_sin_f32 v247, v241\nv_mul_f32 v244, v242, v244\nv_mul_f32 v245, v242, v245\nv_mul_f32 v246, v240, v246\nv_mul_f32 v247, v240, v247\n"
    "v_mul_f32_dpp v248, v20, v244 row_newbcast:3 row_mask:0xf bank_mask:0xf\nv_mul_f32_dpp v249, v20, v245 row_newbcast:3 row_mask:0xf bank_mask:0xf\n"
    "v_mul_f32_dpp v250, v22, v246 row_newbcast:3 row_mask:0xf bank_mask:0xf\nv_mul_f32_dpp v251, v22, v247 row_newbcast:3 row_mask:0xf bank_mask:0xf\n"
    "v_fmac_f32_dpp v248, -v21, v245 row_newbcast:3 row_mask:0xf bank_mask:0xf\nv_fmac_f32_dpp v249, v21, v244 row_newbcast:3 row_mask:0xf bank_mask:0xf\n"
    "v_fmac_f32_dpp v250, v23, v247 row_newbcast:3 row_mask:0xf bank_mask:0xf\nv_fmac_f32_dpp v251, -v23, v246 row_newbcast:3 row_mask:0xf bank_mask:0xf\n"
    "v_cvt_pk_f16_f32 v235, v244, v245\nv_cvt_pk_f16_f32 v227, v246, v247\nv_cvt_pk_f16_f32 v239, v248, v249\nv_cvt_pk_f16_f32 v231, v250, v251\ns_waitcnt lgkmcnt(0)\n"
    "v_readfirstlane_b32 s44, v57\nv_mov_b32_dpp v16, v16 row_ror:12 row_mask:0xf bank_mask:0xf\nv_mov_b32_dpp v17, v17 row_ror:12 row_mask:0xf bank_mask:0xf\n"
    "v_mfma_f32_16x16x32_f16 v[40:43], v[224:227], v[232:235], v[40:43]\nv_mov_b32_dpp v18, v18 row_ror:12 row_mask:0xf bank_mask:0xf\n"
    "v_mov_b32_dpp v19, v19 row_ror:12 row_mask:0xf bank_mask:0xf\nv_mfma_f32_16x16x32_f16 v[44:47], v[224:227], v[236:239], v[44:47]\n"
    "v_mov_b32_dpp v20, v20 row_ror:12 row_mask:0xf bank_mask:0xf\nv_mov_b32_dpp v21, v21 row_ror:12 row_mask:0xf bank_mask:0xf\n"
    "v_mfma_f32_16x16x32_f16 v[48:51], v[228:231], v[232:235], v[48:51]\nv_mov_b32_dpp v22, v22 row_ror:12 row_mask:0xf bank_mask:0xf\n"
    "v_mov_b32_dpp v23, v23 row_ror:12 row_mask:0xf bank_mask:0xf\nv_mfma_f32_16x16x32_f16 v[52:55], v[228:231], v[236:239], v[52:55]\ns_sub_u32 s27, s27, 1\n"
    "s_cmp_eq_u32 s27, 0\ns_cbranch_scc1 .Lkx15\ns_cmp_lt_i32 s44, s10\ns_cbranch_scc1 .Lkgl13\ns_setpc_b64 s[94:95]\n.Lkx15:\ns_nop 4\nv_mul_f32 v40, v40, v24\n"
    "v_mul_f32 v41, v41, v24\nv_mul_f32 v42, v42, v24\nv_mul_f32 v43, v43, v24\nv_mul_f32 v44, v44, v24\nv_mul_f32 v45, v45, v24\nv_mul_f32 v46, v46, v24\n"
    "v_mul_f32 v47, v47, v24\nv_mul_f32 v48, v48, v24\nv_mul_f32 v49, v49, v24\nv_mul_f32 v50, v50, v24\nv_mul_f32 v51, v51, v24\nv_mul_f32 v52, v52, v24\n"
    "v_mul_f32 v53, v53, v24\nv_mul_f32 v54, v54, v24\nv_mul_f32 v55, v55, v24\nv_max3_f32 v240, abs(v40), abs(v41), abs(v42)\nv_max3_f32 v241, abs(v43), abs(v44), abs(v45)\n"
    "v_max3_f32 v240, v240, abs(v46), abs(v47)\nv_max3_f32 v241, v241, abs(v48), abs(v49)\nv_max3_f32 v240, v240, abs(v50), abs(v51)\n"
    "v_max3_f32 v241, v241, abs(v52), abs(v53)\nv_max3_f32 v240, v240, abs(v54), abs(v55)\nv_max_f32 v240, v240, v241\ns_nop 1\n"
    "v_max_f32_dpp v240, v240, v240 row_ror:1 row_mask:0xf bank_mask:0xf\ns_nop 1\nv_max_f32_dpp v240, v240, v240 row_ror:2 row_mask:0xf bank_mask:0xf\ns_nop 1\n"
    "v_max_f32_dpp v240, v240, v240 row_ror:4 row_mask:0xf bank_mask:0xf\ns_nop 1\nv_max_f32_dpp v240, v240, v240 row_ror:8 row_mask:0xf bank_mask:0xf\nv_mov_b32 v241, v240\n"
    "s_nop 1\ns_cbranch_execz .Lhop\nv_permlane16_swap_b32 v240, v241\nv_max_f32 v240, v240, v241\nv_mov_b32 v241, v240\ns_nop 1\nv_permlane32_swap_b32 v240, v241\n"
    "v_max_f32 v240, v240, v241\ns_mov_b64 exec, 1\nds_write_b32 v34, v240\ns_mov_b64 exec, -1\ns_setpc_b64 s[94:95]\n"
    ".Lgm14:\ns_waitcnt vmcnt(0)\nv_readfirstlane_b32 s11, v166\ns_mov_b32 s81, 0x3b000000\ns_mov_b32 s84, -1\ns_mov_b32 s85, 0\nv_mov_b32 v241, 0x3f803f80\n"
    "s_mul_i32 s11, s11, 0x9E3779B1\ns_add_u32 s11, s11, 0x7F4A7C15\nv_mov_b32 v160, v27\nv_xor_b32 v161, 32, v160\nv_xor_b32 v162, 64, v160\nv_xor_b32 v163, 0x60, v160\n"
    "v_mov_b32 v240, 0\nv_writelane_b32 v240, 2, 0\ns_mov_b32 s86, 0x100000\n.Ls18:\nds_read_b32 v243, v56 offset:0\ns_waitcnt lgkmcnt(0)\nv_cmp_gt_i32 vcc, 2, v243\n"
    "s_sub_u32 s86, s86, 1\ns_cbranch_scc1 .Ls19\ns_cbranch_vccz .Ls19\ns_sleep 1\ns_branch .Ls18\n.Ls19:\nds_read_b64_tr_b16 v[208:209], v176 offset:0\n"
    "ds_read_b64_tr_b16 v[210:211], v176 offset:256\nds_read_b64_tr_b16 v[212:213], v176 offset:3072\nds_read_b64_tr_b16 v[214:215], v176 offset:3328\n"
    "ds_read_b128 v[144:147], v160 offset:0\nds_read_b64_tr_b16 v[216:217], v176 offset:6144\nds_read_b64_tr_b16 v[218:219], v176 offset:6400\n"
    "ds_read_b128 v[148:151], v161 offset:0\nds_read_b64_tr_b16 v[220:221], v176 offset:9216\nds_read_b64_tr_b16 v[222:223], v176 offset:9472\n"
    "ds_read_b128 v[152:155], v162 offset:0\nds_read_b64_tr_b16 v[224:225], v176 offset:1024\nds_read_b64_tr_b16 v[226:227], v176 offset:1280\n"
    "ds_read_b128 v[156:159], v163 offset:0\nds_read_b64_tr_b16 v[228:229], v176 offset:4096\nds_read_b64_tr_b16 v[230:231], v176 offset:4352\ns_waitcnt lgkmcnt(11)\n"
    "v_dot2c_f32_bf16 v112, v241, v144\nv_dot2c_f32_bf16 v113, v144, v144\nv_dot2c_f32_bf16 v114, v241, v145\nv_dot2c_f32_bf16 v115, v145, v145\n"
    "v_dot2c_f32_bf16 v116, v241, v146\nv_dot2c_f32_bf16 v117, v146, v146\nv_dot2c_f32_bf16 v118, v241, v147\nv_dot2c_f32_bf16 v119, v147, v147\ns_waitcnt lgkmcnt(8)\n"
    "v_dot2c_f32_bf16 v112, v241, v148\nv_dot2c_f32_bf16 v113, v148, v148\nv_dot2c_f32_bf16 v114, v241, v149\nv_dot2c_f32_bf16 v115, v149, v149\n"
    "v_dot2c_f32_bf16 v116, v241, v150\nv_dot2c_f32_bf16 v117, v150, v150\nv_dot2c_f32_bf16 v118, v241, v151\nv_dot2c_f32_bf16 v119, v151, v151\ns_waitcnt lgkmcnt(5)\n"
    "v_dot2c_f32_bf16 v112, v241, v152\nv_dot2c_f32_bf16 v113, v152, v152\nv_dot2c_f32_bf16 v114, v241, v153\nv_dot2c_f32_bf16 v115, v153, v153\n"
    "v_dot2c_f32_bf16 v116, v241, v154\nv_dot2c_f32_bf16 v117, v154, v154\nv_dot2c_f32_bf16 v118, v241, v155\nv_dot2c_f32_bf16 v119, v155, v155\ns_waitcnt lgkmcnt(2)\n"
    "v_dot2c_f32_bf16 v112, v241, v156\nv_dot2c_f32_bf16 v113, v156, v156\nv_dot2c_f32_bf16 v114, v241, v157\nv_dot2c_f32_bf16 v115, v157, v157\n"
    "v_dot2c_f32_bf16 v116, v241, v158\nv_dot2c_f32_bf16 v117, v158, v158\nv_dot2c_f32_bf16 v118, v241, v159\nv_dot2c_f32_bf16 v119, v159, v159\n"
    "v_mfma_f32_32x32x16_bf16 v[64:79], v[208:211], v[144:147], v[64:79]\nds_read_b64_tr_b16 v[208:209], v176 offset:7168\nds_read_b64_tr_b16 v[210:211], v176 offset:7424\n"
    "ds_add_u32 v30, v240\nv_mfma_f32_32x32x16_bf16 v[64:79], v[212:215], v[148:151], v[64:79]\nds_read_b64_tr_b16 v[212:213], v176 offset:10240\n"
    "ds_read_b64_tr_b16 v[214:215], v176 offset:10496\nds_read_b32 v243, v56\nv_mfma_f32_32x32x16_bf16 v[64:79], v[216:219], v[152:155], v[64:79]\n"
    "ds_read_b64_tr_b16 v[216:217], v176 offset:2048\nds_read_b64_tr_b16 v[218:219], v176 offset:2304\nv_mfma_f32_32x32x16_bf16 v[64:79], v[220:223], v[156:159], v[64:79]\n"
    "ds_read_b64_tr_b16 v[220:221], v176 offset:5120\nds_read_b64_tr_b16 v[222:223], v176 offset:5376\nv_mfma_f32_32x32x16_bf16 v[80:95], v[224:227], v[144:147], v[80:95]\n"
    "ds_read_b64_tr_b16 v[224:225], v176 offset:8192\nds_read_b64_tr_b16 v[226:227], v176 offset:8448\ns_waitcnt lgkmcnt(6)\nv_readfirstlane_b32 s49, v243\n"
    "v_mfma_f32_32x32x16_bf16 v[80:95], v[228:231], v[148:151], v[80:95]\nds_read_b64_tr_b16 v[228:229], v176 offset:11264\nds_read_b64_tr_b16 v[230:231], v176 offset:11520\n"
    "s_cmp_lt_i32 s49, 3\ns_cbranch_scc1 .Lc20\n.Lc21:\nv_mfma_f32_32x32x16_bf16 v[80:95], v[208:211], v[152:155], v[80:95]\nds_read_b64_tr_b16 v[208:209], v177 offset:0\n"
    "ds_read_b64_tr_b16 v[210:211], v177 offset:256\nv_mfma_f32_32x32x16_bf16 v[80:95], v[212:215], v[156:159], v[80:95]\nds_read_b64_tr_b16 v[212:213], v177 offset:3072\n"
    "ds_read_b64_tr_b16 v[214:215], v177 offset:3328\ns_waitcnt lgkmcnt(6)\nv_mfma_f32_32x32x16_bf16 v[96:111], v[216:219], v[144:147], v[96:111]\ns_cmp_lt_i32 s49, 4\n"
    "s_cbranch_scc1 .Lc23\n.Lc24:\nds_read_b128 v[144:147], v160 offset:4096\nds_read_b64_tr_b16 v[216:217], v177 offset:6144\n"
    "ds_read_b64_tr_b16 v[218:219], v177 offset:6400\nv_mfma_f32_32x32x16_bf16 v[96:111], v[220:223], v[148:151], v[96:111]\nds_read_b128 v[148:151], v161 offset:4096\n"
    "ds_read_b64_tr_b16 v[220:221], v177 offset:9216\nds_read_b64_tr_b16 v[222:223], v177 offset:9472\nv_mfma_f32_32x32x16_bf16 v[96:111], v[224:227], v[152:155], v[96:111]\n"
    "ds_read_b128 v[152:155], v162 offset:4096\nds_read_b64_tr_b16 v[224:225], v177 offset:1024\nds_read_b64_tr_b16 v[226:227], v177 offset:1280\ns_waitcnt lgkmcnt(13)\n"
    "v_mfma_f32_32x32x16_bf16 v[96:111], v[228:231], v[156:159], v[96:111]\nds_read_b128 v[156:159], v163 offset:4096\nds_read_b64_tr_b16 v[228:229], v177 offset:4096\n"
    "ds_read_b64_tr_b16 v[230:231], v177 offset:4352\ns_waitcnt lgkmcnt(11)\nv_dot2c_f32_bf16 v112, v241, v144\nv_dot2c_f32_bf16 v113, v144, v144\n"
    "v_dot2c_f32_bf16 v114, v241, v145\nv_dot2c_f32_bf16 v115, v145, v145\nv_dot2c_f32_bf16 v116, v241, v146\nv_dot2c_f32_bf16 v117, v146, v146\n"
    "v_dot2c_f32_bf16 v118, v241, v147\nv_dot2c_f32_bf16 v119, v147, v147\ns_waitcnt lgkmcnt(8)\nv_dot2c_f32_bf16 v112, v241, v148\nv_dot2c_f32_bf16 v113, v148, v148\n"
    "v_dot2c_f32_bf16 v114, v241, v149\nv_dot2c_f32_bf16 v115, v149, v149\nv_dot2c_f32_bf16 v116, v241, v150\nv_dot2c_f32_bf16 v117, v150, v150\n"
    "v_dot2c_f32_bf16 v118, v241, v151\nv_dot2c_f32_bf16 v119, v151, v151\ns_waitcnt lgkmcnt(5)\nv_dot2c_f32_bf16 v112, v241, v152\nv_dot2c_f32_bf16 v113, v152, v152\n"
    "v_dot2c_f32_bf16 v114, v241, v153\nv_dot2c_f32_bf16 v115, v153, v153\nv_dot2c_f32_bf16 v116, v241, v154\nv_dot2c_f32_bf16 v117, v154, v154\n"
    "v_dot2c_f32_bf16 v118, v241, v155\nv_dot2c_f32_bf16 v119, v155, v155\ns_waitcnt lgkmcnt(2)\nv_dot2c_f32_bf16 v112, v241, v156\nv_dot2c_f32_bf16 v113, v156, v156\n"
    "v_dot2c_f32_bf16 v114, v241, v157\nv_dot2c_f32_bf16 v115, v157, v157\nv_dot2c_f32_bf16 v116, v241, v158\nv_dot2c_f32_bf16 v117, v158, v158\n"
    "v_dot2c_f32_bf16 v118, v241, v159\nv_dot2c_f32_bf16 v119, v159, v159\nv_mfma_f32_32x32x16_bf16 v[64:79], v[208:211], v[144:147], v[64:79]\n"
    "ds_read_b64_tr_b16 v[208:209], v177 offset:7168\nds_read_b64_tr_b16 v[210:211], v177 offset:7424\nds_add_u32 v30, v240\n"
    "v_mfma_f32_32x32x16_bf16 v[64:79], v[212:215], v[148:151], v[64:79]\nds_read_b64_tr_b16 v[212:213], v177 offset:10240\nds_read_b64_tr_b16 v[214:215], v177 offset:10496\n"
    "ds_read_b32 v243, v56\ns_nop 0\ns_cbranch_execz .Lhop\nv_mfma_f32_32x32x16_bf16 v[64:79], v[216:219], v[152:155], v[64:79]\n"
    "ds_read_b64_tr_b16 v[216:217], v177 offset:2048\nds_read_b64_tr_b16 v[218:219], v177 offset:2304\nv_mfma_f32_32x32x16_bf16 v[64:79], v[220:223], v[156:159], v[64:79]\n"
    "ds_read_b64_tr_b16 v[220:221], v177 offset:5120\nds_read_b64_tr_b16 v[222:223], v177 offset:5376\nv_mfma_f32_32x32x16_bf16 v[80:95], v[224:227], v[144:147], v[80:95]\n"
    "ds_read_b64_tr_b16 v[224:225], v177 offset:8192\nds_read_b64_tr_b16 v[226:227], v177 offset:8448\ns_waitcnt lgkmcnt(6)\nv_readfirstlane_b32 s49, v243\n"
    "v_mfma_f32_32x32x16_bf16 v[80:95], v[228:231], v[148:151], v[80:95]\nds_read_b64_tr_b16 v[228:229], v177 offset:11264\nds_read_b64_tr_b16 v[230:231], v177 offset:11520\n"
    "s_cmp_lt_i32 s49, 5\ns_cbranch_scc1 .Lc26\n.Lc27:\nv_mfma_f32_32x32x16_bf16 v[80:95], v[208:211], v[152:155], v[80:95]\nds_read_b64_tr_b16 v[208:209], v178 offset:0\n"
    "ds_read_b64_tr_b16 v[210:211], v178 offset:256\nv_mfma_f32_32x32x16_bf16 v[80:95], v[212:215], v[156:159], v[80:95]\nds_read_b64_tr_b16 v[212:213], v178 offset:3072\n"
    "ds_read_b64_tr_b16 v[214:215], v178 offset:3328\ns_waitcnt lgkmcnt(6)\nv_mfma_f32_32x32x16_bf16 v[96:111], v[216:219], v[144:147], v[96:111]\ns_cmp_lt_i32 s49, 6\n"
    "s_cbranch_scc1 .Lc29\n.Lc30:\nds_read_b128 v[144:147], v160 offset:8192\nds_read_b64_tr_b16 v[216:217], v178 offset:6144\n"
    "ds_read_b64_tr_b16 v[218:219], v178 offset:6400\nv_mfma_f32_32x32x16_bf16 v[96:111], v[220:223], v[148:151], v[96:111]\nds_read_b128 v[148:151], v161 offset:8192\n"
    "ds_read_b64_tr_b16 v[220:221], v178 offset:9216\nds_read_b64_tr_b16 v[222:223], v178 offset:9472\nv_mfma_f32_32x32x16_bf16 v[96:111], v[224:227], v[152:155], v[96:111]\n"
    "ds_read_b128 v[152:155], v162 offset:8192\nds_read_b64_tr_b16 v[224:225], v178 offset:1024\nds_read_b64_tr_b16 v[226:227], v178 offset:1280\ns_waitcnt lgkmcnt(13)\n"
    "v_mfma_f32_32x32x16_bf16 v[96:111], v[228:231], v[156:159], v[96:111]\nds_read_b128 v[156:159], v163 offset:8192\nds_read_b64_tr_b16 v[228:229], v178 offset:4096\n"
    "ds_read_b64_tr_b16 v[230:231], v178 offset:4352\ns_waitcnt lgkmcnt(11)\nv_dot2c_f32_bf16 v112, v241, v144\nv_dot2c_f32_bf16 v113, v144, v144\n"
    "v_dot2c_f32_bf16 v114, v241, v145\nv_dot2c_f32_bf16 v115, v145, v145\nv_dot2c_f32_bf16 v116, v241, v146\nv_dot2c_f32_bf16 v117, v146, v146\n"
    "v_dot2c_f32_bf16 v118, v241, v147\nv_dot2c_f32_bf16 v119, v147, v147\ns_waitcnt lgkmcnt(8)\nv_dot2c_f32_bf16 v112, v241, v148\nv_dot2c_f32_bf16 v113, v148, v148\n"
    "v_dot2c_f32_bf16 v114, v241, v149\nv_dot2c_f32_bf16 v115, v149, v149\nv_dot2c_f32_bf16 v116, v241, v150\nv_dot2c_f32_bf16 v117, v150, v150\n"
    "v_dot2c_f32_bf16 v118, v241, v151\nv_dot2c_f32_bf16 v119, v151, v151\ns_waitcnt lgkmcnt(5)\nv_dot2c_f32_bf16 v112, v241, v152\nv_dot2c_f32_bf16 v113, v152, v152\n"
    "v_dot2c_f32_bf16 v114, v241, v153\nv_dot2c_f32_bf16 v115, v153, v153\nv_dot2c_f32_bf16 v116, v241, v154\nv_dot2c_f32_bf16 v117, v154, v154\n"
    "v_dot2c_f32_bf16 v118, v241, v155\nv_dot2c_f32_bf16 v119, v155, v155\ns_waitcnt lgkmcnt(2)\nv_dot2c_f32_bf16 v112, v241, v156\nv_dot2c_f32_bf16 v113, v156, v156\n"
    "v_dot2c_f32_bf16 v114, v241, v157\nv_dot2c_f32_bf16 v115, v157, v157\nv_dot2c_f32_bf16 v116, v241, v158\nv_dot2c_f32_bf16 v117, v158, v158\n"
    "v_dot2c_f32_bf16 v118, v241, v159\nv_dot2c_f32_bf16 v119, v159, v159\nv_mfma_f32_32x32x16_bf16 v[64:79], v[208:211], v[144:147], v[64:79]\n"
    "ds_read_b64_tr_b16 v[208:209], v178 offset:7168\nds_read_b64_tr_b16 v[210:211], v178 offset:7424\nds_add_u32 v30, v240\n"
    "v_mfma_f32_32x32x16_bf16 v[64:79], v[212:215], v[148:151], v[64:79]\nds_read_b64_tr_b16 v[212:213], v178 offset:10240\nds_read_b64_tr_b16 v[214:215], v178 offset:10496\n"
    "ds_read_b32 v243, v56\nv_mfma_f32_32x32x16_bf16 v[64:79], v[216:219], v[152:155], v[64:79]\nds_read_b64_tr_b16 v[216:217], v178 offset:2048\n"
    "ds_read_b64_tr_b16 v[218:219], v178 offset:2304\nv_mfma_f32_32x32x16_bf16 v[64:79], v[220:223], v[156:159], v[64:79]\nds_read_b64_tr_b16 v[220:221], v178 offset:5120\n"
    "ds_read_b64_tr_b16 v[222:223], v178 offset:5376\nv_mfma_f32_32x32x16_bf16 v[80:95], v[224:227], v[144:147], v[80:95]\nds_read_b64_tr_b16 v[224:225], v178 offset:8192\n"
    "ds_read_b64_tr_b16 v[226:227], v178 offset:8448\ns_waitcnt lgkmcnt(6)\nv_readfirstlane_b32 s49, v243\n"
    "v_mfma_f32_32x32x16_bf16 v[80:95], v[228:231], v[148:151], v[80:95]\nds_read_b64_tr_b16 v[228:229], v178 offset:11264\nds_read_b64_tr_b16 v[230:231], v178 offset:11520\n"
    "s_cmp_lt_i32 s49, 7\ns_cbranch_scc1 .Lc32\n.Lc33:\nv_mfma_f32_32x32x16_bf16 v[80:95], v[208:211], v[152:155], v[80:95]\nds_read_b64_tr_b16 v[208:209], v179 offset:0\n"
    "ds_read_b64_tr_b16 v[210:211], v179 offset:256\nv_mfma_f32_32x32x16_bf16 v[80:95], v[212:215], v[156:159], v[80:95]\nds_read_b64_tr_b16 v[212:213], v179 offset:3072\n"
    "ds_read_b64_tr_b16 v[214:215], v179 offset:3328\ns_waitcnt lgkmcnt(6)\nv_mfma_f32_32x32x16_bf16 v[96:111], v[216:219], v[144:147], v[96:111]\ns_cmp_lt_i32 s49, 8\n"
    "s_cbranch_scc1 .Lc35\n.Lc36:\nds_read_b128 v[144:147], v160 offset:0\nds_read_b64_tr_b16 v[216:217], v179 offset:6144\nds_read_b64_tr_b16 v[218:219], v179 offset:6400\n"
    "v_mfma_f32_32x32x16_bf16 v[96:111], v[220:223], v[148:151], v[96:111]\nds_read_b128 v[148:151], v161 offset:0\nds_read_b64_tr_b16 v[220:221], v179 offset:9216\n"
    "ds_read_b64_tr_b16 v[222:223], v179 offset:9472\nv_mfma_f32_32x32x16_bf16 v[96:111], v[224:227], v[152:155], v[96:111]\nds_read_b128 v[152:155], v162 offset:0\n"
    "ds_read_b64_tr_b16 v[224:225], v179 offset:1024\nds_read_b64_tr_b16 v[226:227], v179 offset:1280\ns_waitcnt lgkmcnt(13)\n"
    "v_mfma_f32_32x32x16_bf16 v[96:111], v[228:231], v[156:159], v[96:111]\nds_read_b128 v[156:159], v163 offset:0\nds_read_b64_tr_b16 v[228:229], v179 offset:4096\n"
    "ds_read_b64_tr_b16 v[230:231], v179 offset:4352\ns_waitcnt lgkmcnt(11)\nv_dot2c_f32_bf16 v112, v241, v144\nv_dot2c_f32_bf16 v113, v144, v144\n"
    "v_dot2c_f32_bf16 v114, v241, v145\nv_dot2c_f32_bf16 v115, v145, v145\nv_dot2c_f32_bf16 v116, v241, v146\nv_dot2c_f32_bf16 v117, v146, v146\n"
    "v_dot2c_f32_bf16 v118, v241, v147\nv_dot2c_f32_bf16 v119, v147, v147\ns_waitcnt lgkmcnt(8)\nv_dot2c_f32_bf16 v112, v241, v148\nv_dot2c_f32_bf16 v113, v148, v148\n"
    "v_dot2c_f32_bf16 v114, v241, v149\nv_dot2c_f32_bf16 v115, v149, v149\nv_dot2c_f32_bf16 v116, v241, v150\nv_dot2c_f32_bf16 v117, v150, v150\n"
    "v_dot2c_f32_bf16 v118, v241, v151\nv_dot2c_f32_bf16 v119, v151, v151\ns_waitcnt lgkmcnt(5)\nv_dot2c_f32_bf16 v112, v241, v152\nv_dot2c_f32_bf16 v113, v152, v152\n"
    "v_dot2c_f32_bf16 v114, v241, v153\nv_dot2c_f32_bf16 v115, v153, v153\nv_dot2c_f32_bf16 v116, v241, v154\nv_dot2c_f32_bf16 v117, v154, v154\n"
    "v_dot2c_f32_bf16 v118, v241, v155\nv_dot2c_f32_bf16 v119, v155, v155\ns_waitcnt lgkmcnt(2)\nv_dot2c_f32_bf16 v112, v241, v156\nv_dot2c_f32_bf16 v113, v156, v156\n"
    "v_dot2c_f32_bf16 v114, v241, v157\nv_dot2c_f32_bf16 v115, v157, v157\nv_dot2c_f32_bf16 v116, v241, v158\nv_dot2c_f32_bf16 v117, v158, v158\n"
    "v_dot2c_f32_bf16 v118, v241, v159\nv_dot2c_f32_bf16 v119, v159, v159\nv_mfma_f32_32x32x16_bf16 v[64:79], v[208:211], v[144:147], v[64:79]\n"
    "ds_read_b64_tr_b16 v[208:209], v179 offset:7168\nds_read_b64_tr_b16 v[210:211], v179 offset:7424\nds_add_u32 v30, v240\n"
    "v_mfma_f32_32x32x16_bf16 v[64:79], v[212:215], v[148:151], v[64:79]\nds_read_b64_tr_b16 v[212:213], v179 offset:10240\nds_read_b64_tr_b16 v[214:215], v179 offset:10496\n"
    "s_nop 0\ns_cbranch_execz .Lhop\nds_read_b32 v243, v56\nv_mfma_f32_32x32x16_bf16 v[64:79], v[216:219], v[152:155], v[64:79]\n"
    "ds_read_b64_tr_b16 v[216:217], v179 offset:2048\nds_read_b64_tr_b16 v[218:219], v179 offset:2304\nv_mfma_f32_32x32x16_bf16 v[64:79], v[220:223], v[156:159], v[64:79]\n"
    "ds_read_b64_tr_b16 v[220:221], v179 offset:5120\nds_read_b64_tr_b16 v[222:223], v179 offset:5376\nv_mfma_f32_32x32x16_bf16 v[80:95], v[224:227], v[144:147], v[80:95]\n"
    "ds_read_b64_tr_b16 v[224:225], v179 offset:8192\nds_read_b64_tr_b16 v[226:227], v179 offset:8448\ns_waitcnt lgkmcnt(6)\nv_readfirstlane_b32 s49, v243\n"
    "v_mfma_f32_32x32x16_bf16 v[80:95], v[228:231], v[148:151], v[80:95]\nds_read_b64_tr_b16 v[228:229], v179 offset:11264\nds_read_b64_tr_b16 v[230:231], v179 offset:11520\n"
    "s_cmp_lt_i32 s49, 9\ns_cbranch_scc1 .Lc38\n.Lc39:\nv_mfma_f32_32x32x16_bf16 v[80:95], v[208:211], v[152:155], v[80:95]\nds_read_b64_tr_b16 v[208:209], v180 offset:0\n"
    "ds_read_b64_tr_b16 v[210:211], v180 offset:256\nv_mfma_f32_32x32x16_bf16 v[80:95], v[212:215], v[156:159], v[80:95]\nds_read_b64_tr_b16 v[212:213], v180 offset:3072\n"
    "ds_read_b64_tr_b16 v[214:215], v180 offset:3328\ns_waitcnt lgkmcnt(6)\nv_mfma_f32_32x32x16_bf16 v[96:111], v[216:219], v[144:147], v[96:111]\ns_cmp_lt_i32 s49, 10\n"
    "s_cbranch_scc1 .Lc41\n.Lc42:\nds_read_b128 v[144:147], v160 offset:4096\nds_read_b64_tr_b16 v[216:217], v180 offset:6144\n"
    "ds_read_b64_tr_b16 v[218:219], v180 offset:6400\nv_mfma_f32_32x32x16_bf16 v[96:111], v[220:223], v[148:151], v[96:111]\nds_read_b128 v[148:151], v161 offset:4096\n"
    "ds_read_b64_tr_b16 v[220:221], v180 offset:9216\nds_read_b64_tr_b16 v[222:223], v180 offset:9472\nv_mfma_f32_32x32x16_bf16 v[96:111], v[224:227], v[152:155], v[96:111]\n"
    "ds_read_b128 v[152:155], v162 offset:4096\nds_read_b64_tr_b16 v[224:225], v180 offset:1024\nds_read_b64_tr_b16 v[226:227], v180 offset:1280\ns_waitcnt lgkmcnt(13)\n"
    "v_mfma_f32_32x32x16_bf16 v[96:111], v[228:231], v[156:159], v[96:111]\nds_read_b128 v[156:159], v163 offset:4096\nds_read_b64_tr_b16 v[228:229], v180 offset:4096\n"
    "ds_read_b64_tr_b16 v[230:231], v180 offset:4352\ns_waitcnt lgkmcnt(11)\nv_dot2c_f32_bf16 v112, v241, v144\nv_dot2c_f32_bf16 v113, v144, v144\n"
    "v_dot2c_f32_bf16 v114, v241, v145\nv_dot2c_f32_bf16 v115, v145, v145\nv_dot2c_f32_bf16 v116, v241, v146\nv_dot2c_f32_bf16 v117, v146, v146\n"
    "v_dot2c_f32_bf16 v118, v241, v147\nv_dot2c_f32_bf16 v119, v147, v147\ns_waitcnt lgkmcnt(8)\nv_dot2c_f32_bf16 v112, v241, v148\nv_dot2c_f32_bf16 v113, v148, v148\n"
    "v_dot2c_f32_bf16 v114, v241, v149\nv_dot2c_f32_bf16 v115, v149, v149\nv_dot2c_f32_bf16 v116, v241, v150\nv_dot2c_f32_bf16 v117, v150, v150\n"
    "v_dot2c_f32_bf16 v118, v241, v151\nv_dot2c_f32_bf16 v119, v151, v151\ns_waitcnt lgkmcnt(5)\nv_dot2c_f32_bf16 v112, v241, v152\nv_dot2c_f32_bf16 v113, v152, v152\n"
    "v_dot2c_f32_bf16 v114, v241, v153\nv_dot2c_f32_bf16 v115, v153, v153\nv_dot2c_f32_bf16 v116, v241, v154\nv_dot2c_f32_bf16 v117, v154, v154\n"
    "v_dot2c_f32_bf16 v118, v241, v155\nv_dot2c_f32_bf16 v119, v155, v155\ns_waitcnt lgkmcnt(2)\nv_dot2c_f32_bf16 v112, v241, v156\nv_dot2c_f32_bf16 v113, v156, v156\n"
    "v_dot2c_f32_bf16 v114, v241, v157\nv_dot2c_f32_bf16 v115, v157, v157\nv_dot2c_f32_bf16 v116, v241, v158\nv_dot2c_f32_bf16 v117, v158, v158\n"
    "v_dot2c_f32_bf16 v118, v241, v159\nv_dot2c_f32_bf16 v119, v159, v159\nv_mfma_f32_32x32x16_bf16 v[64:79], v[208:211], v[144:147], v[64:79]\n"
    "ds_read_b64_tr_b16 v[208:209], v180 offset:7168\nds_read_b64_tr_b16 v[210:211], v180 offset:7424\nds_add_u32 v30, v240\n"
    "v_mfma_f32_32x32x16_bf16 v[64:79], v[212:215], v[148:151], v[64:79]\nds_read_b64_tr_b16 v[212:213], v180 offset:10240\nds_read_b64_tr_b16 v[214:215], v180 offset:10496\n"
    "ds_read_b32 v243, v56\nv_mfma_f32_32x32x16_bf16 v[64:79], v[216:219], v[152:155], v[64:79]\nds_read_b64_tr_b16 v[216:217], v180 offset:2048\n"
    "ds_read_b64_tr_b16 v[218:219], v180 offset:2304\nv_mfma_f32_32x32x16_bf16 v[64:79], v[220:223], v[156:159], v[64:79]\nds_read_b64_tr_b16 v[220:221], v180 offset:5120\n"
    "ds_read_b64_tr_b16 v[222:223], v180 offset:5376\nv_mfma_f32_32x32x16_bf16 v[80:95], v[224:227], v[144:147], v[80:95]\nds_read_b64_tr_b16 v[224:225], v180 offset:8192\n"
    "ds_read_b64_tr_b16 v[226:227], v180 offset:8448\ns_waitcnt lgkmcnt(6)\nv_readfirstlane_b32 s49, v243\n"
    "v_mfma_f32_32x32x16_bf16 v[80:95], v[228:231], v[148:151], v[80:95]\nds_read_b64_tr_b16 v[228:229], v180 offset:11264\nds_read_b64_tr_b16 v[230:231], v180 offset:11520\n"
    "s_cmp_lt_i32 s49, 11\ns_cbranch_scc1 .Lc44\n.Lc45:\nv_mfma_f32_32x32x16_bf16 v[80:95], v[208:211], v[152:155], v[80:95]\nds_read_b64_tr_b16 v[208:209], v181 offset:0\n"
    "ds_read_b64_tr_b16 v[210:211], v181 offset:256\nv_mfma_f32_32x32x16_bf16 v[80:95], v[212:215], v[156:159], v[80:95]\nds_read_b64_tr_b16 v[212:213], v181 offset:3072\n"
    "ds_read_b64_tr_b16 v[214:215], v181 offset:3328\ns_waitcnt lgkmcnt(6)\nv_mfma_f32_32x32x16_bf16 v[96:111], v[216:219], v[144:147], v[96:111]\ns_cmp_lt_i32 s49, 12\n"
    "s_cbranch_scc1 .Lc47\n.Lc48:\nds_read_b128 v[144:147], v160 offset:8192\nds_read_b64_tr_b16 v[216:217], v181 offset:6144\n"
    "ds_read_b64_tr_b16 v[218:219], v181 offset:6400\nv_mfma_f32_32x32x16_bf16 v[96:111], v[220:223], v[148:151], v[96:111]\nds_read_b128 v[148:151], v161 offset:8192\n"
    "ds_read_b64_tr_b16 v[220:221], v181 offset:9216\nds_read_b64_tr_b16 v[222:223], v181 offset:9472\nv_mfma_f32_32x32x16_bf16 v[96:111], v[224:227], v[152:155], v[96:111]\n"
    "ds_read_b128 v[152:155], v162 offset:8192\nds_read_b64_tr_b16 v[224:225], v181 offset:1024\nds_read_b64_tr_b16 v[226:227], v181 offset:1280\ns_waitcnt lgkmcnt(13)\n"
    "v_mfma_f32_32x32x16_bf16 v[96:111], v[228:231], v[156:159], v[96:111]\nds_read_b128 v[156:159], v163 offset:8192\nds_read_b64_tr_b16 v[228:229], v181 offset:4096\n"
    "ds_read_b64_tr_b16 v[230:231], v181 offset:4352\ns_waitcnt lgkmcnt(11)\nv_dot2c_f32_bf16 v112, v241, v144\nv_dot2c_f32_bf16 v113, v144, v144\n"
    "v_dot2c_f32_bf16 v114, v241, v145\nv_dot2c_f32_bf16 v115, v145, v145\nv_dot2c_f32_bf16 v116, v241, v146\nv_dot2c_f32_bf16 v117, v146, v146\n"
    "v_dot2c_f32_bf16 v118, v241, v147\nv_dot2c_f32_bf16 v119, v147, v147\ns_waitcnt lgkmcnt(8)\nv_dot2c_f32_bf16 v112, v241, v148\nv_dot2c_f32_bf16 v113, v148, v148\n"
    "v_dot2c_f32_bf16 v114, v241, v149\nv_dot2c_f32_bf16 v115, v149, v149\nv_dot2c_f32_bf16 v116, v241, v150\nv_dot2c_f32_bf16 v117, v150, v150\n"
    "v_dot2c_f32_bf16 v118, v241, v151\nv_dot2c_f32_bf16 v119, v151, v151\ns_waitcnt lgkmcnt(5)\nv_dot2c_f32_bf16 v112, v241, v152\nv_dot2c_f32_bf16 v113, v152, v152\n"
    "v_dot2c_f32_bf16 v114, v241, v153\nv_dot2c_f32_bf16 v115, v153, v153\nv_dot2c_f32_bf16 v116, v241, v154\nv_dot2c_f32_bf16 v117, v154, v154\n"
    "v_dot2c_f32_bf16 v118, v241, v155\nv_dot2c_f32_bf16 v119, v155, v155\ns_waitcnt lgkmcnt(2)\nv_dot2c_f32_bf16 v112, v241, v156\nv_dot2c_f32_bf16 v113, v156, v156\n"
    "v_dot2c_f32_bf16 v114, v241, v157\nv_dot2c_f32_bf16 v115, v157, v157\nv_dot2c_f32_bf16 v116, v241, v158\nv_dot2c_f32_bf16 v117, v158, v158\n"
    "v_dot2c_f32_bf16 v118, v241, v159\nv_dot2c_f32_bf16 v119, v159, v159\nv_mfma_f32_32x32x16_bf16 v[64:79], v[208:211], v[144:147], v[64:79]\n"
    "ds_read_b64_tr_b16 v[208:209], v181 offset:7168\nds_read_b64_tr_b16 v[210:211], v181 offset:7424\nds_add_u32 v30, v240\n"
    "v_mfma_f32_32x32x16_bf16 v[64:79], v[212:215], v[148:151], v[64:79]\nds_read_b64_tr_b16 v[212:213], v181 offset:10240\ns_nop 0\ns_cbranch_execz .Lhop\n"
    "ds_read_b64_tr_b16 v[214:215], v181 offset:10496\nds_read_b32 v243, v56\nv_mfma_f32_32x32x16_bf16 v[64:79], v[216:219], v[152:155], v[64:79]\n"
    "ds_read_b64_tr_b16 v[216:217], v181 offset:2048\nds_read_b64_tr_b16 v[218:219], v181 offset:2304\nv_mfma_f32_32x32x16_bf16 v[64:79], v[220:223], v[156:159], v[64:79]\n"
    "ds_read_b64_tr_b16 v[220:221], v181 offset:5120\nds_read_b64_tr_b16 v[222:223], v181 offset:5376\nv_mfma_f32_32x32x16_bf16 v[80:95], v[224:227], v[144:147], v[80:95]\n"
    "ds_read_b64_tr_b16 v[224:225], v181 offset:8192\nds_read_b64_tr_b16 v[226:227], v181 offset:8448\ns_waitcnt lgkmcnt(6)\nv_readfirstlane_b32 s49, v243\n"
    "v_mfma_f32_32x32x16_bf16 v[80:95], v[228:231], v[148:151], v[80:95]\nds_read_b64_tr_b16 v[228:229], v181 offset:11264\nds_read_b64_tr_b16 v[230:231], v181 offset:11520\n"
    "s_cmp_lt_i32 s49, 13\ns_cbranch_scc1 .Lc50\n.Lc51:\nv_mfma_f32_32x32x16_bf16 v[80:95], v[208:211], v[152:155], v[80:95]\nds_read_b64_tr_b16 v[208:209], v182 offset:0\n"
    "ds_read_b64_tr_b16 v[210:211], v182 offset:256\nv_mfma_f32_32x32x16_bf16 v[80:95], v[212:215], v[156:159], v[80:95]\nds_read_b64_tr_b16 v[212:213], v182 offset:3072\n"
    "ds_read_b64_tr_b16 v[214:215], v182 offset:3328\ns_waitcnt lgkmcnt(6)\nv_mfma_f32_32x32x16_bf16 v[96:111], v[216:219], v[144:147], v[96:111]\ns_cmp_lt_i32 s49, 14\n"
    "s_cbranch_scc1 .Lc53\n.Lc54:\nds_read_b128 v[144:147], v160 offset:0\nds_read_b64_tr_b16 v[216:217], v182 offset:6144\nds_read_b64_tr_b16 v[218:219], v182 offset:6400\n"
    "v_mfma_f32_32x32x16_bf16 v[96:111], v[220:223], v[148:151], v[96:111]\nds_read_b128 v[148:151], v161 offset:0\nds_read_b64_tr_b16 v[220:221], v182 offset:9216\n"
    "ds_read_b64_tr_b16 v[222:223], v182 offset:9472\nv_mfma_f32_32x32x16_bf16 v[96:111], v[224:227], v[152:155], v[96:111]\nds_read_b128 v[152:155], v162 offset:0\n"
    "ds_read_b64_tr_b16 v[224:225], v182 offset:1024\nds_read_b64_tr_b16 v[226:227], v182 offset:1280\ns_waitcnt lgkmcnt(13)\n"
    "v_mfma_f32_32x32x16_bf16 v[96:111], v[228:231], v[156:159], v[96:111]\nds_read_b128 v[156:159], v163 offset:0\nds_read_b64_tr_b16 v[228:229], v182 offset:4096\n"
    "ds_read_b64_tr_b16 v[230:231], v182 offset:4352\ns_waitcnt lgkmcnt(11)\nv_dot2c_f32_bf16 v112, v241, v144\nv_dot2c_f32_bf16 v113, v144, v144\n"
    "v_dot2c_f32_bf16 v114, v241, v145\nv_dot2c_f32_bf16 v115, v145, v145\nv_dot2c_f32_bf16 v116, v241, v146\nv_dot2c_f32_bf16 v117, v146, v146\n"
    "v_dot2c_f32_bf16 v118, v241, v147\nv_dot2c_f32_bf16 v119, v147, v147\ns_waitcnt lgkmcnt(8)\nv_dot2c_f32_bf16 v112, v241, v148\nv_dot2c_f32_bf16 v113, v148, v148\n"
    "v_dot2c_f32_bf16 v114, v241, v149\nv_dot2c_f32_bf16 v115, v149, v149\nv_dot2c_f32_bf16 v116, v241, v150\nv_dot2c_f32_bf16 v117, v150, v150\n"
    "v_dot2c_f32_bf16 v118, v241, v151\nv_dot2c_f32_bf16 v119, v151, v151\ns_waitcnt lgkmcnt(5)\nv_dot2c_f32_bf16 v112, v241, v152\nv_dot2c_f32_bf16 v113, v152, v152\n"
    "v_dot2c_f32_bf16 v114, v241, v153\nv_dot2c_f32_bf16 v115, v153, v153\nv_dot2c_f32_bf16 v116, v241, v154\nv_dot2c_f32_bf16 v117, v154, v154\n"
    "v_dot2c_f32_bf16 v118, v241, v155\nv_dot2c_f32_bf16 v119, v155, v155\ns_waitcnt lgkmcnt(2)\nv_dot2c_f32_bf16 v112, v241, v156\nv_dot2c_f32_bf16 v113, v156, v156\n"
    "v_dot2c_f32_bf16 v114, v241, v157\nv_dot2c_f32_bf16 v115, v157, v157\nv_dot2c_f32_bf16 v116, v241, v158\nv_dot2c_f32_bf16 v117, v158, v158\n"
    "v_dot2c_f32_bf16 v118, v241, v159\nv_dot2c_f32_bf16 v119, v159, v159\nv_mfma_f32_32x32x16_bf16 v[64:79], v[208:211], v[144:147], v[64:79]\n"
    "ds_read_b64_tr_b16 v[208:209], v182 offset:7168\nds_read_b64_tr_b16 v[210:211], v182 offset:7424\nds_add_u32 v30, v240\n"
    "v_mfma_f32_32x32x16_bf16 v[64:79], v[212:215], v[148:151], v[64:79]\nds_read_b64_tr_b16 v[212:213], v182 offset:10240\nds_read_b64_tr_b16 v[214:215], v182 offset:10496\n"
    "ds_read_b32 v243, v56\nv_mfma_f32_32x32x16_bf16 v[64:79], v[216:219], v[152:155], v[64:79]\nds_read_b64_tr_b16 v[216:217], v182 offset:2048\n"
    "ds_read_b64_tr_b16 v[218:219], v182 offset:2304\nv_mfma_f32_32x32x16_bf16 v[64:79], v[220:223], v[156:159], v[64:79]\nds_read_b64_tr_b16 v[220:221], v182 offset:5120\n"
    "ds_read_b64_tr_b16 v[222:223], v182 offset:5376\nv_mfma_f32_32x32x16_bf16 v[80:95], v[224:227], v[144:147], v[80:95]\nds_read_b64_tr_b16 v[224:225], v182 offset:8192\n"
    "ds_read_b64_tr_b16 v[226:227], v182 offset:8448\ns_waitcnt lgkmcnt(6)\nv_readfirstlane_b32 s49, v243\n"
    "v_mfma_f32_32x32x16_bf16 v[80:95], v[228:231], v[148:151], v[80:95]\nds_read_b64_tr_b16 v[228:229], v182 offset:11264\nds_read_b64_tr_b16 v[230:231], v182 offset:11520\n"
    "s_cmp_lt_i32 s49, 15\ns_cbranch_scc1 .Lc56\n.Lc57:\nv_mfma_f32_32x32x16_bf16 v[80:95], v[208:211], v[152:155], v[80:95]\nds_read_b64_tr_b16 v[208:209], v183 offset:0\n"
    "ds_read_b64_tr_b16 v[210:211], v183 offset:256\nv_mfma_f32_32x32x16_bf16 v[80:95], v[212:215], v[156:159], v[80:95]\nds_read_b64_tr_b16 v[212:213], v183 offset:3072\n"
    "ds_read_b64_tr_b16 v[214:215], v183 offset:3328\ns_waitcnt lgkmcnt(6)\nv_mfma_f32_32x32x16_bf16 v[96:111], v[216:219], v[144:147], v[96:111]\ns_cmp_lt_i32 s49, 16\n"
    "s_cbranch_scc1 .Lc59\n.Lc60:\nds_read_b128 v[144:147], v160 offset:4096\nds_read_b64_tr_b16 v[216:217], v183 offset:6144\n"
    "ds_read_b64_tr_b16 v[218:219], v183 offset:6400\nv_mfma_f32_32x32x16_bf16 v[96:111], v[220:223], v[148:151], v[96:111]\nds_read_b128 v[148:151], v161 offset:4096\n"
    "ds_read_b64_tr_b16 v[220:221], v183 offset:9216\nds_read_b64_tr_b16 v[222:223], v183 offset:9472\nv_mfma_f32_32x32x16_bf16 v[96:111], v[224:227], v[152:155], v[96:111]\n"
    "ds_read_b128 v[152:155], v162 offset:4096\nds_read_b64_tr_b16 v[224:225], v183 offset:1024\nds_read_b64_tr_b16 v[226:227], v183 offset:1280\ns_waitcnt lgkmcnt(13)\n"
    "v_mfma_f32_32x32x16_bf16 v[96:111], v[228:231], v[156:159], v[96:111]\nds_read_b128 v[156:159], v163 offset:4096\nds_read_b64_tr_b16 v[228:229], v183 offset:4096\n"
    "ds_read_b64_tr_b16 v[230:231], v183 offset:4352\ns_waitcnt lgkmcnt(11)\nv_dot2c_f32_bf16 v112, v241, v144\nv_dot2c_f32_bf16 v113, v144, v144\n"
    "v_dot2c_f32_bf16 v114, v241, v145\nv_dot2c_f32_bf16 v115, v145, v145\nv_dot2c_f32_bf16 v116, v241, v146\nv_dot2c_f32_bf16 v117, v146, v146\n"
    "v_dot2c_f32_bf16 v118, v241, v147\nv_dot2c_f32_bf16 v119, v147, v147\ns_waitcnt lgkmcnt(8)\nv_dot2c_f32_bf16 v112, v241, v148\nv_dot2c_f32_bf16 v113, v148, v148\n"
    "v_dot2c_f32_bf16 v114, v241, v149\nv_dot2c_f32_bf16 v115, v149, v149\nv_dot2c_f32_bf16 v116, v241, v150\nv_dot2c_f32_bf16 v117, v150, v150\n"
    "v_dot2c_f32_bf16 v118, v241, v151\nv_dot2c_f32_bf16 v119, v151, v151\ns_waitcnt lgkmcnt(5)\nv_dot2c_f32_bf16 v112, v241, v152\nv_dot2c_f32_bf16 v113, v152, v152\n"
    "v_dot2c_f32_bf16 v114, v241, v153\nv_dot2c_f32_bf16 v115, v153, v153\nv_dot2c_f32_bf16 v116, v241, v154\nv_dot2c_f32_bf16 v117, v154, v154\n"
    "v_dot2c_f32_bf16 v118, v241, v155\nv_dot2c_f32_bf16 v119, v155, v155\ns_waitcnt lgkmcnt(2)\nv_dot2c_f32_bf16 v112, v241, v156\nv_dot2c_f32_bf16 v113, v156, v156\n"
    "v_dot2c_f32_bf16 v114, v241, v157\nv_dot2c_f32_bf16 v115, v157, v157\nv_dot2c_f32_bf16 v116, v241, v158\nv_dot2c_f32_bf16 v117, v158, v158\n"
    "v_dot2c_f32_bf16 v118, v241, v159\nv_dot2c_f32_bf16 v119, v159, v159\nv_mfma_f32_32x32x16_bf16 v[64:79], v[208:211], v[144:147], v[64:79]\n"
    "ds_read_b64_tr_b16 v[208:209], v183 offset:7168\nds_read_b64_tr_b16 v[210:211], v183 offset:7424\nds_add_u32 v30, v240\n"
    "v_mfma_f32_32x32x16_bf16 v[64:79], v[212:215], v[148:151], v[64:79]\ns_nop 0\ns_cbranch_execz .Lhop\nds_read_b64_tr_b16 v[212:213], v183 offset:10240\n"
    "ds_read_b64_tr_b16 v[214:215], v183 offset:10496\nv_add_f32 v112, v112, v114\nv_add_f32 v113, v113, v115\nv_add_f32 v116, v116, v118\nv_add_f32 v117, v117, v119\n"
    "v_mfma_f32_32x32x16_bf16 v[64:79], v[216:219], v[152:155], v[64:79]\nds_read_b64_tr_b16 v[216:217], v183 offset:2048\nds_read_b64_tr_b16 v[218:219], v183 offset:2304\n"
    "v_add_f32 v112, v112, v116\nv_add_f32 v113, v113, v117\nv_mfma_f32_32x32x16_bf16 v[64:79], v[220:223], v[156:159], v[64:79]\nds_read_b32 v243, v28\n"
    "ds_read_b32 v124, v38 offset:0\nds_read_b32 v128, v38 offset:128\nds_read_b32 v132, v38 offset:256\nds_read_b64_tr_b16 v[220:221], v183 offset:5120\n"
    "ds_read_b64_tr_b16 v[222:223], v183 offset:5376\nv_permlane32_swap_b32 v112, v113\nv_add_f32 v138, v112, v113\nv_add_f32 v139, v112, v113\n"
    "v_mfma_f32_32x32x16_bf16 v[80:95], v[224:227], v[144:147], v[80:95]\nds_read_b64_tr_b16 v[224:225], v183 offset:8192\nds_read_b64_tr_b16 v[226:227], v183 offset:8448\n"
    "v_permlane32_swap_b32 v138, v139\nv_mul_f32 v138, s81, v138\nv_mul_f32 v112, v138, v138\nv_cvt_pk_bf16_f32 v138, -v138, -v138\ns_waitcnt lgkmcnt(12)\n"
    "v_mfma_f32_32x32x16_bf16 v[80:95], v[228:231], v[148:151], v[80:95]\nds_read_b64_tr_b16 v[228:229], v183 offset:11264\nds_read_b64_tr_b16 v[230:231], v183 offset:11520\n"
    "v_cndmask_b32 v120, 0, v138, s[84:85]\nv_fma_f32 v139, v139, s81, -v112\nv_max_f32 v139, 0, v139\nv_cvt_pk_bf16_f32 v139, 0, v139\n"
    "v_mfma_f32_32x32x16_bf16 v[80:95], v[208:211], v[152:155], v[80:95]\ns_waitcnt lgkmcnt(6)\nv_cmp_gt_i32 vcc, v29, v243\nv_add_f32 v139, 0x3727c5ac, v139\n"
    "v_cvt_pk_bf16_f32 v139, 0, v139\nv_sqrt_f32 v139, v139\nv_mfma_f32_32x32x16_bf16 v[80:95], v[212:215], v[156:159], v[80:95]\nv_cvt_pk_bf16_f32 v139, 0, v139\n"
    "v_rcp_f32 v136, v139\nv_mov_b64 v[232:233], 0\nv_mov_b64 v[234:235], 0\ns_cbranch_vccnz .Lt62\n.Lt63:\n"
    "v_mfma_f32_32x32x16_bf16 v[64:79], v[124:127], v[120:123], v[64:79]\nv_mov_b32 v137, v136\nv_mov_b64 v[236:237], 0\nv_mov_b64 v[238:239], 0\nv_mov_b64 v[244:245], 0\n"
    "v_mov_b64 v[246:247], 0\nv_mfma_f32_32x32x16_bf16 v[80:95], v[128:131], v[120:123], v[80:95]\nv_mov_b64 v[248:249], 0\nv_mov_b64 v[250:251], 0\n"
    "s_mov_b32 s44, 0x00ff00ff\ns_mov_b32 s45, 0xff00ff00\ns_mov_b32 s46, s44\ns_mov_b32 s47, s44\ns_waitcnt lgkmcnt(0)\n"
    "v_mfma_f32_32x32x16_bf16 v[96:111], v[216:219], v[144:147], v[96:111]\nv_mul_f32 v64, v64, v136\nv_mul_f32 v65, v65, v136\nv_mul_f32 v66, v66, v136\n"
    "v_mul_f32 v67, v67, v136\nv_cvt_pk_bf16_f32 v64, v64, v65\nv_cvt_pk_bf16_f32 v65, v66, v67\nds_write_b64 v37, v[64:65] offset:0\n"
    "v_mfma_f32_32x32x16_bf16 v[96:111], v[220:223], v[148:151], v[96:111]\nv_mul_f32 v68, v68, v136\nv_mul_f32 v69, v69, v136\nv_mul_f32 v70, v70, v136\n"
    "v_mul_f32 v71, v71, v136\nv_cvt_pk_bf16_f32 v68, v68, v69\nv_cvt_pk_bf16_f32 v69, v70, v71\nds_write_b64 v37, v[68:69] offset:16\n"
    "v_mfma_f32_32x32x16_bf16 v[96:111], v[224:227], v[152:155], v[96:111]\nv_mul_f32 v72, v72, v136\nv_mul_f32 v73, v73, v136\nv_mul_f32 v74, v74, v136\n"
    "v_mul_f32 v75, v75, v136\nv_cvt_pk_bf16_f32 v72, v72, v73\nv_cvt_pk_bf16_f32 v73, v74, v75\nds_write_b64 v37, v[72:73] offset:32\n"
    "v_mfma_f32_32x32x16_bf16 v[96:111], v[228:231], v[156:159], v[96:111]\nv_mul_f32 v76, v76, v136\nv_mul_f32 v77, v77, v136\nv_mul_f32 v78, v78, v136\n"
    "v_mul_f32 v79, v79, v136\nv_cvt_pk_bf16_f32 v76, v76, v77\nv_cvt_pk_bf16_f32 v77, v78, v79\nds_write_b64 v37, v[76:77] offset:48\n"
    "v_mfma_f32_32x32x16_bf16 v[96:111], v[132:135], v[120:123], v[96:111]\nv_pk_mul_f32 v[80:81], v[80:81], v[136:137]\nv_pk_mul_f32 v[82:83], v[82:83], v[136:137]\n"
    "v_cvt_pk_bf16_f32 v80, v80, v81\nv_cvt_pk_bf16_f32 v81, v82, v83\nds_write_b64 v37, v[80:81] offset:64\nv_pk_mul_f32 v[84:85], v[84:85], v[136:137]\n"
    "v_pk_mul_f32 v[86:87], v[86:87], v[136:137]\nv_cvt_pk_bf16_f32 v84, v84, v85\nv_cvt_pk_bf16_f32 v85, v86, v87\nds_write_b64 v37, v[84:85] offset:80\n"
    "v_pk_mul_f32 v[88:89], v[88:89], v[136:137]\nv_pk_mul_f32 v[90:91], v[90:91], v[136:137]\nv_cvt_pk_bf16_f32 v88, v88, v89\nv_cvt_pk_bf16_f32 v89, v90, v91\n"
    "ds_write_b64 v37, v[88:89] offset:96\nv_pk_mul_f32 v[92:93], v[92:93], v[136:137]\nv_pk_mul_f32 v[94:95], v[94:95], v[136:137]\nv_cvt_pk_bf16_f32 v92, v92, v93\n"
    "v_cvt_pk_bf16_f32 v93, v94, v95\nds_write_b64 v37, v[92:93] offset:112\nv_pk_mul_f32 v[96:97], v[96:97], v[136:137]\nv_pk_mul_f32 v[98:99], v[98:99], v[136:137]\n"
    "v_cvt_pk_bf16_f32 v96, v96, v97\nv_cvt_pk_bf16_f32 v97, v98, v99\nds_write_b64 v37, v[96:97] offset:128\nv_pk_mul_f32 v[100:101], v[100:101], v[136:137]\n"
    "v_pk_mul_f32 v[102:103], v[102:103], v[136:137]\nv_cvt_pk_bf16_f32 v100, v100, v101\nv_cvt_pk_bf16_f32 v101, v102, v103\nds_write_b64 v37, v[100:101] offset:144\n"
    "v_pk_mul_f32 v[104:105], v[104:105], v[136:137]\nv_pk_mul_f32 v[106:107], v[106:107], v[136:137]\nv_cvt_pk_bf16_f32 v104, v104, v105\nv_cvt_pk_bf16_f32 v105, v106, v107\n"
    "ds_write_b64 v37, v[104:105] offset:160\nv_pk_mul_f32 v[108:109], v[108:109], v[136:137]\nv_pk_mul_f32 v[110:111], v[110:111], v[136:137]\n"
    "v_cvt_pk_bf16_f32 v108, v108, v109\nv_cvt_pk_bf16_f32 v109, v110, v111\nds_write_b64 v37, v[108:109] offset:176\ns_branch .Latt12\n.Lc20:\ns_mov_b32 s86, 0x100000\n"
    ".Lc22:\nds_read_b32 v243, v56\ns_waitcnt lgkmcnt(0)\nv_readfirstlane_b32 s49, v243\ns_sub_u32 s86, s86, 1\ns_cbranch_scc1 .Lc21\ns_cmp_ge_i32 s49, 3\n"
    "s_cbranch_scc1 .Lc21\ns_branch .Lc22\n.Lc23:\ns_mov_b32 s86, 0x100000\n.Lc25:\nds_read_b32 v243, v56\ns_waitcnt lgkmcnt(0)\nv_readfirstlane_b32 s49, v243\n"
    "s_sub_u32 s86, s86, 1\ns_cbranch_scc1 .Lc24\ns_cmp_ge_i32 s49, 4\ns_cbranch_scc1 .Lc24\ns_branch .Lc25\n.Lc26:\ns_mov_b32 s86, 0x100000\n.Lc28:\nds_read_b32 v243, v56\n"
    "s_waitcnt lgkmcnt(0)\nv_readfirstlane_b32 s49, v243\ns_sub_u32 s86, s86, 1\ns_cbranch_scc1 .Lc27\ns_cmp_ge_i32 s49, 5\ns_cbranch_execz .Lhop\ns_cbranch_scc1 .Lc27\n"
    "s_branch .Lc28\n.Lc29:\ns_mov_b32 s86, 0x100000\n.Lc31:\nds_read_b32 v243, v56\ns_waitcnt lgkmcnt(0)\nv_readfirstlane_b32 s49, v243\ns_sub_u32 s86, s86, 1\n"
    "s_cbranch_scc1 .Lc30\ns_cmp_ge_i32 s49, 6\ns_cbranch_scc1 .Lc30\ns_branch .Lc31\n.Lc32:\ns_mov_b32 s86, 0x100000\n.Lc34:\nds_read_b32 v243, v56\ns_waitcnt lgkmcnt(0)\n"
    "v_readfirstlane_b32 s49, v243\ns_sub_u32 s86, s86, 1\ns_cbranch_scc1 .Lc33\ns_cmp_ge_i32 s49, 7\ns_cbranch_scc1 .Lc33\ns_branch .Lc34\n.Lc35:\ns_mov_b32 s86, 0x100000\n"
    ".Lc37:\nds_read_b32 v243, v56\ns_waitcnt lgkmcnt(0)\nv_readfirstlane_b32 s49, v243\ns_sub_u32 s86, s86, 1\ns_cbranch_scc1 .Lc36\ns_cmp_ge_i32 s49, 8\n"
    "s_cbranch_scc1 .Lc36\ns_branch .Lc37\n.Lc38:\ns_mov_b32 s86, 0x100000\n.Lc40:\nds_read_b32 v243, v56\ns_waitcnt lgkmcnt(0)\nv_readfirstlane_b32 s49, v243\n"
    "s_sub_u32 s86, s86, 1\ns_cbranch_scc1 .Lc39\ns_cmp_ge_i32 s49, 9\ns_cbranch_scc1 .Lc39\ns_branch .Lc40\n.Lc41:\ns_mov_b32 s86, 0x100000\n.Lc43:\nds_read_b32 v243, v56\n"
    "s_waitcnt lgkmcnt(0)\nv_readfirstlane_b32 s49, v243\ns_sub_u32 s86, s86, 1\ns_cbranch_scc1 .Lc42\ns_cmp_ge_i32 s49, 10\ns_cbranch_scc1 .Lc42\ns_branch .Lc43\n.Lc44:\n"
    "s_mov_b32 s86, 0x100000\n.Lc46:\nds_read_b32 v243, v56\ns_waitcnt lgkmcnt(0)\nv_readfirstlane_b32 s49, v243\ns_sub_u32 s86, s86, 1\ns_cbranch_scc1 .Lc45\n"
    "s_cmp_ge_i32 s49, 11\ns_cbranch_scc1 .Lc45\ns_branch .Lc46\n.Lc47:\ns_mov_b32 s86, 0x100000\n.Lc49:\nds_read_b32 v243, v56\ns_waitcnt lgkmcnt(0)\n"
    "v_readfirstlane_b32 s49, v243\ns_sub_u32 s86, s86, 1\ns_cbranch_scc1 .Lc48\ns_cmp_ge_i32 s49, 12\ns_cbranch_scc1 .Lc48\ns_branch .Lc49\n.Lc50:\ns_mov_b32 s86, 0x100000\n"
    ".Lc52:\nds_read_b32 v243, v56\ns_waitcnt lgkmcnt(0)\nv_readfirstlane_b32 s49, v243\ns_sub_u32 s86, s86, 1\ns_cbranch_scc1 .Lc51\ns_cmp_ge_i32 s49, 13\n"
    "s_cbranch_scc1 .Lc51\ns_branch .Lc52\n.Lc53:\ns_mov_b32 s86, 0x100000\n.Lc55:\nds_read_b32 v243, v56\ns_waitcnt lgkmcnt(0)\nv_readfirstlane_b32 s49, v243\n"
    "s_sub_u32 s86, s86, 1\ns_cbranch_scc1 .Lc54\ns_cmp_ge_i32 s49, 14\ns_cbranch_scc1 .Lc54\ns_branch .Lc55\n.Lc56:\ns_mov_b32 s86, 0x100000\n.Lc58:\nds_read_b32 v243, v56\n"
    "s_waitcnt lgkmcnt(0)\nv_readfirstlane_b32 s49, v243\ns_sub_u32 s86, s86, 1\ns_cbranch_scc1 .Lc57\ns_cmp_ge_i32 s49, 15\ns_cbranch_scc1 .Lc57\ns_branch .Lc58\n.Lc59:\n"
    "s_mov_b32 s86, 0x100000\n.Lc61:\nds_read_b32 v243, v56\ns_waitcnt lgkmcnt(0)\nv_readfirstlane_b32 s49, v243\ns_sub_u32 s86, s86, 1\ns_cbranch_scc1 .Lc60\n"
    "s_cmp_ge_i32 s49, 16\ns_cbranch_scc1 .Lc60\ns_branch .Lc61\n.Lt62:\ns_mov_b32 s86, 0x100000\n.Ls64:\nds_read_b32 v243, v28 offset:0\ns_waitcnt lgkmcnt(0)\n"
    "v_cmp_gt_i32 vcc, v29, v243\ns_sub_u32 s86, s86, 1\ns_cbranch_scc1 .Ls65\ns_cbranch_vccz .Ls65\ns_branch .Ls64\n.Ls65:\nds_read_b32 v124, v38 offset:0\n"
    "ds_read_b32 v128, v38 offset:128\nds_read_b32 v132, v38 offset:256\ns_waitcnt lgkmcnt(0)\ns_branch .Lt63\n"
    ".Lhpre66:\nv_and_b32 v224, 127, v0\ns_getpc_b64 s[44:45]\ns_add_u32 s44, s44, g_tabs@rel32@lo+4\ns_addc_u32 s45, s45, g_tabs@rel32@hi+12\nv_lshlrev_b32 v224, 4, v224\n"
    "global_load_dwordx4 v[48:51], v224, s[44:45] offset:2064\nglobal_load_dwordx4 v[52:55], v224, s[44:45]\nglobal_load_dwordx2 v[62:63], v224, s[44:45] offset:16\n"
    "s_lshl_b32 s43, s22, 17\ns_lshl_b32 s42, s13, 15\ns_add_u32 s44, s43, s42\ns_add_u32 s44, s44, 0xe01000\nv_and_b32 v231, 63, v0\nv_lshlrev_b32 v59, 4, v231\n"
    "s_add_u32 s68, s4, s44\ns_addc_u32 s69, s5, 0\ns_add_u32 s70, s68, 8192\ns_addc_u32 s71, s69, 0\ns_add_u32 s72, s68, 16384\ns_addc_u32 s73, s69, 0\n"
    "s_add_u32 s74, s68, 24576\ns_addc_u32 s75, s69, 0\nv_bfe_u32 v251, v0, 5, 1\nv_and_b32 v250, 31, v0\ns_lshl_b32 s43, s13, 11\ns_add_u32 s42, s43, 0x1d000\n"
    "v_lshl_add_u32 v40, v251, 8, s42\nv_lshl_add_u32 v40, v250, 2, v40\nv_and_b32 v249, 63, v0\nv_lshl_add_u32 v41, v249, 4, s42\ns_mov_b32 s41, 0x1f600\n"
    "v_lshl_add_u32 v42, v249, 4, s41\nv_bfe_u32 v248, v0, 3, 3\nv_and_b32 v247, 7, v0\ns_lshl_b32 s40, s22, 19\ns_lshl_b32 s39, s13, 17\ns_lshl_b32 s38, s21, 7\n"
    "s_add_u32 s37, s12, s40\ns_add_u32 s37, s37, s39\ns_add_u32 s37, s37, s38\ns_add_u32 s37, s37, 0x200000\nv_lshl_add_u32 v43, v248, 13, s37\n"
    "v_lshl_add_u32 v43, v247, 4, v43\ns_add_u32 s36, s12, s40\ns_add_u32 s36, s36, s39\ns_add_u32 s36, s36, s38\ns_add_u32 s36, s36, 0x210000\n"
    "v_lshl_add_u32 v44, v248, 13, s36\nv_lshl_add_u32 v44, v247, 4, v44\ns_lshl_b32 s35, s13, 8\ns_add_u32 s34, s35, 0x27600\nv_lshl_add_u32 v45, v249, 2, s34\n"
    "s_add_u32 s33, s43, 0x1d400\nv_lshl_add_u32 v46, v251, 8, s33\nv_lshl_add_u32 v46, v250, 2, v46\nv_and_b32 v251, 31, v0\nv_bfe_u32 v250, v0, 5, 1\n"
    "s_lshl_b32 s43, s21, 15\ns_lshl_b32 s42, s22, 8\ns_lshl_b32 s41, s13, 6\ns_add_u32 s40, s43, s42\ns_add_u32 s40, s40, s41\nv_lshl_add_u32 v47, v251, 10, s40\n"
    "v_lshl_add_u32 v47, v250, 4, v47\nv_and_b32 v249, 63, v0\ns_lshr_b32 s39, s13, 1\ns_lshl_b32 s38, s39, 13\ns_and_b32 s37, s13, 1\ns_lshl_b32 s36, s37, 11\n"
    "s_add_u32 s35, s38, s36\ns_add_u32 s35, s35, 0x1f600\nv_lshl_add_u32 v60, v249, 4, s35\nv_and_b32 v248, 1, v0\ns_lshl_b32 s34, s39, 3\ns_add_u32 s33, s34, 0x27fc0\n"
    "v_lshl_add_u32 v61, v248, 2, s33\ns_add_u32 s32, s34, 0x27d80\ns_cbranch_execz .Lhop\nv_mov_b32 v252, s32\nv_and_b32 v247, 0x7f, v0\ns_mul_i32 s31, s39, 0x2100\n"
    "s_add_u32 s30, s31, 0x18000\nv_lshl_add_u32 v253, v247, 4, s30\ns_lshr_b32 s43, s13, 1\ns_mul_i32 s42, s43, 0x600\ns_add_u32 s41, s42, 0x1c200\nv_mov_b32 v254, s41\n"
    "s_lshl_b32 s40, s13, 2\ns_add_u32 s39, s40, 0x27f80\nv_mov_b32 v25, s39\nv_mov_b64 v[232:233], 0\nv_mov_b64 v[234:235], 0\nv_mov_b64 v[236:237], 0\n"
    "v_mov_b64 v[238:239], 0\nv_mov_b64 v[244:245], 0\nv_mov_b64 v[246:247], 0\nv_mov_b64 v[248:249], 0\nv_mov_b64 v[250:251], 0\ns_setpc_b64 s[94:95]\n.Lhcon67_0:\n"
    "s_add_u32 s43, s12, 0x400000\nv_lshl_add_u32 v16, v0, 4, s43\ns_lshl_b32 s42, s18, 13\ns_add_u32 s41, s12, s42\ns_add_u32 s41, s41, 0x200000\n"
    "v_lshl_add_u32 v17, v0, 4, s41\nv_lshrrev_b32 v231, 3, v0\nv_mul_u32_u24 v230, 0x50, v231\nv_and_b32 v229, 7, v0\nv_add_u32 v18, 0x1d550, v230\n"
    "v_lshl_add_u32 v18, v229, 3, v18\nv_bfe_u32 v228, v0, 4, 3\nv_xor_b32 v227, v0, v228\ns_mov_b32 s40, 0x19400\nv_lshl_add_u32 v19, v227, 3, s40\nv_bfe_u32 v226, v0, 4, 1\n"
    "v_xor_b32 v225, v0, v226\ns_mov_b32 s39, 0x17c00\nv_lshl_add_u32 v20, v225, 3, s39\ns_lshr_b32 s43, s12, 23\ns_add_u32 s42, s43, 3\nv_lshrrev_b32 v231, s42, v0\n"
    "s_lshr_b32 s41, s12, 22\ns_sub_u32 s40, 15, s41\nv_lshlrev_b32 v230, s40, v231\ns_lshr_b32 s39, s12, 20\ns_add_u32 s38, s39, 7\nv_and_b32 v229, s38, v0\n"
    "s_lshl_b32 s37, s18, 7\ns_add_u32 s36, s37, 0x600000\ns_lshl_b32 s35, s24, 18\ns_lshl_b32 s34, s25, 8\ns_add_u32 s33, s35, s34\ns_add_u32 s33, s33, 0x1200000\n"
    "s_cmp_lt_u32 s12, 1\ns_cselect_b32 s32, s36, s33\nv_add_u32 v21, s32, v230\nv_lshl_add_u32 v21, v229, 4, v21\nv_and_b32 v231, 63, v0\nv_lshlrev_b32 v230, 4, v231\n"
    "v_mov_b32 v229, 0xfc0\ns_lshr_b32 s43, s13, 1\ns_mul_i32 s42, s43, 26\nv_add_u32 v228, s42, v231\nv_cmp_gt_u32 vcc, 64, v228\ns_nop 1\n"
    "v_cndmask_b32 v227, v229, v230, vcc\ns_lshr_b32 s41, s3, 2\ns_mul_i32 s40, s41, 0x1950\ns_mul_i32 s39, s13, 0x150\ns_add_u32 s38, s40, s39\ns_add_u32 s38, s38, 0x1d000\n"
    "s_mul_i32 s37, s41, 0x3180\ns_and_b32 s36, s13, 1\ns_mul_i32 s35, s36, 0xfc0\ns_add_u32 s34, s37, s35\ns_add_u32 s34, s34, 0x20300\ns_cmp_lt_u32 s13, 2\n"
    "s_cselect_b32 s33, s38, s34\nv_add_u32 v22, s33, v227\nv_and_b32 v226, 31, v0\nv_sub_u32 v225, 0x3ff, v226\nv_bfe_u32 v224, v0, 5, 1\nv_lshl_add_u32 v175, v224, 3, v225\n"
    "v_and_b32 v174, 3, v175\nv_mul_u32_u24 v173, 0x840, v174\nv_lshrrev_b32 v172, 2, v175\ns_mul_i32 s32, s43, 0x2100\ns_add_u32 s31, s32, 0x17800\nv_add_u32 v23, s31, v173\n"
    "v_lshl_add_u32 v23, v172, 3, v23\ns_lshr_b32 s43, s12, 23\ns_add_u32 s42, s43, 4\nv_mov_b32 v26, s42\ns_setpc_b64 s[94:95]\n.Lhcon67_1:\nv_and_b32 v231, 63, v0\n"
    "v_add_u32 v230, 3, v231\nv_lshrrev_b32 v229, 2, v230\nv_mul_u32_u24 v228, 0x50, v229\nv_and_b32 v227, 3, v230\ns_lshr_b32 s43, s13, 1\ns_mul_i32 s42, s43, 0x1950\n"
    "s_add_u32 s41, s42, 0x1d500\nv_add_u32 v10, s41, v228\nv_lshl_add_u32 v10, v227, 4, v10\nv_add_u32 v226, 2, v231\nv_lshrrev_b32 v225, 2, v226\n"
    "v_mul_u32_u24 v224, 0x50, v225\nv_and_b32 v175, 3, v226\ns_add_u32 s40, s42, 0x1d508\nv_add_u32 v11, s40, v224\nv_lshl_add_u32 v11, v175, 4, v11\n"
    "v_bfe_u32 v174, v0, 2, 4\nv_mul_u32_u24 v173, 0x50, v174\nv_and_b32 v172, 3, v0\ns_add_u32 s39, s42, 0x1d550\nv_add_u32 v12, s39, v173\nv_lshl_add_u32 v12, v172, 4, v12\n"
    "s_and_b32 s38, s13, 1\nv_add_u32 v171, 1, v231\nv_lshrrev_b32 v170, 5, v171\nv_mul_u32_u24 v169, 0x240, v170\nv_and_b32 v168, 31, v171\ns_mul_i32 s37, s43, 0x3180\n"
    "s_add_u32 s36, s37, 0x20540\nv_add_u32 v167, s36, v169\nv_lshl_add_u32 v167, v168, 4, v167\nv_lshrrev_b32 v166, 6, v171\nv_mul_u32_u24 v165, 0x480, v166\n"
    "v_and_b32 v164, 63, v171\ns_add_u32 s35, s37, 0x22280\nv_add_u32 v163, s35, v165\nv_lshl_add_u32 v163, v164, 4, v163\ns_cmp_lt_u32 s38, 1\ns_cselect_b64 vcc, -1, 0\n"
    "v_cndmask_b32 v13, v163, v167, vcc\ns_and_b32 s43, s13, 1\nv_and_b32 v231, 63, v0\nv_add_u32 v230, 2, v231\nv_lshrrev_b32 v229, 5, v230\nv_mul_u32_u24 v228, 0x240, v229\n"
    "v_and_b32 v227, 31, v230\ns_lshr_b32 s42, s13, 1\ns_mul_i32 s41, s42, 0x3180\ns_add_u32 s40, s41, 0x20540\nv_add_u32 v226, s40, v228\nv_lshl_add_u32 v226, v227, 4, v226\n"
    "s_add_u32 s39, s41, 0x22b80\nv_lshl_add_u32 v225, v231, 4, s39\ns_cmp_lt_u32 s43, 1\ns_cselect_b64 vcc, -1, 0\nv_cndmask_b32 v14, v225, v226, vcc\n"
    "v_add_u32 v224, 1, v231\nv_lshrrev_b32 v175, 5, v224\nv_mul_u32_u24 v174, 0x240, v175\nv_and_b32 v173, 31, v224\nv_add_u32 v172, s40, v174\n"
    "v_lshl_add_u32 v172, v173, 4, v172\nv_lshrrev_b32 v171, 6, v224\nv_mul_u32_u24 v170, 0x480, v171\nv_and_b32 v169, 63, v224\ns_add_u32 s38, s41, 0x22280\n"
    "v_add_u32 v168, s38, v170\nv_lshl_add_u32 v168, v169, 4, v168\ns_cmp_lt_u32 s43, 1\ns_cselect_b64 vcc, -1, 0\nv_cndmask_b32 v167, v168, v172, vcc\n"
    "s_mov_b32 s37, 0xfffffb40\ns_cmp_lt_u32 s43, 1\ns_cselect_b32 s36, s37, 0x1f00\ns_add_u32 s35, s41, s36\ns_add_u32 s35, s35, 0x20300\nv_mov_b32 v166, s35\n"
    "v_cmp_gt_u32 vcc, 64, v224\ns_nop 1\nv_cndmask_b32 v15, v166, v167, vcc\ns_add_u32 s34, s41, 0x1fe40\nv_mov_b32 v165, s34\nv_cmp_gt_u32 vcc, 64, v230\ns_nop 1\n"
    "v_cndmask_b32 v35, v165, v226, vcc\ns_lshl_b32 s33, s18, 13\ns_lshl_b32 s32, s13, 11\ns_add_u32 s31, s33, s32\ns_cbranch_execz .Lhop\ns_add_u32 s31, s31, 0x800000\n"
    "v_lshl_add_u32 v37, v231, 4, s31\nv_and_b32 v231, 7, v0\ns_lshl_b32 s43, s2, 7\ns_add_u32 s42, s43, 0xc00000\nv_lshl_add_u32 v38, v231, 4, s42\ns_setpc_b64 s[94:95]\n"
    ".Lhcon67_2:\ns_lshr_b32 s43, s13, 1\ns_lshl_b32 s42, s43, 3\ns_add_u32 s41, s42, 0x27fb0\nv_mov_b32 v24, s41\nv_add_u32 v231, 0xffffff86, v0\n"
    "v_lshrrev_b32 v230, 3, v231\nv_mul_u32_u24 v229, 0x50, v230\nv_and_b32 v228, 7, v231\nv_add_u32 v30, 0x1d004, v229\nv_lshl_add_u32 v30, v228, 3, v30\n"
    "v_add_u32 v227, 0xffffff87, v0\nv_lshrrev_b32 v226, 3, v227\nv_mul_u32_u24 v225, 0x50, v226\nv_and_b32 v224, 7, v227\nv_add_u32 v31, 0x1d000, v225\n"
    "v_lshl_add_u32 v31, v224, 3, v31\ns_and_b32 s40, s13, 1\nv_bfe_u32 v175, v0, 4, 1\nv_mul_u32_u24 v174, 0x480, v175\nv_mov_b32 v173, 0\ns_cmp_lt_u32 s40, 1\n"
    "s_cselect_b64 vcc, -1, 0\nv_cndmask_b32 v172, v173, v174, vcc\nv_bfe_u32 v171, v0, 1, 1\nv_mul_u32_u24 v170, 0x240, v171\nv_bfe_u32 v169, v0, 5, 1\n"
    "v_bfe_u32 v168, v0, 2, 2\nv_and_b32 v167, 1, v0\ns_mul_i32 s39, s43, 0x3180\ns_add_u32 s38, s39, 0x20300\nv_add_u32 v27, s38, v172\nv_add_u32 v27, v27, v170\n"
    "v_lshl_add_u32 v27, v169, 7, v27\nv_lshl_add_u32 v27, v168, 4, v27\nv_lshl_add_u32 v27, v167, 3, v27\nv_and_b32 v166, 3, v0\ns_add_u32 s37, s39, 0x22280\n"
    "v_lshl_add_u32 v29, v169, 8, s37\nv_lshl_add_u32 v29, v168, 5, v29\nv_lshl_add_u32 v29, v166, 3, v29\nv_add_u32 v28, s37, v174\nv_lshl_add_u32 v28, v169, 8, v28\n"
    "v_lshl_add_u32 v28, v168, 5, v28\nv_lshl_add_u32 v28, v166, 3, v28\nv_and_b32 v165, 31, v0\nv_bfe_u32 v164, v0, 3, 1\nv_xor_b32 v163, v169, v164\n"
    "s_mul_i32 s36, s43, 0x2800\ns_add_u32 s35, s36, 0x18000\nv_lshl_add_u32 v32, v165, 4, s35\nv_lshl_add_u32 v32, v163, 3, v32\nv_xor_b32 v162, v169, v168\n"
    "s_add_u32 s34, s36, 0x18c00\nv_lshl_add_u32 v33, v165, 5, s34\nv_lshl_add_u32 v33, v162, 3, v33\nv_bfe_u32 v161, v0, 4, 2\nv_xor_b32 v160, v0, v161\n"
    "s_mov_b32 s33, 0x18400\nv_lshl_add_u32 v34, v160, 3, s33\nv_xor_b32 v36, 16, v33\ns_bitcmp0_b32 s13, 0\ns_cselect_b32 s30, 256, 0\nv_add_u32 v39, s30, v23\n"
    "s_setpc_b64 s[94:95]\n"
    ".Llstb7:\nv_mov_b32 v72, 1\nds_write_b32 v56, v72 offset:32\ns_mov_b32 s58, 2\ns_mov_b32 s57, 98304\ns_add_u32 s50, s50, 1\ns_and_b32 s50, s50, 7\n"
    "s_sub_u32 s62, s61, 16384\ns_max_i32 s62, s62, 0\ns_add_u32 s62, s8, s62\ns_addc_u32 s63, s9, 0\nv_and_b32 v251, 63, v0\nv_lshlrev_b32 v71, 4, v251\n"
    "v_bfe_u32 v250, v0, 2, 4\nv_and_b32 v249, 3, v0\ns_mov_b32 s43, 0x1002000\nv_lshl_add_u32 v57, v250, 7, s43\nv_lshl_add_u32 v57, v249, 4, v57\ns_lshl_b32 s42, s19, 7\n"
    "s_lshl_b32 s41, s20, 2\ns_add_u32 s40, s42, s41\ns_add_u32 s40, s40, 0x1004000\nv_mov_b32 v58, s40\ns_lshl_b32 s39, s19, 17\ns_lshl_b32 s38, s20, 11\n"
    "s_add_u32 s37, s39, s38\nv_lshl_add_u32 v79, v251, 4, s37\nv_mov_b32 v80, 0x1000000\ns_lshl_b32 s36, s25, 7\ns_add_u32 s35, s36, 0xe04000\n"
    "v_lshl_add_u32 v188, v250, 15, s35\nv_lshl_add_u32 v188, v249, 12, v188\ns_lshl_b32 s34, s18, 13\ns_add_u32 s33, s34, 0x201000\nv_lshl_add_u32 v186, v251, 4, s33\n"
    "v_and_b32 v248, 7, v0\ns_lshl_b32 s32, s2, 7\ns_add_u32 s31, s32, 0x400000\nv_lshl_add_u32 v187, v248, 4, s31\nv_mov_b64 v[180:181], -1\nv_mov_b64 v[182:183], -1\n"
    "global_load_dword v78, v80, s[4:5] sc1\ns_mov_b32 m0, 146432\nv_xor_b32 v72, 32, v71\nglobal_load_lds_dwordx4 v72, s[62:63] offset:2048\nv_xor_b32 v73, 48, v71\n"
    "global_load_lds_dwordx4 v73, s[62:63] offset:3072\ns_mov_b32 m0, 150528\nv_xor_b32 v72, 4160, v71\nglobal_load_lds_dwordx4 v72, s[62:63] offset:0\n"
    "v_xor_b32 v73, 4176, v71\nglobal_load_lds_dwordx4 v73, s[62:63] offset:1024\nv_xor_b32 v72, 4192, v71\nglobal_load_lds_dwordx4 v72, s[62:63] offset:2048\n"
    "v_xor_b32 v73, 4208, v71\nglobal_load_lds_dwordx4 v73, s[62:63] offset:3072\ns_mov_b32 m0, 154624\nv_xor_b32 v72, 8320, v71\n"
    "global_load_lds_dwordx4 v72, s[62:63] offset:0\nv_xor_b32 v73, 8336, v71\nglobal_load_lds_dwordx4 v73, s[62:63] offset:1024\nv_xor_b32 v72, 8352, v71\n"
    "global_load_lds_dwordx4 v72, s[62:63] offset:2048\nv_xor_b32 v73, 8368, v71\nglobal_load_lds_dwordx4 v73, s[62:63] offset:3072\ns_mov_b32 m0, 158720\n"
    "v_xor_b32 v72, 12480, v71\nglobal_load_lds_dwordx4 v72, s[62:63] offset:0\nv_xor_b32 v73, 12496, v71\nglobal_load_lds_dwordx4 v73, s[62:63] offset:1024\n"
    "v_xor_b32 v72, 12512, v71\nglobal_load_lds_dwordx4 v72, s[62:63] offset:2048\nv_xor_b32 v73, 12528, v71\nglobal_load_lds_dwordx4 v73, s[62:63] offset:3072\n"
    "s_call_b64 s[94:95], .Lhpre66\ns_setprio 2\n.Ll68:\nds_read_b128 v[74:77], v56 offset:96\ns_mul_i32 s59, s50, 196608\ns_mul_i32 s56, s50, 12288\ns_add_u32 s52, s66, s59\n"
    "s_addc_u32 s53, s67, 0\ns_mov_b32 m0, s56\ns_nop 0\nglobal_load_lds_dwordx4 v64, s[52:53] offset:0\nglobal_load_lds_dwordx4 v64, s[52:53] offset:1024\n"
    "global_load_lds_dwordx4 v64, s[52:53] offset:2048\ns_add_u32 s56, s56, 3072\ns_cbranch_execz .Lhop\ns_mov_b32 m0, s56\ns_add_u32 s52, s52, 49152\ns_addc_u32 s53, s53, 0\n"
    "global_load_lds_dwordx4 v64, s[52:53] offset:0\nglobal_load_lds_dwordx4 v64, s[52:53] offset:1024\nglobal_load_lds_dwordx4 v64, s[52:53] offset:2048\n"
    "s_add_u32 s56, s56, 3072\ns_mov_b32 m0, s56\ns_add_u32 s52, s52, 49152\ns_addc_u32 s53, s53, 0\nglobal_load_lds_dwordx4 v64, s[52:53] offset:0\n"
    "global_load_lds_dwordx4 v64, s[52:53] offset:1024\nglobal_load_lds_dwordx4 v64, s[52:53] offset:2048\ns_add_u32 s56, s56, 3072\ns_lshl_b32 s59, s50, 7\n"
    "s_add_u32 s54, s48, s59\ns_addc_u32 s55, s49, 0\ns_add_u32 s57, s57, 4096\ns_cmp_eq_u32 s57, 110592\ns_cselect_b32 s57, 98304, s57\ns_sub_u32 s61, s58, 4\n"
    "s_waitcnt lgkmcnt(0)\nv_min3_i32 v74, v74, v75, v76\nv_min_i32 v74, v74, v77\nv_cmp_gt_i32 vcc, s61, v74\ns_add_u32 s59, s58, -1\ns_cmp_lg_u32 s58, 2\n"
    "s_cbranch_scc1 .Ll72\ns_mov_b32 s59, s58\ns_waitcnt vmcnt(23)\n.Ll72:\nv_mov_b32 v70, s59\ns_mov_b64 exec, 1\ns_waitcnt vmcnt(25)\nds_max_i32 v56, v70\n"
    "s_mov_b64 exec, -1\ns_mov_b32 m0, s56\ns_add_u32 s52, s52, 49152\ns_addc_u32 s53, s53, 0\nglobal_load_lds_dwordx4 v64, s[52:53] offset:0\n"
    "global_load_lds_dwordx4 v64, s[52:53] offset:1024\nglobal_load_lds_dwordx4 v64, s[52:53] offset:2048\ns_add_u32 s56, s56, 3072\ns_cbranch_vccnz .Ll69\n.Ll70:\n"
    "s_add_u32 m0, s57, 0\ns_nop 0\nglobal_load_lds_dwordx4 v65, s[54:55] offset:0\nglobal_load_lds_dwordx4 v66, s[54:55] offset:1024\n"
    "global_load_lds_dwordx4 v67, s[54:55] offset:2048\nglobal_load_lds_dwordx4 v68, s[54:55] offset:3072\ns_add_u32 m0, s57, 12288\ns_add_u32 s54, s54, 32768\n"
    "s_addc_u32 s55, s55, 0\nglobal_load_lds_dwordx4 v65, s[54:55] offset:0\nglobal_load_lds_dwordx4 v66, s[54:55] offset:1024\n"
    "global_load_lds_dwordx4 v67, s[54:55] offset:2048\nglobal_load_lds_dwordx4 v68, s[54:55] offset:3072\ns_add_u32 m0, s57, 24576\ns_add_u32 s54, s54, 32768\n"
    "s_addc_u32 s55, s55, 0\nglobal_load_lds_dwordx4 v65, s[54:55] offset:0\nglobal_load_lds_dwordx4 v66, s[54:55] offset:1024\n"
    "global_load_lds_dwordx4 v67, s[54:55] offset:2048\nglobal_load_lds_dwordx4 v68, s[54:55] offset:3072\ns_add_u32 s59, s58, 0\nv_mov_b32 v70, s59\ns_mov_b64 exec, 1\n"
    "s_waitcnt vmcnt(24)\nds_max_i32 v56, v70\ns_mov_b64 exec, -1\ns_add_u32 m0, s57, 36864\ns_add_u32 s54, s54, 32768\ns_addc_u32 s55, s55, 0\n"
    "global_load_lds_dwordx4 v65, s[54:55] offset:0\nglobal_load_lds_dwordx4 v66, s[54:55] offset:1024\nglobal_load_lds_dwordx4 v67, s[54:55] offset:2048\n"
    "global_load_lds_dwordx4 v68, s[54:55] offset:3072\ns_cmp_lg_u32 s58, 12\ns_cbranch_scc1 .Ll73\nglobal_load_dwordx4 v[84:87], v57, s[4:5] sc1\n.Ll73:\n"
    "s_add_u32 s50, s50, 1\ns_and_b32 s50, s50, 7\ns_add_u32 s58, s58, 2\ns_cmp_lt_u32 s58, 16\ns_cbranch_scc1 .Ll68\nv_readfirstlane_b32 s11, v78\n"
    "s_mul_i32 s11, s11, 0x9E3779B1\ns_add_u32 s11, s11, 0x7F4A7C15\nv_xor_b32 v84, s11, v84\nv_xor_b32 v85, s11, v85\nv_xor_b32 v86, s11, v86\nv_xor_b32 v87, s11, v87\n"
    "v_or3_b32 v78, v84, v85, v86\nv_or_b32 v78, v78, v87\nv_cmp_ne_u32 vcc, 0, v78\ns_cmp_eq_u64 vcc, 0\ns_cselect_b32 s48, 18, 17\nv_mov_b32 v73, s48\ns_mov_b64 exec, 1\n"
    "ds_write_b32 v56, v73 offset:4\ns_mov_b64 exec, -1\ns_add_u32 s59, s58, -1\nv_mov_b32 v70, s59\ns_mov_b64 exec, 1\ns_waitcnt vmcnt(16)\nds_max_i32 v56, v70\n"
    "s_mov_b64 exec, -1\nglobal_store_dwordx4 v79, v[180:183], s[4:5] offset:0 sc1\nglobal_store_dwordx4 v79, v[180:183], s[4:5] offset:1024 sc1\nv_add_u32 v79, 0x8000, v79\n"
    "global_store_dwordx4 v79, v[180:183], s[4:5] offset:0 sc1\nglobal_store_dwordx4 v79, v[180:183], s[4:5] offset:1024 sc1\nv_add_u32 v79, 0x8000, v79\n"
    "global_store_dwordx4 v79, v[180:183], s[4:5] offset:0 sc1\nglobal_store_dwordx4 v79, v[180:183], s[4:5] offset:1024 sc1\nv_add_u32 v79, 0x8000, v79\n"
    "global_store_dwordx4 v79, v[180:183], s[4:5] offset:0 sc1\nglobal_store_dwordx4 v79, v[180:183], s[4:5] offset:1024 sc1\nv_add_u32 v79, 0xffffc000, v188\n"
    "v_mov_b32 v240, s11\ns_cbranch_vccnz .Ll73x\nglobal_load_dword v255, v79, s[4:5] sc1\ns_add_u32 s59, s58, 0\nv_mov_b32 v70, s59\ns_mov_b64 exec, 1\ns_waitcnt vmcnt(9)\n"
    "ds_max_i32 v56, v70\ns_mov_b64 exec, -1\ns_setprio 0\ns_waitcnt vmcnt(1)\n.Ll73y:\ns_mov_b64 exec, 1\nglobal_store_dword v58, v240, s[4:5] sc1\ns_mov_b64 exec, -1\n"
    "global_store_dwordx4 v186, v[180:183], s[4:5] offset:-4096 sc1\nglobal_store_dwordx4 v186, v[180:183], s[4:5] offset:-3072 sc1\n"
    "global_store_dwordx4 v186, v[180:183], s[4:5] offset:-2048 sc1\nglobal_store_dwordx4 v186, v[180:183], s[4:5] offset:-1024 sc1\n"
    "global_store_dwordx4 v186, v[180:183], s[4:5] offset:0 sc1\nglobal_store_dwordx4 v186, v[180:183], s[4:5] offset:1024 sc1\n"
    "global_store_dwordx4 v186, v[180:183], s[4:5] offset:2048 sc1\nglobal_store_dwordx4 v186, v[180:183], s[4:5] offset:3072 sc1\n"
    "global_store_dwordx4 v187, v[180:183], s[4:5] sc1\ns_branch .Lafl11\n.Ll69:\ns_mov_b32 s60, 0x100000\n.Ll69x:\nds_read_b128 v[74:77], v56 offset:96\n"
    "s_waitcnt lgkmcnt(0)\nv_min3_i32 v74, v74, v75, v76\nv_min_i32 v74, v74, v77\nv_cmp_gt_i32 vcc, s61, v74\ns_cbranch_vccz .Ll70\ns_sub_u32 s60, s60, 1\n"
    "s_cbranch_scc1 .Ll70\nv_mov_b32 v70, s58\ns_mov_b64 exec, 1\ns_waitcnt vmcnt(12)\nds_max_i32 v56, v70\ns_mov_b64 exec, -1\ns_sleep 1\ns_branch .Ll69x\n"
    ".Lhalo10:\nv_and_b32 v251, 0x7f, v0\ns_lshl_b32 s43, s18, 11\ns_add_u32 s42, s43, 0xe00000\nv_lshl_add_u32 v57, v251, 4, s42\ns_lshr_b32 s41, s2, 4\n"
    "s_lshl_b32 s40, s41, 7\ns_and_b32 s39, s2, 15\ns_lshl_b32 s38, s39, 2\ns_add_u32 s37, s40, s38\ns_add_u32 s37, s37, 0x1002000\nv_mov_b32 v177, s37\n"
    "v_mov_b32 v178, 0x27f98\nv_bfe_u32 v251, v0, 5, 1\ns_nop 0\ns_cbranch_execz .Lhop\nv_mul_u32_u24 v250, 0xc00, v251\nv_bfe_u32 v249, v0, 4, 1\nv_bfe_u32 v248, v0, 2, 2\n"
    "v_and_b32 v247, 3, v0\ns_lshl_b32 s43, s13, 10\ns_add_u32 s42, s43, 0xfffffc00\nv_add_u32 v180, s42, v250\nv_lshl_add_u32 v180, v249, 9, v180\n"
    "v_lshl_add_u32 v180, v248, 6, v180\nv_lshl_add_u32 v180, v247, 3, v180\nv_lshl_add_u32 v180, v249, 5, v180\nv_and_b32 v246, 15, v0\nv_bfe_u32 v245, v0, 4, 2\n"
    "v_xor_b32 v244, v245, v246\ns_mov_b32 s41, 0x23c00\nv_lshl_add_u32 v181, v246, 10, s41\nv_lshl_add_u32 v181, v244, 4, v181\ns_lshl_b32 s40, s13, 7\n"
    "s_add_u32 s39, s40, 0x27b80\nv_lshl_add_u32 v58, v245, 4, s39\nv_mul_u32_u24 v243, 0xd0, v246\ns_lshl_b32 s38, s13, 6\ns_add_u32 s37, s38, 0x17fc0\n"
    "v_add_u32 v184, s37, v243\nv_lshl_add_u32 v184, v245, 3, v184\ns_mov_b32 s36, 0x27f80\nv_lshl_add_u32 v185, v247, 2, s36\ns_add_u32 s44, s23, 0\ns_and_b32 s44, s44, 7\n"
    "s_mul_i32 s45, s44, 12288\ns_lshl_b32 s44, s44, 7\nv_add_u32 v192, s45, v180\nv_xor_b32 v208, s44, v181\nv_xor_b32 v193, 32, v192\nv_xor_b32 v209, 64, v208\n"
    "s_add_u32 s44, s23, 1\ns_and_b32 s44, s44, 7\ns_mul_i32 s45, s44, 12288\ns_lshl_b32 s44, s44, 7\nv_add_u32 v194, s45, v180\nv_xor_b32 v210, s44, v181\n"
    "v_xor_b32 v195, 32, v194\nv_xor_b32 v211, 64, v210\ns_add_u32 s44, s23, 2\ns_and_b32 s44, s44, 7\ns_mul_i32 s45, s44, 12288\ns_lshl_b32 s44, s44, 7\n"
    "v_add_u32 v196, s45, v180\nv_xor_b32 v212, s44, v181\nv_xor_b32 v197, 32, v196\nv_xor_b32 v213, 64, v212\ns_add_u32 s44, s23, 3\ns_and_b32 s44, s44, 7\n"
    "s_mul_i32 s45, s44, 12288\ns_lshl_b32 s44, s44, 7\nv_add_u32 v198, s45, v180\nv_xor_b32 v214, s44, v181\nv_xor_b32 v199, 32, v198\nv_xor_b32 v215, 64, v214\n"
    "s_add_u32 s44, s23, 4\ns_and_b32 s44, s44, 7\ns_mul_i32 s45, s44, 12288\ns_lshl_b32 s44, s44, 7\nv_add_u32 v200, s45, v180\nv_xor_b32 v216, s44, v181\n"
    "v_xor_b32 v201, 32, v200\nv_xor_b32 v217, 64, v216\ns_add_u32 s44, s23, 5\ns_and_b32 s44, s44, 7\ns_mul_i32 s45, s44, 12288\ns_lshl_b32 s44, s44, 7\n"
    "v_add_u32 v202, s45, v180\nv_xor_b32 v218, s44, v181\nv_xor_b32 v203, 32, v202\nv_xor_b32 v219, 64, v218\ns_add_u32 s44, s23, 6\ns_and_b32 s44, s44, 7\n"
    "s_mul_i32 s45, s44, 12288\ns_lshl_b32 s44, s44, 7\nv_add_u32 v204, s45, v180\nv_xor_b32 v220, s44, v181\nv_xor_b32 v205, 32, v204\nv_xor_b32 v221, 64, v220\n"
    "s_add_u32 s44, s23, 7\ns_and_b32 s44, s44, 7\ns_mul_i32 s45, s44, 12288\ns_lshl_b32 s44, s44, 7\nv_add_u32 v206, s45, v180\nv_xor_b32 v222, s44, v181\n"
    "v_xor_b32 v207, 32, v206\nv_xor_b32 v223, 64, v222\ns_cmp_eq_u32 s13, 1\ns_cbranch_scc1 .Lh74\ns_waitcnt vmcnt(0)\nv_lshl_or_b32 v168, v169, 16, v168\n"
    "v_lshl_or_b32 v169, v171, 16, v170\nv_lshl_or_b32 v170, v173, 16, v172\nv_lshl_or_b32 v171, v175, 16, v174\nv_readfirstlane_b32 s11, v166\n"
    "global_store_dwordx4 v57, v[168:171], s[4:5] sc1\ns_mul_i32 s11, s11, 0x9E3779B1\ns_add_u32 s11, s11, 0x7F4A7C15\n.Lh74:\ns_call_b64 s[94:95], .Lhpre66\n"
    "s_mul_i32 s88, s13, 12\ns_add_u32 s88, s88, -8\ns_lshl_b32 s90, s88, 10\ns_add_u32 s90, s92, s90\ns_addc_u32 s91, s93, 0\ns_sub_u32 s89, 12, 1\ns_mov_b64 exec, 0\n"
    "s_swappc_b64 s[94:95], s[90:91]\n.Lz207:\nv_mov_b32 v176, 1\nv_mov_b32 v179, s11\ns_mov_b64 exec, 1\ns_waitcnt vmcnt(0)\nds_write_b32 v25, v176 offset:16\n"
    "s_mov_b64 exec, -1\ns_cmp_lg_u32 s13, 3\ns_cbranch_scc1 .Lh75\ns_mov_b32 s86, 0x100000\n.Ls76:\nds_read_b32 v176, v178 offset:0\ns_waitcnt lgkmcnt(0)\n"
    "v_cmp_gt_i32 vcc, 1, v176\ns_sub_u32 s86, s86, 1\ns_cbranch_scc1 .Ls77\ns_cbranch_vccz .Ls77\ns_sleep 1\ns_branch .Ls76\n.Ls77:\ns_mov_b64 exec, 1\n"
    "global_store_dword v177, v179, s[4:5] sc1\ns_mov_b64 exec, -1\n.Lh75:\ns_mov_b32 s48, 0\ns_cmp_lt_i32 s48, 3\ns_cbranch_scc1 .Lc78\n.Lc79:\nds_read_b128 v[64:67], v208\n"
    "ds_read_b128 v[68:71], v209\nds_read_b128 v[72:75], v210\nds_read_b128 v[76:79], v211\nds_read_b64_tr_b16 v[128:129], v192 offset:0\n"
    "ds_read_b64_tr_b16 v[130:131], v192 offset:256\nds_read_b64_tr_b16 v[132:133], v193 offset:0\nds_read_b64_tr_b16 v[134:135], v193 offset:256\n"
    "ds_read_b64_tr_b16 v[136:137], v192 offset:6144\nds_read_b64_tr_b16 v[138:139], v192 offset:6400\nds_read_b64_tr_b16 v[140:141], v193 offset:6144\n"
    "ds_read_b64_tr_b16 v[142:143], v193 offset:6400\ns_waitcnt lgkmcnt(0)\ns_setprio 3\nv_mfma_f32_16x16x32_bf16 v[152:155], v[64:67], v[64:67], 0\n"
    "v_mfma_f32_16x16x32_bf16 v[152:155], v[68:71], v[68:71], v[152:155]\nv_mfma_f32_16x16x32_bf16 v[152:155], v[72:75], v[72:75], v[152:155]\n"
    "v_mfma_f32_16x16x32_bf16 v[152:155], v[76:79], v[76:79], v[152:155]\nv_mfma_f32_16x16x32_bf16 v[144:147], v[128:131], v[64:67], 0\n"
    "v_mfma_f32_16x16x32_bf16 v[148:151], v[132:135], v[64:67], 0\nv_mfma_f32_16x16x32_bf16 v[144:147], v[136:139], v[68:71], v[144:147]\n"
    "v_mfma_f32_16x16x32_bf16 v[148:151], v[140:143], v[68:71], v[148:151]\ns_setprio 0\ns_cmp_lt_i32 s48, 3\ns_cbranch_scc1 .Lc81\n.Lc82:\nds_read_b128 v[80:83], v212\n"
    "ds_read_b128 v[84:87], v213\nds_read_b128 v[88:91], v214\nds_read_b128 v[92:95], v215\nds_read_b64_tr_b16 v[128:129], v194 offset:0\n"
    "ds_read_b64_tr_b16 v[130:131], v194 offset:256\nds_read_b64_tr_b16 v[132:133], v195 offset:0\nds_read_b64_tr_b16 v[134:135], v195 offset:256\n"
    "ds_read_b64_tr_b16 v[136:137], v194 offset:6144\nds_read_b64_tr_b16 v[138:139], v194 offset:6400\nds_read_b64_tr_b16 v[140:141], v195 offset:6144\n"
    "ds_read_b64_tr_b16 v[142:143], v195 offset:6400\ns_waitcnt lgkmcnt(0)\ns_setprio 3\nv_mfma_f32_16x16x32_bf16 v[152:155], v[80:83], v[80:83], v[152:155]\n"
    "v_mfma_f32_16x16x32_bf16 v[152:155], v[84:87], v[84:87], v[152:155]\nv_mfma_f32_16x16x32_bf16 v[152:155], v[88:91], v[88:91], v[152:155]\n"
    "v_mfma_f32_16x16x32_bf16 v[152:155], v[92:95], v[92:95], v[152:155]\nv_mfma_f32_16x16x32_bf16 v[144:147], v[128:131], v[72:75], v[144:147]\n"
    "v_mfma_f32_16x16x32_bf16 v[148:151], v[132:135], v[72:75], v[148:151]\ns_cbranch_execz .Lhop\nv_mfma_f32_16x16x32_bf16 v[144:147], v[136:139], v[76:79], v[144:147]\n"
    "v_mfma_f32_16x16x32_bf16 v[148:151], v[140:143], v[76:79], v[148:151]\ns_setprio 0\ns_cmp_lt_i32 s48, 5\ns_cbranch_scc1 .Lc84\n.Lc85:\nds_read_b128 v[96:99], v216\n"
    "ds_read_b128 v[100:103], v217\nds_read_b128 v[104:107], v218\nds_read_b128 v[108:111], v219\nds_read_b64_tr_b16 v[128:129], v196 offset:0\n"
    "ds_read_b64_tr_b16 v[130:131], v196 offset:256\nds_read_b64_tr_b16 v[132:133], v197 offset:0\nds_read_b64_tr_b16 v[134:135], v197 offset:256\n"
    "ds_read_b64_tr_b16 v[136:137], v196 offset:6144\nds_read_b64_tr_b16 v[138:139], v196 offset:6400\nds_read_b64_tr_b16 v[140:141], v197 offset:6144\n"
    "ds_read_b64_tr_b16 v[142:143], v197 offset:6400\ns_waitcnt lgkmcnt(0)\ns_setprio 3\nv_mfma_f32_16x16x32_bf16 v[152:155], v[96:99], v[96:99], v[152:155]\n"
    "v_mfma_f32_16x16x32_bf16 v[152:155], v[100:103], v[100:103], v[152:155]\nv_mfma_f32_16x16x32_bf16 v[152:155], v[104:107], v[104:107], v[152:155]\n"
    "v_mfma_f32_16x16x32_bf16 v[152:155], v[108:111], v[108:111], v[152:155]\nv_mfma_f32_16x16x32_bf16 v[144:147], v[128:131], v[80:83], v[144:147]\n"
    "v_mfma_f32_16x16x32_bf16 v[148:151], v[132:135], v[80:83], v[148:151]\nv_mfma_f32_16x16x32_bf16 v[144:147], v[136:139], v[84:87], v[144:147]\n"
    "v_mfma_f32_16x16x32_bf16 v[148:151], v[140:143], v[84:87], v[148:151]\ns_setprio 0\ns_cmp_lt_i32 s48, 7\ns_cbranch_scc1 .Lc87\n.Lc88:\nds_read_b128 v[112:115], v220\n"
    "ds_read_b128 v[116:119], v221\nds_read_b128 v[120:123], v222\nds_read_b128 v[124:127], v223\nds_read_b64_tr_b16 v[128:129], v198 offset:0\n"
    "ds_read_b64_tr_b16 v[130:131], v198 offset:256\nds_read_b64_tr_b16 v[132:133], v199 offset:0\nds_read_b64_tr_b16 v[134:135], v199 offset:256\n"
    "ds_read_b64_tr_b16 v[136:137], v198 offset:6144\nds_read_b64_tr_b16 v[138:139], v198 offset:6400\nds_read_b64_tr_b16 v[140:141], v199 offset:6144\n"
    "ds_read_b64_tr_b16 v[142:143], v199 offset:6400\ns_waitcnt lgkmcnt(0)\ns_setprio 3\nv_mfma_f32_16x16x32_bf16 v[152:155], v[112:115], v[112:115], v[152:155]\n"
    "v_mfma_f32_16x16x32_bf16 v[152:155], v[116:119], v[116:119], v[152:155]\nv_mfma_f32_16x16x32_bf16 v[152:155], v[120:123], v[120:123], v[152:155]\n"
    "v_mfma_f32_16x16x32_bf16 v[152:155], v[124:127], v[124:127], v[152:155]\nv_mfma_f32_16x16x32_bf16 v[144:147], v[128:131], v[88:91], v[144:147]\n"
    "v_mfma_f32_16x16x32_bf16 v[148:151], v[132:135], v[88:91], v[148:151]\nv_mfma_f32_16x16x32_bf16 v[144:147], v[136:139], v[92:95], v[144:147]\n"
    "v_mfma_f32_16x16x32_bf16 v[148:151], v[140:143], v[92:95], v[148:151]\ns_setprio 0\ns_cmp_lt_i32 s48, 9\ns_cbranch_scc1 .Lc90\n.Lc91:\n"
    "ds_read_b64_tr_b16 v[128:129], v200 offset:0\nds_read_b64_tr_b16 v[130:131], v200 offset:256\nds_read_b64_tr_b16 v[132:133], v201 offset:0\n"
    "ds_read_b64_tr_b16 v[134:135], v201 offset:256\nds_read_b64_tr_b16 v[136:137], v200 offset:6144\nds_read_b64_tr_b16 v[138:139], v200 offset:6400\n"
    "ds_read_b64_tr_b16 v[140:141], v201 offset:6144\nds_read_b64_tr_b16 v[142:143], v201 offset:6400\ns_waitcnt lgkmcnt(0)\ns_setprio 3\n"
    "v_mfma_f32_16x16x32_bf16 v[144:147], v[128:131], v[96:99], v[144:147]\nv_mfma_f32_16x16x32_bf16 v[148:151], v[132:135], v[96:99], v[148:151]\n"
    "v_mfma_f32_16x16x32_bf16 v[144:147], v[136:139], v[100:103], v[144:147]\nv_mfma_f32_16x16x32_bf16 v[148:151], v[140:143], v[100:103], v[148:151]\ns_setprio 0\n"
    "s_mov_b32 s44, 0xaaaaaaaa\ns_mov_b32 s45, s44\ns_mov_b32 s46, 0xcccccccc\ns_mov_b32 s47, s46\nv_cndmask_b32 v156, v152, v153, s[44:45]\n"
    "v_cndmask_b32 v157, v154, v155, s[44:45]\ns_mov_b32 s44, 0x00f0000f\ns_mov_b32 s45, 0xf0000f00\nv_cndmask_b32 v156, v156, v157, s[46:47]\ns_mov_b64 s[46:47], 0xffff\n"
    "v_cndmask_b32 v156, 0, v156, s[44:45]\nv_cndmask_b32 v157, 0, v153, s[46:47]\ns_mov_b32 s44, 0x3b000000\ns_nop 0\nv_permlane32_swap_b32 v156, v157\n"
    "v_add_f32 v158, v156, v157\nv_add_f32 v159, v156, v157\ns_nop 1\nv_permlane16_swap_b32 v158, v159\nv_add_f32 v156, v158, v159\nv_add_f32 v157, v158, v159\ns_nop 1\n"
    "v_permlane32_swap_b32 v156, v157\nv_mul_f32 v157, s44, v157\nv_mul_f32 v158, v157, v157\nv_fma_f32 v158, v156, s44, -v158\nv_max_f32 v158, 0, v158\n"
    "v_cvt_pk_bf16_f32 v158, 0, v158\nv_add_f32 v158, 0x3727c5ac, v158\nv_cvt_pk_bf16_f32 v158, 0, v158\nv_sqrt_f32 v158, v158\nv_cvt_pk_bf16_f32 v157, 0, -v157\n"
    "v_cvt_pk_bf16_f32 v158, 0, v158\nv_rcp_f32 v158, v158\nv_mov_b32 v159, 1\ns_mov_b32 s46, 0xffff0000\ns_mov_b32 s44, 0x00020002\ns_mov_b32 s45, s44\ns_cmp_lt_i32 s48, 11\n"
    "s_cbranch_scc1 .Lc93\n.Lc94:\nds_read_b64_tr_b16 v[128:129], v202 offset:0\nds_read_b64_tr_b16 v[130:131], v202 offset:256\nds_read_b64_tr_b16 v[132:133], v203 offset:0\n"
    "ds_read_b64_tr_b16 v[134:135], v203 offset:256\nds_read_b64_tr_b16 v[136:137], v202 offset:6144\nds_read_b64_tr_b16 v[138:139], v202 offset:6400\n"
    "ds_read_b64_tr_b16 v[140:141], v203 offset:6144\nds_read_b64_tr_b16 v[142:143], v203 offset:6400\ns_waitcnt lgkmcnt(0)\ns_setprio 3\n"
    "v_mfma_f32_16x16x32_bf16 v[144:147], v[128:131], v[104:107], v[144:147]\nv_mfma_f32_16x16x32_bf16 v[148:151], v[132:135], v[104:107], v[148:151]\n"
    "v_mfma_f32_16x16x32_bf16 v[144:147], v[136:139], v[108:111], v[144:147]\nv_mfma_f32_16x16x32_bf16 v[148:151], v[140:143], v[108:111], v[148:151]\ns_setprio 0\n"
    "s_cmp_lt_i32 s48, 13\ns_cbranch_scc1 .Lc96\n.Lc97:\nds_read_b64_tr_b16 v[128:129], v204 offset:0\nds_read_b64_tr_b16 v[130:131], v204 offset:256\n"
    "ds_read_b64_tr_b16 v[132:133], v205 offset:0\nds_read_b64_tr_b16 v[134:135], v205 offset:256\nds_read_b64_tr_b16 v[136:137], v204 offset:6144\n"
    "ds_read_b64_tr_b16 v[138:139], v204 offset:6400\nds_read_b64_tr_b16 v[140:141], v205 offset:6144\nds_read_b64_tr_b16 v[142:143], v205 offset:6400\ns_waitcnt lgkmcnt(0)\n"
    "s_setprio 3\nv_mfma_f32_16x16x32_bf16 v[144:147], v[128:131], v[112:115], v[144:147]\nv_mfma_f32_16x16x32_bf16 v[148:151], v[132:135], v[112:115], v[148:151]\n"
    "v_mfma_f32_16x16x32_bf16 v[144:147], v[136:139], v[116:119], v[144:147]\nv_mfma_f32_16x16x32_bf16 v[148:151], v[140:143], v[116:119], v[148:151]\ns_setprio 0\n"
    "s_cmp_lt_i32 s48, 15\ns_cbranch_scc1 .Lc99\n.Lc100:\nds_read_b32 v156, v56 offset:4\nds_read_b64_tr_b16 v[128:129], v206 offset:0\n"
    "ds_read_b64_tr_b16 v[130:131], v206 offset:256\nds_read_b64_tr_b16 v[132:133], v207 offset:0\nds_read_b64_tr_b16 v[134:135], v207 offset:256\n"
    "ds_read_b64_tr_b16 v[136:137], v206 offset:6144\nds_read_b64_tr_b16 v[138:139], v206 offset:6400\ns_cbranch_execz .Lhop\nds_read_b64_tr_b16 v[140:141], v207 offset:6144\n"
    "ds_read_b64_tr_b16 v[142:143], v207 offset:6400\ns_waitcnt lgkmcnt(0)\ns_setprio 3\nv_mfma_f32_16x16x32_bf16 v[144:147], v[128:131], v[120:123], v[144:147]\n"
    "v_mfma_f32_16x16x32_bf16 v[148:151], v[132:135], v[120:123], v[148:151]\nv_mfma_f32_16x16x32_bf16 v[144:147], v[136:139], v[124:127], v[144:147]\n"
    "v_mfma_f32_16x16x32_bf16 v[148:151], v[140:143], v[124:127], v[148:151]\ns_setprio 3\nv_readfirstlane_b32 s48, v156\ns_nop 4\nv_and_b32 v160, s46, v144\n"
    "v_and_b32 v161, s46, v145\nv_and_b32 v162, s46, v146\nv_and_b32 v163, s46, v147\nv_and_b32 v164, s46, v148\nv_and_b32 v165, s46, v149\nv_and_b32 v166, s46, v150\n"
    "v_and_b32 v167, s46, v151\nv_sub_f32 v168, v144, v160\nv_sub_f32 v169, v145, v161\nv_sub_f32 v170, v146, v162\nv_sub_f32 v171, v147, v163\nv_sub_f32 v172, v148, v164\n"
    "v_sub_f32 v173, v149, v165\nv_sub_f32 v174, v150, v166\nv_sub_f32 v175, v151, v167\nv_cvt_pk_bf16_f32 v160, v160, v168\nv_cvt_pk_bf16_f32 v161, v161, v169\n"
    "v_cvt_pk_bf16_f32 v162, v162, v170\nv_cvt_pk_bf16_f32 v163, v163, v171\nv_cvt_pk_bf16_f32 v164, v164, v172\nv_cvt_pk_bf16_f32 v165, v165, v173\n"
    "v_cvt_pk_bf16_f32 v166, v166, v174\nv_cvt_pk_bf16_f32 v167, v167, v175\ns_mov_b64 exec, s[44:45]\nds_write_b128 v58, v[160:163]\nds_write_b128 v58, v[164:167] offset:64\n"
    "ds_write_b32 v25, v159 offset:32\ns_mov_b64 exec, -1\ns_setprio 0\n"
    ".Lafl11:\nglobal_load_dwordx4 v[224:227], v47, s[8:9] offset:0\nglobal_load_dwordx4 v[228:231], v47, s[8:9] offset:32\ns_mov_b32 s44, 0x00ff00ff\n"
    "s_mov_b32 s45, 0xff00ff00\ns_mov_b32 s46, s44\ns_mov_b32 s47, s44\ns_sub_u32 s26, s48, 17\ns_cmp_eq_u32 s26, 0\ns_cbranch_scc1 .La102\n"
    "global_load_dwordx4 v[64:67], v59, s[68:69] offset:-4096 sc1\nglobal_load_dwordx4 v[68:71], v59, s[68:69] offset:-3072 sc1\n"
    "global_load_dwordx4 v[72:75], v59, s[68:69] offset:-2048 sc1\nglobal_load_dwordx4 v[76:79], v59, s[68:69] offset:-1024 sc1\n"
    "global_load_dwordx4 v[80:83], v59, s[68:69] offset:0 sc1\nglobal_load_dwordx4 v[84:87], v59, s[68:69] offset:1024 sc1\n"
    "global_load_dwordx4 v[88:91], v59, s[68:69] offset:2048 sc1\nglobal_load_dwordx4 v[92:95], v59, s[68:69] offset:3072 sc1\n"
    "global_load_dwordx4 v[96:99], v59, s[70:71] offset:-4096 sc1\nglobal_load_dwordx4 v[100:103], v59, s[70:71] offset:-3072 sc1\n"
    "global_load_dwordx4 v[104:107], v59, s[70:71] offset:-2048 sc1\nglobal_load_dwordx4 v[108:111], v59, s[70:71] offset:-1024 sc1\n"
    "global_load_dwordx4 v[112:115], v59, s[70:71] offset:0 sc1\nglobal_load_dwordx4 v[116:119], v59, s[70:71] offset:1024 sc1\n"
    "global_load_dwordx4 v[120:123], v59, s[70:71] offset:2048 sc1\nglobal_load_dwordx4 v[124:127], v59, s[70:71] offset:3072 sc1\n.La104:\ns_cmp_lt_u32 s13, 2\n"
    "s_cbranch_scc1 .La105\nv_fmac_f32_dpp v144, v144, v157 row_newbcast:1 row_mask:0xf bank_mask:0xf\n"
    "v_fmac_f32_dpp v145, v145, v157 row_newbcast:1 row_mask:0xf bank_mask:0xf\nv_fmac_f32_dpp v146, v146, v157 row_newbcast:1 row_mask:0xf bank_mask:0xf\n"
    "v_fmac_f32_dpp v147, v147, v157 row_newbcast:1 row_mask:0xf bank_mask:0xf\nv_fmac_f32_dpp v148, v148, v157 row_newbcast:1 row_mask:0xf bank_mask:0xf\n"
    "v_fmac_f32_dpp v149, v149, v157 row_newbcast:1 row_mask:0xf bank_mask:0xf\nv_fmac_f32_dpp v150, v150, v157 row_newbcast:1 row_mask:0xf bank_mask:0xf\n"
    "v_fmac_f32_dpp v151, v151, v157 row_newbcast:1 row_mask:0xf bank_mask:0xf\nv_mul_f32 v144, v144, v158\nv_mul_f32 v145, v145, v158\nv_mul_f32 v146, v146, v158\n"
    "v_mul_f32 v147, v147, v158\nv_mul_f32 v148, v148, v158\nv_mul_f32 v149, v149, v158\nv_mul_f32 v150, v150, v158\nv_mul_f32 v151, v151, v158\n"
    "v_cvt_pk_bf16_f32 v144, v144, v145\nv_cvt_pk_bf16_f32 v145, v146, v147\nv_cvt_pk_bf16_f32 v146, v148, v149\nv_cvt_pk_bf16_f32 v147, v150, v151\ns_mov_b32 s86, 0x100000\n"
    ".Ls107:\nds_read_b32 v240, v185 offset:96\ns_waitcnt lgkmcnt(0)\nv_cmp_gt_i32 vcc, 16, v240\ns_sub_u32 s86, s86, 1\ns_cbranch_scc1 .Ls108\ns_cbranch_vccz .Ls108\n"
    "s_sleep 1\ns_branch .Ls107\n.Ls108:\nds_write2_b64 v184, v[144:145], v[146:147] offset1:4\ns_branch .La106\n.Lc78:\ns_mov_b32 s86, 0x100000\n.Lc80:\n"
    "ds_read_b32 v156, v56\ns_waitcnt lgkmcnt(0)\nv_readfirstlane_b32 s48, v156\ns_sub_u32 s86, s86, 1\ns_cbranch_scc1 .Lc79\ns_cmp_ge_i32 s48, 3\ns_cbranch_scc1 .Lc79\n"
    "s_sleep 1\ns_branch .Lc80\n.Lc81:\ns_mov_b32 s86, 0x100000\n.Lc83:\nds_read_b32 v176, v56\ns_waitcnt lgkmcnt(0)\nv_readfirstlane_b32 s48, v176\ns_sub_u32 s86, s86, 1\n"
    "s_cbranch_scc1 .Lc82\ns_cmp_ge_i32 s48, 3\ns_cbranch_scc1 .Lc82\ns_sleep 1\ns_branch .Lc83\n.Lc84:\ns_mov_b32 s86, 0x100000\n.Lc86:\nds_read_b32 v176, v56\n"
    "s_waitcnt lgkmcnt(0)\nv_readfirstlane_b32 s48, v176\ns_sub_u32 s86, s86, 1\ns_cbranch_scc1 .Lc85\ns_cmp_ge_i32 s48, 5\ns_cbranch_scc1 .Lc85\ns_sleep 1\ns_branch .Lc86\n"
    ".Lc87:\ns_mov_b32 s86, 0x100000\n.Lc89:\nds_read_b32 v176, v56\ns_waitcnt lgkmcnt(0)\nv_readfirstlane_b32 s48, v176\ns_sub_u32 s86, s86, 1\ns_cbranch_scc1 .Lc88\n"
    "s_cmp_ge_i32 s48, 7\ns_cbranch_scc1 .Lc88\ns_sleep 1\ns_branch .Lc89\n.Lc90:\ns_mov_b32 s86, 0x100000\n.Lc92:\nds_read_b32 v176, v56\ns_waitcnt lgkmcnt(0)\n"
    "v_readfirstlane_b32 s48, v176\ns_sub_u32 s86, s86, 1\ns_cbranch_scc1 .Lc91\ns_cmp_ge_i32 s48, 9\ns_cbranch_scc1 .Lc91\ns_sleep 1\ns_branch .Lc92\n.Lc93:\n"
    "s_mov_b32 s86, 0x100000\n.Lc95:\nds_read_b32 v176, v56\ns_waitcnt lgkmcnt(0)\nv_readfirstlane_b32 s48, v176\ns_sub_u32 s86, s86, 1\ns_cbranch_scc1 .Lc94\n"
    "s_cmp_ge_i32 s48, 11\ns_cbranch_scc1 .Lc94\ns_sleep 1\ns_branch .Lc95\n.Lc96:\ns_mov_b32 s86, 0x100000\n.Lc98:\nds_read_b32 v176, v56\ns_waitcnt lgkmcnt(0)\n"
    "v_readfirstlane_b32 s48, v176\ns_sub_u32 s86, s86, 1\ns_cbranch_scc1 .Lc97\ns_cmp_ge_i32 s48, 13\ns_cbranch_scc1 .Lc97\ns_sleep 1\ns_branch .Lc98\n.Lc99:\n"
    "s_mov_b32 s86, 0x100000\n.Lc101:\nds_read_b32 v176, v56\ns_waitcnt lgkmcnt(0)\nv_readfirstlane_b32 s48, v176\ns_sub_u32 s86, s86, 1\ns_cbranch_scc1 .Lc100\n"
    "s_cmp_ge_i32 s48, 15\ns_cbranch_scc1 .Lc100\ns_sleep 1\ns_branch .Lc101\n.La105:\ns_xor_b32 s48, s26, 1\ns_or_b32 s48, s48, s13\ns_cbranch_scc1 .La106\n"
    "global_load_dword v255, v188, s[4:5] sc1\n.La106:\n"
    ".Latt12:\ns_setprio 1\ns_waitcnt lgkmcnt(0)\ns_barrier\ns_cbranch_execz .Lhop\nds_read_b128 v[192:195], v6\nds_read_b128 v[196:199], v6 offset:208\n"
    "s_mov_b64 exec, s[44:45]\nds_read_b128 v[232:235], v7\nds_read_b128 v[236:239], v7 offset:208\ns_mov_b64 exec, -1\nds_read_b64_tr_b16 v[200:201], v8 offset:0\n"
    "ds_read_b64_tr_b16 v[202:203], v8 offset:32\nds_read_b64_tr_b16 v[204:205], v8 offset:208\nds_read_b64_tr_b16 v[206:207], v8 offset:240\ns_mov_b32 s48, 0x3eb8aa3b\n"
    "v_mov_b32 v220, 0x3f803f80\nv_mov_b32 v221, 0x3f803f80\nv_mov_b32 v222, 0x3f803f80\nv_mov_b32 v223, 0x3f803f80\ns_waitcnt lgkmcnt(5)\n"
    "v_mfma_f32_16x16x32_bf16 v[208:211], v[192:195], v[232:235], v[2:5]\ns_waitcnt lgkmcnt(4)\nv_mfma_f32_16x16x32_bf16 v[212:215], v[196:199], v[236:239], v[2:5]\ns_nop 5\n"
    "v_max3_f32 v216, v208, v209, v210\ns_nop 0\nv_max3_f32 v217, v212, v213, v214\nv_max_f32 v216, v216, v211\nv_max_f32 v217, v217, v215\ns_nop 1\n"
    "v_permlane16_swap_b32 v216, v217\nv_max_f32 v218, v216, v217\nv_max_f32 v217, v216, v217\ns_nop 1\nv_permlane32_swap_b32 v218, v217\nv_max_f32 v218, v218, v217\n"
    "v_mul_f32 v216, 0xbeb8aa3b, v218\nv_mul_f32 v217, 0xbeb8aa3b, v218\ns_nop 1\nv_permlane16_swap_b32 v216, v217\n"
    "v_pk_fma_f32 v[208:209], v[208:209], s[48:49], v[216:217] op_sel:[0,0,0] op_sel_hi:[1,0,0]\n"
    "v_pk_fma_f32 v[210:211], v[210:211], s[48:49], v[216:217] op_sel:[0,0,0] op_sel_hi:[1,0,0]\n"
    "v_pk_fma_f32 v[212:213], v[212:213], s[48:49], v[216:217] op_sel:[0,0,1] op_sel_hi:[1,0,1]\n"
    "v_pk_fma_f32 v[214:215], v[214:215], s[48:49], v[216:217] op_sel:[0,0,1] op_sel_hi:[1,0,1]\nv_exp_f32 v208, v208\nv_exp_f32 v209, v209\nv_exp_f32 v210, v210\n"
    "v_exp_f32 v211, v211\nv_exp_f32 v212, v212\nv_exp_f32 v213, v213\nv_exp_f32 v214, v214\nv_exp_f32 v215, v215\ns_mov_b64 exec, s[46:47]\n"
    "v_cvt_pk_bf16_f32 v244, v208, v209\nv_cvt_pk_bf16_f32 v245, v210, v211\nv_cvt_pk_bf16_f32 v248, v212, v213\nv_cvt_pk_bf16_f32 v249, v214, v215\ns_not_b64 exec, exec\n"
    "v_cvt_pk_bf16_f32 v246, v208, v209\nv_cvt_pk_bf16_f32 v247, v210, v211\nv_cvt_pk_bf16_f32 v250, v212, v213\nv_cvt_pk_bf16_f32 v251, v214, v215\ns_mov_b64 exec, -1\n"
    "s_waitcnt lgkmcnt(0)\nv_mfma_f32_16x16x32_bf16 v[208:211], v[220:223], v[244:247], 0\nv_mfma_f32_16x16x32_bf16 v[212:215], v[220:223], v[248:251], 0\n"
    "v_mfma_f32_16x16x32_bf16 v[192:195], v[200:203], v[244:247], 0\nv_mfma_f32_16x16x32_bf16 v[196:199], v[204:207], v[248:251], 0\ns_nop 4\nv_rcp_f32 v216, v208\n"
    "v_rcp_f32 v217, v212\ns_nop 0\nv_pk_mul_f32 v[192:193], v[192:193], v[216:217] op_sel:[0,0] op_sel_hi:[1,0]\n"
    "v_pk_mul_f32 v[194:195], v[194:195], v[216:217] op_sel:[0,0] op_sel_hi:[1,0]\nv_pk_mul_f32 v[196:197], v[196:197], v[216:217] op_sel:[0,1] op_sel_hi:[1,1]\n"
    "v_pk_mul_f32 v[198:199], v[198:199], v[216:217] op_sel:[0,1] op_sel_hi:[1,1]\nv_cvt_pk_bf16_f32 v200, v192, v193\nv_cvt_pk_bf16_f32 v201, v194, v195\n"
    "v_cvt_pk_bf16_f32 v202, v196, v197\nv_cvt_pk_bf16_f32 v203, v198, v199\ns_nop 0\nv_permlane16_swap_b32 v200, v202\nv_permlane16_swap_b32 v201, v203\n"
    "global_store_dwordx4 v9, v[200:203], s[4:5] sc1\ns_setprio 0\ns_cmp_gt_u32 s3, 3\ns_cbranch_scc1 .Lhb110\n"
    "s_cmp_lg_u32 s12, 0\ns_cbranch_scc1 .Lb109\ns_cmp_eq_u32 s27, 0\ns_cbranch_scc1 .Lb118\ns_mov_b32 s10, 64\ns_call_b64 s[94:95], .Lkgl13\n.Lb118:\n"
    "ds_write_b128 v33, v[40:43] offset:0\nds_write_b128 v33, v[44:47] offset:1024\nds_write_b128 v33, v[48:51] offset:2048\nds_write_b128 v33, v[52:55] offset:3072\n"
    "v_mov_b32 v240, v35\nv_mov_b32 v241, v39\nglobal_load_dwordx4 v[252:255], v39, s[4:5] sc1\n.Lb109:\ns_waitcnt lgkmcnt(0)\ns_barrier\ns_lshl_b32 s43, s21, 15\n"
    "s_add_u32 s42, s12, s43\nv_lshl_add_u32 v192, v0, 4, s42\ns_lshl_b32 s41, s13, 10\ns_add_u32 s51, s41, 0x1f600\ns_mov_b32 s40, 0x1f600\nv_lshl_add_u32 v57, v0, 4, s40\n"
    "v_and_b32 v239, 3, v0\ns_mov_b32 s39, 0x27f80\nv_lshl_add_u32 v58, v239, 2, s39\nv_and_b32 v238, 0x7f, v0\ns_lshr_b32 s38, s13, 1\ns_mul_i32 s37, s38, 0x2100\n"
    "s_add_u32 s36, s37, 0x18000\nv_lshl_add_u32 v59, v238, 4, s36\nv_and_b32 v237, 31, v0\ns_mov_b32 s35, 0x27600\nv_lshl_add_u32 v39, v237, 2, s35\ns_lshl_b32 s34, s22, 13\n"
    "s_lshl_b32 s33, s21, 7\ns_add_u32 s32, s12, s34\ns_add_u32 s32, s32, s33\ns_add_u32 s32, s32, 0x400000\nv_lshl_add_u32 v56, v237, 2, s32\nv_add_u32 v193, 4096, v192\n"
    "v_add_u32 v194, 8192, v192\nv_add_u32 v195, 12288, v192\nv_add_u32 v196, 16384, v192\nv_add_u32 v197, 20480, v192\nv_add_u32 v198, 24576, v192\n"
    "v_add_u32 v199, 28672, v192\ns_mov_b32 s80, 0x10000\ns_cmp_lg_u32 s12, 0\ns_cbranch_scc1 .Lgb111\ns_add_u32 s43, s12, 0x400000\nv_lshl_add_u32 v16, v0, 4, s43\n"
    "s_lshl_b32 s42, s18, 13\ns_add_u32 s41, s12, s42\ns_add_u32 s41, s41, 0x200000\nv_lshl_add_u32 v17, v0, 4, s41\nv_lshrrev_b32 v127, 3, v0\n"
    "v_mul_u32_u24 v126, 0x50, v127\nv_and_b32 v125, 7, v0\nv_add_u32 v18, 0x1d550, v126\nv_lshl_add_u32 v18, v125, 3, v18\nv_bfe_u32 v124, v0, 4, 3\n"
    "v_xor_b32 v123, v0, v124\ns_mov_b32 s40, 0x19400\nv_lshl_add_u32 v19, v123, 3, s40\nv_bfe_u32 v122, v0, 4, 1\nv_xor_b32 v121, v0, v122\ns_mov_b32 s39, 0x17c00\n"
    "v_lshl_add_u32 v20, v121, 3, s39\ns_lshr_b32 s43, s12, 23\ns_add_u32 s42, s43, 3\nv_lshrrev_b32 v127, s42, v0\ns_lshr_b32 s41, s12, 22\ns_sub_u32 s40, 15, s41\n"
    "v_lshlrev_b32 v126, s40, v127\ns_lshr_b32 s39, s12, 20\ns_add_u32 s38, s39, 7\nv_and_b32 v125, s38, v0\ns_lshl_b32 s37, s18, 7\ns_add_u32 s36, s37, 0x600000\n"
    "s_lshl_b32 s35, s24, 18\ns_lshl_b32 s34, s25, 8\ns_add_u32 s33, s35, s34\ns_add_u32 s33, s33, 0x1200000\ns_cmp_lt_u32 s12, 1\ns_cselect_b32 s32, s36, s33\n"
    "v_add_u32 v21, s32, v126\ns_cbranch_execz .Lhop\nv_lshl_add_u32 v21, v125, 4, v21\nv_and_b32 v127, 63, v0\nv_lshlrev_b32 v126, 4, v127\nv_mov_b32 v125, 0xfc0\n"
    "s_lshr_b32 s43, s13, 1\ns_mul_i32 s42, s43, 26\nv_add_u32 v124, s42, v127\nv_cmp_gt_u32 vcc, 64, v124\ns_nop 1\nv_cndmask_b32 v123, v125, v126, vcc\n"
    "s_lshr_b32 s41, s3, 2\ns_mul_i32 s40, s41, 0x1950\ns_mul_i32 s39, s13, 0x150\ns_add_u32 s38, s40, s39\ns_add_u32 s38, s38, 0x1d000\ns_mul_i32 s37, s41, 0x3180\n"
    "s_and_b32 s36, s13, 1\ns_mul_i32 s35, s36, 0xfc0\ns_add_u32 s34, s37, s35\ns_add_u32 s34, s34, 0x20300\ns_cmp_lt_u32 s13, 2\ns_cselect_b32 s33, s38, s34\n"
    "v_add_u32 v22, s33, v123\nv_and_b32 v122, 31, v0\n.Lg115:\ns_waitcnt vmcnt(0)\nv_xor_b32 v252, s11, v252\nv_xor_b32 v253, s11, v253\nv_xor_b32 v254, s11, v254\n"
    "v_xor_b32 v255, s11, v255\nv_or3_b32 v224, v252, v253, v254\nv_or_b32 v224, v224, v255\nv_cmp_ne_u32 vcc, 0, v224\ns_cbranch_vccz .Lg116\ns_sub_u32 s80, s80, 1\n"
    "s_cbranch_scc1 .Lg116\ns_sleep 2\nglobal_load_dwordx4 v[252:255], v241, s[4:5] sc1\ns_branch .Lg115\n.Lg116:\ns_mov_b32 s80, 0x10000\ns_mov_b32 s86, 0x100000\n.Ls119:\n"
    "ds_read_b32 v244, v58 offset:64\ns_waitcnt lgkmcnt(0)\nv_cmp_gt_i32 vcc, 2, v244\ns_sub_u32 s86, s86, 1\ns_cbranch_scc1 .Ls120\ns_cbranch_vccz .Ls120\ns_sleep 1\n"
    "s_branch .Ls119\n.Ls120:\ns_sleep 6\ns_branch .Lgst112\n.Lgb111:\nv_alignbit_b32 v228, v11, v10, 16\nv_alignbit_b32 v229, v12, v11, 16\n"
    "v_alignbit_b32 v230, v13, v12, 16\nv_alignbit_b32 v231, v14, v13, 16\nds_write_b128 v59, v[10:13]\nv_mov_b32 v232, v11\nv_mov_b32 v233, v12\nv_mov_b32 v234, v13\n"
    "v_mov_b32 v235, v14\nds_write_b128 v59, v[228:231] offset:2112\nv_alignbit_b32 v236, v12, v11, 16\nv_alignbit_b32 v237, v13, v12, 16\nv_alignbit_b32 v238, v14, v13, 16\n"
    "v_alignbit_b32 v239, v15, v14, 16\nds_write_b128 v59, v[232:235] offset:4224\nds_write_b128 v59, v[236:239] offset:6336\ns_waitcnt vmcnt(0)\ns_sleep 10\n.Lgst112:\n"
    "s_add_u32 m0, s51, 0\ns_nop 2\nglobal_load_lds_dwordx4 v192, s[4:5] sc1\ns_add_u32 m0, s51, 4096\ns_nop 2\nglobal_load_lds_dwordx4 v193, s[4:5] sc1\n"
    "s_add_u32 m0, s51, 8192\ns_nop 2\nglobal_load_lds_dwordx4 v194, s[4:5] sc1\ns_add_u32 m0, s51, 12288\ns_nop 2\nglobal_load_lds_dwordx4 v195, s[4:5] sc1\n"
    "s_add_u32 m0, s51, 16384\ns_nop 2\nglobal_load_lds_dwordx4 v196, s[4:5] sc1\ns_add_u32 m0, s51, 20480\ns_nop 2\nglobal_load_lds_dwordx4 v197, s[4:5] sc1\n"
    "s_add_u32 m0, s51, 24576\ns_nop 2\nglobal_load_lds_dwordx4 v198, s[4:5] sc1\ns_add_u32 m0, s51, 28672\ns_nop 2\nglobal_load_lds_dwordx4 v199, s[4:5] sc1\n"
    "s_cmp_lg_u32 s12, 0\ns_cbranch_scc1 .Lb118y\nv_sub_u32 v121, 0x3ff, v122\nv_bfe_u32 v120, v0, 5, 1\nv_lshl_add_u32 v119, v120, 3, v121\nv_and_b32 v118, 3, v119\n"
    "v_mul_u32_u24 v117, 0x840, v118\nv_lshrrev_b32 v116, 2, v119\ns_mul_i32 s32, s43, 0x2100\ns_add_u32 s31, s32, 0x17800\nv_add_u32 v23, s31, v117\n"
    "v_lshl_add_u32 v23, v116, 3, v23\ns_lshl_b32 s43, s13, 2\ns_add_u32 s42, s43, 0x27f80\nv_mov_b32 v25, s42\ns_lshr_b32 s41, s12, 23\ns_add_u32 s40, s41, 4\n"
    "v_mov_b32 v26, s40\nv_and_b32 v127, 31, v0\nv_mul_u32_u24 v126, 0x50, v127\nv_bfe_u32 v125, v0, 5, 1\ns_lshr_b32 s39, s13, 1\ns_mul_i32 s38, s39, 0x1950\n"
    "s_and_b32 s37, s13, 1\ns_mul_i32 s36, s37, 0xa00\ns_add_u32 s35, s38, s36\ns_add_u32 s35, s35, 0x1d000\nv_add_u32 v27, s35, v126\nv_lshl_add_u32 v27, v125, 4, v27\n"
    "v_bfe_u32 v124, v0, 1, 3\nv_xor_b32 v123, v125, v124\ns_mul_i32 s34, s39, 0x2800\ns_lshl_b32 s33, s37, 11\ns_add_u32 s32, s34, s33\ns_add_u32 s32, s32, 0x19400\n"
    "v_lshl_add_u32 v28, v127, 6, s32\nv_lshl_add_u32 v28, v123, 3, v28\nv_xor_b32 v29, 16, v28\nv_xor_b32 v30, 32, v28\nv_xor_b32 v31, 48, v28\ns_lshr_b32 s43, s12, 23\n"
    "s_lshl_b32 s42, s43, 13\ns_lshr_b32 s41, s18, 4\ns_lshl_b32 s40, s41, 7\ns_and_b32 s39, s18, 15\ns_lshl_b32 s38, s39, 2\ns_add_u32 s37, s42, s40\n"
    "s_add_u32 s37, s37, s38\ns_add_u32 s37, s37, 0x1008000\nv_mov_b32 v32, s37\nv_bfe_u32 v127, v0, 2, 4\nv_and_b32 v126, 3, v0\ns_mov_b32 s36, 0x1008000\n"
    "v_lshl_add_u32 v125, v127, 7, s36\nv_lshl_add_u32 v125, v126, 4, v125\nv_and_b32 v124, 7, v0\ns_lshl_b32 s35, s24, 3\nv_add_u32 v123, s35, v124\n"
    "v_lshrrev_b32 v122, 2, v123\nv_and_b32 v121, 3, v123\ns_mov_b32 s34, 0x100a000\nv_lshl_add_u32 v120, v122, 7, s34\nv_lshl_add_u32 v120, v121, 4, v120\n"
    "s_cmp_lt_u32 s12, 1\ns_cselect_b64 vcc, -1, 0\nv_cndmask_b32 v33, v120, v125, vcc\nv_and_b32 v119, 63, v0\ns_lshl_b32 s33, s18, 13\ns_lshl_b32 s32, s13, 11\n"
    "s_add_u32 s31, s33, s32\ns_add_u32 s31, s31, 0x600000\nv_lshl_add_u32 v40, v119, 4, s31\nv_and_b32 v127, 15, v0\ns_lshl_b32 s43, s18, 10\ns_lshl_b32 s42, s13, 8\n"
    "s_add_u32 s41, s43, s42\ns_add_u32 s41, s41, 0xd00000\nv_lshl_add_u32 v41, v127, 4, s41\nv_mov_b64 v[52:53], -1\nv_mov_b64 v[54:55], -1\ns_branch .Lb118x\n.Lb118y:\n"
    "s_add_u32 s43, s12, 0x400000\nv_lshl_add_u32 v16, v0, 4, s43\ns_lshl_b32 s42, s18, 13\ns_add_u32 s41, s12, s42\ns_add_u32 s41, s41, 0x200000\n"
    "v_lshl_add_u32 v17, v0, 4, s41\ns_lshr_b32 s43, s12, 23\ns_cbranch_execz .Lhop\ns_add_u32 s42, s43, 3\nv_lshrrev_b32 v127, s42, v0\ns_lshr_b32 s41, s12, 22\n"
    "s_sub_u32 s40, 15, s41\nv_lshlrev_b32 v126, s40, v127\ns_lshr_b32 s39, s12, 20\ns_add_u32 s38, s39, 7\nv_and_b32 v125, s38, v0\ns_lshl_b32 s37, s18, 7\n"
    "s_add_u32 s36, s37, 0x600000\ns_lshl_b32 s35, s24, 18\ns_lshl_b32 s34, s25, 8\ns_add_u32 s33, s35, s34\ns_add_u32 s33, s33, 0x1200000\ns_cmp_lt_u32 s12, 1\n"
    "s_cselect_b32 s32, s36, s33\nv_add_u32 v21, s32, v126\nv_lshl_add_u32 v21, v125, 4, v21\ns_add_u32 s31, s43, 4\nv_mov_b32 v26, s31\ns_lshr_b32 s43, s12, 23\n"
    "s_lshl_b32 s42, s43, 13\ns_lshr_b32 s41, s18, 4\ns_lshl_b32 s40, s41, 7\ns_and_b32 s39, s18, 15\ns_lshl_b32 s38, s39, 2\ns_add_u32 s37, s42, s40\n"
    "s_add_u32 s37, s37, s38\ns_add_u32 s37, s37, 0x1008000\nv_mov_b32 v32, s37\nv_bfe_u32 v127, v0, 2, 4\nv_and_b32 v126, 3, v0\ns_mov_b32 s36, 0x1008000\n"
    "v_lshl_add_u32 v125, v127, 7, s36\nv_lshl_add_u32 v125, v126, 4, v125\nv_and_b32 v124, 7, v0\ns_lshl_b32 s35, s24, 3\nv_add_u32 v123, s35, v124\n"
    "v_lshrrev_b32 v122, 2, v123\nv_and_b32 v121, 3, v123\ns_mov_b32 s34, 0x100a000\nv_lshl_add_u32 v120, v122, 7, s34\nv_lshl_add_u32 v120, v121, 4, v120\n"
    "s_cmp_lt_u32 s12, 1\ns_cselect_b64 vcc, -1, 0\nv_cndmask_b32 v33, v120, v125, vcc\ns_mov_b32 s33, 0x100e000\nv_lshl_add_u32 v240, v127, 7, s33\n"
    "v_lshl_add_u32 v240, v126, 4, v240\n.Lb118x:\ns_setprio 3\ns_waitcnt vmcnt(7)\n.Lr121:\nds_read_b128 v[208:211], v57 offset:0\ns_waitcnt lgkmcnt(0)\n"
    "v_max3_u32 v244, v208, v209, 0\nv_max3_u32 v244, v210, v211, v244\nv_cmp_eq_u32 vcc, -1, v244\ns_cbranch_vccnz .Lw122\n.Lw122x:\ns_barrier\ns_waitcnt vmcnt(6)\n.Lr123:\n"
    "ds_read_b128 v[212:215], v57 offset:4096\ns_waitcnt lgkmcnt(0)\nv_max3_u32 v244, v212, v213, 0\nv_max3_u32 v244, v214, v215, v244\nv_cmp_eq_u32 vcc, -1, v244\n"
    "s_cbranch_vccnz .Lw124\n.Lw124x:\ns_barrier\ns_waitcnt vmcnt(5)\n.Lr125:\nds_read_b128 v[216:219], v57 offset:8192\ns_waitcnt vmcnt(4)\n"
    "ds_read_b128 v[220:223], v57 offset:12288\ns_waitcnt lgkmcnt(1)\nv_max3_u32 v244, v216, v217, 0\nv_max3_u32 v244, v218, v219, v244\ns_waitcnt lgkmcnt(0)\n"
    "v_max3_u32 v244, v220, v221, v244\nv_max3_u32 v244, v222, v223, v244\nv_cmp_eq_u32 vcc, -1, v244\ns_cbranch_vccnz .Lw126\n.Lw126x:\ns_barrier\ns_waitcnt vmcnt(3)\n"
    ".Lr127:\nds_read_b128 v[224:227], v57 offset:16384\ns_waitcnt vmcnt(2)\nds_read_b128 v[228:231], v57 offset:20480\ns_waitcnt lgkmcnt(1)\nv_max3_u32 v244, v224, v225, 0\n"
    "v_max3_u32 v244, v226, v227, v244\ns_waitcnt lgkmcnt(0)\nv_max3_u32 v244, v228, v229, v244\nv_max3_u32 v244, v230, v231, v244\nv_cmp_eq_u32 vcc, -1, v244\n"
    "s_cbranch_vccnz .Lw128\n.Lw128x:\ns_waitcnt vmcnt(0)\ns_cmp_lg_u32 s13, 1\ns_cbranch_scc1 .Lw128z\nglobal_load_dwordx4 v[252:255], v240, s[4:5] sc1\n.Lw128z:\n"
    "s_cmp_lg_u32 s12, 0\ns_cbranch_scc1 .Lw128y\nglobal_store_dwordx4 v40, v[52:55], s[4:5] sc1\nglobal_store_dwordx4 v40, v[52:55], s[4:5] offset:1024 sc1\n"
    "global_store_dwordx4 v41, v[52:55], s[4:5] sc1\n.Lw128y:\ns_barrier\n.Lr129:\nds_read_b128 v[232:235], v57 offset:24576\nds_read_b128 v[236:239], v57 offset:28672\n"
    "s_waitcnt lgkmcnt(1)\nv_max3_u32 v244, v232, v233, 0\nv_max3_u32 v244, v234, v235, v244\ns_waitcnt lgkmcnt(0)\nv_max3_u32 v244, v236, v237, v244\n"
    "v_max3_u32 v244, v238, v239, v244\nv_cmp_eq_u32 vcc, -1, v244\ns_cbranch_vccnz .Lw130\n.Lw130x:\ns_setprio 0\ns_barrier\n"
    "s_add_u32 s58, s4, 8192\ns_addc_u32 s59, s5, 0\ns_add_u32 s60, s4, 16384\ns_addc_u32 s61, s5, 0\ns_add_u32 s62, s4, 24576\ns_addc_u32 s63, s5, 0\n"
    "s_mov_b32 s45, 0x3c000000\ns_cmp_lg_u32 s13, 0\ns_waitcnt vmcnt(0)\ns_cbranch_scc1 .Lg135\ns_setprio 3\ns_mov_b32 exec_hi, 0\ns_barrier\n"
    "ds_read2_b32 v[44:45], v39 offset0:0 offset1:32\nds_read2_b32 v[46:47], v39 offset0:64 offset1:96\nds_read2_b32 v[48:49], v39 offset0:128 offset1:160\n"
    "ds_read2_b32 v[50:51], v39 offset0:192 offset1:224\ns_waitcnt lgkmcnt(0)\nv_pk_add_f32 v[44:45], v[44:45], v[46:47]\nv_pk_add_f32 v[48:49], v[48:49], v[50:51]\n"
    "v_pk_add_f32 v[40:41], v[44:45], v[48:49]\nv_mul_f32 v40, s45, v40\nv_mul_f32 v42, v40, v40\nv_fma_f32 v41, v41, s45, -v42\nv_max_f32 v41, 0, v41\nv_sqrt_f32 v41, v41\n"
    "s_nop 0\nv_cvt_pk_bf16_f32 v40, v40, v41\nglobal_store_dword v56, v40, s[4:5] sc1\ns_mov_b32 exec_hi, -1\ns_setprio 0\ns_branch .Lln114\n.Lg135:\ns_cmp_eq_u32 s13, 1\n"
    "s_cbranch_scc1 .Lg134\n.Lg133:\ns_barrier\ns_branch .Lln114\n.Lg134:\ns_cmp_lg_u32 s12, 0\ns_cbranch_scc1 .Lg136\ns_mov_b32 s80, 0x10000\n.Lg132:\n"
    "v_xor_b32 v252, s11, v252\nv_xor_b32 v253, s11, v253\nv_xor_b32 v254, s11, v254\nv_xor_b32 v255, s11, v255\nv_or3_b32 v224, v252, v253, v254\nv_or_b32 v224, v224, v255\n"
    "v_cmp_ne_u32 vcc, 0, v224\ns_cbranch_vccz .Lg133\ns_sub_u32 s80, s80, 1\ns_cbranch_scc1 .Lg133\ns_sleep 2\nglobal_load_dwordx4 v[252:255], v240, s[4:5] sc1\n"
    "s_waitcnt vmcnt(0)\ns_branch .Lg132\n.Lg136:\ns_sub_u32 s87, s100, 300\nv_subrev_u32 v252, s87, v252\nv_subrev_u32 v253, s87, v253\nv_subrev_u32 v254, s87, v254\n"
    "v_subrev_u32 v255, s87, v255\nv_max3_u32 v252, v252, v253, v254\nv_max_u32 v252, v252, v255\ns_nop 1\n"
    "v_max_u32_dpp v252, v252, v252 row_ror:1 row_mask:0xf bank_mask:0xf\ns_nop 1\nv_max_u32_dpp v252, v252, v252 row_ror:2 row_mask:0xf bank_mask:0xf\ns_nop 1\n"
    "v_max_u32_dpp v252, v252, v252 row_ror:4 row_mask:0xf bank_mask:0xf\ns_nop 1\nv_max_u32_dpp v252, v252, v252 row_ror:8 row_mask:0xf bank_mask:0xf\nv_mov_b32 v253, v252\n"
    "s_nop 1\nv_permlane16_swap_b32 v252, v253\nv_max_u32 v252, v252, v253\nv_mov_b32 v253, v252\ns_nop 1\nv_permlane32_swap_b32 v252, v253\nv_max_u32 v252, v252, v253\n"
    "s_nop 0\ns_cbranch_execz .Lhop\nv_readfirstlane_b32 s87, v252\ns_min_u32 s87, s87, 400\ns_max_u32 s87, s87, 300\ns_sub_u32 s87, s87, 300\ns_add_u32 s78, s78, 5200\n"
    "s_barrier\ns_memrealtime s[100:101]\ns_memtime s[76:77]\ns_waitcnt lgkmcnt(0)\ns_sub_u32 s76, s76, s78\ns_max_i32 s76, s76, 0\ns_mul_i32 s76, s76, 49\n"
    "s_lshr_b32 s76, s76, 10\ns_sub_u32 s87, s87, s76\ns_max_i32 s87, s87, 0\ns_waitcnt lgkmcnt(0)\ns_add_u32 s87, s87, s100\ns_add_u32 s87, s87, 30\n.Lj137:\n"
    "s_memrealtime s[100:101]\ns_waitcnt lgkmcnt(0)\ns_sub_u32 s101, s87, s100\ns_cmp_le_i32 s101, 0\ns_cbranch_scc0 .Lj137\ns_branch .Lln114\n"
    ".Lhb110:\ns_cmp_lg_u32 s12, 0\ns_cbranch_scc1 .Lh138\ns_setprio 1\ns_cmp_eq_u32 s13, 0\ns_cbranch_scc1 .Lh138y\n.Lh138x:\ns_cmp_eq_u32 s26, 0\ns_waitcnt lgkmcnt(0)\n"
    "s_barrier\ns_cbranch_scc1 .Lh139\n.Lh140:\nds_read_b64 v[216:217], v252\nds_read_b128 v[192:195], v60 offset:0\nds_read_b128 v[196:199], v60 offset:1024\n"
    "ds_read_b128 v[200:203], v60 offset:4096\nds_read_b128 v[204:207], v60 offset:5120\nv_mov_b32 v220, 0\nv_mov_b32 v221, 2\ns_mov_b64 exec, 1\n"
    "ds_write_b32 v25, v221 offset:64\ns_mov_b64 exec, -1\nds_write_b16 v254, v220 offset:1400\ns_waitcnt vmcnt(17)\n"
    "global_load_dwordx4 v[128:131], v59, s[72:73] offset:-4096 sc1\nv_add_u32_sdwa v240, v254, v48 dst_sel:DWORD dst_unused:UNUSED_PAD src0_sel:DWORD src1_sel:WORD_0\n"
    "v_add_u32_sdwa v241, v254, v48 dst_sel:DWORD dst_unused:UNUSED_PAD src0_sel:DWORD src1_sel:WORD_1\n"
    "v_add_u32_sdwa v242, v254, v49 dst_sel:DWORD dst_unused:UNUSED_PAD src0_sel:DWORD src1_sel:WORD_0\n"
    "v_add_u32_sdwa v243, v254, v49 dst_sel:DWORD dst_unused:UNUSED_PAD src0_sel:DWORD src1_sel:WORD_1\n"
    "v_add_u32_sdwa v244, v254, v50 dst_sel:DWORD dst_unused:UNUSED_PAD src0_sel:DWORD src1_sel:WORD_0\nglobal_load_dwordx4 v[132:135], v59, s[72:73] offset:-3072 sc1\n"
    "v_add_u32_sdwa v245, v254, v50 dst_sel:DWORD dst_unused:UNUSED_PAD src0_sel:DWORD src1_sel:WORD_1\n"
    "v_add_u32_sdwa v246, v254, v51 dst_sel:DWORD dst_unused:UNUSED_PAD src0_sel:DWORD src1_sel:WORD_0\n"
    "v_add_u32_sdwa v247, v254, v51 dst_sel:DWORD dst_unused:UNUSED_PAD src0_sel:DWORD src1_sel:WORD_1\ns_waitcnt lgkmcnt(6)\nv_mov_b32 v218, 3\nv_add_f32 v216, v216, v217\n"
    "global_load_dwordx4 v[136:139], v59, s[72:73] offset:-2048 sc1\nv_lshrrev_b32 v216, 23, v216\nv_sub_u32 v216, 0x10c, v216\nv_min_u32 v216, 0xfc, v216\n"
    "v_sub_u32 v217, 0xfe, v216\nv_lshlrev_b32 v216, 23, v216\nglobal_load_dwordx4 v[140:143], v59, s[72:73] offset:-1024 sc1\nv_lshlrev_b32 v217, 23, v217\n"
    "s_waitcnt lgkmcnt(2)\nv_pk_add_f32 v[208:209], v[192:193], v[200:201]\nv_pk_add_f32 v[210:211], v[194:195], v[202:203]\nv_pk_add_f32 v[212:213], v[196:197], v[204:205]\n"
    "v_pk_add_f32 v[214:215], v[198:199], v[206:207]\nglobal_load_dwordx4 v[144:147], v59, s[72:73] offset:0 sc1\n"
    "v_pk_mul_f32 v[208:209], v[208:209], v[216:217] op_sel_hi:[1,0]\nv_pk_mul_f32 v[210:211], v[210:211], v[216:217] op_sel_hi:[1,0]\n"
    "v_pk_mul_f32 v[212:213], v[212:213], v[216:217] op_sel_hi:[1,0]\nv_pk_mul_f32 v[214:215], v[214:215], v[216:217] op_sel_hi:[1,0]\nv_cvt_pk_f16_f32 v208, v208, v209\n"
    "global_load_dwordx4 v[148:151], v59, s[72:73] offset:1024 sc1\nv_cvt_pk_f16_f32 v209, v210, v211\nv_cvt_pk_f16_f32 v210, v212, v213\nv_cvt_pk_f16_f32 v211, v214, v215\n"
    "ds_write_b16 v240, v208\nds_write_b16_d16_hi v241, v208\nglobal_load_dwordx4 v[152:155], v59, s[72:73] offset:2048 sc1\nds_write_b16 v242, v209\n"
    "ds_write_b16_d16_hi v243, v209\nds_write_b16 v244, v210\nds_write_b16_d16_hi v245, v210\nds_write_b16 v246, v211\n"
    "global_load_dwordx4 v[156:159], v59, s[72:73] offset:3072 sc1\nds_write_b16_d16_hi v247, v211\ns_mov_b64 exec, 1\nds_write_b32 v25, v218 offset:64\n"
    "ds_write_b32 v252, v217 offset:32\ns_mov_b64 exec, -1\nv_add_u32_sdwa v232, v254, v52 dst_sel:DWORD dst_unused:UNUSED_PAD src0_sel:DWORD src1_sel:WORD_0\n"
    "v_add_u32_sdwa v233, v254, v52 dst_sel:DWORD dst_unused:UNUSED_PAD src0_sel:DWORD src1_sel:WORD_1\n"
    "v_add_u32_sdwa v234, v254, v53 dst_sel:DWORD dst_unused:UNUSED_PAD src0_sel:DWORD src1_sel:WORD_0\n"
    "v_add_u32_sdwa v235, v254, v53 dst_sel:DWORD dst_unused:UNUSED_PAD src0_sel:DWORD src1_sel:WORD_1\nglobal_load_dwordx4 v[160:163], v59, s[74:75] offset:-4096 sc1\n"
    "v_add_u32_sdwa v236, v254, v54 dst_sel:DWORD dst_unused:UNUSED_PAD src0_sel:DWORD src1_sel:WORD_0\n"
    "v_add_u32_sdwa v237, v254, v54 dst_sel:DWORD dst_unused:UNUSED_PAD src0_sel:DWORD src1_sel:WORD_1\n"
    "v_add_u32_sdwa v238, v254, v55 dst_sel:DWORD dst_unused:UNUSED_PAD src0_sel:DWORD src1_sel:WORD_0\n"
    "v_add_u32_sdwa v239, v254, v55 dst_sel:DWORD dst_unused:UNUSED_PAD src0_sel:DWORD src1_sel:WORD_1\n"
    "v_add_u32_sdwa v248, v254, v62 dst_sel:DWORD dst_unused:UNUSED_PAD src0_sel:DWORD src1_sel:WORD_0\nglobal_load_dwordx4 v[164:167], v59, s[74:75] offset:-3072 sc1\n"
    "v_add_u32_sdwa v249, v254, v62 dst_sel:DWORD dst_unused:UNUSED_PAD src0_sel:DWORD src1_sel:WORD_1\n"
    "v_add_u32_sdwa v250, v254, v63 dst_sel:DWORD dst_unused:UNUSED_PAD src0_sel:DWORD src1_sel:WORD_0\ns_mov_b32 s86, 0x100000\n.Ls147:\nds_read_b32 v216, v61 offset:0\n"
    "s_waitcnt lgkmcnt(0)\nv_cmp_gt_i32 vcc, 3, v216\ns_sub_u32 s86, s86, 1\ns_cbranch_scc1 .Ls148\ns_cbranch_vccz .Ls148\ns_branch .Ls147\n.Ls148:\nds_read_u16 v200, v232\n"
    "ds_read_u16 v201, v233\nglobal_load_dwordx4 v[168:171], v59, s[74:75] offset:-2048 sc1\nds_read_u16 v202, v234\nds_read_u16 v203, v235\nds_read_u16 v204, v236\n"
    "ds_read_u16 v205, v237\nds_read_u16 v206, v238\nglobal_load_dwordx4 v[172:175], v59, s[74:75] offset:-1024 sc1\nds_read_u16 v207, v239\nds_read_u16 v208, v248\n"
    "ds_read_u16 v209, v249\nds_read_u16 v210, v250\ns_waitcnt lgkmcnt(0)\nv_lshl_or_b32 v192, v201, 16, v200\nglobal_load_dwordx4 v[176:179], v59, s[74:75] offset:0 sc1\n"
    "v_lshl_or_b32 v193, v203, 16, v202\nv_lshl_or_b32 v194, v205, 16, v204\nv_lshl_or_b32 v195, v207, 16, v206\nv_lshl_or_b32 v196, v209, 16, v208\nv_mov_b32 v197, v210\n"
    "global_load_dwordx4 v[180:183], v59, s[74:75] offset:1024 sc1\nv_alignbit_b32 v212, v193, v192, 16\nv_alignbit_b32 v213, v194, v193, 16\n"
    "v_alignbit_b32 v214, v195, v194, 16\nv_alignbit_b32 v215, v196, v195, 16\nds_write_b128 v253, v[192:195]\nglobal_load_dwordx4 v[184:187], v59, s[74:75] offset:2048 sc1\n"
    "v_mov_b32 v216, v193\nv_mov_b32 v217, v194\nv_mov_b32 v218, v195\nv_mov_b32 v219, v196\nds_write_b128 v253, v[212:215] offset:2112\n"
    "global_load_dwordx4 v[188:191], v59, s[74:75] offset:3072 sc1\nv_alignbit_b32 v220, v194, v193, 16\nv_alignbit_b32 v221, v195, v194, 16\n"
    "v_alignbit_b32 v222, v196, v195, 16\ns_nop 0\ns_cbranch_execz .Lhop\nv_alignbit_b32 v223, v197, v196, 16\nds_write_b128 v253, v[216:219] offset:4224\n"
    "ds_write_b128 v253, v[220:223] offset:6336\ns_waitcnt vmcnt(16)\nv_permlane32_swap_b32 v224, v226\nv_permlane32_swap_b32 v225, v227\nv_permlane32_swap_b32 v228, v230\n"
    "v_permlane32_swap_b32 v229, v231\nv_mov_b64 v[240:241], v[224:225]\nv_mov_b64 v[242:243], v[226:227]\nv_mov_b64 v[244:245], v[228:229]\nv_mov_b64 v[246:247], v[230:231]\n"
    "v_mov_b64 v[252:253], -1\nv_mov_b64 v[254:255], -1\ns_branch .Lhwo113\n.Lh138y:\nv_mov_b32 v240, s11\nv_add_u32 v58, 0x2000, v58\ns_mov_b64 exec, 1\ns_waitcnt vmcnt(19)\n"
    "global_store_dword v58, v240, s[4:5] sc1\ns_mov_b64 exec, -1\ns_branch .Lh138x\n.Lh138:\ns_barrier\nglobal_load_dword v240, v47, s[4:5] offset:0 sc1\n"
    "global_load_dword v241, v47, s[4:5] offset:128 sc1\nglobal_load_dword v242, v47, s[4:5] offset:512 sc1\nglobal_load_dword v243, v47, s[4:5] offset:640 sc1\n"
    "global_load_dword v244, v47, s[4:5] offset:1024 sc1\nglobal_load_dword v245, v47, s[4:5] offset:1152 sc1\nglobal_load_dword v246, v47, s[4:5] offset:1536 sc1\n"
    "global_load_dword v247, v47, s[4:5] offset:1664 sc1\nglobal_load_dwordx4 v[128:131], v59, s[72:73] offset:-4096 sc1\nv_bfe_u32 v223, v0, 5, 1\nv_and_b32 v222, 31, v0\n"
    "s_lshl_b32 s43, s13, 11\nglobal_load_dwordx4 v[132:135], v59, s[72:73] offset:-3072 sc1\ns_add_u32 s42, s43, 0x1d000\nv_lshl_add_u32 v40, v223, 8, s42\n"
    "v_lshl_add_u32 v40, v222, 2, v40\nv_and_b32 v221, 63, v0\nglobal_load_dwordx4 v[136:139], v59, s[72:73] offset:-2048 sc1\nv_lshl_add_u32 v41, v221, 4, s42\n"
    "s_mov_b32 s41, 0x1f600\nv_lshl_add_u32 v42, v221, 4, s41\nv_bfe_u32 v220, v0, 3, 3\nglobal_load_dwordx4 v[140:143], v59, s[72:73] offset:-1024 sc1\n"
    "v_and_b32 v219, 7, v0\ns_lshl_b32 s40, s22, 19\ns_lshl_b32 s39, s13, 17\ns_lshl_b32 s38, s21, 7\nglobal_load_dwordx4 v[144:147], v59, s[72:73] offset:0 sc1\n"
    "s_add_u32 s37, s12, s40\ns_add_u32 s37, s37, s39\ns_add_u32 s37, s37, s38\nglobal_load_dwordx4 v[148:151], v59, s[72:73] offset:1024 sc1\ns_add_u32 s37, s37, 0x200000\n"
    "v_lshl_add_u32 v43, v220, 13, s37\nv_lshl_add_u32 v43, v219, 4, v43\ns_add_u32 s36, s12, s40\nglobal_load_dwordx4 v[152:155], v59, s[72:73] offset:2048 sc1\n"
    "s_add_u32 s36, s36, s39\ns_add_u32 s36, s36, s38\ns_add_u32 s36, s36, 0x210000\nv_lshl_add_u32 v44, v220, 13, s36\n"
    "global_load_dwordx4 v[156:159], v59, s[72:73] offset:3072 sc1\nv_lshl_add_u32 v44, v219, 4, v44\ns_lshl_b32 s35, s13, 8\ns_add_u32 s34, s35, 0x27600\n"
    "v_lshl_add_u32 v45, v221, 2, s34\nglobal_load_dwordx4 v[160:163], v59, s[74:75] offset:-4096 sc1\ns_add_u32 s33, s43, 0x1d400\nv_lshl_add_u32 v46, v223, 8, s33\n"
    "v_lshl_add_u32 v46, v222, 2, v46\nglobal_load_dwordx4 v[164:167], v59, s[74:75] offset:-3072 sc1\ns_add_u32 s32, s12, 0x400000\nv_lshl_add_u32 v16, v0, 4, s32\n"
    "s_lshl_b32 s31, s18, 13\ns_add_u32 s30, s12, s31\nglobal_load_dwordx4 v[168:171], v59, s[74:75] offset:-2048 sc1\ns_add_u32 s30, s30, 0x200000\n"
    "v_lshl_add_u32 v17, v0, 4, s30\ns_lshr_b32 s43, s12, 23\ns_add_u32 s42, s43, 3\nglobal_load_dwordx4 v[172:175], v59, s[74:75] offset:-1024 sc1\n"
    "v_lshrrev_b32 v223, s42, v0\ns_lshr_b32 s41, s12, 22\ns_sub_u32 s40, 15, s41\nv_lshlrev_b32 v222, s40, v223\nglobal_load_dwordx4 v[176:179], v59, s[74:75] offset:0 sc1\n"
    "s_lshr_b32 s39, s12, 20\ns_add_u32 s38, s39, 7\nv_and_b32 v221, s38, v0\nglobal_load_dwordx4 v[180:183], v59, s[74:75] offset:1024 sc1\ns_lshl_b32 s37, s18, 7\n"
    "s_add_u32 s36, s37, 0x600000\ns_lshl_b32 s35, s24, 18\ns_lshl_b32 s34, s25, 8\nglobal_load_dwordx4 v[184:187], v59, s[74:75] offset:2048 sc1\ns_add_u32 s33, s35, s34\n"
    "s_add_u32 s33, s33, 0x1200000\ns_cmp_lt_u32 s12, 1\ns_cselect_b32 s32, s36, s33\nglobal_load_dwordx4 v[188:191], v59, s[74:75] offset:3072 sc1\nv_add_u32 v21, s32, v222\n"
    "v_lshl_add_u32 v21, v221, 4, v21\ns_add_u32 s31, s43, 4\nv_mov_b32 v26, s31\ns_waitcnt vmcnt(16)\ns_mov_b32 s80, 0x10000\nv_max3_u32 v232, v240, v241, v242\n"
    "v_max3_u32 v233, v243, v244, v245\nv_max3_u32 v232, v232, v246, v247\nv_max_u32 v232, v232, v233\nv_cmp_eq_u32 vcc, -1, v232\ns_cbranch_vccnz .Lw146\n"
    ".Lhwo113:\nv_lshlrev_b32 v192, 16, v240\nv_and_b32 v193, 0xffff0000, v240\nv_lshlrev_b32 v194, 16, v241\nv_and_b32 v195, 0xffff0000, v241\nv_lshlrev_b32 v196, 16, v242\n"
    "v_and_b32 v197, 0xffff0000, v242\nv_lshlrev_b32 v198, 16, v243\nv_and_b32 v199, 0xffff0000, v243\nv_lshlrev_b32 v200, 16, v244\nv_and_b32 v201, 0xffff0000, v244\n"
    "v_lshlrev_b32 v202, 16, v245\nv_and_b32 v203, 0xffff0000, v245\nv_lshlrev_b32 v204, 16, v246\nv_and_b32 v205, 0xffff0000, v246\nv_lshlrev_b32 v206, 16, v247\n"
    "v_and_b32 v207, 0xffff0000, v247\ns_add_u32 s58, s4, 8192\ns_addc_u32 s59, s5, 0\ns_add_u32 s60, s4, 16384\ns_addc_u32 s61, s5, 0\ns_add_u32 s62, s4, 24576\n"
    "s_addc_u32 s63, s5, 0\ns_add_u32 s54, s4, 2097152\ns_addc_u32 s55, s5, 0\ns_mov_b32 s48, 0x3f803f80\ns_waitcnt lgkmcnt(0)\ns_setprio 2\ns_barrier\n"
    "ds_read_b128 v[208:211], v42 offset:0\nds_read_b128 v[212:215], v42 offset:1024\nds_read_b128 v[216:219], v42 offset:2048\nds_read_b128 v[220:223], v42 offset:3072\n"
    "s_waitcnt lgkmcnt(3)\nv_mfma_f32_32x32x16_bf16 v[192:207], v[64:67], v[208:211], v[192:207]\ns_barrier\nds_read_b128 v[224:227], v42 offset:4096\ns_waitcnt lgkmcnt(3)\n"
    "v_mfma_f32_32x32x16_bf16 v[192:207], v[68:71], v[212:215], v[192:207]\nds_read_b128 v[228:231], v42 offset:5120\ns_waitcnt lgkmcnt(3)\n"
    "v_mfma_f32_32x32x16_bf16 v[192:207], v[72:75], v[216:219], v[192:207]\nds_read_b128 v[232:235], v42 offset:6144\ns_waitcnt lgkmcnt(3)\n"
    "v_mfma_f32_32x32x16_bf16 v[192:207], v[76:79], v[220:223], v[192:207]\nds_read_b128 v[208:211], v42 offset:7168\ns_waitcnt lgkmcnt(3)\n"
    "v_mfma_f32_32x32x16_bf16 v[192:207], v[80:83], v[224:227], v[192:207]\ns_barrier\nds_read_b128 v[212:215], v42 offset:8192\ns_waitcnt lgkmcnt(3)\n"
    "v_mfma_f32_32x32x16_bf16 v[192:207], v[84:87], v[228:231], v[192:207]\nds_read_b128 v[216:219], v42 offset:9216\ns_cbranch_execz .Lhop\ns_waitcnt lgkmcnt(3)\n"
    "v_mfma_f32_32x32x16_bf16 v[192:207], v[88:91], v[232:235], v[192:207]\nds_read_b128 v[220:223], v42 offset:10240\ns_waitcnt lgkmcnt(3)\n"
    "v_mfma_f32_32x32x16_bf16 v[192:207], v[92:95], v[208:211], v[192:207]\nds_read_b128 v[224:227], v42 offset:11264\ns_waitcnt lgkmcnt(3)\n"
    "v_mfma_f32_32x32x16_bf16 v[192:207], v[96:99], v[212:215], v[192:207]\nds_read_b128 v[228:231], v42 offset:12288\ns_waitcnt lgkmcnt(3)\n"
    "v_mfma_f32_32x32x16_bf16 v[192:207], v[100:103], v[216:219], v[192:207]\nds_read_b128 v[232:235], v42 offset:13312\ns_waitcnt lgkmcnt(3)\n"
    "v_mfma_f32_32x32x16_bf16 v[192:207], v[104:107], v[220:223], v[192:207]\nds_read_b128 v[208:211], v42 offset:14336\ns_waitcnt lgkmcnt(3)\n"
    "v_mfma_f32_32x32x16_bf16 v[192:207], v[108:111], v[224:227], v[192:207]\nds_read_b128 v[212:215], v42 offset:15360\ns_waitcnt lgkmcnt(3)\n"
    "v_mfma_f32_32x32x16_bf16 v[192:207], v[112:115], v[228:231], v[192:207]\ns_barrier\nds_read_b128 v[216:219], v42 offset:16384\nds_read_b128 v[220:223], v42 offset:17408\n"
    "s_waitcnt lgkmcnt(4)\nv_mfma_f32_32x32x16_bf16 v[192:207], v[116:119], v[232:235], v[192:207]\nds_read_b128 v[224:227], v42 offset:18432\ns_waitcnt lgkmcnt(4)\n"
    "v_mfma_f32_32x32x16_bf16 v[192:207], v[120:123], v[208:211], v[192:207]\nds_read_b128 v[228:231], v42 offset:19456\ns_waitcnt lgkmcnt(4)\n"
    "v_mfma_f32_32x32x16_bf16 v[192:207], v[124:127], v[212:215], v[192:207]\nds_read_b128 v[232:235], v42 offset:20480\ns_waitcnt vmcnt(8) lgkmcnt(4)\n"
    "v_mfma_f32_32x32x16_bf16 v[192:207], v[128:131], v[216:219], v[192:207]\nds_read_b128 v[208:211], v42 offset:21504\nds_read_b128 v[212:215], v42 offset:22528\n"
    "s_waitcnt lgkmcnt(5)\nv_mfma_f32_32x32x16_bf16 v[192:207], v[132:135], v[220:223], v[192:207]\nds_read_b128 v[216:219], v42 offset:23552\ns_waitcnt lgkmcnt(5)\n"
    "v_mfma_f32_32x32x16_bf16 v[192:207], v[136:139], v[224:227], v[192:207]\ns_barrier\nds_read_b128 v[220:223], v42 offset:24576\ns_waitcnt lgkmcnt(5)\n"
    "v_mfma_f32_32x32x16_bf16 v[192:207], v[140:143], v[228:231], v[192:207]\nds_read_b128 v[224:227], v42 offset:25600\ns_waitcnt lgkmcnt(5)\n"
    "v_mfma_f32_32x32x16_bf16 v[192:207], v[144:147], v[232:235], v[192:207]\nds_read_b128 v[228:231], v42 offset:26624\nds_read_b128 v[232:235], v42 offset:27648\n"
    "s_waitcnt lgkmcnt(6)\nv_mfma_f32_32x32x16_bf16 v[192:207], v[148:151], v[208:211], v[192:207]\nds_read_b128 v[208:211], v42 offset:28672\ns_waitcnt lgkmcnt(6)\n"
    "v_mfma_f32_32x32x16_bf16 v[192:207], v[152:155], v[212:215], v[192:207]\nds_read_b128 v[212:215], v42 offset:29696\ns_waitcnt lgkmcnt(6)\n"
    "v_mfma_f32_32x32x16_bf16 v[192:207], v[156:159], v[216:219], v[192:207]\nds_read_b128 v[216:219], v42 offset:30720\ns_waitcnt vmcnt(0) lgkmcnt(6)\n"
    "v_mfma_f32_32x32x16_bf16 v[192:207], v[160:163], v[220:223], v[192:207]\nds_read_b128 v[220:223], v42 offset:31744\ns_waitcnt lgkmcnt(6)\n"
    "v_mfma_f32_32x32x16_bf16 v[192:207], v[164:167], v[224:227], v[192:207]\ns_waitcnt lgkmcnt(5)\nv_mfma_f32_32x32x16_bf16 v[192:207], v[168:171], v[228:231], v[192:207]\n"
    "s_waitcnt lgkmcnt(4)\nv_mfma_f32_32x32x16_bf16 v[192:207], v[172:175], v[232:235], v[192:207]\ns_waitcnt lgkmcnt(3)\n"
    "v_mfma_f32_32x32x16_bf16 v[192:207], v[176:179], v[208:211], v[192:207]\ns_waitcnt lgkmcnt(2)\nv_mfma_f32_32x32x16_bf16 v[192:207], v[180:183], v[212:215], v[192:207]\n"
    "s_waitcnt lgkmcnt(1)\nv_mfma_f32_32x32x16_bf16 v[192:207], v[184:187], v[216:219], v[192:207]\ns_waitcnt lgkmcnt(0)\n"
    "v_mfma_f32_32x32x16_bf16 v[192:207], v[188:191], v[220:223], v[192:207]\ns_nop 11\nv_cvt_pk_bf16_f32 v208, v192, v193\nv_cvt_pk_bf16_f32 v209, v194, v195\n"
    "v_cvt_pk_bf16_f32 v210, v196, v197\nv_cvt_pk_bf16_f32 v211, v198, v199\nv_cvt_pk_bf16_f32 v212, v200, v201\nv_cvt_pk_bf16_f32 v213, v202, v203\n"
    "v_cvt_pk_bf16_f32 v214, v204, v205\nv_cvt_pk_bf16_f32 v215, v206, v207\nv_dot2_f32_bf16 v56, v208, s48, 0\nv_dot2_f32_bf16 v57, v208, v208, 0\n"
    "v_dot2_f32_bf16 v58, v209, s48, 0\nv_dot2_f32_bf16 v59, v209, v209, 0\nv_dot2_f32_bf16 v56, v210, s48, v56\nv_dot2_f32_bf16 v57, v210, v210, v57\n"
    "v_dot2_f32_bf16 v58, v211, s48, v58\nv_dot2_f32_bf16 v59, v211, v211, v59\nv_dot2_f32_bf16 v56, v212, s48, v56\nv_dot2_f32_bf16 v57, v212, v212, v57\n"
    "v_dot2_f32_bf16 v58, v213, s48, v58\nv_dot2_f32_bf16 v59, v213, v213, v59\nv_dot2_f32_bf16 v56, v214, s48, v56\nv_dot2_f32_bf16 v57, v214, v214, v57\n"
    "v_dot2_f32_bf16 v58, v215, s48, v58\nv_dot2_f32_bf16 v59, v215, v215, v59\nds_write2_b32 v40, v208, v209 offset1:32\ns_nop 1\nv_pk_add_f32 v[56:57], v[56:57], v[58:59]\n"
    "s_nop 1\nv_permlane32_swap_b32 v56, v57\nv_add_f32 v56, v56, v57\nds_write_b32 v45, v56\nds_write2_b32 v40, v210, v211 offset0:128 offset1:160\n"
    "ds_read_b128 v[216:219], v41\nds_write2_b32 v46, v212, v213 offset1:32\ns_waitcnt lgkmcnt(3)\ns_barrier\nds_write2_b32 v46, v214, v215 offset0:128 offset1:160\n"
    "ds_read_b128 v[220:223], v41 offset:1024\ns_setprio 0\ns_waitcnt lgkmcnt(3)\nglobal_store_dwordx4 v43, v[216:219], s[4:5] sc1\ns_waitcnt lgkmcnt(0)\n"
    "global_store_dwordx4 v44, v[220:223], s[4:5] sc1\ns_cmp_lg_u32 s12, 0\ns_cbranch_scc1 .Lz149\ns_call_b64 s[94:95], .Lhcon67_1\ns_call_b64 s[94:95], .Lhcon67_0\n"
    "s_call_b64 s[94:95], .Lhcon67_2\ns_sleep 2\n.Lz149:\nds_read_b64 v[128:129], v23 offset:896\nds_read_b64 v[130:131], v23 offset:904\n"
    "ds_read_b64 v[132:133], v23 offset:864\nds_read_b64 v[134:135], v23 offset:872\nds_read_b64 v[136:137], v23 offset:832\nds_read_b64 v[138:139], v23 offset:840\n"
    "ds_read_b64 v[140:141], v23 offset:800\nds_read_b64 v[142:143], v23 offset:808\nds_read_b64 v[144:145], v23 offset:704\nds_read_b64 v[146:147], v23 offset:712\n"
    "ds_read_b64 v[148:149], v23 offset:672\nds_read_b64 v[150:151], v23 offset:680\nds_read_b64 v[152:153], v23 offset:640\nds_read_b64 v[154:155], v23 offset:648\n"
    "ds_read_b64 v[156:157], v23 offset:608\nds_read_b64 v[158:159], v23 offset:616\nds_read_b64 v[160:161], v39 offset:512\nds_read_b64 v[162:163], v39 offset:520\n"
    "ds_read_b64 v[164:165], v39 offset:480\nds_read_b64 v[166:167], v39 offset:488\nds_read_b64 v[168:169], v39 offset:448\nds_read_b64 v[170:171], v39 offset:456\n"
    "ds_read_b64 v[172:173], v39 offset:416\ns_nop 0\ns_cbranch_execz .Lhop\nds_read_b64 v[174:175], v39 offset:424\nds_read_b64 v[176:177], v39 offset:320\n"
    "ds_read_b64 v[178:179], v39 offset:328\nds_read_b64 v[180:181], v39 offset:288\nds_read_b64 v[182:183], v39 offset:296\nds_read_b64 v[184:185], v39 offset:256\n"
    "ds_read_b64 v[186:187], v39 offset:264\nds_read_b64 v[188:189], v39 offset:224\nds_read_b64 v[190:191], v39 offset:232\n"
    ".Lln114:\ns_cmp_gt_u32 s3, 3\ns_waitcnt lgkmcnt(0)\ns_barrier\nglobal_load_dwordx4 v[240:243], v17, s[4:5] sc1\nglobal_load_dwordx4 v[208:211], v16, s[4:5] sc1\n"
    "global_load_dwordx4 v[212:215], v16, s[58:59] sc1\nglobal_load_dwordx4 v[216:219], v16, s[60:61] sc1\nglobal_load_dwordx4 v[220:223], v16, s[62:63] sc1\n"
    "s_cbranch_scc1 .Llc151\nds_read_b128 v[10:13], v59\nds_read_b32 v14, v59 offset:4236\nds_read_u16 v15, v59 offset:6350\nds_read_b64 v[64:65], v23 offset:2016\n"
    "ds_read_b64 v[66:67], v23 offset:2024\nds_read_b64 v[68:69], v23 offset:1984\nds_read_b64 v[70:71], v23 offset:1992\nds_read_b64 v[72:73], v23 offset:1952\n"
    "ds_read_b64 v[74:75], v23 offset:1960\nds_read_b64 v[76:77], v23 offset:1920\nds_read_b64 v[78:79], v23 offset:1928\nds_read_b64 v[80:81], v23 offset:1888\n"
    "ds_read_b64 v[82:83], v23 offset:1896\nds_read_b64 v[84:85], v23 offset:1856\nds_read_b64 v[86:87], v23 offset:1864\nds_read_b64 v[88:89], v23 offset:1824\n"
    "ds_read_b64 v[90:91], v23 offset:1832\nds_read_b64 v[92:93], v23 offset:1792\nds_read_b64 v[94:95], v23 offset:1800\nds_read_b64 v[96:97], v23 offset:1760\n"
    "ds_read_b64 v[98:99], v23 offset:1768\nds_read_b64 v[100:101], v23 offset:1728\nds_read_b64 v[102:103], v23 offset:1736\nds_read_b64 v[104:105], v23 offset:1696\n"
    "ds_read_b64 v[106:107], v23 offset:1704\nds_read_b64 v[108:109], v23 offset:1664\nds_read_b64 v[110:111], v23 offset:1672\nds_read_b64 v[112:113], v23 offset:1632\n"
    "ds_read_b64 v[114:115], v23 offset:1640\nds_read_b64 v[116:117], v23 offset:1600\nds_read_b64 v[118:119], v23 offset:1608\nds_read_b64 v[120:121], v23 offset:1568\n"
    "ds_read_b64 v[122:123], v23 offset:1576\nds_read_b64 v[124:125], v23 offset:1536\nds_read_b64 v[126:127], v23 offset:1544\nds_read_b64 v[128:129], v23 offset:1504\n"
    "ds_read_b64 v[130:131], v23 offset:1512\nds_read_b64 v[132:133], v23 offset:1472\nds_read_b64 v[134:135], v23 offset:1480\nds_read_b64 v[136:137], v23 offset:1440\n"
    "ds_read_b64 v[138:139], v23 offset:1448\nds_read_b64 v[140:141], v23 offset:1408\nds_read_b64 v[142:143], v23 offset:1416\nds_read_b64 v[144:145], v23 offset:1376\n"
    "ds_read_b64 v[146:147], v23 offset:1384\nds_read_b64 v[148:149], v23 offset:1344\nds_read_b64 v[150:151], v23 offset:1352\nds_read_b64 v[152:153], v23 offset:1312\n"
    "ds_read_b64 v[154:155], v23 offset:1320\nds_read_b64 v[156:157], v23 offset:1280\nds_read_b64 v[158:159], v23 offset:1288\nds_read_b64 v[160:161], v23 offset:1248\n"
    "ds_read_b64 v[162:163], v23 offset:1256\nds_read_b64 v[164:165], v23 offset:1216\nds_read_b64 v[166:167], v23 offset:1224\nds_read_b64 v[168:169], v23 offset:1184\n"
    "ds_read_b64 v[170:171], v23 offset:1192\nds_read_b64 v[172:173], v23 offset:1152\nds_read_b64 v[174:175], v23 offset:1160\nds_read_b64 v[176:177], v23 offset:1120\n"
    "ds_read_b64 v[178:179], v23 offset:1128\nds_read_b64 v[180:181], v23 offset:1088\nds_read_b64 v[182:183], v23 offset:1096\nds_read_b64 v[184:185], v23 offset:1056\n"
    "ds_read_b64 v[186:187], v23 offset:1064\nds_read_b64 v[188:189], v23 offset:1024\nds_read_b64 v[190:191], v23 offset:1032\nds_read_b64 v[192:193], v23 offset:992\n"
    "ds_read_b64 v[194:195], v23 offset:1000\nds_read_b64 v[196:197], v23 offset:960\nds_read_b64 v[198:199], v23 offset:968\nds_read_b64 v[200:201], v23 offset:928\n"
    "ds_read_b64 v[202:203], v23 offset:936\n.Llc151:\nv_mov_b64 v[236:237], 0\nv_mov_b64 v[238:239], 0\ns_cmp_lg_u32 s12, 0\ns_cbranch_scc1 .Ln152\n"
    "s_load_dword s17, s[16:17], 0\nv_mov_b32 v232, 115200\nds_read_u16 v233, v232 offset:1024\nds_read_u16 v234, v232 offset:2560\nds_read_b32 v235, v232 offset:48032\n"
    "ds_read_b32 v232, v232 offset:48040\ns_waitcnt lgkmcnt(0)\nv_fma_mix_f32 v233, v233, -v235, 0 op_sel_hi:[1,0,0]\nv_fma_mix_f32 v234, v234, -v232, 0 op_sel_hi:[1,0,0]\n"
    "s_lshl_b32 s16, s17, 16\ns_and_b32 s17, s17, 0xffff0000\nv_readfirstlane_b32 s14, v235\nv_readfirstlane_b32 s15, v232\nv_readfirstlane_b32 s28, v233\n"
    "v_readfirstlane_b32 s29, v234\ns_lshr_b32 s26, s14, 16\ns_lshr_b32 s27, s15, 16\n.Ln152:\nds_write_b128 v22, v[236:239]\ns_mov_b32 s44, 0x3f80\n"
    "s_mov_b32 s46, 0x3e800000\ns_mov_b32 s48, 0x3727c5ac\ns_mov_b32 s80, 0x10000\n.Ln154:\ns_waitcnt vmcnt(4)\nv_lshlrev_b32 v56, 16, v240\nv_lshlrev_b32 v57, 16, v241\n"
    "v_lshlrev_b32 v58, 16, v242\nv_lshlrev_b32 v59, 16, v243\nv_and_b32 v60, 0xffff0000, v240\nv_and_b32 v61, 0xffff0000, v241\nv_and_b32 v62, 0xffff0000, v242\n"
    "v_and_b32 v63, 0xffff0000, v243\nv_max3_u32 v232, v240, v241, v242\nv_max_u32 v232, v232, v243\nv_cmp_eq_u32 s[50:51], -1, v232\ns_waitcnt vmcnt(3)\n"
    "v_dot2_f32_bf16 v224, v208, s44, 0\nv_dot2_f32_bf16 v228, v208, v208, 0\nv_dot2_f32_bf16 v225, v209, s44, 0\nv_dot2_f32_bf16 v229, v209, v209, 0\n"
    "v_dot2_f32_bf16 v226, v210, s44, 0\nv_dot2_f32_bf16 v230, v210, v210, 0\nv_dot2_f32_bf16 v227, v211, s44, 0\nv_dot2_f32_bf16 v231, v211, v211, 0\ns_waitcnt vmcnt(2)\n"
    "s_nop 0\ns_cbranch_execz .Lhop\nv_dot2_f32_bf16 v224, v212, s44, v224\nv_dot2_f32_bf16 v228, v212, v212, v228\nv_dot2_f32_bf16 v225, v213, s44, v225\n"
    "v_dot2_f32_bf16 v229, v213, v213, v229\nv_dot2_f32_bf16 v226, v214, s44, v226\nv_dot2_f32_bf16 v230, v214, v214, v230\nv_dot2_f32_bf16 v227, v215, s44, v227\n"
    "v_dot2_f32_bf16 v231, v215, v215, v231\ns_waitcnt vmcnt(1)\nv_dot2_f32_bf16 v224, v216, s44, v224\nv_dot2_f32_bf16 v228, v216, v216, v228\n"
    "v_dot2_f32_bf16 v225, v217, s44, v225\nv_dot2_f32_bf16 v229, v217, v217, v229\nv_dot2_f32_bf16 v226, v218, s44, v226\nv_dot2_f32_bf16 v230, v218, v218, v230\n"
    "v_dot2_f32_bf16 v227, v219, s44, v227\nv_dot2_f32_bf16 v231, v219, v219, v231\ns_waitcnt vmcnt(0)\nv_dot2_f32_bf16 v224, v220, s44, v224\n"
    "v_dot2_f32_bf16 v228, v220, v220, v228\nv_dot2_f32_bf16 v225, v221, s44, v225\nv_dot2_f32_bf16 v229, v221, v221, v229\nv_dot2_f32_bf16 v226, v222, s44, v226\n"
    "v_dot2_f32_bf16 v230, v222, v222, v230\nv_dot2_f32_bf16 v227, v223, s44, v227\nv_dot2_f32_bf16 v231, v223, v223, v231\n"
    "v_pk_mul_f32 v[224:225], v[224:225], s[46:47] op_sel_hi:[1,0]\ns_nop 0\nv_pk_mul_f32 v[226:227], v[226:227], s[46:47] op_sel_hi:[1,0]\nv_cmp_u_f32 vcc, v228, v229\n"
    "v_cmp_u_f32 s[52:53], v230, v231\nv_pk_fma_f32 v[232:233], v[224:225], v[224:225], s[48:49] op_sel_hi:[1,1,0] neg_lo:[0,0,1] neg_hi:[0,0,1]\n"
    "v_pk_fma_f32 v[234:235], v[226:227], v[226:227], s[48:49] op_sel_hi:[1,1,0] neg_lo:[0,0,1] neg_hi:[0,0,1]\n"
    "v_pk_fma_f32 v[228:229], v[228:229], s[46:47], v[232:233] op_sel_hi:[1,0,1] neg_lo:[0,0,1] neg_hi:[0,0,1]\n"
    "v_pk_fma_f32 v[230:231], v[230:231], s[46:47], v[234:235] op_sel_hi:[1,0,1] neg_lo:[0,0,1] neg_hi:[0,0,1]\nv_max_f32 v228, s48, v228\nv_max_f32 v229, s48, v229\n"
    "v_max_f32 v230, s48, v230\nv_max_f32 v231, s48, v231\nv_rsq_f32 v228, v228\nv_rsq_f32 v229, v229\nv_rsq_f32 v230, v230\nv_rsq_f32 v231, v231\n"
    "v_pk_add_f32 v[244:245], v[56:57], v[224:225] neg_lo:[0,1] neg_hi:[0,1]\nv_pk_add_f32 v[246:247], v[58:59], v[226:227] neg_lo:[0,1] neg_hi:[0,1]\n"
    "v_pk_add_f32 v[248:249], v[60:61], v[224:225] neg_lo:[0,1] neg_hi:[0,1]\nv_pk_add_f32 v[250:251], v[62:63], v[226:227] neg_lo:[0,1] neg_hi:[0,1]\n"
    "s_or_b64 vcc, vcc, s[50:51]\nv_pk_mul_f32 v[244:245], v[244:245], v[228:229]\nv_pk_mul_f32 v[246:247], v[246:247], v[230:231]\n"
    "v_pk_mul_f32 v[248:249], v[248:249], v[228:229]\nv_pk_mul_f32 v[250:251], v[250:251], v[230:231]\ns_or_b64 vcc, vcc, s[52:53]\nv_cvt_pk_f16_f32 v232, v244, v245\n"
    "v_cvt_pk_f16_f32 v233, v246, v247\nv_cvt_pk_f16_f32 v234, v248, v249\nds_write_b64 v18, v[232:233]\nv_cvt_pk_f16_f32 v235, v250, v251\n"
    "ds_write_b64 v18, v[234:235] offset:6480\ns_cbranch_scc1 .Llw157\n.Llx158:\ns_cmp_gt_u32 s3, 3\ns_cbranch_scc1 .Lhcv155\n"
    "s_waitcnt lgkmcnt(0)\ns_barrier\ns_memrealtime s[100:101]\nds_read_b128 v[220:223], v27 offset:1392\nds_read_b128 v[224:227], v27 offset:1360\n"
    "ds_read_b128 v[228:231], v27 offset:1312\nv_fmac_f32 v56, s16, v244\nv_fmac_f32 v57, s16, v245\nv_fmac_f32 v58, s16, v246\nv_fmac_f32 v59, s16, v247\n"
    "v_fmac_f32 v60, s17, v248\nv_fmac_f32 v61, s17, v249\nv_fmac_f32 v62, s17, v250\nv_fmac_f32 v63, s17, v251\ns_waitcnt lgkmcnt(2)\n"
    "v_mfma_f32_32x32x16_f16 v[204:219], v[64:67], v[220:223], 0\nds_read_b128 v[232:235], v27 offset:1280\nds_read_b128 v[236:239], v27 offset:1232\ns_cmp_lg_u32 s13, 0\n"
    "s_cbranch_scc1 .Lz159\ns_waitcnt lgkmcnt(0)\nv_mov_b32 v40, s100\ns_mov_b64 exec, 1\nglobal_store_dword v32, v40, s[4:5] sc1\ns_mov_b64 exec, -1\n.Lz159:\n"
    "s_waitcnt lgkmcnt(3)\nv_mfma_f32_32x32x16_f16 v[204:219], v[68:71], v[224:227], v[204:219]\nds_read_b128 v[240:243], v27 offset:1200\n"
    "ds_read_b128 v[220:223], v27 offset:1152\ns_waitcnt lgkmcnt(4)\nv_mfma_f32_32x32x16_f16 v[204:219], v[72:75], v[228:231], v[204:219]\n"
    "ds_read_b128 v[224:227], v27 offset:1120\nds_read_b128 v[228:231], v27 offset:1072\ns_waitcnt lgkmcnt(5)\n"
    "v_mfma_f32_32x32x16_f16 v[204:219], v[76:79], v[232:235], v[204:219]\nds_read_b128 v[232:235], v27 offset:1040\ns_waitcnt lgkmcnt(5)\n"
    "v_mfma_f32_32x32x16_f16 v[204:219], v[80:83], v[236:239], v[204:219]\nds_read_b128 v[236:239], v27 offset:992\ns_waitcnt lgkmcnt(5)\n"
    "v_mfma_f32_32x32x16_f16 v[204:219], v[84:87], v[240:243], v[204:219]\nds_read_b128 v[240:243], v27 offset:960\ns_waitcnt lgkmcnt(5)\n"
    "v_mfma_f32_32x32x16_f16 v[204:219], v[88:91], v[220:223], v[204:219]\nds_read_b128 v[220:223], v27 offset:912\ns_waitcnt lgkmcnt(5)\n"
    "v_mfma_f32_32x32x16_f16 v[204:219], v[92:95], v[224:227], v[204:219]\nds_read_b128 v[224:227], v27 offset:880\ns_waitcnt lgkmcnt(5)\n"
    "v_mfma_f32_32x32x16_f16 v[204:219], v[96:99], v[228:231], v[204:219]\nds_read_b128 v[228:231], v27 offset:832\ns_waitcnt lgkmcnt(5)\n"
    "v_mfma_f32_32x32x16_f16 v[204:219], v[100:103], v[232:235], v[204:219]\nds_read_b128 v[232:235], v27 offset:800\ns_waitcnt lgkmcnt(5)\n"
    "v_mfma_f32_32x32x16_f16 v[204:219], v[104:107], v[236:239], v[204:219]\nds_read_b128 v[236:239], v27 offset:752\ns_waitcnt lgkmcnt(5)\n"
    "v_mfma_f32_32x32x16_f16 v[204:219], v[108:111], v[240:243], v[204:219]\nds_read_b128 v[240:243], v27 offset:720\ns_waitcnt lgkmcnt(5)\n"
    "v_mfma_f32_32x32x16_f16 v[204:219], v[112:115], v[220:223], v[204:219]\nds_read_b128 v[220:223], v27 offset:672\ns_waitcnt lgkmcnt(5)\n"
    "v_mfma_f32_32x32x16_f16 v[204:219], v[116:119], v[224:227], v[204:219]\nds_read_b128 v[224:227], v27 offset:640\ns_waitcnt lgkmcnt(5)\n"
    "v_mfma_f32_32x32x16_f16 v[204:219], v[120:123], v[228:231], v[204:219]\nds_read_b128 v[228:231], v27 offset:592\ns_waitcnt lgkmcnt(5)\n"
    "v_mfma_f32_32x32x16_f16 v[204:219], v[124:127], v[232:235], v[204:219]\nds_read_b128 v[232:235], v27 offset:560\ns_waitcnt lgkmcnt(5)\n"
    "v_mfma_f32_32x32x16_f16 v[204:219], v[128:131], v[236:239], v[204:219]\nds_read_b128 v[236:239], v27 offset:512\ns_waitcnt lgkmcnt(5)\n"
    "v_mfma_f32_32x32x16_f16 v[204:219], v[132:135], v[240:243], v[204:219]\nds_read_b128 v[240:243], v27 offset:480\ns_waitcnt lgkmcnt(5)\n"
    "v_mfma_f32_32x32x16_f16 v[204:219], v[136:139], v[220:223], v[204:219]\nds_read_b128 v[220:223], v27 offset:432\ns_waitcnt lgkmcnt(5)\n"
    "v_mfma_f32_32x32x16_f16 v[204:219], v[140:143], v[224:227], v[204:219]\nds_read_b128 v[224:227], v27 offset:400\ns_waitcnt lgkmcnt(5)\n"
    "v_mfma_f32_32x32x16_f16 v[204:219], v[144:147], v[228:231], v[204:219]\nds_read_b128 v[228:231], v27 offset:352\ns_waitcnt lgkmcnt(5)\n"
    "v_mfma_f32_32x32x16_f16 v[204:219], v[148:151], v[232:235], v[204:219]\nds_read_b128 v[232:235], v27 offset:320\ns_waitcnt lgkmcnt(5)\n"
    "v_mfma_f32_32x32x16_f16 v[204:219], v[152:155], v[236:239], v[204:219]\nds_read_b128 v[236:239], v27 offset:272\ns_waitcnt lgkmcnt(5)\ns_cbranch_execz .Lhop\n"
    "v_mfma_f32_32x32x16_f16 v[204:219], v[156:159], v[240:243], v[204:219]\nds_read_b128 v[240:243], v27 offset:240\ns_waitcnt lgkmcnt(5)\n"
    "v_mfma_f32_32x32x16_f16 v[204:219], v[160:163], v[220:223], v[204:219]\nds_read_b128 v[220:223], v27 offset:192\ns_waitcnt lgkmcnt(5)\n"
    "v_mfma_f32_32x32x16_f16 v[204:219], v[164:167], v[224:227], v[204:219]\nds_read_b128 v[224:227], v27 offset:160\ns_waitcnt lgkmcnt(5)\n"
    "v_mfma_f32_32x32x16_f16 v[204:219], v[168:171], v[228:231], v[204:219]\nds_read_b128 v[228:231], v27 offset:112\ns_waitcnt lgkmcnt(5)\n"
    "v_mfma_f32_32x32x16_f16 v[204:219], v[172:175], v[232:235], v[204:219]\nds_read_b128 v[232:235], v27 offset:80\ns_waitcnt lgkmcnt(5)\n"
    "v_mfma_f32_32x32x16_f16 v[204:219], v[176:179], v[236:239], v[204:219]\nds_read_b128 v[236:239], v27 offset:32\ns_waitcnt lgkmcnt(5)\n"
    "v_mfma_f32_32x32x16_f16 v[204:219], v[180:183], v[240:243], v[204:219]\ns_waitcnt lgkmcnt(4)\nv_mfma_f32_32x32x16_f16 v[204:219], v[184:187], v[220:223], v[204:219]\n"
    "s_waitcnt lgkmcnt(3)\nv_mfma_f32_32x32x16_f16 v[204:219], v[188:191], v[224:227], v[204:219]\ns_waitcnt lgkmcnt(2)\n"
    "v_mfma_f32_32x32x16_f16 v[204:219], v[192:195], v[228:231], v[204:219]\ns_waitcnt lgkmcnt(1)\nv_mfma_f32_32x32x16_f16 v[204:219], v[196:199], v[232:235], v[204:219]\n"
    "s_waitcnt lgkmcnt(0)\nv_mfma_f32_32x32x16_f16 v[204:219], v[200:203], v[236:239], v[204:219]\ns_nop 11\nv_cvt_pk_bf16_f32 v204, v204, v205\n"
    "v_cvt_pk_bf16_f32 v205, v206, v207\nds_write_b64 v28, v[204:205] offset:0\nv_cvt_pk_bf16_f32 v208, v208, v209\nv_cvt_pk_bf16_f32 v209, v210, v211\n"
    "ds_write_b64 v29, v[208:209] offset:0\nv_cvt_pk_bf16_f32 v212, v212, v213\nv_cvt_pk_bf16_f32 v213, v214, v215\nds_write_b64 v30, v[212:213] offset:0\n"
    "v_cvt_pk_bf16_f32 v216, v216, v217\nv_cvt_pk_bf16_f32 v217, v218, v219\nds_write_b64 v31, v[216:217] offset:0\ns_cmp_lg_u32 s12, 0\ns_cbranch_scc1 .Lz161\n"
    "v_bfe_u32 v95, v0, 5, 1\nv_and_b32 v94, 31, v0\nv_lshlrev_b32 v39, 9, v95\nv_lshl_add_u32 v39, v94, 2, v39\nv_and_b32 v93, 63, v0\ns_lshl_b32 s43, s24, 15\n"
    "s_and_b32 s42, s25, 3\ns_lshl_b32 s41, s42, 13\ns_lshl_b32 s40, s13, 11\ns_add_u32 s39, s43, s41\ns_add_u32 s39, s39, s40\ns_add_u32 s39, s39, 0xd00000\n"
    "v_lshl_add_u32 v34, v93, 2, s39\ns_lshl_b32 s38, s19, 17\ns_lshl_b32 s37, s13, 15\ns_add_u32 s82, s38, s37\ns_add_u32 s82, s82, 0x600800\ns_lshl_b32 s36, s23, 8\n"
    "s_add_u32 s35, s43, s41\ns_add_u32 s35, s35, s40\ns_add_u32 s35, s35, s36\ns_add_u32 s35, s35, 0xd00000\nv_lshl_add_u32 v35, v93, 2, s35\ns_lshr_b32 s34, s18, 4\n"
    "s_lshl_b32 s33, s34, 7\ns_and_b32 s32, s18, 15\ns_lshl_b32 s31, s32, 2\ns_add_u32 s30, s33, s31\ns_add_u32 s30, s30, 0x100e000\nv_mov_b32 v141, s30\n"
    "s_add_u32 s82, s4, s82\ns_addc_u32 s83, s5, 0\ns_add_u32 s86, s23, 0\ns_and_b32 s86, s86, 7\ns_lshl_b32 s44, s86, 12\ns_add_u32 s52, s82, s44\ns_addc_u32 s53, s83, 0\n"
    "s_add_u32 s86, s23, 1\ns_and_b32 s86, s86, 7\ns_lshl_b32 s45, s86, 12\ns_add_u32 s54, s82, s45\ns_addc_u32 s55, s83, 0\ns_add_u32 s86, s23, 2\ns_and_b32 s86, s86, 7\n"
    "s_lshl_b32 s46, s86, 12\ns_add_u32 s56, s82, s46\ns_addc_u32 s57, s83, 0\ns_add_u32 s86, s23, 3\ns_and_b32 s86, s86, 7\ns_lshl_b32 s47, s86, 12\ns_add_u32 s58, s82, s47\n"
    "s_addc_u32 s59, s83, 0\ns_add_u32 s86, s23, 4\ns_and_b32 s86, s86, 7\ns_lshl_b32 s48, s86, 12\ns_add_u32 s60, s82, s48\ns_addc_u32 s61, s83, 0\ns_add_u32 s86, s23, 5\n"
    "s_and_b32 s86, s86, 7\ns_lshl_b32 s49, s86, 12\ns_add_u32 s62, s82, s49\ns_addc_u32 s63, s83, 0\ns_add_u32 s86, s23, 6\ns_and_b32 s86, s86, 7\ns_lshl_b32 s50, s86, 12\n"
    "s_add_u32 s64, s82, s50\ns_addc_u32 s65, s83, 0\ns_add_u32 s86, s23, 7\ns_and_b32 s86, s86, 7\ns_lshl_b32 s51, s86, 12\ns_add_u32 s66, s82, s51\ns_addc_u32 s67, s83, 0\n"
    "s_mov_b32 s81, 0x3b000000\ns_mov_b32 s84, -1\ns_mov_b32 s85, 0\nv_mov_b32 v241, 0x3f803f80\nv_mov_b64 v[120:121], 0\nv_mov_b64 v[122:123], 0\nv_mov_b64 v[124:125], 0\n"
    "v_mov_b64 v[126:127], 0\nv_mov_b64 v[128:129], 0\nv_mov_b64 v[130:131], 0\nv_mov_b64 v[132:133], 0\nv_mov_b64 v[134:135], 0\nv_mad_u32_u24 v242, s44, 3, v36\n"
    "s_mov_b32 s80, 0x10000\nds_read_b64_tr_b16 v[208:209], v242 offset:0\nds_read_b64_tr_b16 v[210:211], v242 offset:256\nds_read_b64_tr_b16 v[212:213], v242 offset:3072\n"
    "ds_read_b64_tr_b16 v[214:215], v242 offset:3328\nds_read_b64_tr_b16 v[216:217], v242 offset:6144\nds_read_b64_tr_b16 v[218:219], v242 offset:6400\n"
    "ds_read_b64_tr_b16 v[220:221], v242 offset:9216\nds_read_b64_tr_b16 v[222:223], v242 offset:9472\nds_read_b64_tr_b16 v[224:225], v242 offset:1024\n"
    "ds_read_b64_tr_b16 v[226:227], v242 offset:1280\nds_read_b64_tr_b16 v[228:229], v242 offset:4096\nds_read_b64_tr_b16 v[230:231], v242 offset:4352\n"
    "ds_read_b32 v124, v38 offset:0\nds_read_b32 v128, v38 offset:128\nds_read_b32 v132, v38 offset:256\n.Lz161:\ns_cmp_lt_u32 s3, 2\ns_waitcnt lgkmcnt(0)\ns_barrier\n"
    "s_memrealtime s[88:89]\nds_read_b64 v[44:45], v19\nds_read_b64 v[46:47], v19 offset:10240\ns_cbranch_scc1 .Le163\nds_read_b64 v[48:49], v20\n"
    "ds_read_b64 v[50:51], v20 offset:10240\ns_waitcnt lgkmcnt(0)\nv_dot2c_f32_bf16 v56, s26, v48\nv_dot2c_f32_bf16 v57, s14, v48\nv_dot2c_f32_bf16 v58, s26, v49\n"
    "v_dot2c_f32_bf16 v59, s14, v49\nv_dot2c_f32_bf16 v60, s27, v50\nv_dot2c_f32_bf16 v61, s15, v50\nv_dot2c_f32_bf16 v62, s27, v51\nv_dot2c_f32_bf16 v63, s15, v51\n"
    "s_branch .Lz160\n.Le163:\ns_cmp_lg_u32 s13, 0\ns_cbranch_scc1 .Lz160\nglobal_load_dwordx4 v[40:43], v33, s[4:5] sc1\n.Lz160:\ns_waitcnt lgkmcnt(0)\n"
    "v_dot2c_f32_bf16 v56, s26, v44\nv_dot2c_f32_bf16 v57, s14, v44\nv_dot2c_f32_bf16 v58, s26, v45\nv_dot2c_f32_bf16 v59, s14, v45\nv_dot2c_f32_bf16 v60, s27, v46\n"
    "v_dot2c_f32_bf16 v61, s15, v46\nv_dot2c_f32_bf16 v62, s27, v47\nv_dot2c_f32_bf16 v63, s15, v47\nv_cvt_pk_bf16_f32 v52, v56, v60\nv_cvt_pk_bf16_f32 v53, v57, v61\n"
    "v_cvt_pk_bf16_f32 v54, v58, v62\nv_cvt_pk_bf16_f32 v55, v59, v63\nglobal_store_dwordx4 v21, v[52:55], s[4:5] sc1\ns_cmp_lg_u32 s3, 0\ns_cbranch_scc1 .Lz162\n"
    "s_waitcnt vmcnt(1)\ns_sub_u32 s87, s100, 300\nv_subrev_u32 v40, s87, v40\ns_cbranch_execz .Lhop\nv_subrev_u32 v41, s87, v41\nv_subrev_u32 v42, s87, v42\n"
    "v_subrev_u32 v43, s87, v43\nv_max3_u32 v40, v40, v41, v42\nv_max_u32 v40, v40, v43\ns_nop 1\nv_max_u32_dpp v40, v40, v40 row_ror:1 row_mask:0xf bank_mask:0xf\ns_nop 1\n"
    "v_max_u32_dpp v40, v40, v40 row_ror:2 row_mask:0xf bank_mask:0xf\ns_nop 1\nv_max_u32_dpp v40, v40, v40 row_ror:4 row_mask:0xf bank_mask:0xf\ns_nop 1\n"
    "v_max_u32_dpp v40, v40, v40 row_ror:8 row_mask:0xf bank_mask:0xf\nv_mov_b32 v41, v40\ns_nop 1\nv_permlane16_swap_b32 v40, v41\nv_max_u32 v40, v40, v41\n"
    "v_mov_b32 v41, v40\ns_nop 1\nv_permlane32_swap_b32 v40, v41\nv_max_u32 v40, v40, v41\ns_nop 0\nv_readfirstlane_b32 s87, v40\ns_min_u32 s87, s87, 370\n"
    "s_max_u32 s87, s87, 300\ns_sub_u32 s87, s87, 300\ns_mov_b32 s100, s88\n.Lz162:\ns_cmp_eq_u32 s12, 0\ns_cbranch_scc1 .Lqg165\n"
    ".Lfin164:\ns_lshl_b32 s43, s18, 13\ns_add_u32 s42, s43, 0x1200000\nv_lshl_add_u32 v16, v0, 4, s42\nv_lshrrev_b32 v239, 3, v0\nv_and_b32 v238, 7, v0\n"
    "s_lshl_b32 s41, s25, 16\ns_lshl_b32 s40, s24, 7\ns_add_u32 s39, s41, s40\nv_lshl_add_u32 v17, v239, 10, s39\nv_lshl_add_u32 v17, v238, 4, v17\ns_lshl_b32 s38, s3, 4\n"
    "v_lshl_add_u32 v18, v0, 4, s38\nv_mul_u32_u24 v237, 0x410, v238\nv_lshl_add_u32 v22, v239, 2, v237\ns_mov_b32 s80, 0x10000\ns_cmp_lg_u32 s3, 0\ns_cbranch_scc1 .Lz169\n"
    "s_waitcnt lgkmcnt(0)\ns_add_u32 s87, s87, s100\ns_add_u32 s87, s87, 25\n.Lj170:\ns_memrealtime s[100:101]\ns_waitcnt lgkmcnt(0)\ns_sub_u32 s101, s87, s100\n"
    "s_cmp_le_i32 s101, 0\ns_cbranch_scc0 .Lj170\n.Lz169:\ns_barrier\n.Lf167:\nglobal_load_dwordx4 v[44:47], v16, s[4:5] sc1\ns_waitcnt vmcnt(0)\nds_write_b128 v18, v[44:47]\n"
    "v_max3_u32 v48, v44, v45, v46\nv_max_u32 v48, v48, v47\nv_cmp_eq_u32 vcc, -1, v48\ns_waitcnt lgkmcnt(0)\ns_cbranch_vccnz .Lf168\n.Lf168x:\ns_barrier\n"
    "ds_read2st64_b32 v[44:45], v22 offset1:1\nds_read2st64_b32 v[46:47], v22 offset0:2 offset1:3\ns_waitcnt lgkmcnt(0)\nglobal_store_dwordx4 v17, v[44:47], s[6:7] sc1\n"
    "s_endpgm\n.Lf168:\ns_sub_u32 s80, s80, 1\ns_cbranch_scc0 .Lf167\ns_branch .Lf168x\n"
    ".Lhcv155:\ns_setprio 0\ns_bitcmp1_b32 s13, 0\ns_cbranch_scc1 .Lk172\ns_cmp_lg_u32 s12, 0\ns_waitcnt lgkmcnt(0)\ns_barrier\nds_read_b64 v[192:193], v11 offset:0\n"
    "ds_read_b128 v[194:197], v10 offset:0\nds_read_b64 v[198:199], v12 offset:0\nds_read_b64 v[200:201], v11 offset:1280\nds_read_b128 v[202:205], v10 offset:1280\n"
    "ds_read_b64 v[206:207], v12 offset:1280\nds_read_b64 v[208:209], v11 offset:2560\nds_read_b128 v[210:213], v10 offset:2560\nds_read_b64 v[214:215], v12 offset:2560\n"
    "s_cbranch_scc1 .Lz175\nglobal_store_dwordx4 v37, v[252:255], s[4:5] offset:0 sc1\nglobal_store_dwordx4 v37, v[252:255], s[4:5] offset:1024 sc1\n"
    "global_store_dwordx4 v37, v[252:255], s[54:55] offset:0 sc1\nglobal_store_dwordx4 v37, v[252:255], s[54:55] offset:1024 sc1\ns_cmp_lg_u32 s13, 2\ns_cbranch_scc1 .Lz175\n"
    "global_store_dwordx4 v38, v[252:255], s[4:5] sc1\n.Lz175:\ns_waitcnt lgkmcnt(6)\nv_alignbit_b32 v216, v194, v193, 16\nv_alignbit_b32 v217, v195, v194, 16\n"
    "v_alignbit_b32 v218, v196, v195, 16\nv_alignbit_b32 v219, v197, v196, 16\nv_alignbit_b32 v220, v198, v197, 16\nv_pk_add_f16 v224, v193, v216\n"
    "v_pk_add_f16 v225, v194, v217\nv_pk_add_f16 v226, v195, v218\nv_pk_add_f16 v227, v196, v219\nv_pk_add_f16 v228, v197, v220\nv_pk_add_f16 v236, v216, v225\n"
    "v_pk_add_f16 v237, v217, v226\nv_pk_add_f16 v238, v218, v227\nv_pk_add_f16 v239, v219, v228\nds_write_b64 v13, v[236:237] offset:4040\nv_pk_add_f16 v232, v224, v195\n"
    "v_pk_add_f16 v233, v225, v196\nv_pk_add_f16 v234, v226, v197\nv_pk_add_f16 v235, v227, v198\nds_write_b64 v14, v[238:239] offset:4032\nv_pk_add_f16 v232, v232, v225\n"
    "v_pk_add_f16 v233, v233, v226\nv_pk_add_f16 v234, v234, v227\nv_pk_add_f16 v235, v235, v228\nds_write_b128 v13, v[232:235] offset:0\ns_mov_b64 exec, 1\n"
    "ds_write_b32 v25, v26 offset:48\ns_mov_b64 exec, -1\ns_waitcnt lgkmcnt(6)\nv_alignbit_b32 v216, v202, v201, 16\nv_alignbit_b32 v217, v203, v202, 16\n"
    "v_alignbit_b32 v218, v204, v203, 16\nv_alignbit_b32 v219, v205, v204, 16\nv_alignbit_b32 v220, v206, v205, 16\nv_pk_add_f16 v224, v201, v216\n"
    "v_pk_add_f16 v225, v202, v217\nv_pk_add_f16 v226, v203, v218\nv_pk_add_f16 v227, v204, v219\nv_pk_add_f16 v228, v205, v220\nv_pk_add_f16 v236, v216, v225\n"
    "v_pk_add_f16 v237, v217, v226\nv_pk_add_f16 v238, v218, v227\nv_pk_add_f16 v239, v219, v228\nds_write_b64 v13, v[236:237] offset:5192\nv_pk_add_f16 v232, v224, v203\n"
    "v_pk_add_f16 v233, v225, v204\nv_pk_add_f16 v234, v226, v205\nv_pk_add_f16 v235, v227, v206\nds_write_b64 v14, v[238:239] offset:5184\nv_pk_add_f16 v232, v232, v225\n"
    "v_pk_add_f16 v233, v233, v226\nv_pk_add_f16 v234, v234, v227\nv_pk_add_f16 v235, v235, v228\nds_write_b128 v13, v[232:235] offset:1152\ns_waitcnt lgkmcnt(6)\n"
    "v_alignbit_b32 v216, v210, v209, 16\nv_alignbit_b32 v217, v211, v210, 16\nv_alignbit_b32 v218, v212, v211, 16\nv_alignbit_b32 v219, v213, v212, 16\n"
    "v_alignbit_b32 v220, v214, v213, 16\nv_pk_add_f16 v224, v209, v216\nv_pk_add_f16 v225, v210, v217\nv_pk_add_f16 v226, v211, v218\ns_cbranch_execz .Lhop\n"
    "v_pk_add_f16 v227, v212, v219\nv_pk_add_f16 v228, v213, v220\nv_pk_add_f16 v236, v216, v225\nv_pk_add_f16 v237, v217, v226\nv_pk_add_f16 v238, v218, v227\n"
    "v_pk_add_f16 v239, v219, v228\nds_write_b64 v15, v[236:237] offset:6344\nv_pk_add_f16 v232, v224, v211\nv_pk_add_f16 v233, v225, v212\nv_pk_add_f16 v234, v226, v213\n"
    "v_pk_add_f16 v235, v227, v214\nds_write_b64 v35, v[238:239] offset:6336\nv_pk_add_f16 v232, v232, v225\nv_pk_add_f16 v233, v233, v226\nv_pk_add_f16 v234, v234, v227\n"
    "v_pk_add_f16 v235, v235, v228\nds_write_b128 v15, v[232:235] offset:2304\ns_setprio 0\nds_read_b64_tr_b16 v[40:41], v27 offset:1984\n"
    "ds_read_b64_tr_b16 v[42:43], v27 offset:2048\nds_read_b64_tr_b16 v[44:45], v27 offset:1728\nds_read_b64_tr_b16 v[46:47], v27 offset:1792\n"
    "ds_read_b64_tr_b16 v[48:49], v27 offset:1408\nds_read_b64_tr_b16 v[50:51], v27 offset:1472\nds_read_b64_tr_b16 v[52:53], v27 offset:1152\n"
    "ds_read_b64_tr_b16 v[54:55], v27 offset:1216\nds_read_b64_tr_b16 v[240:241], v27 offset:832\nds_read_b64_tr_b16 v[242:243], v27 offset:896\ns_waitcnt lgkmcnt(8)\n"
    "v_mfma_f32_32x32x16_f16 v[192:207], v[40:43], v[128:131], 0\nds_read_b64_tr_b16 v[40:41], v27 offset:576\nds_read_b64_tr_b16 v[42:43], v27 offset:640\n"
    "s_waitcnt lgkmcnt(8)\nv_mfma_f32_32x32x16_f16 v[192:207], v[44:47], v[132:135], v[192:207]\nds_read_b64_tr_b16 v[44:45], v27 offset:6016\n"
    "ds_read_b64_tr_b16 v[46:47], v27 offset:6080\nv_fmac_f32 v56, s16, v244\nv_fmac_f32 v57, s16, v245\ns_waitcnt lgkmcnt(8)\n"
    "v_mfma_f32_32x32x16_f16 v[192:207], v[48:51], v[136:139], v[192:207]\nds_read_b64_tr_b16 v[48:49], v27 offset:5760\nds_read_b64_tr_b16 v[50:51], v27 offset:5824\n"
    "v_fmac_f32 v58, s16, v246\nv_fmac_f32 v59, s16, v247\ns_waitcnt lgkmcnt(8)\nv_mfma_f32_32x32x16_f16 v[192:207], v[52:55], v[140:143], v[192:207]\n"
    "ds_read_b64_tr_b16 v[52:53], v27 offset:5440\nds_read_b64_tr_b16 v[54:55], v27 offset:5504\nv_fmac_f32 v60, s17, v248\nv_fmac_f32 v61, s17, v249\ns_waitcnt lgkmcnt(8)\n"
    "v_mfma_f32_32x32x16_f16 v[192:207], v[240:243], v[160:163], v[192:207]\nds_read_b64_tr_b16 v[240:241], v27 offset:5184\nds_read_b64_tr_b16 v[242:243], v27 offset:5248\n"
    "v_fmac_f32 v62, s17, v250\nv_fmac_f32 v63, s17, v251\ns_waitcnt lgkmcnt(8)\nv_mfma_f32_32x32x16_f16 v[192:207], v[40:43], v[164:167], v[192:207]\n"
    "ds_read_b64_tr_b16 v[40:41], v27 offset:4864\nds_read_b64_tr_b16 v[42:43], v27 offset:4928\ns_waitcnt lgkmcnt(8)\n"
    "v_mfma_f32_32x32x16_f16 v[192:207], v[44:47], v[144:147], v[192:207]\nds_read_b64_tr_b16 v[44:45], v27 offset:4608\nds_read_b64_tr_b16 v[46:47], v27 offset:4672\n"
    "s_waitcnt lgkmcnt(8)\nv_mfma_f32_32x32x16_f16 v[192:207], v[48:51], v[148:151], v[192:207]\nds_read_b32 v244, v30\nds_read_b64 v[246:247], v31\n"
    "ds_read_b32 v245, v30 offset:6480\nds_read_b64 v[248:249], v31 offset:6480\ns_waitcnt lgkmcnt(10)\nv_mfma_f32_32x32x16_f16 v[192:207], v[52:55], v[152:155], v[192:207]\n"
    "s_waitcnt lgkmcnt(8)\nv_mfma_f32_32x32x16_f16 v[192:207], v[240:243], v[156:159], v[192:207]\ns_waitcnt lgkmcnt(6)\n"
    "v_mfma_f32_32x32x16_f16 v[192:207], v[40:43], v[176:179], v[192:207]\ns_waitcnt lgkmcnt(0)\nv_fma_mix_f32 v56, v244, s28, v56 op_sel:[1,0,0] op_sel_hi:[1,0,0]\n"
    "v_fma_mix_f32 v57, v246, s28, v57 op_sel:[0,0,0] op_sel_hi:[1,0,0]\nv_fma_mix_f32 v58, v246, s28, v58 op_sel:[1,0,0] op_sel_hi:[1,0,0]\ns_waitcnt lgkmcnt(4)\n"
    "v_mfma_f32_32x32x16_f16 v[192:207], v[44:47], v[180:183], v[192:207]\nv_fma_mix_f32 v59, v247, s28, v59 op_sel:[0,0,0] op_sel_hi:[1,0,0]\n"
    "v_fma_mix_f32 v60, v245, s29, v60 op_sel:[1,0,0] op_sel_hi:[1,0,0]\nv_fma_mix_f32 v61, v248, s29, v61 op_sel:[0,0,0] op_sel_hi:[1,0,0]\n"
    "v_fma_mix_f32 v62, v248, s29, v62 op_sel:[1,0,0] op_sel_hi:[1,0,0]\nv_fma_mix_f32 v63, v249, s29, v63 op_sel:[0,0,0] op_sel_hi:[1,0,0]\ns_nop 6\n"
    "v_cvt_pk_bf16_f32 v192, v192, v193\nv_cvt_pk_bf16_f32 v193, v194, v195\nds_write_b64 v32, v[192:193] offset:1024\nv_cvt_pk_bf16_f32 v196, v196, v197\n"
    "v_cvt_pk_bf16_f32 v197, v198, v199\nds_write_b64 v32, v[196:197] offset:1536\nv_cvt_pk_bf16_f32 v200, v200, v201\nv_cvt_pk_bf16_f32 v201, v202, v203\n"
    "ds_write_b64 v32, v[200:201] offset:2048\nv_cvt_pk_bf16_f32 v204, v204, v205\nv_cvt_pk_bf16_f32 v205, v206, v207\nds_write_b64 v32, v[204:205] offset:2560\n"
    "s_branch .Lk173\n.Lk172:\ns_cmp_lg_u32 s12, 0\ns_waitcnt lgkmcnt(0)\ns_barrier\nds_read_b64 v[192:193], v11 offset:0\nds_read_b128 v[194:197], v10 offset:0\n"
    "ds_read_b64 v[198:199], v12 offset:0\nds_read_b64 v[200:201], v11 offset:1280\nds_read_b128 v[202:205], v10 offset:1280\nds_read_b64 v[206:207], v12 offset:1280\n"
    "s_cbranch_scc1 .Lz176\nglobal_store_dwordx4 v37, v[252:255], s[4:5] offset:0 sc1\nglobal_store_dwordx4 v37, v[252:255], s[4:5] offset:1024 sc1\n"
    "global_store_dwordx4 v37, v[252:255], s[54:55] offset:0 sc1\nglobal_store_dwordx4 v37, v[252:255], s[54:55] offset:1024 sc1\ns_cmp_lg_u32 s13, 2\ns_cbranch_scc1 .Lz176\n"
    "global_store_dwordx4 v38, v[252:255], s[4:5] sc1\n.Lz176:\ns_waitcnt lgkmcnt(3)\nv_alignbit_b32 v216, v193, v192, 16\nv_alignbit_b32 v217, v194, v193, 16\n"
    "v_alignbit_b32 v218, v195, v194, 16\nv_alignbit_b32 v219, v196, v195, 16\nv_alignbit_b32 v220, v197, v196, 16\nv_alignbit_b32 v221, v198, v197, 16\n"
    "v_alignbit_b32 v222, v199, v198, 16\nv_pk_add_f16 v224, v192, v216\nv_pk_add_f16 v225, v193, v217\nv_pk_add_f16 v226, v194, v218\nv_pk_add_f16 v227, v195, v219\n"
    "v_pk_add_f16 v228, v196, v220\nv_pk_add_f16 v229, v197, v221\nv_pk_add_f16 v230, v198, v222\nv_pk_add_f16 v223, v226, v227\nv_pk_add_f16 v231, v228, v229\n"
    "v_pk_add_f16 v232, v224, v196\nv_pk_add_f16 v233, v225, v197\nv_pk_add_f16 v234, v226, v198\nv_pk_add_f16 v235, v227, v199\nv_pk_add_f16 v208, v225, v223\n"
    "v_pk_add_f16 v209, v223, v228\nv_pk_add_f16 v210, v227, v231\nv_pk_add_f16 v211, v231, v230\ns_cbranch_execz .Lhop\nv_pk_add_f16 v236, v208, v216\n"
    "v_pk_add_f16 v237, v209, v217\nv_pk_add_f16 v238, v210, v218\nv_pk_add_f16 v239, v211, v219\nds_write_b128 v14, v[236:239] offset:0\nv_pk_add_f16 v232, v232, v208\n"
    "v_pk_add_f16 v233, v233, v209\nv_pk_add_f16 v234, v234, v210\nv_pk_add_f16 v235, v235, v211\nds_write_b128 v13, v[232:235] offset:0\ns_waitcnt lgkmcnt(2)\n"
    "v_alignbit_b32 v216, v201, v200, 16\nv_alignbit_b32 v217, v202, v201, 16\nv_alignbit_b32 v218, v203, v202, 16\nv_alignbit_b32 v219, v204, v203, 16\n"
    "v_alignbit_b32 v220, v205, v204, 16\nv_alignbit_b32 v221, v206, v205, 16\nv_alignbit_b32 v222, v207, v206, 16\nv_pk_add_f16 v224, v200, v216\n"
    "v_pk_add_f16 v225, v201, v217\nv_pk_add_f16 v226, v202, v218\nv_pk_add_f16 v227, v203, v219\nv_pk_add_f16 v228, v204, v220\nv_pk_add_f16 v229, v205, v221\n"
    "v_pk_add_f16 v230, v206, v222\nv_pk_add_f16 v223, v226, v227\nv_pk_add_f16 v231, v228, v229\nv_pk_add_f16 v232, v224, v204\nv_pk_add_f16 v233, v225, v205\n"
    "v_pk_add_f16 v234, v226, v206\nv_pk_add_f16 v235, v227, v207\nv_pk_add_f16 v208, v225, v223\nv_pk_add_f16 v209, v223, v228\nv_pk_add_f16 v210, v227, v231\n"
    "v_pk_add_f16 v211, v231, v230\nv_pk_add_f16 v236, v208, v216\nv_pk_add_f16 v237, v209, v217\nv_pk_add_f16 v238, v210, v218\nv_pk_add_f16 v239, v211, v219\n"
    "ds_write_b128 v14, v[236:239] offset:1152\nv_pk_add_f16 v232, v232, v208\nv_pk_add_f16 v233, v233, v209\nv_pk_add_f16 v234, v234, v210\nv_pk_add_f16 v235, v235, v211\n"
    "ds_write_b128 v15, v[232:235] offset:1152\nv_fmac_f32 v56, s16, v244\nv_fmac_f32 v57, s16, v245\nv_fmac_f32 v58, s16, v246\nv_fmac_f32 v59, s16, v247\n"
    "v_fmac_f32 v60, s17, v248\nv_fmac_f32 v61, s17, v249\nv_fmac_f32 v62, s17, v250\nv_fmac_f32 v63, s17, v251\nds_read_b32 v244, v30\nds_read_b64 v[246:247], v31\n"
    "ds_read_b32 v245, v30 offset:6480\nds_read_b64 v[248:249], v31 offset:6480\ns_setprio 0\nds_read_b32 v250, v24\nds_read_b64_tr_b16 v[40:41], v28 offset:512\n"
    "ds_read_b64_tr_b16 v[42:43], v28 offset:640\nds_read_b64_tr_b16 v[44:45], v28 offset:0\nds_read_b64_tr_b16 v[46:47], v28 offset:128\n"
    "ds_read_b64_tr_b16 v[48:49], v29 offset:512\nds_read_b64_tr_b16 v[50:51], v29 offset:640\nds_read_b64_tr_b16 v[52:53], v29 offset:0\n"
    "ds_read_b64_tr_b16 v[54:55], v29 offset:128\nds_read_b64_tr_b16 v[240:241], v28 offset:2816\nds_read_b64_tr_b16 v[242:243], v28 offset:2944\ns_waitcnt lgkmcnt(8)\n"
    "v_mfma_f32_32x32x16_f16 v[224:239], v[40:43], v[160:163], 0\nv_cmp_gt_i32 vcc, v26, v250\nds_read_b64_tr_b16 v[40:41], v28 offset:2304\n"
    "ds_read_b64_tr_b16 v[42:43], v28 offset:2432\ns_waitcnt lgkmcnt(8)\nv_mfma_f32_32x32x16_f16 v[224:239], v[44:47], v[164:167], v[224:239]\n"
    "ds_read_b64_tr_b16 v[44:45], v29 offset:2816\nds_read_b64_tr_b16 v[46:47], v29 offset:2944\ns_waitcnt lgkmcnt(8)\n"
    "v_mfma_f32_32x32x16_f16 v[208:223], v[48:51], v[168:171], 0\nds_read_b64_tr_b16 v[48:49], v29 offset:2304\nds_read_b64_tr_b16 v[50:51], v29 offset:2432\n"
    "s_waitcnt lgkmcnt(8)\nv_mfma_f32_32x32x16_f16 v[208:223], v[52:55], v[172:175], v[208:223]\ns_cbranch_vccnz .Lw177\n.Lr178:\nds_read_b64_tr_b16 v[52:53], v27 offset:832\n"
    "ds_read_b64_tr_b16 v[54:55], v27 offset:896\ns_waitcnt lgkmcnt(8)\nv_mfma_f32_32x32x16_f16 v[224:239], v[240:243], v[176:179], v[224:239]\n"
    "ds_read_b64_tr_b16 v[240:241], v27 offset:576\nds_read_b64_tr_b16 v[242:243], v27 offset:640\nv_fma_mix_f32 v56, v244, s28, v56 op_sel:[1,0,0] op_sel_hi:[1,0,0]\n"
    "v_fma_mix_f32 v57, v246, s28, v57 op_sel:[0,0,0] op_sel_hi:[1,0,0]\ns_waitcnt lgkmcnt(8)\nv_mfma_f32_32x32x16_f16 v[224:239], v[40:43], v[180:183], v[224:239]\n"
    "ds_read_b64_tr_b16 v[40:41], v27 offset:256\nds_read_b64_tr_b16 v[42:43], v27 offset:320\nv_fma_mix_f32 v58, v246, s28, v58 op_sel:[1,0,0] op_sel_hi:[1,0,0]\n"
    "v_fma_mix_f32 v59, v247, s28, v59 op_sel:[0,0,0] op_sel_hi:[1,0,0]\ns_waitcnt lgkmcnt(8)\nv_mfma_f32_32x32x16_f16 v[208:223], v[44:47], v[184:187], v[208:223]\n"
    "ds_read_b64_tr_b16 v[44:45], v27 offset:0\nds_read_b64_tr_b16 v[46:47], v27 offset:64\nv_fma_mix_f32 v60, v245, s29, v60 op_sel:[1,0,0] op_sel_hi:[1,0,0]\n"
    "v_fma_mix_f32 v61, v248, s29, v61 op_sel:[0,0,0] op_sel_hi:[1,0,0]\ns_waitcnt lgkmcnt(8)\nv_mfma_f32_32x32x16_f16 v[208:223], v[48:51], v[188:191], v[208:223]\n"
    "ds_read_b64_tr_b16 v[48:49], v27 offset:4864\nds_read_b64_tr_b16 v[50:51], v27 offset:4928\nv_fma_mix_f32 v62, v248, s29, v62 op_sel:[1,0,0] op_sel_hi:[1,0,0]\n"
    "v_fma_mix_f32 v63, v249, s29, v63 op_sel:[0,0,0] op_sel_hi:[1,0,0]\ns_waitcnt lgkmcnt(8)\nv_mfma_f32_32x32x16_f16 v[192:207], v[52:55], v[128:131], 0\n"
    "ds_read_b64_tr_b16 v[52:53], v27 offset:4608\nds_read_b64_tr_b16 v[54:55], v27 offset:4672\ns_waitcnt lgkmcnt(8)\n"
    "v_mfma_f32_32x32x16_f16 v[192:207], v[240:243], v[132:135], v[192:207]\nds_read_b64_tr_b16 v[240:241], v27 offset:4288\nds_read_b64_tr_b16 v[242:243], v27 offset:4352\n"
    "v_cvt_pk_bf16_f32 v224, v224, v225\nv_cvt_pk_bf16_f32 v225, v226, v227\nds_write_b64 v33, v[224:225] offset:0\ns_waitcnt lgkmcnt(9)\n"
    "v_mfma_f32_32x32x16_f16 v[192:207], v[40:43], v[136:139], v[192:207]\nds_read_b64_tr_b16 v[40:41], v27 offset:4032\nds_read_b64_tr_b16 v[42:43], v27 offset:4096\n"
    "v_cvt_pk_bf16_f32 v228, v228, v229\nv_cvt_pk_bf16_f32 v229, v230, v231\nds_write_b64 v36, v[228:229] offset:0\ns_waitcnt lgkmcnt(10)\n"
    "v_mfma_f32_32x32x16_f16 v[192:207], v[44:47], v[140:143], v[192:207]\nv_add_f32 v232, v232, v216\nv_add_f32 v233, v233, v217\nv_add_f32 v234, v234, v218\n"
    "v_add_f32 v235, v235, v219\ns_waitcnt lgkmcnt(8)\nv_mfma_f32_32x32x16_f16 v[192:207], v[48:51], v[144:147], v[192:207]\nv_add_f32 v236, v236, v220\n"
    "v_add_f32 v237, v237, v221\nv_add_f32 v238, v238, v222\nv_add_f32 v239, v239, v223\ns_waitcnt lgkmcnt(6)\n"
    "v_mfma_f32_32x32x16_f16 v[192:207], v[52:55], v[148:151], v[192:207]\nv_cvt_pk_bf16_f32 v232, v232, v233\ns_nop 0\ns_cbranch_execz .Lhop\n"
    "v_cvt_pk_bf16_f32 v233, v234, v235\nds_write_b64 v33, v[232:233] offset:1024\ns_waitcnt lgkmcnt(5)\n"
    "v_mfma_f32_32x32x16_f16 v[192:207], v[240:243], v[152:155], v[192:207]\nv_cvt_pk_bf16_f32 v236, v236, v237\nv_cvt_pk_bf16_f32 v237, v238, v239\n"
    "ds_write_b64 v36, v[236:237] offset:1024\ns_waitcnt lgkmcnt(3)\nv_mfma_f32_32x32x16_f16 v[192:207], v[40:43], v[156:159], v[192:207]\ns_nop 11\n"
    "v_cvt_pk_bf16_f32 v192, v192, v193\nv_cvt_pk_bf16_f32 v193, v194, v195\nds_write_b64 v32, v[192:193] offset:0\nv_cvt_pk_bf16_f32 v196, v196, v197\n"
    "v_cvt_pk_bf16_f32 v197, v198, v199\nds_write_b64 v32, v[196:197] offset:512\n.Lk173:\ns_setprio 1\ns_waitcnt vmcnt(0) lgkmcnt(0)\ns_barrier\nds_read_b64 v[208:209], v19\n"
    "ds_read_b64 v[210:211], v19 offset:10240\nds_read_b64 v[212:213], v20\nds_read_b64 v[214:215], v20 offset:10240\nds_read_b64 v[216:217], v34\n"
    "ds_read_b64 v[218:219], v34 offset:10240\ns_cmp_lg_u32 s12, 0\ns_waitcnt lgkmcnt(4)\nv_dot2c_f32_bf16 v56, s26, v208\nv_dot2c_f32_bf16 v57, s14, v208\n"
    "v_dot2c_f32_bf16 v58, s26, v209\nv_dot2c_f32_bf16 v59, s14, v209\nv_dot2c_f32_bf16 v60, s27, v210\nv_dot2c_f32_bf16 v61, s15, v210\nv_dot2c_f32_bf16 v62, s27, v211\n"
    "v_dot2c_f32_bf16 v63, s15, v211\ns_waitcnt lgkmcnt(2)\nv_dot2c_f32_bf16 v56, s26, v212\nv_dot2c_f32_bf16 v57, s14, v212\nv_dot2c_f32_bf16 v58, s26, v213\n"
    "v_dot2c_f32_bf16 v59, s14, v213\nv_dot2c_f32_bf16 v60, s27, v214\nv_dot2c_f32_bf16 v61, s15, v214\nv_dot2c_f32_bf16 v62, s27, v215\nv_dot2c_f32_bf16 v63, s15, v215\n"
    "s_waitcnt lgkmcnt(0)\nv_dot2c_f32_bf16 v56, s26, v216\nv_dot2c_f32_bf16 v57, s14, v216\nv_dot2c_f32_bf16 v58, s26, v217\nv_dot2c_f32_bf16 v59, s14, v217\n"
    "v_dot2c_f32_bf16 v60, s27, v218\nv_dot2c_f32_bf16 v61, s15, v218\nv_dot2c_f32_bf16 v62, s27, v219\nv_dot2c_f32_bf16 v63, s15, v219\nv_cvt_pk_bf16_f32 v52, v56, v60\n"
    "v_cvt_pk_bf16_f32 v53, v57, v61\nv_cvt_pk_bf16_f32 v54, v58, v62\nv_cvt_pk_bf16_f32 v55, v59, v63\nglobal_store_dwordx4 v21, v[52:55], s[4:5] sc1\ns_setprio 0\n"
    "s_cbranch_scc1 .Lfin164\n"
    "s_lshl_b32 s43, s19, 2\ns_max_u32 s42, s43, 1\ns_lshl_b32 s41, s42, 15\ns_lshl_b32 s40, s13, 13\ns_add_u32 s46, s41, s40\ns_add_u32 s46, s46, 0x5f9000\n"
    "v_bfe_u32 v223, v0, 4, 2\nv_and_b32 v222, 15, v0\nv_lshl_add_u32 v224, v223, 9, 64\nv_lshl_add_u32 v224, v222, 2, v224\ns_add_u32 s48, s4, s46\ns_addc_u32 s49, s5, 0\n"
    "s_setprio 3\ns_barrier\nglobal_load_dword v192, v224, s[48:49] offset:-4096 sc1\nglobal_load_dword v193, v224, s[48:49] offset:-3968 sc1\n"
    "global_load_dword v194, v224, s[48:49] offset:-3840 sc1\nglobal_load_dword v195, v224, s[48:49] offset:-3712 sc1\n"
    "global_load_dword v196, v224, s[48:49] offset:-2048 sc1\nglobal_load_dword v197, v224, s[48:49] offset:-1920 sc1\n"
    "global_load_dword v198, v224, s[48:49] offset:-1792 sc1\nglobal_load_dword v199, v224, s[48:49] offset:-1664 sc1\nglobal_load_dword v200, v224, s[48:49] offset:0 sc1\n"
    "global_load_dword v201, v224, s[48:49] offset:128 sc1\nglobal_load_dword v202, v224, s[48:49] offset:256 sc1\nglobal_load_dword v203, v224, s[48:49] offset:384 sc1\n"
    "global_load_dword v204, v224, s[48:49] offset:2048 sc1\nglobal_load_dword v205, v224, s[48:49] offset:2176 sc1\nglobal_load_dword v206, v224, s[48:49] offset:2304 sc1\n"
    "global_load_dword v207, v224, s[48:49] offset:2432 sc1\ns_setprio 0\ns_mov_b32 s12, 8388608\nv_add_u32 v9, 8388608, v9\nv_bfe_u32 v223, v0, 5, 1\n"
    "v_mul_u32_u24 v222, 0xc00, v223\nv_bfe_u32 v221, v0, 4, 1\nv_bfe_u32 v220, v0, 2, 2\nv_and_b32 v219, 3, v0\ns_mul_i32 s43, s13, 0x6000\ns_add_u32 s42, s43, 0x400\n"
    "v_add_u32 v229, s42, v222\nv_lshl_add_u32 v229, v221, 9, v229\nv_lshl_add_u32 v229, v220, 6, v229\nv_lshl_add_u32 v229, v219, 3, v229\n"
    "v_lshl_add_u32 v229, v221, 5, v229\nv_add_u32 v218, s42, v222\nv_lshl_add_u32 v218, v221, 9, v218\nv_lshl_add_u32 v218, v220, 6, v218\n"
    "v_lshl_add_u32 v218, v219, 3, v218\nv_lshl_add_u32 v218, v221, 5, v218\nv_xor_b32 v230, 32, v218\nv_bfe_u32 v217, v0, 4, 2\ns_lshr_b32 s41, s13, 1\n"
    "s_lshl_b32 s40, s41, 7\ns_and_b32 s39, s13, 1\ns_lshl_b32 s38, s39, 6\ns_add_u32 s37, s40, s38\ns_add_u32 s37, s37, 0x27c80\nv_lshl_add_u32 v231, v217, 4, s37\n"
    "ds_read_b128 v[40:43], v231\nds_read_b64_tr_b16 v[128:129], v229 offset:0\nds_read_b64_tr_b16 v[130:131], v229 offset:256\n"
    "ds_read_b64_tr_b16 v[132:133], v229 offset:6144\nds_read_b64_tr_b16 v[134:135], v229 offset:6400\nds_read_b64_tr_b16 v[136:137], v229 offset:12288\n"
    "ds_read_b64_tr_b16 v[138:139], v229 offset:12544\nds_read_b64_tr_b16 v[140:141], v229 offset:18432\nds_read_b64_tr_b16 v[142:143], v229 offset:18688\n"
    "ds_read_b64_tr_b16 v[144:145], v230 offset:0\nds_read_b64_tr_b16 v[146:147], v230 offset:256\nds_read_b64_tr_b16 v[148:149], v230 offset:6144\n"
    "ds_read_b64_tr_b16 v[150:151], v230 offset:6400\nds_read_b64_tr_b16 v[152:153], v230 offset:12288\nds_read_b64_tr_b16 v[154:155], v230 offset:12544\n"
    "ds_read_b64_tr_b16 v[156:157], v230 offset:18432\nds_read_b64_tr_b16 v[158:159], v230 offset:18688\nds_read_b64_tr_b16 v[160:161], v229 offset:1024\n"
    "ds_read_b64_tr_b16 v[162:163], v229 offset:1280\nds_read_b64_tr_b16 v[164:165], v229 offset:7168\nds_read_b64_tr_b16 v[166:167], v229 offset:7424\n"
    "ds_read_b64_tr_b16 v[168:169], v229 offset:13312\nds_read_b64_tr_b16 v[170:171], v229 offset:13568\nds_read_b64_tr_b16 v[172:173], v229 offset:19456\n"
    "ds_read_b64_tr_b16 v[174:175], v229 offset:19712\nds_read_b64_tr_b16 v[176:177], v230 offset:1024\nds_read_b64_tr_b16 v[178:179], v230 offset:1280\n"
    "ds_read_b64_tr_b16 v[180:181], v230 offset:7168\nds_read_b64_tr_b16 v[182:183], v230 offset:7424\nds_read_b64_tr_b16 v[184:185], v230 offset:13312\n"
    "ds_read_b64_tr_b16 v[186:187], v230 offset:13568\nds_read_b64_tr_b16 v[188:189], v230 offset:19456\nds_read_b64_tr_b16 v[190:191], v230 offset:19712\n"
    "s_lshl_b32 s43, s22, 17\ns_lshl_b32 s42, s13, 15\ns_add_u32 s44, s43, s42\ns_add_u32 s44, s44, 0xe01000\nv_and_b32 v215, 63, v0\nv_lshlrev_b32 v59, 4, v215\n"
    "s_cbranch_execz .Lhop\ns_add_u32 s68, s4, s44\ns_addc_u32 s69, s5, 0\ns_add_u32 s70, s68, 8192\ns_addc_u32 s71, s69, 0\ns_add_u32 s72, s68, 16384\n"
    "s_addc_u32 s73, s69, 0\ns_add_u32 s74, s68, 24576\ns_addc_u32 s75, s69, 0\nv_and_b32 v223, 63, v0\ns_lshl_b32 s43, s13, 12\ns_add_u32 s42, s43, 0x23000\n"
    "v_lshl_add_u32 v225, v223, 4, s42\ns_lshl_b32 s41, s13, 10\ns_add_u32 s40, s41, 0x23000\nv_lshl_add_u32 v226, v223, 4, s40\nv_and_b32 v222, 15, v0\n"
    "s_lshl_b32 s39, s13, 7\ns_add_u32 s38, s39, 0x27d80\nv_lshl_add_u32 v227, v222, 3, s38\ns_mov_b32 s37, 0x27d80\nv_lshl_add_u32 v228, v222, 3, s37\n"
    "v_mul_u32_u24 v221, 0xd0, v222\nv_bfe_u32 v220, v0, 4, 2\ns_lshr_b32 s36, s13, 1\ns_lshl_b32 s35, s36, 6\ns_and_b32 s34, s13, 1\ns_lshl_b32 s33, s34, 5\n"
    "s_add_u32 s32, s35, s33\ns_add_u32 s32, s32, 0x18040\nv_add_u32 v240, s32, v221\nv_lshl_add_u32 v240, v220, 3, v240\nv_and_b32 v219, 3, v0\ns_mov_b32 s31, 0x27f80\n"
    "v_lshl_add_u32 v241, v219, 2, s31\ns_mov_b32 s46, 0x3f803f80\ns_mov_b32 s47, 0x3b000000\ns_mov_b32 s80, 0x10000\nv_mov_b32 v242, 3\nv_mov_b64 v[232:233], 0\n"
    "v_mov_b64 v[234:235], 0\nv_mov_b64 v[236:237], 0\nv_mov_b64 v[238:239], 0\nv_mov_b64 v[244:245], 0\nv_mov_b64 v[246:247], 0\nv_mov_b64 v[248:249], 0\n"
    "v_mov_b64 v[250:251], 0\ns_waitcnt lgkmcnt(0)\nv_lshlrev_b32 v44, 16, v40\nv_lshlrev_b32 v45, 16, v41\nv_lshlrev_b32 v46, 16, v42\nv_lshlrev_b32 v47, 16, v43\n"
    "v_and_b32 v40, 0xffff0000, v40\nv_and_b32 v41, 0xffff0000, v41\nv_and_b32 v42, 0xffff0000, v42\nv_and_b32 v43, 0xffff0000, v43\nv_add_f32 v40, v40, v44\n"
    "v_add_f32 v41, v41, v45\nv_add_f32 v42, v42, v46\nv_add_f32 v43, v43, v47\n.Lq181:\ns_setprio 3\ns_waitcnt vmcnt(0)\n"
    "v_mfma_f32_16x16x32_bf16 v[208:211], v[128:131], v[192:195], 0\nv_mfma_f32_16x16x32_bf16 v[212:215], v[144:147], v[192:195], 0\n"
    "v_mfma_f32_16x16x32_bf16 v[216:219], v[160:163], v[192:195], 0\nv_mfma_f32_16x16x32_bf16 v[220:223], v[176:179], v[192:195], 0\n"
    "v_mfma_f32_16x16x32_bf16 v[208:211], v[132:135], v[196:199], v[208:211]\nv_mfma_f32_16x16x32_bf16 v[212:215], v[148:151], v[196:199], v[212:215]\n"
    "v_mfma_f32_16x16x32_bf16 v[216:219], v[164:167], v[196:199], v[216:219]\nv_mfma_f32_16x16x32_bf16 v[220:223], v[180:183], v[196:199], v[220:223]\n"
    "v_mfma_f32_16x16x32_bf16 v[208:211], v[136:139], v[200:203], v[208:211]\nv_mfma_f32_16x16x32_bf16 v[212:215], v[152:155], v[200:203], v[212:215]\n"
    "v_mfma_f32_16x16x32_bf16 v[216:219], v[168:171], v[200:203], v[216:219]\nv_mfma_f32_16x16x32_bf16 v[220:223], v[184:187], v[200:203], v[220:223]\n"
    "v_mfma_f32_16x16x32_bf16 v[208:211], v[140:143], v[204:207], v[208:211]\nv_mfma_f32_16x16x32_bf16 v[212:215], v[156:159], v[204:207], v[212:215]\n"
    "v_mfma_f32_16x16x32_bf16 v[216:219], v[172:175], v[204:207], v[216:219]\nv_mfma_f32_16x16x32_bf16 v[220:223], v[188:191], v[204:207], v[220:223]\n"
    "v_dot2_f32_bf16 v48, v192, s46, 0\nv_dot2_f32_bf16 v49, v192, v192, 0\nv_dot2_f32_bf16 v50, v193, s46, 0\nv_dot2_f32_bf16 v51, v193, v193, 0\n"
    "v_dot2_f32_bf16 v48, v194, s46, v48\nv_dot2_f32_bf16 v49, v194, v194, v49\nv_dot2_f32_bf16 v50, v195, s46, v50\nv_dot2_f32_bf16 v51, v195, v195, v51\n"
    "v_dot2_f32_bf16 v48, v196, s46, v48\nv_dot2_f32_bf16 v49, v196, v196, v49\nv_dot2_f32_bf16 v50, v197, s46, v50\nv_dot2_f32_bf16 v51, v197, v197, v51\n"
    "v_dot2_f32_bf16 v48, v198, s46, v48\nv_dot2_f32_bf16 v49, v198, v198, v49\nv_dot2_f32_bf16 v50, v199, s46, v50\nv_dot2_f32_bf16 v51, v199, v199, v51\n"
    "v_dot2_f32_bf16 v48, v200, s46, v48\nv_dot2_f32_bf16 v49, v200, v200, v49\nv_dot2_f32_bf16 v50, v201, s46, v50\nv_dot2_f32_bf16 v51, v201, v201, v51\n"
    "v_dot2_f32_bf16 v48, v202, s46, v48\nv_dot2_f32_bf16 v49, v202, v202, v49\nv_dot2_f32_bf16 v50, v203, s46, v50\nv_dot2_f32_bf16 v51, v203, v203, v51\n"
    "v_dot2_f32_bf16 v48, v204, s46, v48\nv_dot2_f32_bf16 v49, v204, v204, v49\nv_dot2_f32_bf16 v50, v205, s46, v50\nv_dot2_f32_bf16 v51, v205, v205, v51\n"
    "v_dot2_f32_bf16 v48, v206, s46, v48\nv_dot2_f32_bf16 v49, v206, v206, v49\nv_dot2_f32_bf16 v50, v207, s46, v50\nv_dot2_f32_bf16 v51, v207, v207, v51\ns_setprio 0\n"
    "ds_write_b128 v225, v[208:211] offset:0\nds_write_b128 v225, v[212:215] offset:1024\nds_write_b128 v225, v[216:219] offset:2048\n"
    "ds_write_b128 v225, v[220:223] offset:3072\nv_add_f32 v48, v48, v50\nv_add_f32 v49, v49, v51\nv_cmp_u_f32 vcc, v48, v49\ns_nop 0\nv_permlane16_swap_b32 v48, v49\n"
    "v_add_f32 v50, v48, v49\nv_add_f32 v49, v48, v49\ns_nop 1\nv_permlane32_swap_b32 v50, v49\nv_add_f32 v50, v50, v49\nv_mov_b32 v48, v50\nv_mov_b32 v49, v50\ns_nop 1\n"
    "v_permlane16_swap_b32 v48, v49\ns_cbranch_vccnz .Lq182\n.Lq182x:\ns_mov_b64 exec, 0xffff\nds_write_b64 v227, v[48:49]\ns_mov_b64 exec, 1\n"
    "ds_write_b32 v25, v242 offset:16\ns_mov_b64 exec, -1\ns_mov_b32 s86, 0x100000\n.Ls183:\nds_read_b32 v48, v241 offset:16\ns_waitcnt lgkmcnt(0)\nv_cmp_gt_i32 vcc, 3, v48\n"
    "s_sub_u32 s86, s86, 1\ns_cbranch_scc1 .Ls184\ns_cbranch_vccz .Ls184\ns_sleep 1\ns_branch .Ls183\n.Ls184:\nds_read_b64 v[48:49], v228 offset:0\n"
    "ds_read_b64 v[50:51], v228 offset:128\nds_read_b64 v[52:53], v228 offset:256\nds_read_b64 v[54:55], v228 offset:384\nds_read_b128 v[208:211], v226 offset:0\n"
    "ds_read_b128 v[212:215], v226 offset:4096\nds_read_b128 v[216:219], v226 offset:8192\nds_read_b128 v[220:223], v226 offset:12288\ns_waitcnt lgkmcnt(4)\n"
    "v_add_f32 v48, v48, v50\nv_add_f32 v49, v49, v51\nv_add_f32 v52, v52, v54\nv_add_f32 v53, v53, v55\nv_add_f32 v48, v48, v52\nv_add_f32 v49, v49, v53\n"
    "v_mul_f32 v48, s47, v48\nv_mul_f32 v50, v48, v48\nv_fma_f32 v49, v49, s47, -v50\nv_max_f32 v49, 0, v49\nv_cvt_pk_bf16_f32 v49, 0, v49\ns_nop 0\ns_cbranch_execz .Lhop\n"
    "v_add_f32 v49, 0x3727c5ac, v49\nv_cvt_pk_bf16_f32 v49, 0, v49\nv_sqrt_f32 v49, v49\nv_cvt_pk_bf16_f32 v48, 0, -v48\nv_cvt_pk_bf16_f32 v49, 0, v49\nv_rcp_f32 v49, v49\n"
    "s_waitcnt lgkmcnt(0)\nv_add_f32 v208, v208, v216\nv_add_f32 v209, v209, v217\nv_add_f32 v210, v210, v218\nv_add_f32 v211, v211, v219\nv_add_f32 v212, v212, v220\n"
    "v_add_f32 v213, v213, v221\nv_add_f32 v214, v214, v222\nv_add_f32 v215, v215, v223\nv_fmac_f32 v208, v48, v40\nv_fmac_f32 v209, v48, v41\nv_fmac_f32 v210, v48, v42\n"
    "v_fmac_f32 v211, v48, v43\nv_add_f32 v208, v208, v212\nv_add_f32 v209, v209, v213\nv_add_f32 v210, v210, v214\nv_add_f32 v211, v211, v215\nv_mul_f32 v208, v208, v49\n"
    "v_mul_f32 v209, v209, v49\nv_mul_f32 v210, v210, v49\nv_mul_f32 v211, v211, v49\nv_cvt_pk_bf16_f32 v44, v208, v209\nv_cvt_pk_bf16_f32 v45, v210, v211\n"
    "ds_write_b64 v240, v[44:45]\ns_setprio 0\nv_add_u32 v37, 10485760, v37\nglobal_store_dwordx4 v37, v[252:255], s[4:5] sc1\n"
    "global_store_dwordx4 v37, v[252:255], s[4:5] offset:1024 sc1\ns_or_b32 s44, s2, s13\ns_cbranch_scc1 .Lq181y\nv_mov_b32 v208, 16777216\nv_mov_b32 v209, s11\n"
    "s_mov_b64 exec, 1\nglobal_store_dword v208, v209, s[4:5] sc1\ns_mov_b64 exec, -1\n.Lq181y:\nv_bfe_u32 v215, v0, 5, 1\nv_and_b32 v214, 31, v0\ns_lshl_b32 s43, s21, 15\n"
    "s_lshl_b32 s42, s22, 13\ns_lshl_b32 s41, s13, 11\ns_add_u32 s40, s43, s42\ns_add_u32 s40, s40, s41\ns_add_u32 s40, s40, 0x600000\nv_lshl_add_u32 v47, v215, 8, s40\n"
    "v_lshl_add_u32 v47, v214, 2, v47\ns_mov_b32 s44, 0x00ff00ff\ns_mov_b32 s45, 0xff00ff00\ns_mov_b32 s46, s44\ns_mov_b32 s47, s44\ns_branch .Latt12\n"
    ".Lqg165:\ns_mov_b32 s12, 8388608\nv_add_u32 v9, 8388608, v9\ns_cmp_lg_u32 s3, 0\ns_cbranch_scc1 .Lz185\ns_waitcnt lgkmcnt(0)\ns_add_u32 s87, s87, s100\n"
    "s_add_u32 s87, s87, 32\n.Lj186:\ns_memrealtime s[100:101]\ns_waitcnt lgkmcnt(0)\ns_sub_u32 s101, s87, s100\ns_cmp_le_i32 s101, 0\ns_cbranch_scc0 .Lj186\n.Lz185:\n"
    "s_barrier\nglobal_load_dword v144, v39, s[52:53] offset:-2048 sc1\nglobal_load_dword v145, v39, s[52:53] offset:-1920 sc1\n"
    "global_load_dword v146, v39, s[52:53] offset:-1792 sc1\nglobal_load_dword v147, v39, s[52:53] offset:-1664 sc1\nglobal_load_dword v148, v39, s[52:53] offset:-1024 sc1\n"
    "global_load_dword v149, v39, s[52:53] offset:-896 sc1\nglobal_load_dword v150, v39, s[52:53] offset:-768 sc1\nglobal_load_dword v151, v39, s[52:53] offset:-640 sc1\n"
    "global_load_dword v152, v39, s[52:53] offset:0 sc1\nglobal_load_dword v153, v39, s[52:53] offset:128 sc1\nglobal_load_dword v154, v39, s[52:53] offset:256 sc1\n"
    "global_load_dword v155, v39, s[52:53] offset:384 sc1\nglobal_load_dword v156, v39, s[52:53] offset:1024 sc1\nglobal_load_dword v157, v39, s[52:53] offset:1152 sc1\n"
    "global_load_dword v158, v39, s[52:53] offset:1280 sc1\nglobal_load_dword v159, v39, s[52:53] offset:1408 sc1\nglobal_load_dword v160, v39, s[54:55] offset:-2048 sc1\n"
    "global_load_dword v161, v39, s[54:55] offset:-1920 sc1\nglobal_load_dword v162, v39, s[54:55] offset:-1792 sc1\nglobal_load_dword v163, v39, s[54:55] offset:-1664 sc1\n"
    "global_load_dword v164, v39, s[54:55] offset:-1024 sc1\nglobal_load_dword v165, v39, s[54:55] offset:-896 sc1\nglobal_load_dword v166, v39, s[54:55] offset:-768 sc1\n"
    "global_load_dword v167, v39, s[54:55] offset:-640 sc1\nglobal_load_dword v168, v39, s[54:55] offset:0 sc1\nglobal_load_dword v169, v39, s[54:55] offset:128 sc1\n"
    "global_load_dword v170, v39, s[54:55] offset:256 sc1\nglobal_load_dword v171, v39, s[54:55] offset:384 sc1\nglobal_load_dword v172, v39, s[54:55] offset:1024 sc1\n"
    "global_load_dword v173, v39, s[54:55] offset:1152 sc1\nglobal_load_dword v174, v39, s[54:55] offset:1280 sc1\nglobal_load_dword v175, v39, s[54:55] offset:1408 sc1\n"
    "global_load_dword v176, v39, s[56:57] offset:-2048 sc1\nglobal_load_dword v177, v39, s[56:57] offset:-1920 sc1\nglobal_load_dword v178, v39, s[56:57] offset:-1792 sc1\n"
    "global_load_dword v179, v39, s[56:57] offset:-1664 sc1\nglobal_load_dword v180, v39, s[56:57] offset:-1024 sc1\nglobal_load_dword v181, v39, s[56:57] offset:-896 sc1\n"
    "global_load_dword v182, v39, s[56:57] offset:-768 sc1\nglobal_load_dword v183, v39, s[56:57] offset:-640 sc1\nglobal_load_dword v184, v39, s[56:57] offset:0 sc1\n"
    "global_load_dword v185, v39, s[56:57] offset:128 sc1\nglobal_load_dword v186, v39, s[56:57] offset:256 sc1\nglobal_load_dword v187, v39, s[56:57] offset:384 sc1\n"
    "global_load_dword v188, v39, s[56:57] offset:1024 sc1\nglobal_load_dword v189, v39, s[56:57] offset:1152 sc1\nglobal_load_dword v190, v39, s[56:57] offset:1280 sc1\n"
    "global_load_dword v191, v39, s[56:57] offset:1408 sc1\n.Ld187:\ns_waitcnt vmcnt(44)\nv_dot2_f32_bf16 v112, v144, v241, 0\nv_dot2_f32_bf16 v113, v144, v144, 0\n"
    "v_dot2_f32_bf16 v114, v145, v241, 0\nv_dot2_f32_bf16 v115, v145, v145, 0\nv_dot2_f32_bf16 v116, v146, v241, 0\nv_dot2_f32_bf16 v117, v146, v146, 0\n"
    "v_dot2_f32_bf16 v118, v147, v241, 0\nv_dot2_f32_bf16 v119, v147, v147, 0\ns_waitcnt vmcnt(40)\nv_dot2_f32_bf16 v112, v148, v241, v112\n"
    "v_dot2_f32_bf16 v113, v148, v148, v113\nv_dot2_f32_bf16 v114, v149, v241, v114\nv_dot2_f32_bf16 v115, v149, v149, v115\nv_dot2_f32_bf16 v116, v150, v241, v116\n"
    "v_dot2_f32_bf16 v117, v150, v150, v117\nv_dot2_f32_bf16 v118, v151, v241, v118\nv_dot2_f32_bf16 v119, v151, v151, v119\ns_waitcnt vmcnt(36)\n"
    "v_dot2_f32_bf16 v112, v152, v241, v112\nv_dot2_f32_bf16 v113, v152, v152, v113\nv_dot2_f32_bf16 v114, v153, v241, v114\nv_dot2_f32_bf16 v115, v153, v153, v115\n"
    "v_dot2_f32_bf16 v116, v154, v241, v116\nv_dot2_f32_bf16 v117, v154, v154, v117\nv_dot2_f32_bf16 v118, v155, v241, v118\nv_dot2_f32_bf16 v119, v155, v155, v119\n"
    "s_waitcnt vmcnt(32)\nv_dot2_f32_bf16 v112, v156, v241, v112\nv_dot2_f32_bf16 v113, v156, v156, v113\nv_dot2_f32_bf16 v114, v157, v241, v114\n"
    "v_dot2_f32_bf16 v115, v157, v157, v115\nv_dot2_f32_bf16 v116, v158, v241, v116\nv_dot2_f32_bf16 v117, v158, v158, v117\nv_dot2_f32_bf16 v118, v159, v241, v118\n"
    "v_dot2_f32_bf16 v119, v159, v159, v119\ns_nop 0\ns_cbranch_execz .Lhop\nv_pk_add_f32 v[112:113], v[112:113], v[114:115]\ns_nop 1\n"
    "v_pk_add_f32 v[116:117], v[116:117], v[118:119]\nv_pk_add_f32 v[112:113], v[112:113], v[116:117]\nv_cmp_u_f32 vcc, v112, v113\ns_nop 0\nv_permlane32_swap_b32 v112, v113\n"
    "v_add_f32 v112, v112, v113\ns_cbranch_vccnz .Ld188\n.Ld189:\nglobal_store_dword v35, v112, s[4:5] sc1\nv_mfma_f32_32x32x16_bf16 v[64:79], v[208:211], v[144:147], 0\n"
    "ds_read_b64_tr_b16 v[208:209], v242 offset:7168\nds_read_b64_tr_b16 v[210:211], v242 offset:7424\nglobal_load_dword v192, v39, s[58:59] offset:-2048 sc1\n"
    "global_load_dword v193, v39, s[58:59] offset:-1920 sc1\nv_mfma_f32_32x32x16_bf16 v[64:79], v[212:215], v[148:151], v[64:79]\n"
    "ds_read_b64_tr_b16 v[212:213], v242 offset:10240\nds_read_b64_tr_b16 v[214:215], v242 offset:10496\nglobal_load_dword v194, v39, s[58:59] offset:-1792 sc1\n"
    "global_load_dword v195, v39, s[58:59] offset:-1664 sc1\nv_mfma_f32_32x32x16_bf16 v[64:79], v[216:219], v[152:155], v[64:79]\n"
    "ds_read_b64_tr_b16 v[216:217], v242 offset:2048\nds_read_b64_tr_b16 v[218:219], v242 offset:2304\nglobal_load_dword v196, v39, s[58:59] offset:-1024 sc1\n"
    "global_load_dword v197, v39, s[58:59] offset:-896 sc1\ns_waitcnt lgkmcnt(6)\nv_mfma_f32_32x32x16_bf16 v[64:79], v[220:223], v[156:159], v[64:79]\n"
    "ds_read_b64_tr_b16 v[220:221], v242 offset:5120\nds_read_b64_tr_b16 v[222:223], v242 offset:5376\nglobal_load_dword v198, v39, s[58:59] offset:-768 sc1\n"
    "global_load_dword v199, v39, s[58:59] offset:-640 sc1\nv_mfma_f32_32x32x16_bf16 v[80:95], v[224:227], v[144:147], 0\nds_read_b64_tr_b16 v[224:225], v242 offset:8192\n"
    "ds_read_b64_tr_b16 v[226:227], v242 offset:8448\nglobal_load_dword v200, v39, s[58:59] offset:0 sc1\nglobal_load_dword v201, v39, s[58:59] offset:128 sc1\n"
    "v_mad_u32_u24 v240, s45, 3, v36\nv_mfma_f32_32x32x16_bf16 v[80:95], v[228:231], v[148:151], v[80:95]\nds_read_b64_tr_b16 v[228:229], v242 offset:11264\n"
    "ds_read_b64_tr_b16 v[230:231], v242 offset:11520\nglobal_load_dword v202, v39, s[58:59] offset:256 sc1\nglobal_load_dword v203, v39, s[58:59] offset:384 sc1\n"
    "s_waitcnt lgkmcnt(6)\nv_mfma_f32_32x32x16_bf16 v[80:95], v[208:211], v[152:155], v[80:95]\nds_read_b64_tr_b16 v[208:209], v240 offset:0\n"
    "ds_read_b64_tr_b16 v[210:211], v240 offset:256\nglobal_load_dword v204, v39, s[58:59] offset:1024 sc1\nglobal_load_dword v205, v39, s[58:59] offset:1152 sc1\n"
    "v_mfma_f32_32x32x16_bf16 v[80:95], v[212:215], v[156:159], v[80:95]\nds_read_b64_tr_b16 v[212:213], v240 offset:3072\nds_read_b64_tr_b16 v[214:215], v240 offset:3328\n"
    "global_load_dword v206, v39, s[58:59] offset:1280 sc1\nglobal_load_dword v207, v39, s[58:59] offset:1408 sc1\n"
    "v_mfma_f32_32x32x16_bf16 v[96:111], v[216:219], v[144:147], 0\nds_read_b64_tr_b16 v[216:217], v240 offset:6144\nds_read_b64_tr_b16 v[218:219], v240 offset:6400\n"
    "s_waitcnt vmcnt(33)\nv_max3_u32 v244, v160, v161, v162\nv_max3_u32 v243, v163, v164, v165\ns_waitcnt lgkmcnt(6)\n"
    "v_mfma_f32_32x32x16_bf16 v[96:111], v[220:223], v[148:151], v[96:111]\nds_read_b64_tr_b16 v[220:221], v240 offset:9216\nds_read_b64_tr_b16 v[222:223], v240 offset:9472\n"
    "v_max3_u32 v244, v244, v166, v167\nv_max3_u32 v243, v243, v168, v169\nv_max3_u32 v244, v244, v170, v171\n"
    "v_mfma_f32_32x32x16_bf16 v[96:111], v[224:227], v[152:155], v[96:111]\nds_read_b64_tr_b16 v[224:225], v240 offset:1024\nds_read_b64_tr_b16 v[226:227], v240 offset:1280\n"
    "v_max3_u32 v243, v243, v172, v173\nv_max3_u32 v244, v244, v174, v175\nv_max_u32 v243, v243, v244\nv_cmp_eq_u32 vcc, -1, v243\n"
    "v_mfma_f32_32x32x16_bf16 v[96:111], v[228:231], v[156:159], v[96:111]\nds_read_b64_tr_b16 v[228:229], v240 offset:4096\nds_read_b64_tr_b16 v[230:231], v240 offset:4352\n"
    "s_cbranch_vccnz .Lx192\n.Lx193:\ns_waitcnt lgkmcnt(6)\nv_mfma_f32_32x32x16_bf16 v[64:79], v[208:211], v[160:163], v[64:79]\n"
    "ds_read_b64_tr_b16 v[208:209], v240 offset:7168\nds_read_b64_tr_b16 v[210:211], v240 offset:7424\nglobal_load_dword v144, v39, s[60:61] offset:-2048 sc1\n"
    "global_load_dword v145, v39, s[60:61] offset:-1920 sc1\nv_mfma_f32_32x32x16_bf16 v[64:79], v[212:215], v[164:167], v[64:79]\n"
    "ds_read_b64_tr_b16 v[212:213], v240 offset:10240\nds_read_b64_tr_b16 v[214:215], v240 offset:10496\nglobal_load_dword v146, v39, s[60:61] offset:-1792 sc1\n"
    "global_load_dword v147, v39, s[60:61] offset:-1664 sc1\nv_mfma_f32_32x32x16_bf16 v[64:79], v[216:219], v[168:171], v[64:79]\n"
    "ds_read_b64_tr_b16 v[216:217], v240 offset:2048\nds_read_b64_tr_b16 v[218:219], v240 offset:2304\nglobal_load_dword v148, v39, s[60:61] offset:-1024 sc1\n"
    "global_load_dword v149, v39, s[60:61] offset:-896 sc1\ns_waitcnt lgkmcnt(6)\nv_mfma_f32_32x32x16_bf16 v[64:79], v[220:223], v[172:175], v[64:79]\n"
    "ds_read_b64_tr_b16 v[220:221], v240 offset:5120\nds_read_b64_tr_b16 v[222:223], v240 offset:5376\nglobal_load_dword v150, v39, s[60:61] offset:-768 sc1\n"
    "global_load_dword v151, v39, s[60:61] offset:-640 sc1\nv_mfma_f32_32x32x16_bf16 v[80:95], v[224:227], v[160:163], v[80:95]\n"
    "ds_read_b64_tr_b16 v[224:225], v240 offset:8192\nds_read_b64_tr_b16 v[226:227], v240 offset:8448\nglobal_load_dword v152, v39, s[60:61] offset:0 sc1\n"
    "global_load_dword v153, v39, s[60:61] offset:128 sc1\nv_mad_u32_u24 v242, s46, 3, v36\nv_mfma_f32_32x32x16_bf16 v[80:95], v[228:231], v[164:167], v[80:95]\n"
    "ds_read_b64_tr_b16 v[228:229], v240 offset:11264\nds_read_b64_tr_b16 v[230:231], v240 offset:11520\nglobal_load_dword v154, v39, s[60:61] offset:256 sc1\n"
    "global_load_dword v155, v39, s[60:61] offset:384 sc1\ns_waitcnt lgkmcnt(6)\nv_mfma_f32_32x32x16_bf16 v[80:95], v[208:211], v[168:171], v[80:95]\n"
    "ds_read_b64_tr_b16 v[208:209], v242 offset:0\nds_read_b64_tr_b16 v[210:211], v242 offset:256\nglobal_load_dword v156, v39, s[60:61] offset:1024 sc1\n"
    "global_load_dword v157, v39, s[60:61] offset:1152 sc1\nv_mfma_f32_32x32x16_bf16 v[80:95], v[212:215], v[172:175], v[80:95]\n"
    "ds_read_b64_tr_b16 v[212:213], v242 offset:3072\nds_read_b64_tr_b16 v[214:215], v242 offset:3328\nglobal_load_dword v158, v39, s[60:61] offset:1280 sc1\n"
    "global_load_dword v159, v39, s[60:61] offset:1408 sc1\nv_mfma_f32_32x32x16_bf16 v[96:111], v[216:219], v[160:163], v[96:111]\n"
    "ds_read_b64_tr_b16 v[216:217], v242 offset:6144\nds_read_b64_tr_b16 v[218:219], v242 offset:6400\ns_waitcnt vmcnt(33)\nv_max3_u32 v244, v176, v177, v178\n"
    "v_max3_u32 v243, v179, v180, v181\ns_waitcnt lgkmcnt(6)\nv_mfma_f32_32x32x16_bf16 v[96:111], v[220:223], v[164:167], v[96:111]\n"
    "ds_read_b64_tr_b16 v[220:221], v242 offset:9216\nds_read_b64_tr_b16 v[222:223], v242 offset:9472\nv_max3_u32 v244, v244, v182, v183\nv_max3_u32 v243, v243, v184, v185\n"
    "v_max3_u32 v244, v244, v186, v187\nv_mfma_f32_32x32x16_bf16 v[96:111], v[224:227], v[168:171], v[96:111]\nds_read_b64_tr_b16 v[224:225], v242 offset:1024\ns_nop 0\n"
    "s_cbranch_execz .Lhop\nds_read_b64_tr_b16 v[226:227], v242 offset:1280\nv_max3_u32 v243, v243, v188, v189\nv_max3_u32 v244, v244, v190, v191\nv_max_u32 v243, v243, v244\n"
    "v_cmp_eq_u32 vcc, -1, v243\nv_mfma_f32_32x32x16_bf16 v[96:111], v[228:231], v[172:175], v[96:111]\nds_read_b64_tr_b16 v[228:229], v242 offset:4096\n"
    "ds_read_b64_tr_b16 v[230:231], v242 offset:4352\ns_cbranch_vccnz .Lx194\n.Lx195:\ns_waitcnt lgkmcnt(6)\n"
    "v_mfma_f32_32x32x16_bf16 v[64:79], v[208:211], v[176:179], v[64:79]\nds_read_b64_tr_b16 v[208:209], v242 offset:7168\nds_read_b64_tr_b16 v[210:211], v242 offset:7424\n"
    "global_load_dword v160, v39, s[62:63] offset:-2048 sc1\nglobal_load_dword v161, v39, s[62:63] offset:-1920 sc1\n"
    "v_mfma_f32_32x32x16_bf16 v[64:79], v[212:215], v[180:183], v[64:79]\nds_read_b64_tr_b16 v[212:213], v242 offset:10240\nds_read_b64_tr_b16 v[214:215], v242 offset:10496\n"
    "global_load_dword v162, v39, s[62:63] offset:-1792 sc1\nglobal_load_dword v163, v39, s[62:63] offset:-1664 sc1\n"
    "v_mfma_f32_32x32x16_bf16 v[64:79], v[216:219], v[184:187], v[64:79]\nds_read_b64_tr_b16 v[216:217], v242 offset:2048\nds_read_b64_tr_b16 v[218:219], v242 offset:2304\n"
    "global_load_dword v164, v39, s[62:63] offset:-1024 sc1\nglobal_load_dword v165, v39, s[62:63] offset:-896 sc1\ns_waitcnt lgkmcnt(6)\n"
    "v_mfma_f32_32x32x16_bf16 v[64:79], v[220:223], v[188:191], v[64:79]\nds_read_b64_tr_b16 v[220:221], v242 offset:5120\nds_read_b64_tr_b16 v[222:223], v242 offset:5376\n"
    "global_load_dword v166, v39, s[62:63] offset:-768 sc1\nglobal_load_dword v167, v39, s[62:63] offset:-640 sc1\n"
    "v_mfma_f32_32x32x16_bf16 v[80:95], v[224:227], v[176:179], v[80:95]\nds_read_b64_tr_b16 v[224:225], v242 offset:8192\nds_read_b64_tr_b16 v[226:227], v242 offset:8448\n"
    "global_load_dword v168, v39, s[62:63] offset:0 sc1\nglobal_load_dword v169, v39, s[62:63] offset:128 sc1\nv_mad_u32_u24 v240, s47, 3, v36\n"
    "v_mfma_f32_32x32x16_bf16 v[80:95], v[228:231], v[180:183], v[80:95]\nds_read_b64_tr_b16 v[228:229], v242 offset:11264\nds_read_b64_tr_b16 v[230:231], v242 offset:11520\n"
    "global_load_dword v170, v39, s[62:63] offset:256 sc1\nglobal_load_dword v171, v39, s[62:63] offset:384 sc1\ns_waitcnt lgkmcnt(6)\n"
    "v_mfma_f32_32x32x16_bf16 v[80:95], v[208:211], v[184:187], v[80:95]\nds_read_b64_tr_b16 v[208:209], v240 offset:0\nds_read_b64_tr_b16 v[210:211], v240 offset:256\n"
    "global_load_dword v172, v39, s[62:63] offset:1024 sc1\nglobal_load_dword v173, v39, s[62:63] offset:1152 sc1\n"
    "v_mfma_f32_32x32x16_bf16 v[80:95], v[212:215], v[188:191], v[80:95]\nds_read_b64_tr_b16 v[212:213], v240 offset:3072\nds_read_b64_tr_b16 v[214:215], v240 offset:3328\n"
    "global_load_dword v174, v39, s[62:63] offset:1280 sc1\nglobal_load_dword v175, v39, s[62:63] offset:1408 sc1\n"
    "v_mfma_f32_32x32x16_bf16 v[96:111], v[216:219], v[176:179], v[96:111]\nds_read_b64_tr_b16 v[216:217], v240 offset:6144\nds_read_b64_tr_b16 v[218:219], v240 offset:6400\n"
    "s_waitcnt vmcnt(32)\nv_max3_u32 v244, v192, v193, v194\nv_max3_u32 v243, v195, v196, v197\ns_waitcnt lgkmcnt(6)\n"
    "v_mfma_f32_32x32x16_bf16 v[96:111], v[220:223], v[180:183], v[96:111]\nds_read_b64_tr_b16 v[220:221], v240 offset:9216\nds_read_b64_tr_b16 v[222:223], v240 offset:9472\n"
    "v_max3_u32 v244, v244, v198, v199\nv_max3_u32 v243, v243, v200, v201\nv_max3_u32 v244, v244, v202, v203\n"
    "v_mfma_f32_32x32x16_bf16 v[96:111], v[224:227], v[184:187], v[96:111]\nds_read_b64_tr_b16 v[224:225], v240 offset:1024\nds_read_b64_tr_b16 v[226:227], v240 offset:1280\n"
    "v_max3_u32 v243, v243, v204, v205\nv_max3_u32 v244, v244, v206, v207\nv_max_u32 v243, v243, v244\nv_cmp_eq_u32 vcc, -1, v243\n"
    "v_mfma_f32_32x32x16_bf16 v[96:111], v[228:231], v[188:191], v[96:111]\nds_read_b64_tr_b16 v[228:229], v240 offset:4096\nds_read_b64_tr_b16 v[230:231], v240 offset:4352\n"
    "s_cbranch_vccnz .Lx196\n.Lx197:\ns_waitcnt lgkmcnt(6)\nv_mfma_f32_32x32x16_bf16 v[64:79], v[208:211], v[192:195], v[64:79]\n"
    "ds_read_b64_tr_b16 v[208:209], v240 offset:7168\nds_read_b64_tr_b16 v[210:211], v240 offset:7424\nglobal_load_dword v176, v39, s[64:65] offset:-2048 sc1\n"
    "global_load_dword v177, v39, s[64:65] offset:-1920 sc1\nv_mfma_f32_32x32x16_bf16 v[64:79], v[212:215], v[196:199], v[64:79]\n"
    "ds_read_b64_tr_b16 v[212:213], v240 offset:10240\nds_read_b64_tr_b16 v[214:215], v240 offset:10496\nglobal_load_dword v178, v39, s[64:65] offset:-1792 sc1\n"
    "global_load_dword v179, v39, s[64:65] offset:-1664 sc1\nv_mfma_f32_32x32x16_bf16 v[64:79], v[216:219], v[200:203], v[64:79]\n"
    "ds_read_b64_tr_b16 v[216:217], v240 offset:2048\nds_read_b64_tr_b16 v[218:219], v240 offset:2304\nglobal_load_dword v180, v39, s[64:65] offset:-1024 sc1\n"
    "global_load_dword v181, v39, s[64:65] offset:-896 sc1\ns_waitcnt lgkmcnt(6)\nv_mfma_f32_32x32x16_bf16 v[64:79], v[220:223], v[204:207], v[64:79]\n"
    "ds_read_b64_tr_b16 v[220:221], v240 offset:5120\nds_read_b64_tr_b16 v[222:223], v240 offset:5376\nglobal_load_dword v182, v39, s[64:65] offset:-768 sc1\n"
    "global_load_dword v183, v39, s[64:65] offset:-640 sc1\nv_mfma_f32_32x32x16_bf16 v[80:95], v[224:227], v[192:195], v[80:95]\n"
    "ds_read_b64_tr_b16 v[224:225], v240 offset:8192\nds_read_b64_tr_b16 v[226:227], v240 offset:8448\nglobal_load_dword v184, v39, s[64:65] offset:0 sc1\n"
    "global_load_dword v185, v39, s[64:65] offset:128 sc1\nv_mad_u32_u24 v242, s48, 3, v36\nv_mfma_f32_32x32x16_bf16 v[80:95], v[228:231], v[196:199], v[80:95]\n"
    "ds_read_b64_tr_b16 v[228:229], v240 offset:11264\nds_read_b64_tr_b16 v[230:231], v240 offset:11520\nglobal_load_dword v186, v39, s[64:65] offset:256 sc1\n"
    "global_load_dword v187, v39, s[64:65] offset:384 sc1\ns_waitcnt lgkmcnt(6)\nv_mfma_f32_32x32x16_bf16 v[80:95], v[208:211], v[200:203], v[80:95]\n"
    "ds_read_b64_tr_b16 v[208:209], v242 offset:0\nds_read_b64_tr_b16 v[210:211], v242 offset:256\nglobal_load_dword v188, v39, s[64:65] offset:1024 sc1\n"
    "global_load_dword v189, v39, s[64:65] offset:1152 sc1\nv_mfma_f32_32x32x16_bf16 v[80:95], v[212:215], v[204:207], v[80:95]\n"
    "ds_read_b64_tr_b16 v[212:213], v242 offset:3072\nds_read_b64_tr_b16 v[214:215], v242 offset:3328\nglobal_load_dword v190, v39, s[64:65] offset:1280 sc1\n"
    "global_load_dword v191, v39, s[64:65] offset:1408 sc1\nv_mfma_f32_32x32x16_bf16 v[96:111], v[216:219], v[192:195], v[96:111]\n"
    "ds_read_b64_tr_b16 v[216:217], v242 offset:6144\nds_read_b64_tr_b16 v[218:219], v242 offset:6400\ns_waitcnt vmcnt(32)\nv_max3_u32 v244, v144, v145, v146\n"
    "v_max3_u32 v243, v147, v148, v149\ns_waitcnt lgkmcnt(6)\nv_mfma_f32_32x32x16_bf16 v[96:111], v[220:223], v[196:199], v[96:111]\n"
    "ds_read_b64_tr_b16 v[220:221], v242 offset:9216\nds_read_b64_tr_b16 v[222:223], v242 offset:9472\nv_max3_u32 v244, v244, v150, v151\nv_max3_u32 v243, v243, v152, v153\n"
    "v_max3_u32 v244, v244, v154, v155\nv_mfma_f32_32x32x16_bf16 v[96:111], v[224:227], v[200:203], v[96:111]\ns_nop 0\ns_cbranch_execz .Lhop\n"
    "ds_read_b64_tr_b16 v[224:225], v242 offset:1024\nds_read_b64_tr_b16 v[226:227], v242 offset:1280\nv_max3_u32 v243, v243, v156, v157\nv_max3_u32 v244, v244, v158, v159\n"
    "v_max_u32 v243, v243, v244\nv_cmp_eq_u32 vcc, -1, v243\nv_mfma_f32_32x32x16_bf16 v[96:111], v[228:231], v[204:207], v[96:111]\n"
    "ds_read_b64_tr_b16 v[228:229], v242 offset:4096\nds_read_b64_tr_b16 v[230:231], v242 offset:4352\ns_cbranch_vccnz .Lx198\n.Lx199:\ns_waitcnt lgkmcnt(6)\n"
    "v_mfma_f32_32x32x16_bf16 v[64:79], v[208:211], v[144:147], v[64:79]\nds_read_b64_tr_b16 v[208:209], v242 offset:7168\nds_read_b64_tr_b16 v[210:211], v242 offset:7424\n"
    "global_load_dword v192, v39, s[66:67] offset:-2048 sc1\nglobal_load_dword v193, v39, s[66:67] offset:-1920 sc1\n"
    "v_mfma_f32_32x32x16_bf16 v[64:79], v[212:215], v[148:151], v[64:79]\nds_read_b64_tr_b16 v[212:213], v242 offset:10240\nds_read_b64_tr_b16 v[214:215], v242 offset:10496\n"
    "global_load_dword v194, v39, s[66:67] offset:-1792 sc1\nglobal_load_dword v195, v39, s[66:67] offset:-1664 sc1\n"
    "v_mfma_f32_32x32x16_bf16 v[64:79], v[216:219], v[152:155], v[64:79]\nds_read_b64_tr_b16 v[216:217], v242 offset:2048\nds_read_b64_tr_b16 v[218:219], v242 offset:2304\n"
    "global_load_dword v196, v39, s[66:67] offset:-1024 sc1\nglobal_load_dword v197, v39, s[66:67] offset:-896 sc1\ns_waitcnt lgkmcnt(6)\n"
    "v_mfma_f32_32x32x16_bf16 v[64:79], v[220:223], v[156:159], v[64:79]\nds_read_b64_tr_b16 v[220:221], v242 offset:5120\nds_read_b64_tr_b16 v[222:223], v242 offset:5376\n"
    "global_load_dword v198, v39, s[66:67] offset:-768 sc1\nglobal_load_dword v199, v39, s[66:67] offset:-640 sc1\n"
    "v_mfma_f32_32x32x16_bf16 v[80:95], v[224:227], v[144:147], v[80:95]\nds_read_b64_tr_b16 v[224:225], v242 offset:8192\nds_read_b64_tr_b16 v[226:227], v242 offset:8448\n"
    "global_load_dword v200, v39, s[66:67] offset:0 sc1\nglobal_load_dword v201, v39, s[66:67] offset:128 sc1\nv_mad_u32_u24 v240, s49, 3, v36\n"
    "v_mfma_f32_32x32x16_bf16 v[80:95], v[228:231], v[148:151], v[80:95]\nds_read_b64_tr_b16 v[228:229], v242 offset:11264\nds_read_b64_tr_b16 v[230:231], v242 offset:11520\n"
    "global_load_dword v202, v39, s[66:67] offset:256 sc1\nglobal_load_dword v203, v39, s[66:67] offset:384 sc1\ns_waitcnt lgkmcnt(6)\n"
    "v_mfma_f32_32x32x16_bf16 v[80:95], v[208:211], v[152:155], v[80:95]\nds_read_b64_tr_b16 v[208:209], v240 offset:0\nds_read_b64_tr_b16 v[210:211], v240 offset:256\n"
    "global_load_dword v204, v39, s[66:67] offset:1024 sc1\nglobal_load_dword v205, v39, s[66:67] offset:1152 sc1\n"
    "v_mfma_f32_32x32x16_bf16 v[80:95], v[212:215], v[156:159], v[80:95]\nds_read_b64_tr_b16 v[212:213], v240 offset:3072\nds_read_b64_tr_b16 v[214:215], v240 offset:3328\n"
    "global_load_dword v206, v39, s[66:67] offset:1280 sc1\nglobal_load_dword v207, v39, s[66:67] offset:1408 sc1\n"
    "v_mfma_f32_32x32x16_bf16 v[96:111], v[216:219], v[144:147], v[96:111]\nds_read_b64_tr_b16 v[216:217], v240 offset:6144\nds_read_b64_tr_b16 v[218:219], v240 offset:6400\n"
    "s_waitcnt vmcnt(32)\nv_max3_u32 v244, v160, v161, v162\nv_max3_u32 v243, v163, v164, v165\ns_waitcnt lgkmcnt(6)\n"
    "v_mfma_f32_32x32x16_bf16 v[96:111], v[220:223], v[148:151], v[96:111]\nds_read_b64_tr_b16 v[220:221], v240 offset:9216\nds_read_b64_tr_b16 v[222:223], v240 offset:9472\n"
    "v_max3_u32 v244, v244, v166, v167\nv_max3_u32 v243, v243, v168, v169\nv_max3_u32 v244, v244, v170, v171\n"
    "v_mfma_f32_32x32x16_bf16 v[96:111], v[224:227], v[152:155], v[96:111]\nds_read_b64_tr_b16 v[224:225], v240 offset:1024\nds_read_b64_tr_b16 v[226:227], v240 offset:1280\n"
    "v_max3_u32 v243, v243, v172, v173\nv_max3_u32 v244, v244, v174, v175\nv_max_u32 v243, v243, v244\nv_cmp_eq_u32 vcc, -1, v243\n"
    "v_mfma_f32_32x32x16_bf16 v[96:111], v[228:231], v[156:159], v[96:111]\nds_read_b64_tr_b16 v[228:229], v240 offset:4096\nds_read_b64_tr_b16 v[230:231], v240 offset:4352\n"
    "s_cbranch_vccnz .Lx200\n.Lx201:\nglobal_load_dword v112, v34, s[4:5] offset:0 sc1\nglobal_load_dword v113, v34, s[4:5] offset:256 sc1\ns_waitcnt lgkmcnt(6)\n"
    "v_mfma_f32_32x32x16_bf16 v[64:79], v[208:211], v[160:163], v[64:79]\nds_read_b64_tr_b16 v[208:209], v240 offset:7168\nds_read_b64_tr_b16 v[210:211], v240 offset:7424\n"
    "global_load_dword v114, v34, s[4:5] offset:512 sc1\nglobal_load_dword v115, v34, s[4:5] offset:768 sc1\n"
    "v_mfma_f32_32x32x16_bf16 v[64:79], v[212:215], v[164:167], v[64:79]\nds_read_b64_tr_b16 v[212:213], v240 offset:10240\nds_read_b64_tr_b16 v[214:215], v240 offset:10496\n"
    "global_load_dword v116, v34, s[4:5] offset:1024 sc1\nglobal_load_dword v117, v34, s[4:5] offset:1280 sc1\n"
    "v_mfma_f32_32x32x16_bf16 v[64:79], v[216:219], v[168:171], v[64:79]\nds_read_b64_tr_b16 v[216:217], v240 offset:2048\nds_read_b64_tr_b16 v[218:219], v240 offset:2304\n"
    "v_mad_u32_u24 v242, s50, 3, v36\nglobal_load_dword v118, v34, s[4:5] offset:1536 sc1\nglobal_load_dword v119, v34, s[4:5] offset:1792 sc1\ns_waitcnt lgkmcnt(6)\n"
    "v_mfma_f32_32x32x16_bf16 v[64:79], v[220:223], v[172:175], v[64:79]\nds_read_b64_tr_b16 v[220:221], v240 offset:5120\nds_read_b64_tr_b16 v[222:223], v240 offset:5376\n"
    "s_waitcnt vmcnt(24)\nv_max3_u32 v244, v176, v177, v178\nv_max3_u32 v243, v179, v180, v181\nv_mfma_f32_32x32x16_bf16 v[80:95], v[224:227], v[160:163], v[80:95]\n"
    "ds_read_b64_tr_b16 v[224:225], v240 offset:8192\nds_read_b64_tr_b16 v[226:227], v240 offset:8448\nv_max3_u32 v244, v244, v182, v183\nv_max3_u32 v243, v243, v184, v185\n"
    "v_max3_u32 v244, v244, v186, v187\nv_mfma_f32_32x32x16_bf16 v[80:95], v[228:231], v[164:167], v[80:95]\nds_read_b64_tr_b16 v[228:229], v240 offset:11264\n"
    "ds_read_b64_tr_b16 v[230:231], v240 offset:11520\nv_max3_u32 v243, v243, v188, v189\nv_max3_u32 v244, v244, v190, v191\nv_max_u32 v243, v243, v244\n"
    "v_cmp_eq_u32 vcc, -1, v243\ns_waitcnt lgkmcnt(6)\nv_mfma_f32_32x32x16_bf16 v[80:95], v[208:211], v[168:171], v[80:95]\nds_read_b64_tr_b16 v[208:209], v242 offset:0\n"
    "ds_read_b64_tr_b16 v[210:211], v242 offset:256\ns_cbranch_vccnz .Lx202\n.Lx203:\nv_mfma_f32_32x32x16_bf16 v[80:95], v[212:215], v[172:175], v[80:95]\n"
    "ds_read_b64_tr_b16 v[212:213], v242 offset:3072\nds_read_b64_tr_b16 v[214:215], v242 offset:3328\nv_mad_u32_u24 v240, s51, 3, v36\n"
    "v_mfma_f32_32x32x16_bf16 v[96:111], v[216:219], v[160:163], v[96:111]\nds_read_b64_tr_b16 v[216:217], v242 offset:6144\nds_read_b64_tr_b16 v[218:219], v242 offset:6400\n"
    "s_waitcnt vmcnt(8)\nv_max3_u32 v244, v192, v193, v194\nv_max3_u32 v243, v195, v196, v197\ns_waitcnt lgkmcnt(6)\n"
    "v_mfma_f32_32x32x16_bf16 v[96:111], v[220:223], v[164:167], v[96:111]\nds_read_b64_tr_b16 v[220:221], v242 offset:9216\nds_read_b64_tr_b16 v[222:223], v242 offset:9472\n"
    "v_max3_u32 v244, v244, v198, v199\ns_nop 0\ns_cbranch_execz .Lhop\nv_max3_u32 v243, v243, v200, v201\nv_max3_u32 v244, v244, v202, v203\n"
    "v_mfma_f32_32x32x16_bf16 v[96:111], v[224:227], v[168:171], v[96:111]\nds_read_b64_tr_b16 v[224:225], v240 offset:0\nds_read_b64_tr_b16 v[226:227], v240 offset:256\n"
    "v_max3_u32 v243, v243, v204, v205\nv_max3_u32 v244, v244, v206, v207\nv_max_u32 v243, v243, v244\nv_cmp_eq_u32 vcc, -1, v243\n"
    "v_mfma_f32_32x32x16_bf16 v[96:111], v[228:231], v[172:175], v[96:111]\nds_read_b64_tr_b16 v[228:229], v240 offset:3072\nds_read_b64_tr_b16 v[230:231], v240 offset:3328\n"
    "s_cbranch_vccnz .Lx204\n.Lx205:\ns_waitcnt lgkmcnt(6)\nv_mfma_f32_32x32x16_bf16 v[64:79], v[208:211], v[176:179], v[64:79]\n"
    "ds_read_b64_tr_b16 v[208:209], v240 offset:6144\nds_read_b64_tr_b16 v[210:211], v240 offset:6400\nv_mfma_f32_32x32x16_bf16 v[64:79], v[212:215], v[180:183], v[64:79]\n"
    "ds_read_b64_tr_b16 v[212:213], v240 offset:9216\nds_read_b64_tr_b16 v[214:215], v240 offset:9472\ns_waitcnt vmcnt(0)\nv_add_f32 v138, v112, v113\n"
    "v_add_f32 v139, v114, v115\nv_add_f32 v244, v116, v117\nv_mfma_f32_32x32x16_bf16 v[64:79], v[216:219], v[184:187], v[64:79]\n"
    "ds_read_b64_tr_b16 v[216:217], v242 offset:1024\nds_read_b64_tr_b16 v[218:219], v242 offset:1280\nv_add_f32 v243, v118, v119\nv_add_f32 v138, v138, v139\n"
    "v_add_f32 v244, v244, v243\nv_add_f32 v139, v138, v244\ns_waitcnt lgkmcnt(6)\nv_mfma_f32_32x32x16_bf16 v[64:79], v[220:223], v[188:191], v[64:79]\n"
    "ds_read_b64_tr_b16 v[220:221], v242 offset:4096\nds_read_b64_tr_b16 v[222:223], v242 offset:4352\nv_add_f32 v138, v138, v244\nv_cmp_u_f32 vcc, v138, v138\n"
    "v_mfma_f32_32x32x16_bf16 v[64:79], v[224:227], v[192:195], v[64:79]\nds_read_b64_tr_b16 v[224:225], v242 offset:7168\nds_read_b64_tr_b16 v[226:227], v242 offset:7424\n"
    "s_cbranch_vccnz .Lp190\n.Lp191:\nv_permlane32_swap_b32 v138, v139\nv_mul_f32 v138, s81, v138\nv_mul_f32 v112, v138, v138\n"
    "v_mfma_f32_32x32x16_bf16 v[64:79], v[228:231], v[196:199], v[64:79]\nds_read_b64_tr_b16 v[228:229], v242 offset:10240\nds_read_b64_tr_b16 v[230:231], v242 offset:10496\n"
    "s_memrealtime s[100:101]\ns_memtime s[78:79]\nv_cvt_pk_bf16_f32 v138, -v138, -v138\nv_fma_f32 v139, v139, s81, -v112\nv_cndmask_b32 v120, 0, v138, s[84:85]\n"
    "s_waitcnt lgkmcnt(6)\nv_mfma_f32_32x32x16_bf16 v[64:79], v[208:211], v[200:203], v[64:79]\nds_read_b64_tr_b16 v[208:209], v240 offset:1024\n"
    "ds_read_b64_tr_b16 v[210:211], v240 offset:1280\nv_max_f32 v139, 0, v139\nv_cvt_pk_bf16_f32 v139, 0, v139\nv_add_f32 v139, 0x3727c5ac, v139\n"
    "v_mfma_f32_32x32x16_bf16 v[64:79], v[212:215], v[204:207], v[64:79]\nds_read_b64_tr_b16 v[212:213], v240 offset:4096\nds_read_b64_tr_b16 v[214:215], v240 offset:4352\n"
    "v_mfma_f32_32x32x16_bf16 v[64:79], v[124:127], v[120:123], v[64:79]\nv_cvt_pk_bf16_f32 v139, 0, v139\nv_sqrt_f32 v139, v139\nv_mov_b64 v[232:233], 0\n"
    "v_mov_b64 v[234:235], 0\nv_cvt_pk_bf16_f32 v139, 0, v139\nv_rcp_f32 v136, v139\nv_mfma_f32_32x32x16_bf16 v[80:95], v[128:131], v[120:123], v[80:95]\n"
    "v_mov_b32 v137, v136\nv_mov_b64 v[236:237], 0\nv_mov_b64 v[238:239], 0\nv_mov_b64 v[244:245], 0\nv_mov_b64 v[246:247], 0\n"
    "v_mfma_f32_32x32x16_bf16 v[80:95], v[216:219], v[176:179], v[80:95]\nds_read_b64_tr_b16 v[216:217], v240 offset:7168\nds_read_b64_tr_b16 v[218:219], v240 offset:7424\n"
    "v_mul_f32 v64, v64, v136\nv_mul_f32 v65, v65, v136\nv_mul_f32 v66, v66, v136\ns_waitcnt lgkmcnt(6)\nv_mfma_f32_32x32x16_bf16 v[80:95], v[220:223], v[180:183], v[80:95]\n"
    "ds_read_b64_tr_b16 v[220:221], v240 offset:10240\nds_read_b64_tr_b16 v[222:223], v240 offset:10496\nv_mul_f32 v67, v67, v136\nv_cvt_pk_bf16_f32 v64, v64, v65\n"
    "v_cvt_pk_bf16_f32 v65, v66, v67\nv_mfma_f32_32x32x16_bf16 v[80:95], v[224:227], v[184:187], v[80:95]\nds_read_b64_tr_b16 v[224:225], v242 offset:2048\n"
    "ds_read_b64_tr_b16 v[226:227], v242 offset:2304\nds_write_b64 v37, v[64:65] offset:0\nv_mul_f32 v68, v68, v136\nv_mul_f32 v69, v69, v136\n"
    "v_mfma_f32_32x32x16_bf16 v[80:95], v[228:231], v[188:191], v[80:95]\nds_read_b64_tr_b16 v[228:229], v242 offset:5120\nds_read_b64_tr_b16 v[230:231], v242 offset:5376\n"
    "v_mul_f32 v70, v70, v136\nv_mul_f32 v71, v71, v136\nv_cvt_pk_bf16_f32 v68, v68, v69\ns_waitcnt lgkmcnt(7)\n"
    "v_mfma_f32_32x32x16_bf16 v[80:95], v[208:211], v[192:195], v[80:95]\nds_read_b64_tr_b16 v[208:209], v242 offset:8192\nds_read_b64_tr_b16 v[210:211], v242 offset:8448\n"
    "v_cvt_pk_bf16_f32 v69, v70, v71\nds_write_b64 v37, v[68:69] offset:16\nv_mul_f32 v72, v72, v136\nv_mfma_f32_32x32x16_bf16 v[80:95], v[212:215], v[196:199], v[80:95]\n"
    "ds_read_b64_tr_b16 v[212:213], v242 offset:11264\nds_read_b64_tr_b16 v[214:215], v242 offset:11520\nv_mul_f32 v73, v73, v136\nv_mul_f32 v74, v74, v136\n"
    "v_mul_f32 v75, v75, v136\nv_mfma_f32_32x32x16_bf16 v[80:95], v[216:219], v[200:203], v[80:95]\nds_read_b64_tr_b16 v[216:217], v240 offset:2048\n"
    "ds_read_b64_tr_b16 v[218:219], v240 offset:2304\nv_cvt_pk_bf16_f32 v72, v72, v73\nv_cvt_pk_bf16_f32 v73, v74, v75\nds_write_b64 v37, v[72:73] offset:32\n"
    "s_waitcnt lgkmcnt(8)\nv_mfma_f32_32x32x16_bf16 v[80:95], v[220:223], v[204:207], v[80:95]\nds_read_b64_tr_b16 v[220:221], v240 offset:5120\n"
    "ds_read_b64_tr_b16 v[222:223], v240 offset:5376\nv_mul_f32 v76, v76, v136\nv_mul_f32 v77, v77, v136\nv_mul_f32 v78, v78, v136\n"
    "v_mfma_f32_32x32x16_bf16 v[96:111], v[224:227], v[176:179], v[96:111]\nds_read_b64_tr_b16 v[224:225], v240 offset:8192\nds_read_b64_tr_b16 v[226:227], v240 offset:8448\n"
    "v_mul_f32 v79, v79, v136\nv_cvt_pk_bf16_f32 v76, v76, v77\nv_cvt_pk_bf16_f32 v77, v78, v79\nv_mfma_f32_32x32x16_bf16 v[96:111], v[132:135], v[120:123], v[96:111]\n"
    "ds_write_b64 v37, v[76:77] offset:48\ns_cmp_lg_u32 s13, 1\ns_cbranch_scc1 .Lz206\nv_mov_b32 v140, s100\ns_mov_b64 exec, 1\nglobal_store_dword v141, v140, s[4:5] sc1\n"
    "s_mov_b64 exec, -1\n.Lz206:\nv_mfma_f32_32x32x16_bf16 v[96:111], v[228:231], v[180:183], v[96:111]\nds_read_b64_tr_b16 v[228:229], v240 offset:11264\n"
    "ds_read_b64_tr_b16 v[230:231], v240 offset:11520\nv_mul_f32 v80, v80, v136\nv_mul_f32 v81, v81, v136\nv_mul_f32 v82, v82, v136\nv_mul_f32 v83, v83, v136\n"
    "v_cvt_pk_bf16_f32 v80, v80, v81\ns_waitcnt lgkmcnt(8)\nv_mfma_f32_32x32x16_bf16 v[96:111], v[208:211], v[184:187], v[96:111]\nv_cvt_pk_bf16_f32 v81, v82, v83\n"
    "ds_write_b64 v37, v[80:81] offset:64\nv_mul_f32 v84, v84, v136\nv_mul_f32 v85, v85, v136\nv_mul_f32 v86, v86, v136\n"
    "v_mfma_f32_32x32x16_bf16 v[96:111], v[212:215], v[188:191], v[96:111]\nv_mul_f32 v87, v87, v136\nv_cvt_pk_bf16_f32 v84, v84, v85\nv_cvt_pk_bf16_f32 v85, v86, v87\n"
    "s_cbranch_execz .Lhop\nds_write_b64 v37, v[84:85] offset:80\nv_mul_f32 v88, v88, v136\nv_mfma_f32_32x32x16_bf16 v[96:111], v[216:219], v[192:195], v[96:111]\n"
    "v_mul_f32 v89, v89, v136\nv_mul_f32 v90, v90, v136\nv_mul_f32 v91, v91, v136\nv_cvt_pk_bf16_f32 v88, v88, v89\nv_cvt_pk_bf16_f32 v89, v90, v91\ns_waitcnt lgkmcnt(2)\n"
    "v_mfma_f32_32x32x16_bf16 v[96:111], v[220:223], v[196:199], v[96:111]\nds_write_b64 v37, v[88:89] offset:96\nv_mul_f32 v92, v92, v136\nv_mul_f32 v93, v93, v136\n"
    "v_mul_f32 v94, v94, v136\nv_mul_f32 v95, v95, v136\nv_mfma_f32_32x32x16_bf16 v[96:111], v[224:227], v[200:203], v[96:111]\nv_cvt_pk_bf16_f32 v92, v92, v93\n"
    "v_cvt_pk_bf16_f32 v93, v94, v95\nds_write_b64 v37, v[92:93] offset:112\nv_mfma_f32_32x32x16_bf16 v[96:111], v[228:231], v[204:207], v[96:111]\nv_mov_b64 v[248:249], 0\n"
    "v_mov_b64 v[250:251], 0\ns_mov_b32 s44, 0x00ff00ff\ns_mov_b32 s45, 0xff00ff00\ns_mov_b32 s46, s44\ns_mov_b32 s47, s44\ns_nop 5\n"
    "v_pk_mul_f32 v[96:97], v[96:97], v[136:137]\nv_pk_mul_f32 v[98:99], v[98:99], v[136:137]\nv_cvt_pk_bf16_f32 v96, v96, v97\nv_cvt_pk_bf16_f32 v97, v98, v99\n"
    "ds_write_b64 v37, v[96:97] offset:128\nv_pk_mul_f32 v[100:101], v[100:101], v[136:137]\nv_pk_mul_f32 v[102:103], v[102:103], v[136:137]\n"
    "v_cvt_pk_bf16_f32 v100, v100, v101\nv_cvt_pk_bf16_f32 v101, v102, v103\nds_write_b64 v37, v[100:101] offset:144\nv_pk_mul_f32 v[104:105], v[104:105], v[136:137]\n"
    "v_pk_mul_f32 v[106:107], v[106:107], v[136:137]\nv_cvt_pk_bf16_f32 v104, v104, v105\nv_cvt_pk_bf16_f32 v105, v106, v107\nds_write_b64 v37, v[104:105] offset:160\n"
    "v_pk_mul_f32 v[108:109], v[108:109], v[136:137]\nv_pk_mul_f32 v[110:111], v[110:111], v[136:137]\nv_cvt_pk_bf16_f32 v108, v108, v109\nv_cvt_pk_bf16_f32 v109, v110, v111\n"
    "ds_write_b64 v37, v[108:109] offset:176\ns_branch .Latt12\n"
    ".Ll73x:\ns_add_u32 s59, s58, 0\nv_mov_b32 v70, s59\ns_mov_b64 exec, 1\ns_waitcnt vmcnt(8)\nds_max_i32 v56, v70\ns_mov_b64 exec, -1\ns_setprio 0\ns_waitcnt vmcnt(0)\n"
    "s_branch .Ll73y\n.La102:\ns_waitcnt vmcnt(0)\ns_branch .La104\n.Lw122:\ns_sub_u32 s80, s80, 1\ns_cbranch_scc1 .Lw122x\ns_mov_b64 exec, vcc\ns_add_u32 m0, s51, 0\n"
    "s_nop 2\nglobal_load_lds_dwordx4 v192, s[4:5] sc1\ns_mov_b64 exec, -1\ns_waitcnt vmcnt(0)\ns_branch .Lr121\n.Lw124:\ns_sub_u32 s80, s80, 1\ns_cbranch_scc1 .Lw124x\n"
    "s_mov_b64 exec, vcc\ns_add_u32 m0, s51, 4096\ns_nop 2\nglobal_load_lds_dwordx4 v193, s[4:5] sc1\ns_mov_b64 exec, -1\ns_waitcnt vmcnt(0)\ns_branch .Lr123\n.Lw126:\n"
    "s_sub_u32 s80, s80, 1\ns_cbranch_scc1 .Lw126x\ns_mov_b64 exec, vcc\ns_add_u32 m0, s51, 8192\ns_nop 2\nglobal_load_lds_dwordx4 v194, s[4:5] sc1\ns_add_u32 m0, s51, 12288\n"
    "s_nop 2\nglobal_load_lds_dwordx4 v195, s[4:5] sc1\ns_mov_b64 exec, -1\ns_waitcnt vmcnt(0)\ns_branch .Lr125\n.Lw128:\ns_sub_u32 s80, s80, 1\ns_cbranch_scc1 .Lw128x\n"
    "s_mov_b64 exec, vcc\ns_add_u32 m0, s51, 16384\ns_nop 2\nglobal_load_lds_dwordx4 v196, s[4:5] sc1\ns_add_u32 m0, s51, 20480\ns_nop 2\n"
    "global_load_lds_dwordx4 v197, s[4:5] sc1\ns_mov_b64 exec, -1\ns_waitcnt vmcnt(0)\ns_branch .Lr127\n.Lw130:\ns_sub_u32 s80, s80, 1\ns_cbranch_scc1 .Lw130x\n"
    "s_mov_b64 exec, vcc\ns_add_u32 m0, s51, 24576\ns_nop 2\nglobal_load_lds_dwordx4 v198, s[4:5] sc1\ns_add_u32 m0, s51, 28672\ns_nop 2\n"
    "global_load_lds_dwordx4 v199, s[4:5] sc1\ns_mov_b64 exec, -1\ns_waitcnt vmcnt(0)\ns_branch .Lr129\n.Lh139:\ns_cmp_lg_u32 s13, 0\ns_cbranch_scc1 .Lh142\n"
    "s_mov_b32 s80, 0x10000\nv_mov_b32 v241, 18\n.Lh141:\nglobal_load_dwordx4 v[192:195], v57, s[4:5] sc1\ns_waitcnt vmcnt(0)\nv_xor_b32 v192, s11, v192\n"
    "v_xor_b32 v193, s11, v193\nv_xor_b32 v194, s11, v194\nv_xor_b32 v195, s11, v195\nv_or3_b32 v242, v192, v193, v194\nv_or_b32 v242, v242, v195\nv_cmp_ne_u32 vcc, 0, v242\n"
    "s_cbranch_vccz .Lh143\ns_sub_u32 s80, s80, 1\ns_cbranch_scc1 .Lh143\ns_sleep 4\ns_branch .Lh141\n.Lh143:\ns_mov_b64 exec, 1\nds_write_b32 v56, v241 offset:4\n"
    "s_mov_b64 exec, -1\n.Lh142:\ns_mov_b32 s86, 0x100000\n.Ls144:\nds_read_b32 v242, v56 offset:4\ns_waitcnt lgkmcnt(0)\nv_cmp_gt_i32 vcc, 18, v242\ns_sub_u32 s86, s86, 1\n"
    "s_cbranch_scc1 .Ls145\ns_cbranch_vccz .Ls145\ns_sleep 1\ns_branch .Ls144\n.Ls145:\nglobal_load_dwordx4 v[64:67], v59, s[68:69] offset:-4096 sc1\n"
    "global_load_dwordx4 v[68:71], v59, s[68:69] offset:-3072 sc1\nglobal_load_dwordx4 v[72:75], v59, s[68:69] offset:-2048 sc1\n"
    "global_load_dwordx4 v[76:79], v59, s[68:69] offset:-1024 sc1\nglobal_load_dwordx4 v[80:83], v59, s[68:69] offset:0 sc1\n"
    "global_load_dwordx4 v[84:87], v59, s[68:69] offset:1024 sc1\nglobal_load_dwordx4 v[88:91], v59, s[68:69] offset:2048 sc1\n"
    "global_load_dwordx4 v[92:95], v59, s[68:69] offset:3072 sc1\nglobal_load_dwordx4 v[96:99], v59, s[70:71] offset:-4096 sc1\n"
    "global_load_dwordx4 v[100:103], v59, s[70:71] offset:-3072 sc1\nglobal_load_dwordx4 v[104:107], v59, s[70:71] offset:-2048 sc1\n"
    "global_load_dwordx4 v[108:111], v59, s[70:71] offset:-1024 sc1\nglobal_load_dwordx4 v[112:115], v59, s[70:71] offset:0 sc1\n"
    "global_load_dwordx4 v[116:119], v59, s[70:71] offset:1024 sc1\nglobal_load_dwordx4 v[120:123], v59, s[70:71] offset:2048 sc1\n"
    "global_load_dwordx4 v[124:127], v59, s[70:71] offset:3072 sc1\ns_branch .Lh140\n.Lw146:\ns_sub_u32 s80, s80, 1\ns_cbranch_scc1 .Lhwo113\n"
    "global_load_dword v240, v47, s[4:5] offset:0 sc1\nglobal_load_dword v241, v47, s[4:5] offset:128 sc1\nglobal_load_dword v242, v47, s[4:5] offset:512 sc1\n"
    "global_load_dword v243, v47, s[4:5] offset:640 sc1\nglobal_load_dword v244, v47, s[4:5] offset:1024 sc1\nglobal_load_dword v245, v47, s[4:5] offset:1152 sc1\n"
    "global_load_dword v246, v47, s[4:5] offset:1536 sc1\nglobal_load_dword v247, v47, s[4:5] offset:1664 sc1\ns_waitcnt vmcnt(0)\nv_max3_u32 v232, v240, v241, v242\n"
    "v_max3_u32 v233, v243, v244, v245\ns_nop 0\ns_cbranch_execz .Lhop\nv_max3_u32 v232, v232, v246, v247\nv_max_u32 v232, v232, v233\nv_cmp_eq_u32 vcc, -1, v232\n"
    "s_cbranch_vccnz .Lw146\ns_branch .Lhwo113\n.Llw157:\nv_max3_u32 v232, v208, v209, v210\nv_max3_u32 v232, v232, v211, v212\nv_max3_u32 v232, v232, v213, v214\n"
    "v_max3_u32 v232, v232, v215, v216\nv_max3_u32 v232, v232, v217, v218\nv_max3_u32 v232, v232, v219, v220\nv_max3_u32 v232, v232, v221, v222\nv_max_u32 v232, v232, v223\n"
    "v_cmp_eq_u32 vcc, -1, v232\ns_or_b64 vcc, vcc, s[50:51]\ns_cbranch_scc0 .Llx158\ns_sub_u32 s80, s80, 1\ns_cbranch_scc1 .Llx158\ns_mov_b64 exec, vcc\n"
    "global_load_dwordx4 v[240:243], v17, s[4:5] sc1\nglobal_load_dwordx4 v[208:211], v16, s[4:5] sc1\nglobal_load_dwordx4 v[212:215], v16, s[58:59] sc1\n"
    "global_load_dwordx4 v[216:219], v16, s[60:61] sc1\nglobal_load_dwordx4 v[220:223], v16, s[62:63] sc1\ns_mov_b64 exec, -1\ns_branch .Ln154\n.Lw177:\n"
    "s_mov_b32 s86, 0x100000\n.Ls179:\nds_read_b32 v250, v24 offset:0\ns_waitcnt lgkmcnt(0)\nv_cmp_gt_i32 vcc, v26, v250\ns_sub_u32 s86, s86, 1\ns_cbranch_scc1 .Ls180\n"
    "s_cbranch_vccz .Ls180\ns_sleep 1\ns_branch .Ls179\n.Ls180:\ns_branch .Lr178\n.Lq182:\nv_max3_u32 v52, v192, v193, v194\nv_max3_u32 v52, v52, v195, v196\n"
    "v_max3_u32 v52, v52, v197, v198\nv_max3_u32 v52, v52, v199, v200\nv_max3_u32 v52, v52, v201, v202\nv_max3_u32 v52, v52, v203, v204\nv_max3_u32 v52, v52, v205, v206\n"
    "v_max_u32 v52, v52, v207\nv_cmp_eq_u32 vcc, -1, v52\ns_cbranch_vccz .Lq182x\ns_sub_u32 s80, s80, 1\ns_cbranch_scc1 .Lq182x\ns_setprio 3\ns_waitcnt lgkmcnt(0)\n"
    "global_load_dword v192, v224, s[48:49] offset:-4096 sc1\nglobal_load_dword v193, v224, s[48:49] offset:-3968 sc1\n"
    "global_load_dword v194, v224, s[48:49] offset:-3840 sc1\nglobal_load_dword v195, v224, s[48:49] offset:-3712 sc1\n"
    "global_load_dword v196, v224, s[48:49] offset:-2048 sc1\nglobal_load_dword v197, v224, s[48:49] offset:-1920 sc1\n"
    "global_load_dword v198, v224, s[48:49] offset:-1792 sc1\nglobal_load_dword v199, v224, s[48:49] offset:-1664 sc1\nglobal_load_dword v200, v224, s[48:49] offset:0 sc1\n"
    "global_load_dword v201, v224, s[48:49] offset:128 sc1\nglobal_load_dword v202, v224, s[48:49] offset:256 sc1\nglobal_load_dword v203, v224, s[48:49] offset:384 sc1\n"
    "global_load_dword v204, v224, s[48:49] offset:2048 sc1\nglobal_load_dword v205, v224, s[48:49] offset:2176 sc1\nglobal_load_dword v206, v224, s[48:49] offset:2304 sc1\n"
    "global_load_dword v207, v224, s[48:49] offset:2432 sc1\ns_branch .Lq181\n.Ld188:\nv_max3_u32 v244, v144, v145, v146\nv_max3_u32 v243, v147, v148, v149\n"
    "v_max3_u32 v244, v244, v150, v151\nv_max3_u32 v243, v243, v152, v153\nv_max3_u32 v244, v244, v154, v155\nv_max3_u32 v243, v243, v156, v157\n"
    "v_max3_u32 v244, v244, v158, v159\nv_max_u32 v243, v243, v244\nv_cmp_eq_u32 vcc, -1, v243\ns_cbranch_vccz .Ld189\ns_sub_u32 s80, s80, 1\ns_cbranch_scc1 .Ld189\n"
    "s_sleep 2\nglobal_load_dword v144, v39, s[52:53] offset:-2048 sc1\nglobal_load_dword v145, v39, s[52:53] offset:-1920 sc1\n"
    "global_load_dword v146, v39, s[52:53] offset:-1792 sc1\nglobal_load_dword v147, v39, s[52:53] offset:-1664 sc1\nglobal_load_dword v148, v39, s[52:53] offset:-1024 sc1\n"
    "global_load_dword v149, v39, s[52:53] offset:-896 sc1\nglobal_load_dword v150, v39, s[52:53] offset:-768 sc1\nglobal_load_dword v151, v39, s[52:53] offset:-640 sc1\n"
    "global_load_dword v152, v39, s[52:53] offset:0 sc1\nglobal_load_dword v153, v39, s[52:53] offset:128 sc1\nglobal_load_dword v154, v39, s[52:53] offset:256 sc1\n"
    "global_load_dword v155, v39, s[52:53] offset:384 sc1\nglobal_load_dword v156, v39, s[52:53] offset:1024 sc1\nglobal_load_dword v157, v39, s[52:53] offset:1152 sc1\n"
    "global_load_dword v158, v39, s[52:53] offset:1280 sc1\nglobal_load_dword v159, v39, s[52:53] offset:1408 sc1\ns_waitcnt vmcnt(0)\ns_branch .Ld187\n.Lp190:\n"
    "v_max3_u32 v243, v112, v113, v114\nv_max3_u32 v243, v243, v115, v116\nv_max3_u32 v243, v243, v117, v118\nv_max_u32 v243, v243, v119\nv_cmp_eq_u32 vcc, -1, v243\n"
    "s_cbranch_vccz .Lp191\ns_sub_u32 s80, s80, 1\ns_cbranch_scc1 .Lp191\nglobal_load_dword v112, v34, s[4:5] offset:0 sc1\n"
    "global_load_dword v113, v34, s[4:5] offset:256 sc1\nglobal_load_dword v114, v34, s[4:5] offset:512 sc1\nglobal_load_dword v115, v34, s[4:5] offset:768 sc1\n"
    "global_load_dword v116, v34, s[4:5] offset:1024 sc1\nglobal_load_dword v117, v34, s[4:5] offset:1280 sc1\nglobal_load_dword v118, v34, s[4:5] offset:1536 sc1\n"
    "global_load_dword v119, v34, s[4:5] offset:1792 sc1\ns_waitcnt vmcnt(0)\nv_add_f32 v138, v112, v113\nv_add_f32 v139, v114, v115\nv_add_f32 v244, v116, v117\n"
    "v_add_f32 v243, v118, v119\nv_add_f32 v138, v138, v139\nv_add_f32 v244, v244, v243\nv_add_f32 v139, v138, v244\nv_add_f32 v138, v138, v244\nv_cmp_u_f32 vcc, v138, v138\n"
    "s_nop 0\ns_branch .Lp190\n.Lx192:\ns_sub_u32 s80, s80, 1\ns_cbranch_scc1 .Lx193\ns_sleep 2\nglobal_load_dword v160, v39, s[54:55] offset:-2048 sc1\n"
    "global_load_dword v161, v39, s[54:55] offset:-1920 sc1\nglobal_load_dword v162, v39, s[54:55] offset:-1792 sc1\nglobal_load_dword v163, v39, s[54:55] offset:-1664 sc1\n"
    "global_load_dword v164, v39, s[54:55] offset:-1024 sc1\nglobal_load_dword v165, v39, s[54:55] offset:-896 sc1\nglobal_load_dword v166, v39, s[54:55] offset:-768 sc1\n"
    "global_load_dword v167, v39, s[54:55] offset:-640 sc1\nglobal_load_dword v168, v39, s[54:55] offset:0 sc1\nglobal_load_dword v169, v39, s[54:55] offset:128 sc1\n"
    "global_load_dword v170, v39, s[54:55] offset:256 sc1\nglobal_load_dword v171, v39, s[54:55] offset:384 sc1\nglobal_load_dword v172, v39, s[54:55] offset:1024 sc1\n"
    "global_load_dword v173, v39, s[54:55] offset:1152 sc1\nglobal_load_dword v174, v39, s[54:55] offset:1280 sc1\nglobal_load_dword v175, v39, s[54:55] offset:1408 sc1\n"
    "s_waitcnt vmcnt(0)\nv_max3_u32 v244, v160, v161, v162\nv_max3_u32 v243, v163, v164, v165\nv_max3_u32 v244, v244, v166, v167\nv_max3_u32 v243, v243, v168, v169\n"
    "v_max3_u32 v244, v244, v170, v171\nv_max3_u32 v243, v243, v172, v173\nv_max3_u32 v244, v244, v174, v175\nv_max_u32 v243, v243, v244\nv_cmp_eq_u32 vcc, -1, v243\n"
    "s_cbranch_vccnz .Lx192\ns_branch .Lx193\n.Lx194:\ns_sub_u32 s80, s80, 1\ns_cbranch_scc1 .Lx195\ns_sleep 2\ns_cbranch_execz .Lhop\n"
    "global_load_dword v176, v39, s[56:57] offset:-2048 sc1\nglobal_load_dword v177, v39, s[56:57] offset:-1920 sc1\nglobal_load_dword v178, v39, s[56:57] offset:-1792 sc1\n"
    "global_load_dword v179, v39, s[56:57] offset:-1664 sc1\nglobal_load_dword v180, v39, s[56:57] offset:-1024 sc1\nglobal_load_dword v181, v39, s[56:57] offset:-896 sc1\n"
    "global_load_dword v182, v39, s[56:57] offset:-768 sc1\nglobal_load_dword v183, v39, s[56:57] offset:-640 sc1\nglobal_load_dword v184, v39, s[56:57] offset:0 sc1\n"
    "global_load_dword v185, v39, s[56:57] offset:128 sc1\nglobal_load_dword v186, v39, s[56:57] offset:256 sc1\nglobal_load_dword v187, v39, s[56:57] offset:384 sc1\n"
    "global_load_dword v188, v39, s[56:57] offset:1024 sc1\nglobal_load_dword v189, v39, s[56:57] offset:1152 sc1\nglobal_load_dword v190, v39, s[56:57] offset:1280 sc1\n"
    "global_load_dword v191, v39, s[56:57] offset:1408 sc1\ns_waitcnt vmcnt(0)\nv_max3_u32 v244, v176, v177, v178\nv_max3_u32 v243, v179, v180, v181\n"
    "v_max3_u32 v244, v244, v182, v183\nv_max3_u32 v243, v243, v184, v185\nv_max3_u32 v244, v244, v186, v187\nv_max3_u32 v243, v243, v188, v189\n"
    "v_max3_u32 v244, v244, v190, v191\nv_max_u32 v243, v243, v244\nv_cmp_eq_u32 vcc, -1, v243\ns_cbranch_vccnz .Lx194\ns_branch .Lx195\n.Lx196:\ns_sub_u32 s80, s80, 1\n"
    "s_cbranch_scc1 .Lx197\ns_sleep 2\nglobal_load_dword v192, v39, s[58:59] offset:-2048 sc1\nglobal_load_dword v193, v39, s[58:59] offset:-1920 sc1\n"
    "global_load_dword v194, v39, s[58:59] offset:-1792 sc1\nglobal_load_dword v195, v39, s[58:59] offset:-1664 sc1\nglobal_load_dword v196, v39, s[58:59] offset:-1024 sc1\n"
    "global_load_dword v197, v39, s[58:59] offset:-896 sc1\nglobal_load_dword v198, v39, s[58:59] offset:-768 sc1\nglobal_load_dword v199, v39, s[58:59] offset:-640 sc1\n"
    "global_load_dword v200, v39, s[58:59] offset:0 sc1\nglobal_load_dword v201, v39, s[58:59] offset:128 sc1\nglobal_load_dword v202, v39, s[58:59] offset:256 sc1\n"
    "global_load_dword v203, v39, s[58:59] offset:384 sc1\nglobal_load_dword v204, v39, s[58:59] offset:1024 sc1\nglobal_load_dword v205, v39, s[58:59] offset:1152 sc1\n"
    "global_load_dword v206, v39, s[58:59] offset:1280 sc1\nglobal_load_dword v207, v39, s[58:59] offset:1408 sc1\ns_waitcnt vmcnt(0)\nv_max3_u32 v244, v192, v193, v194\n"
    "v_max3_u32 v243, v195, v196, v197\nv_max3_u32 v244, v244, v198, v199\nv_max3_u32 v243, v243, v200, v201\nv_max3_u32 v244, v244, v202, v203\n"
    "v_max3_u32 v243, v243, v204, v205\nv_max3_u32 v244, v244, v206, v207\nv_max_u32 v243, v243, v244\nv_cmp_eq_u32 vcc, -1, v243\ns_cbranch_vccnz .Lx196\ns_branch .Lx197\n"
    ".Lx198:\ns_sub_u32 s80, s80, 1\ns_cbranch_scc1 .Lx199\ns_sleep 2\nglobal_load_dword v144, v39, s[60:61] offset:-2048 sc1\n"
    "global_load_dword v145, v39, s[60:61] offset:-1920 sc1\nglobal_load_dword v146, v39, s[60:61] offset:-1792 sc1\nglobal_load_dword v147, v39, s[60:61] offset:-1664 sc1\n"
    "global_load_dword v148, v39, s[60:61] offset:-1024 sc1\nglobal_load_dword v149, v39, s[60:61] offset:-896 sc1\nglobal_load_dword v150, v39, s[60:61] offset:-768 sc1\n"
    "global_load_dword v151, v39, s[60:61] offset:-640 sc1\nglobal_load_dword v152, v39, s[60:61] offset:0 sc1\nglobal_load_dword v153, v39, s[60:61] offset:128 sc1\n"
    "global_load_dword v154, v39, s[60:61] offset:256 sc1\nglobal_load_dword v155, v39, s[60:61] offset:384 sc1\nglobal_load_dword v156, v39, s[60:61] offset:1024 sc1\n"
    "global_load_dword v157, v39, s[60:61] offset:1152 sc1\nglobal_load_dword v158, v39, s[60:61] offset:1280 sc1\nglobal_load_dword v159, v39, s[60:61] offset:1408 sc1\n"
    "s_waitcnt vmcnt(0)\nv_max3_u32 v244, v144, v145, v146\nv_max3_u32 v243, v147, v148, v149\nv_max3_u32 v244, v244, v150, v151\nv_max3_u32 v243, v243, v152, v153\n"
    "v_max3_u32 v244, v244, v154, v155\nv_max3_u32 v243, v243, v156, v157\nv_max3_u32 v244, v244, v158, v159\nv_max_u32 v243, v243, v244\nv_cmp_eq_u32 vcc, -1, v243\n"
    "s_cbranch_vccnz .Lx198\ns_branch .Lx199\n.Lx200:\ns_sub_u32 s80, s80, 1\ns_cbranch_scc1 .Lx201\ns_sleep 2\nglobal_load_dword v160, v39, s[62:63] offset:-2048 sc1\n"
    "global_load_dword v161, v39, s[62:63] offset:-1920 sc1\nglobal_load_dword v162, v39, s[62:63] offset:-1792 sc1\nglobal_load_dword v163, v39, s[62:63] offset:-1664 sc1\n"
    "global_load_dword v164, v39, s[62:63] offset:-1024 sc1\nglobal_load_dword v165, v39, s[62:63] offset:-896 sc1\nglobal_load_dword v166, v39, s[62:63] offset:-768 sc1\n"
    "global_load_dword v167, v39, s[62:63] offset:-640 sc1\nglobal_load_dword v168, v39, s[62:63] offset:0 sc1\nglobal_load_dword v169, v39, s[62:63] offset:128 sc1\n"
    "global_load_dword v170, v39, s[62:63] offset:256 sc1\nglobal_load_dword v171, v39, s[62:63] offset:384 sc1\nglobal_load_dword v172, v39, s[62:63] offset:1024 sc1\n"
    "global_load_dword v173, v39, s[62:63] offset:1152 sc1\nglobal_load_dword v174, v39, s[62:63] offset:1280 sc1\nglobal_load_dword v175, v39, s[62:63] offset:1408 sc1\n"
    "s_waitcnt vmcnt(0)\nv_max3_u32 v244, v160, v161, v162\nv_max3_u32 v243, v163, v164, v165\nv_max3_u32 v244, v244, v166, v167\nv_max3_u32 v243, v243, v168, v169\n"
    "v_max3_u32 v244, v244, v170, v171\nv_max3_u32 v243, v243, v172, v173\nv_max3_u32 v244, v244, v174, v175\nv_max_u32 v243, v243, v244\nv_cmp_eq_u32 vcc, -1, v243\n"
    "s_cbranch_vccnz .Lx200\ns_branch .Lx201\n.Lx202:\ns_sub_u32 s80, s80, 1\ns_cbranch_scc1 .Lx203\ns_sleep 2\nglobal_load_dword v176, v39, s[64:65] offset:-2048 sc1\n"
    "global_load_dword v177, v39, s[64:65] offset:-1920 sc1\nglobal_load_dword v178, v39, s[64:65] offset:-1792 sc1\nglobal_load_dword v179, v39, s[64:65] offset:-1664 sc1\n"
    "global_load_dword v180, v39, s[64:65] offset:-1024 sc1\nglobal_load_dword v181, v39, s[64:65] offset:-896 sc1\nglobal_load_dword v182, v39, s[64:65] offset:-768 sc1\n"
    "global_load_dword v183, v39, s[64:65] offset:-640 sc1\nglobal_load_dword v184, v39, s[64:65] offset:0 sc1\nglobal_load_dword v185, v39, s[64:65] offset:128 sc1\n"
    "global_load_dword v186, v39, s[64:65] offset:256 sc1\nglobal_load_dword v187, v39, s[64:65] offset:384 sc1\nglobal_load_dword v188, v39, s[64:65] offset:1024 sc1\n"
    "global_load_dword v189, v39, s[64:65] offset:1152 sc1\nglobal_load_dword v190, v39, s[64:65] offset:1280 sc1\nglobal_load_dword v191, v39, s[64:65] offset:1408 sc1\n"
    "s_waitcnt vmcnt(0)\nv_max3_u32 v244, v176, v177, v178\nv_max3_u32 v243, v179, v180, v181\nv_max3_u32 v244, v244, v182, v183\ns_cbranch_execz .Lhop\n"
    "v_max3_u32 v243, v243, v184, v185\nv_max3_u32 v244, v244, v186, v187\nv_max3_u32 v243, v243, v188, v189\nv_max3_u32 v244, v244, v190, v191\nv_max_u32 v243, v243, v244\n"
    "v_cmp_eq_u32 vcc, -1, v243\ns_cbranch_vccnz .Lx202\ns_branch .Lx203\n.Lx204:\ns_sub_u32 s80, s80, 1\ns_cbranch_scc1 .Lx205\ns_sleep 2\n"
    "global_load_dword v192, v39, s[66:67] offset:-2048 sc1\nglobal_load_dword v193, v39, s[66:67] offset:-1920 sc1\nglobal_load_dword v194, v39, s[66:67] offset:-1792 sc1\n"
    "global_load_dword v195, v39, s[66:67] offset:-1664 sc1\nglobal_load_dword v196, v39, s[66:67] offset:-1024 sc1\nglobal_load_dword v197, v39, s[66:67] offset:-896 sc1\n"
    "global_load_dword v198, v39, s[66:67] offset:-768 sc1\nglobal_load_dword v199, v39, s[66:67] offset:-640 sc1\nglobal_load_dword v200, v39, s[66:67] offset:0 sc1\n"
    "global_load_dword v201, v39, s[66:67] offset:128 sc1\nglobal_load_dword v202, v39, s[66:67] offset:256 sc1\nglobal_load_dword v203, v39, s[66:67] offset:384 sc1\n"
    "global_load_dword v204, v39, s[66:67] offset:1024 sc1\nglobal_load_dword v205, v39, s[66:67] offset:1152 sc1\nglobal_load_dword v206, v39, s[66:67] offset:1280 sc1\n"
    "global_load_dword v207, v39, s[66:67] offset:1408 sc1\ns_waitcnt vmcnt(0)\nv_max3_u32 v244, v192, v193, v194\nv_max3_u32 v243, v195, v196, v197\n"
    "v_max3_u32 v244, v244, v198, v199\nv_max3_u32 v243, v243, v200, v201\nv_max3_u32 v244, v244, v202, v203\nv_max3_u32 v243, v243, v204, v205\n"
    "v_max3_u32 v244, v244, v206, v207\nv_max_u32 v243, v243, v244\nv_cmp_eq_u32 vcc, -1, v243\ns_cbranch_vccnz .Lx204\ns_branch .Lx205\n.fill 189, 4, 0xBF800000\n"
    "s_cbranch_execz .Lhop\n.fill 255, 4, 0xBF800000\ns_cbranch_execz .Lhop\n.Lk_all_end:\n"
    :: "{s[0:1]}"(__builtin_amdgcn_kernarg_segment_ptr()), "{s2}"(blockIdx.x), "{v0}"(threadIdx.x), "{s3}"((uint32_t)(uintptr_t)(__attribute__((address_space(3))) char*)smem)
    : "v255", "s101", "vcc", "scc", "memory");
}

extern "C" void kernel_launch(void* const* d_in, const int* in_sizes, int n_in,
                              void* d_out, int out_size, void* d_ws, size_t ws_size,
                              hipStream_t stream) {
  Args a;
  a.x = (const u16*)d_in[0]; a.w = (const u16*)d_in[1]; a.wo = (const u16*)d_in[2];
  a.A_re = (const u16*)d_in[4]; a.A_im = (const u16*)d_in[5]; a.B_re = (const u16*)d_in[6]; a.B_im = (const u16*)d_in[7];
  a.C_re = (const u16*)d_in[8]; a.C_im = (const u16*)d_in[9]; a.log_dt = (const u16*)d_in[10]; a.Dskip = (const u16*)d_in[11];
  a.out = (u16*)d_out; a.ws = (char*)d_ws;
  k_all<<<256, 512, 0, stream>>>(a);
}
```

```cpp
#include <hip/hip_runtime.h>
#include <stdint.h>

typedef unsigned short u16;
constexpr int rnd_l(int v) { int e = 0; while ((v >> e) >= 256) ++e; if (!e) return v; const int h = 1 << (e - 1), q = v >> e, r = v & ((1 << e) - 1); return (q + ((r > h || (r == h && (q & 1))) ? 1 : 0)) << e; }
constexpr int slot_l(int v) { v = rnd_l(v); int e = 0; while ((v >> e) >= 256) ++e; return e * 128 + (v >> e); }
struct Tabs { u16 img[1032]; u16 prd[1024]; };
constexpr Tabs mk_tabs() {
  Tabs t{};
  for (int i = 0; i < 1032; ++i) {
    const int f = 1023 - i, o = f >> 3, a = (o - 74) / 12, m = f - 8 * (74 + 12 * a);
    int v = -1;
    if (f >= 32 && f - 32 <= 525) v = f - 32;
    if (o >= 74 && o < 122 && m < 64) v = a >= 2 ? 16 * (65 + m) - 8 * (a - 2) : (m < 62 ? 8 * (66 + m) + 4 * a : (m == 62 ? 1024 : -1));
    t.img[i] = (u16)(2 * (v < 0 ? 700 : slot_l(v)));
  }
  for (int w = 0; w < 1024; ++w) {
    const int tile = (w >> 9) * 2 + ((w >> 2) & 1), lane = (w >> 3) & 63;
    const int r = (lane >> 4) * 4 + (w & 3), C = (tile & 1) * 16 + (lane & 15);
    const int lr = 8 * C + (r < 4 ? r : (r == 4 ? 258 : 256 * (r - 4))) + 4 * (tile >> 1);
    const bool ok = r <= 12 && lr <= 2048 && rnd_l(lr) == lr;
    t.prd[w] = (u16)(2 * (ok ? slot_l(lr) : 704 + (lane & 63)));
  }
  return t;
}
extern "C" { extern __device__ __attribute__((aligned(16), used)) const Tabs g_tabs = mk_tabs(); }

struct Args { const u16 *x, *w, *wo, *A_re, *A_im, *B_re, *B_im, *C_re, *C_im, *log_dt, *Dskip; u16* out; char* ws; };

__global__ __launch_bounds__(512, 2) __attribute__((aligned(4096))) void k_all(const Args ar) {
  __shared__ __attribute__((aligned(128))) char smem[163840];
  asm volatile(
    ".Lk_all_beg:\ns_load_dwordx16 s[64:79], s[0:1], 0\ns_load_dwordx8 s[80:87], s[0:1], 0x40\ns_load_dwordx2 s[4:5], s[0:1], 0x60\nv_readfirstlane_b32 s3, v0\n"
    "s_lshr_b32 s25, s2, 3\ns_lshr_b32 s3, s3, 6\ns_cmp_lg_u32 s3, 4\ns_cbranch_scc1 .Loth1\ns_lshr_b32 s51, s2, 5\ns_and_b32 s60, s25, 3\nv_bfe_u32 v64, v0, 2, 4\n"
    "s_lshl1_add_u32 s50, s60, s51\nv_lshrrev_b32 v65, 4, v0\nv_mul_u32_u24 v64, 0xc00, v64\ns_and_b32 s50, s50, 7\nv_and_b32 v66, 2, v65\ns_and_b32 s61, s2, 1\n"
    "v_xor_b32 v66, v66, v0\ns_lshl3_add_u32 s51, s61, s51\nv_and_b32 v66, 3, v66\ns_lshl_b32 s51, s51, 6\nv_lshl_add_u32 v64, v66, 4, v64\ns_mul_i32 m0, s50, 12288\n"
    "v_add_u32 v64, s51, v64\ns_lshl_b32 s59, m0, 4\ns_mov_b32 s58, 3\ns_waitcnt lgkmcnt(0)\ns_sleep 10\ns_add_u32 s52, s66, s59\n.Le5:\ns_addc_u32 s53, s67, 0\n"
    "global_load_lds_dwordx4 v64, s[52:53] offset:0\nglobal_load_lds_dwordx4 v64, s[52:53] offset:1024\nglobal_load_lds_dwordx4 v64, s[52:53] offset:2048\n"
    "s_add_u32 m0, m0, 3072\ns_add_u32 s59, s59, 49152\ns_sub_u32 s58, s58, 1\ns_cbranch_scc1 .Le5x\ns_add_u32 s52, s66, s59\ns_branch .Le5\n.Le5x:\n"
    "s_bfe_u32 s61, s2, 0x20001\nv_and_b32 v65, 3, v65\ns_lshl2_add_u32 s61, s61, s60\nv_xor_b32 v65, v65, v0\ns_lshl_b32 s61, s61, 17\nv_and_b32 v65, 7, v65\n"
    "s_add_u32 s48, s64, s61\nv_bfe_u32 v66, v0, 3, 3\ns_addc_u32 s49, s65, 0\nv_lshlrev_b32 v66, 10, v66\ns_lshl_b32 s59, s50, 7\nv_lshl_add_u32 v65, v65, 4, v66\n"
    "s_add_u32 s54, s48, s59\nv_xor_b32 v66, 64, v65\ns_addc_u32 s55, s49, 0\nv_add_u32 v67, 14336, v65\ns_mov_b32 m0, 98304\nv_add_u32 v68, 21504, v66\n"
    "v_add_u32 v66, 7168, v66\n.Le6:\nglobal_load_lds_dwordx4 v65, s[54:55] offset:0\nglobal_load_lds_dwordx4 v66, s[54:55] offset:1024\n"
    "global_load_lds_dwordx4 v67, s[54:55] offset:2048\nglobal_load_lds_dwordx4 v68, s[54:55] offset:3072\ns_add_u32 s54, s54, 32768\ns_addc_u32 s55, s55, 0\n"
    "s_add_u32 m0, m0, 12288\ns_cmp_lt_u32 m0, 147456\ns_cbranch_scc1 .Le6\ns_barrier\ns_branch .Lcom2\n.Lhop:\ns_sub_u32 s89, s89, 1\ns_cbranch_scc1 .Lhpx\n"
    "s_add_u32 s90, s90, 1024\ns_addc_u32 s91, s91, 0\ns_setpc_b64 s[90:91]\n.Lhpx:\ns_mov_b64 exec, -1\ns_setpc_b64 s[94:95]\n"
    ".Loth1:\ns_and_b32 s24, s2, 7\ns_lshl_b32 s44, s3, 2\ns_lshl_b32 s18, s24, 5\ns_add_u32 s18, s18, s25\ns_getpc_b64 s[92:93]\n.Lk_pc:\n"
    "s_sub_u32 s92, s92, .Lk_pc-.Lk_all_beg+4\ns_subb_u32 s93, s93, 0\nv_mov_b32 v70, 16777216\ns_cmp_gt_u32 s3, 3\ns_cbranch_scc1 .Loth3\nv_and_b32 v65, 63, v0\n"
    "s_lshl3_add_u32 s47, s44, s25\nv_lshl_add_u32 v64, v65, 2, s3\ns_sub_u32 s47, s47, 1984\nv_cmp_gt_u32 vcc, 16, v65\nv_lshl_add_u32 v65, v65, 7, s47\n"
    "s_lshl_b32 s47, s18, 9\nv_cndmask_b32 v64, v65, v64, vcc\ns_lshl_b32 s46, s18, 2\nv_min_u32 v64, (.Lk_all_end-.Lk_all_beg)/128-8, v64\nv_lshlrev_b32 v64, 7, v64\n"
    "s_mov_b64 exec, 0x3ffff\nglobal_load_dword v255, v64, s[92:93] offset:1024\ns_mov_b64 exec, -1\nv_lshlrev_b32 v64, 1, v0\nv_and_b32 v64, 0x1fc, v64\nv_mov_b32 v65, s46\n"
    "v_or_b32 v64, s47, v64\ns_waitcnt lgkmcnt(0)\nglobal_load_dword v160, v64, s[70:71]\nglobal_load_dword v161, v64, s[72:73]\nglobal_load_dword v162, v64, s[78:79]\n"
    "global_load_dword v163, v64, s[80:81]\nglobal_load_dword v164, v64, s[74:75]\nglobal_load_dword v165, v64, s[76:77]\nglobal_load_dword v167, v65, s[82:83]\n"
    "global_load_dword v166, v70, s[4:5] sc1\ns_branch .Lcom2\n.Loth3:\ns_cmp_eq_u32 s3, 5\ns_cbranch_scc1 .Loth4\ns_and_b32 s47, s18, 15\ns_lshl1_add_u32 s47, s47, s3\n"
    "s_lshl_b32 s47, s47, 14\ns_and_b32 s48, s18, 0xf0\nv_and_b32 v69, 32, v0\ns_lshl2_add_u32 s47, s48, s47\nv_and_b32 v71, 31, v0\ns_sub_u32 s47, s47, 94208\n"
    "v_lshl_add_u32 v69, v69, 8, s47\nv_lshl_add_u32 v69, v71, 1, v69\ns_waitcnt lgkmcnt(0)\nglobal_load_ushort v168, v69, s[68:69] offset:-4096\n"
    "global_load_ushort v169, v69, s[68:69] offset:-3072\nglobal_load_ushort v170, v69, s[68:69] offset:-2048\nglobal_load_ushort v171, v69, s[68:69] offset:-1024\n"
    "global_load_ushort v172, v69, s[68:69] offset:0\nglobal_load_ushort v173, v69, s[68:69] offset:1024\nglobal_load_ushort v174, v69, s[68:69] offset:2048\n"
    "global_load_ushort v175, v69, s[68:69] offset:3072\nglobal_load_dword v166, v70, s[4:5] sc1\ns_branch .Lcom2\n.Loth4:\nv_lshlrev_b32 v68, 4, v0\n"
    "v_mov_b32 v80, 0x3f803f80\nv_mov_b32 v81, 0x3f803f80\nv_mov_b32 v82, 0x3f803f80\nv_mov_b32 v83, 0x3f803f80\nv_add_u32 v68, 142336, v68\nv_mov_b64 v[84:85], 0\n"
    "v_mov_b64 v[86:87], 0\nds_write_b128 v68, v[80:83]\ns_mov_b64 exec, 0xff\nds_write_b128 v68, v[84:87] offset:16256\ns_mov_b64 exec, -1\ns_waitcnt lgkmcnt(0)\n"
    ".Lcom2:\ns_and_b32 s24, s2, 7\ns_lshr_b32 s25, s2, 3\ns_and_b32 s13, s3, 3\ns_lshl_b32 s43, s24, 5\ns_add_u32 s18, s43, s25\ns_lshr_b32 s42, s24, 1\n"
    "s_lshl_b32 s41, s42, 2\ns_and_b32 s40, s25, 3\ns_add_u32 s19, s41, s40\ns_and_b32 s39, s24, 1\ns_lshl_b32 s38, s39, 3\ns_lshr_b32 s37, s25, 2\ns_add_u32 s20, s38, s37\n"
    "s_lshl_b32 s36, s24, 3\ns_and_b32 s35, s25, 7\ns_add_u32 s21, s36, s35\ns_lshr_b32 s22, s25, 3\ns_lshl_b32 s34, s40, 1\ns_add_u32 s33, s37, s34\ns_and_b32 s23, s33, 7\n"
    "s_lshl_b32 s44, s18, 2\ns_mov_b64 s[6:7], s[86:87]\ns_mov_b64 s[8:9], s[64:65]\ns_mov_b32 s12, 0\nv_bfe_u32 v251, v0, 4, 2\nv_lshlrev_b32 v250, 2, v251\n"
    "s_add_u32 s43, s19, s3\ns_cmp_lt_u32 s43, 1\ns_cselect_b32 s42, 8, 0\nv_and_b32 v249, 7, v0\nv_add_u32 v248, 1, v249\nv_max_u32 v247, s42, v248\n"
    "v_sub_u32 v2, v250, v247\nv_add_u32 v246, 9, v249\nv_sub_u32 v40, v246, v247\nv_add_u32 v3, 1, v2\nv_add_u32 v4, 2, v2\nv_add_u32 v5, 3, v2\ns_nop 0\n"
    "s_cbranch_execz .Lhop\nv_cmp_lt_u32 s[36:37], v2, v40\nv_cmp_lt_u32 s[38:39], v3, v40\nv_cmp_lt_u32 s[40:41], v4, v40\nv_cmp_lt_u32 s[42:43], v5, v40\n"
    "v_mov_b32 v40, 0xf149f2ca\nv_cndmask_b32 v2, v40, 0, s[36:37]\nv_cndmask_b32 v3, v40, 0, s[38:39]\nv_cndmask_b32 v4, v40, 0, s[40:41]\n"
    "v_cndmask_b32 v5, v40, 0, s[42:43]\nv_and_b32 v251, 15, v0\nv_max_u32 v250, 1, v251\nv_mul_u32_u24 v249, 0x1a0, v250\nv_bfe_u32 v248, v0, 4, 2\ns_mul_i32 s43, s3, 0xd00\n"
    "s_add_u32 s42, s43, 0x18040\nv_add_u32 v6, s42, v249\nv_lshl_add_u32 v6, v248, 4, v6\nv_and_b32 v247, 7, v0\nv_mul_u32_u24 v246, 0x1a0, v247\n"
    "s_add_u32 s41, s43, 0x18d00\nv_add_u32 v7, s41, v246\nv_lshl_add_u32 v7, v248, 4, v7\nv_bfe_u32 v245, v0, 2, 2\nv_lshl_add_u32 v244, v248, 2, v245\n"
    "v_max_u32 v243, 1, v244\nv_mul_u32_u24 v242, 0x1a0, v243\nv_and_b32 v241, 3, v0\ns_add_u32 s40, s43, 0x18080\nv_add_u32 v8, s40, v242\nv_lshl_add_u32 v8, v241, 3, v8\n"
    "v_bfe_u32 v240, v0, 4, 1\ns_lshl_b32 s39, s19, 7\ns_lshl_b32 s38, s3, 4\ns_add_u32 s37, s39, s38\nv_add_u32 v239, s37, v240\nv_lshl_add_u32 v239, v247, 1, v239\n"
    "v_lshrrev_b32 v238, 5, v239\nv_bfe_u32 v237, v0, 3, 1\nv_bfe_u32 v236, v0, 5, 1\nv_and_b32 v235, 31, v239\ns_lshl_b32 s36, s20, 11\nv_lshl_add_u32 v9, v238, 15, s36\n"
    "v_lshl_add_u32 v9, v237, 10, v9\nv_lshl_add_u32 v9, v236, 9, v9\nv_lshl_add_u32 v9, v235, 4, v9\nv_mov_b32 v56, 163712\ns_add_u32 s16, s84, s44\ns_addc_u32 s17, s85, 0\n"
    "s_cmp_eq_u32 s3, 4\ns_cbranch_scc1 .Llstb7\ns_barrier\ns_cmp_gt_u32 s3, 3\ns_cbranch_scc1 .Lhalo10\n"
    "v_bfe_u32 v251, v0, 5, 1\nv_bfe_u32 v250, v0, 2, 2\nv_bfe_u32 v249, v0, 4, 1\nv_xor_b32 v248, v249, v251\nv_and_b32 v247, 3, v0\nv_lshlrev_b32 v36, 9, v251\n"
    "v_lshl_add_u32 v36, v250, 6, v36\nv_lshl_add_u32 v36, v248, 5, v36\nv_lshl_add_u32 v36, v247, 3, v36\nv_and_b32 v246, 31, v0\nv_bfe_u32 v245, v0, 1, 3\n"
    "v_xor_b32 v244, v251, v245\ns_mul_i32 s43, s13, 0x3000\ns_add_u32 s42, s43, 0x18000\nv_lshl_add_u32 v27, v246, 7, s42\nv_lshl_add_u32 v27, v244, 4, v27\n"
    "v_mul_u32_u24 v243, 0xd0, v246\ns_mul_i32 s41, s13, 0x1a00\ns_add_u32 s40, s41, 0x18d00\nv_add_u32 v37, s40, v243\nv_lshl_add_u32 v37, v251, 3, v37\n"
    "s_mov_b32 s39, 0x27c00\nv_lshl_add_u32 v38, v246, 2, s39\ns_mov_b32 s38, 0x27fe0\nv_lshl_add_u32 v242, v247, 2, s38\nv_and_b32 v241, 4, v0\nv_lshlrev_b32 v240, 4, v241\n"
    "v_sub_u32 v28, v242, v240\nv_mov_b32 v239, 16\nv_cmp_gt_u32 vcc, 1, v241\ns_nop 1\nv_cndmask_b32 v29, 1, v239, vcc\ns_lshl_b32 s37, s13, 2\ns_add_u32 s36, s37, 0x27fe0\n"
    "v_mov_b32 v238, s36\nv_and_b32 v237, 63, v0\ns_mov_b32 s35, 0x27e00\nv_lshl_add_u32 v236, v237, 2, s35\nv_cmp_gt_u32 vcc, 1, v237\ns_nop 1\n"
    "v_cndmask_b32 v30, v236, v238, vcc\ns_and_b32 s34, s23, 7\ns_mul_i32 s33, s34, 0x3000\nv_lshl_add_u32 v176, v251, 9, s33\nv_lshl_add_u32 v176, v250, 6, v176\n"
    "v_lshl_add_u32 v176, v248, 5, v176\nv_lshl_add_u32 v176, v247, 3, v176\ns_add_u32 s32, s23, 1\ns_and_b32 s31, s32, 7\ns_mul_i32 s30, s31, 0x3000\n"
    "v_lshl_add_u32 v177, v251, 9, s30\nv_lshl_add_u32 v177, v250, 6, v177\nv_lshl_add_u32 v177, v248, 5, v177\nv_lshl_add_u32 v177, v247, 3, v177\nv_bfe_u32 v251, v0, 5, 1\n"
    "v_bfe_u32 v250, v0, 2, 2\nv_bfe_u32 v249, v0, 4, 1\nv_xor_b32 v248, v249, v251\nv_and_b32 v247, 3, v0\ns_add_u32 s43, s23, 2\ns_and_b32 s42, s43, 7\n"
    "s_mul_i32 s41, s42, 0x3000\nv_lshl_add_u32 v178, v251, 9, s41\nv_lshl_add_u32 v178, v250, 6, v178\nv_lshl_add_u32 v178, v248, 5, v178\n"
    "v_lshl_add_u32 v178, v247, 3, v178\ns_add_u32 s40, s23, 3\ns_and_b32 s39, s40, 7\ns_mul_i32 s38, s39, 0x3000\nv_lshl_add_u32 v179, v251, 9, s38\n"
    "v_lshl_add_u32 v179, v250, 6, v179\nv_lshl_add_u32 v179, v248, 5, v179\nv_lshl_add_u32 v179, v247, 3, v179\ns_add_u32 s37, s23, 4\ns_and_b32 s36, s37, 7\n"
    "s_mul_i32 s35, s36, 0x3000\nv_lshl_add_u32 v180, v251, 9, s35\nv_lshl_add_u32 v180, v250, 6, v180\nv_lshl_add_u32 v180, v248, 5, v180\n"
    "v_lshl_add_u32 v180, v247, 3, v180\ns_add_u32 s34, s23, 5\ns_and_b32 s33, s34, 7\ns_mul_i32 s32, s33, 0x3000\nv_lshl_add_u32 v181, v251, 9, s32\n"
    "v_lshl_add_u32 v181, v250, 6, v181\nv_lshl_add_u32 v181, v248, 5, v181\nv_lshl_add_u32 v181, v247, 3, v181\nv_bfe_u32 v251, v0, 5, 1\nv_bfe_u32 v250, v0, 2, 2\n"
    "v_bfe_u32 v249, v0, 4, 1\nv_xor_b32 v248, v249, v251\nv_and_b32 v247, 3, v0\ns_add_u32 s43, s23, 6\ns_and_b32 s42, s43, 7\ns_mul_i32 s41, s42, 0x3000\n"
    "v_lshl_add_u32 v182, v251, 9, s41\nv_lshl_add_u32 v182, v250, 6, v182\nv_lshl_add_u32 v182, v248, 5, v182\nv_lshl_add_u32 v182, v247, 3, v182\ns_add_u32 s40, s23, 7\n"
    "s_and_b32 s39, s40, 7\ns_mul_i32 s38, s39, 0x3000\nv_lshl_add_u32 v183, v251, 9, s38\nv_lshl_add_u32 v183, v250, 6, v183\ns_nop 0\ns_cbranch_execz .Lhop\n"
    "v_lshl_add_u32 v183, v248, 5, v183\nv_lshl_add_u32 v183, v247, 3, v183\ns_mul_i32 s37, s13, 0xc00\ns_add_u32 s36, s37, 0x1f600\nv_lshl_add_u32 v33, v0, 4, s36\n"
    "s_lshl_b32 s35, s13, 2\ns_add_u32 s34, s35, 0x27d80\nv_mov_b32 v34, s34\nv_bfe_u32 v246, v0, 2, 4\ns_mov_b32 s33, 0x1006000\nv_lshl_add_u32 v35, v246, 7, s33\n"
    "v_lshl_add_u32 v35, v247, 4, v35\ns_lshr_b32 s32, s21, 2\ns_lshl_b32 s31, s32, 7\ns_add_u32 s30, s31, 0x1004000\nv_lshl_add_u32 v39, v247, 4, s30\n"
    "v_and_b32 v251, 15, v0\nv_mov_b32 v250, 0x102\nv_min_u32 v249, 12, v251\ns_mov_b32 s43, 0xfffffc00\nv_lshl_add_u32 v248, v249, 8, s43\nv_cmp_gt_u32 vcc, 5, v251\n"
    "s_nop 1\nv_cndmask_b32 v247, v248, v250, vcc\nv_cmp_gt_u32 vcc, 4, v251\ns_nop 1\nv_cndmask_b32 v25, v247, v251, vcc\nv_lshlrev_b32 v26, 3, v251\nv_cvt_f32_u32 v25, v25\n"
    "v_cvt_f32_u32 v26, v26\ns_mov_b32 s10, 4\ns_mov_b32 s27, 4\ns_add_u32 s94, s92, .Lgm14-.Lk_all_beg+4\ns_addc_u32 s95, s93, 0\nv_mov_b64 v[40:41], 0\n"
    "v_mov_b64 v[42:43], 0\nv_mov_b64 v[44:45], 0\nv_mov_b64 v[46:47], 0\nv_mov_b64 v[48:49], 0\nv_mov_b64 v[50:51], 0\nv_mov_b64 v[52:53], 0\nv_mov_b64 v[54:55], 0\n"
    "v_mov_b64 v[64:65], 0\nv_mov_b64 v[66:67], 0\nv_mov_b64 v[68:69], 0\nv_mov_b64 v[70:71], 0\nv_mov_b64 v[72:73], 0\nv_mov_b64 v[74:75], 0\nv_mov_b64 v[76:77], 0\n"
    "v_mov_b64 v[78:79], 0\nv_mov_b64 v[80:81], 0\nv_mov_b64 v[82:83], 0\nv_mov_b64 v[84:85], 0\nv_mov_b64 v[86:87], 0\nv_mov_b64 v[88:89], 0\nv_mov_b64 v[90:91], 0\n"
    "v_mov_b64 v[92:93], 0\nv_mov_b64 v[94:95], 0\nv_mov_b64 v[96:97], 0\nv_mov_b64 v[98:99], 0\nv_mov_b64 v[100:101], 0\nv_mov_b64 v[102:103], 0\nv_mov_b64 v[104:105], 0\n"
    "v_mov_b64 v[106:107], 0\nv_mov_b64 v[108:109], 0\nv_mov_b64 v[110:111], 0\nv_mov_b64 v[112:113], 0\nv_mov_b64 v[114:115], 0\nv_mov_b64 v[116:117], 0\n"
    "v_mov_b64 v[118:119], 0\nv_mov_b64 v[120:121], 0\nv_mov_b64 v[122:123], 0\nv_mov_b64 v[124:125], 0\nv_mov_b64 v[126:127], 0\nv_mov_b64 v[128:129], 0\n"
    "v_mov_b64 v[130:131], 0\nv_mov_b64 v[132:133], 0\nv_mov_b64 v[134:135], 0\ns_waitcnt vmcnt(1)\n"
    ".Lkgp9:\ns_bitcmp1_b32 s3, 1\ns_cselect_b32 s44, 0, 16\nv_lshlrev_b32 v240, s44, v167\nv_and_b32 v224, 1, v0\nv_and_b32 v240, 0xffff0000, v240\n"
    "v_lshlrev_b32 v224, 4, v224\ns_mov_b32 s45, 0x3fb8aa3b\nv_mul_f32 v241, s45, v240\nv_xor_b32 v224, 16, v224\nv_fma_f32 v242, v240, s45, -v241\nv_rndne_f32 v243, v241\n"
    "v_fmamk_f32 v242, v240, 0x32a5705f, v242\nv_sub_f32 v241, v241, v243\nv_add_f32 v241, v241, v242\nv_exp_f32 v241, v241\nv_cvt_i32_f32 v243, v243\n"
    "v_lshlrev_b32 v160, v224, v160\nv_lshlrev_b32 v161, v224, v161\nv_lshlrev_b32 v162, v224, v162\nv_lshlrev_b32 v163, v224, v163\nv_lshlrev_b32 v164, v224, v164\n"
    "v_lshlrev_b32 v165, v224, v165\nv_ldexp_f32 v241, v241, v243\nv_and_b32 v160, 0xffff0000, v160\nv_and_b32 v161, 0xffff0000, v161\nv_and_b32 v162, 0xffff0000, v162\n"
    "v_and_b32 v163, 0xffff0000, v163\nv_and_b32 v164, 0xffff0000, v164\nv_and_b32 v165, 0xffff0000, v165\nv_cvt_pk_bf16_f32 v241, 0, v241\nv_mul_f32 v164, v241, v164\n"
    "v_mul_f32 v165, v241, v165\nv_mul_f32 v16, v241, v160\nv_mul_f32 v17, v241, v161\nv_mul_f32 v242, v162, v164\nv_mul_f32 v243, v163, v165\nv_mul_f32 v244, v162, v165\n"
    "v_mul_f32 v245, v163, v164\nv_sub_f32 v242, v242, v243\nv_add_f32 v243, v244, v245\nv_mul_f32 v16, s45, v16\nv_mul_f32 v17, 0.15915494, v17\nv_mul_f32 v244, v242, v242\n"
    "v_mul_f32 v245, v243, v243\nv_add_f32 v244, v244, v245\nv_log_f32 v244, v244\nv_max_f32 v246, abs(v242), abs(v243)\nv_min_f32 v247, abs(v242), abs(v243)\n"
    "v_mul_f32 v18, 0.5, v244\nv_frexp_mant_f32 v248, v246\nv_max_f32 v244, 0xc2700000, v18\nv_rcp_f32 v248, v248\nv_frexp_exp_i32_f32 v246, v246\n"
    "v_max_f32_dpp v244, v244, v244 row_ror:1 row_mask:0xf bank_mask:0xf\nv_frexp_exp_i32_f32 v249, v247\nv_frexp_mant_f32 v247, v247\n"
    "v_max_f32_dpp v244, v244, v244 row_ror:2 row_mask:0xf bank_mask:0xf\nv_mul_f32 v248, v247, v248\nv_sub_u32 v246, v249, v246\n"
    "v_max_f32_dpp v244, v244, v244 row_ror:4 row_mask:0xf bank_mask:0xf\nv_ldexp_f32 v246, v248, v246\nv_mov_b32 v248, 0xbc7a590c\n"
    "v_max_f32_dpp v244, v244, v244 row_ror:8 row_mask:0xf bank_mask:0xf\nv_mul_f32 v247, v246, v246\nv_mov_b32 v245, v244\nv_fmac_f32 v248, 0x3b2d2a58, v247\n"
    "v_fmaak_f32 v248, v247, v248, 0x3d29fb3f\nv_permlane16_swap_b32 v244, v245\nv_fmaak_f32 v248, v247, v248, 0xbd97d4d7\nv_max_f32 v244, v244, v245\n"
    "v_fmaak_f32 v248, v247, v248, 0x3dd931b2\nv_mov_b32 v245, v244\nv_fmaak_f32 v248, v247, v248, 0xbe1160e6\nv_fmaak_f32 v248, v247, v248, 0x3e4cb8bf\n"
    "v_permlane32_swap_b32 v244, v245\nv_fmaak_f32 v248, v247, v248, 0xbeaaaa62\nv_max_f32 v244, v244, v245\nv_mul_f32 v247, v247, v248\nv_ceil_f32 v244, v244\n"
    "v_fmac_f32 v246, v246, v247\nv_add_f32 v244, 0xc1600000, v244\nv_cmp_gt_f32 vcc, abs(v243), abs(v242)\nv_sub_f32 v247, 0x3fc90fdb, v246\nv_sub_f32 v18, v18, v244\n"
    "v_cndmask_b32 v246, v246, v247, vcc\nv_cmp_gt_f32 vcc, 0, v242\nv_sub_f32 v247, 0x40490fdb, v246\nv_exp_f32 v24, v244\nv_cndmask_b32 v246, v246, v247, vcc\n"
    "s_brev_b32 s44, -2\nv_max_f32 v246, 0, v246\nv_mul_f32 v244, 0x43000000, v16\nv_bfi_b32 v246, s44, v246, v243\nv_mul_f32 v245, 0x43000000, v17\n"
    "v_mul_f32 v19, 0xbe22f983, v246\nv_mul_f32 v247, 4.0, v16\nv_mul_f32 v248, 4.0, v17\nv_fract_f32 v245, v245\nv_fract_f32 v248, v248\nv_exp_f32 v244, v244\n"
    "v_exp_f32 v247, v247\nv_cos_f32 v20, v245\nv_sin_f32 v21, v245\nv_cos_f32 v22, v248\nv_sin_f32 v23, v248\nv_mul_f32 v20, v244, v20\nv_mul_f32 v21, v244, v21\n"
    "v_mul_f32 v22, v247, v22\nv_mul_f32 v23, v247, v23\n"
    ".Lkgl13:\nv_mov_b32_dpp v240, v18 row_newbcast:0 row_mask:0xf bank_mask:0xf\nv_mov_b32_dpp v241, v19 row_newbcast:0 row_mask:0xf bank_mask:0xf\n"
    "v_mul_f32_dpp v242, v16, v26 row_newbcast:0 row_mask:0xf bank_mask:0xf\nv_mul_f32_dpp v243, v17, v26 row_newbcast:0 row_mask:0xf bank_mask:0xf\n"
    "v_fmac_f32_dpp v240, v16, v25 row_newbcast:0 row_mask:0xf bank_mask:0xf\nv_fmac_f32_dpp v241, -v17, v25 row_newbcast:0 row_mask:0xf bank_mask:0xf\n"
    "v_fract_f32 v243, v243\nv_exp_f32 v242, v242\nv_fract_f32 v241, v241\ns_cbranch_execz .Lhop\nv_exp_f32 v240, v240\nv_cos_f32 v244, v243\nv_sin_f32 v245, v243\n"
    "v_cos_f32 v246, v241\nv_sin_f32 v247, v241\nv_mul_f32 v244, v242, v244\nv_mul_f32 v245, v242, v245\nv_mul_f32 v246, v240, v246\nv_mul_f32 v247, v240, v247\n"
    "v_mul_f32_dpp v248, v20, v244 row_newbcast:0 row_mask:0xf bank_mask:0xf\nv_mul_f32_dpp v249, v20, v245 row_newbcast:0 row_mask:0xf bank_mask:0xf\n"
    "v_mul_f32_dpp v250, v22, v246 row_newbcast:0 row_mask:0xf bank_mask:0xf\nv_mul_f32_dpp v251, v22, v247 row_newbcast:0 row_mask:0xf bank_mask:0xf\n"
    "v_fmac_f32_dpp v248, -v21, v245 row_newbcast:0 row_mask:0xf bank_mask:0xf\nv_fmac_f32_dpp v249, v21, v244 row_newbcast:0 row_mask:0xf bank_mask:0xf\n"
    "v_fmac_f32_dpp v250, v23, v247 row_newbcast:0 row_mask:0xf bank_mask:0xf\nv_fmac_f32_dpp v251, -v23, v246 row_newbcast:0 row_mask:0xf bank_mask:0xf\n"
    "v_cvt_pk_f16_f32 v232, v244, v245\nv_cvt_pk_f16_f32 v224, v246, v247\nv_cvt_pk_f16_f32 v236, v248, v249\nv_cvt_pk_f16_f32 v228, v250, v251\n"
    "v_mov_b32_dpp v240, v18 row_newbcast:1 row_mask:0xf bank_mask:0xf\nv_mov_b32_dpp v241, v19 row_newbcast:1 row_mask:0xf bank_mask:0xf\n"
    "v_mul_f32_dpp v242, v16, v26 row_newbcast:1 row_mask:0xf bank_mask:0xf\nv_mul_f32_dpp v243, v17, v26 row_newbcast:1 row_mask:0xf bank_mask:0xf\n"
    "v_fmac_f32_dpp v240, v16, v25 row_newbcast:1 row_mask:0xf bank_mask:0xf\nv_fmac_f32_dpp v241, -v17, v25 row_newbcast:1 row_mask:0xf bank_mask:0xf\n"
    "v_fract_f32 v243, v243\nv_exp_f32 v242, v242\nv_fract_f32 v241, v241\nv_exp_f32 v240, v240\nv_cos_f32 v244, v243\nv_sin_f32 v245, v243\nv_cos_f32 v246, v241\n"
    "v_sin_f32 v247, v241\nv_mul_f32 v244, v242, v244\nv_mul_f32 v245, v242, v245\nv_mul_f32 v246, v240, v246\nv_mul_f32 v247, v240, v247\n"
    "v_mul_f32_dpp v248, v20, v244 row_newbcast:1 row_mask:0xf bank_mask:0xf\nv_mul_f32_dpp v249, v20, v245 row_newbcast:1 row_mask:0xf bank_mask:0xf\n"
    "v_mul_f32_dpp v250, v22, v246 row_newbcast:1 row_mask:0xf bank_mask:0xf\nv_mul_f32_dpp v251, v22, v247 row_newbcast:1 row_mask:0xf bank_mask:0xf\n"
    "v_fmac_f32_dpp v248, -v21, v245 row_newbcast:1 row_mask:0xf bank_mask:0xf\nv_fmac_f32_dpp v249, v21, v244 row_newbcast:1 row_mask:0xf bank_mask:0xf\n"
    "v_fmac_f32_dpp v250, v23, v247 row_newbcast:1 row_mask:0xf bank_mask:0xf\nv_fmac_f32_dpp v251, -v23, v246 row_newbcast:1 row_mask:0xf bank_mask:0xf\n"
    "v_cvt_pk_f16_f32 v233, v244, v245\nv_cvt_pk_f16_f32 v225, v246, v247\nv_cvt_pk_f16_f32 v237, v248, v249\nv_cvt_pk_f16_f32 v229, v250, v251\n"
    "v_mov_b32_dpp v240, v18 row_newbcast:2 row_mask:0xf bank_mask:0xf\nv_mov_b32_dpp v241, v19 row_newbcast:2 row_mask:0xf bank_mask:0xf\n"
    "v_mul_f32_dpp v242, v16, v26 row_newbcast:2 row_mask:0xf bank_mask:0xf\nv_mul_f32_dpp v243, v17, v26 row_newbcast:2 row_mask:0xf bank_mask:0xf\n"
    "v_fmac_f32_dpp v240, v16, v25 row_newbcast:2 row_mask:0xf bank_mask:0xf\nv_fmac_f32_dpp v241, -v17, v25 row_newbcast:2 row_mask:0xf bank_mask:0xf\n"
    "v_fract_f32 v243, v243\nv_exp_f32 v242, v242\nv_fract_f32 v241, v241\nv_exp_f32 v240, v240\nv_cos_f32 v244, v243\nv_sin_f32 v245, v243\nv_cos_f32 v246, v241\n"
    "v_sin_f32 v247, v241\nv_mul_f32 v244, v242, v244\nv_mul_f32 v245, v242, v245\nv_mul_f32 v246, v240, v246\nv_mul_f32 v247, v240, v247\n"
    "v_mul_f32_dpp v248, v20, v244 row_newbcast:2 row_mask:0xf bank_mask:0xf\nv_mul_f32_dpp v249, v20, v245 row_newbcast:2 row_mask:0xf bank_mask:0xf\n"
    "v_mul_f32_dpp v250, v22, v246 row_newbcast:2 row_mask:0xf bank_mask:0xf\nv_mul_f32_dpp v251, v22, v247 row_newbcast:2 row_mask:0xf bank_mask:0xf\n"
    "v_fmac_f32_dpp v248, -v21, v245 row_newbcast:2 row_mask:0xf bank_mask:0xf\nv_fmac_f32_dpp v249, v21, v244 row_newbcast:2 row_mask:0xf bank_mask:0xf\n"
    "v_fmac_f32_dpp v250, v23, v247 row_newbcast:2 row_mask:0xf bank_mask:0xf\nv_fmac_f32_dpp v251, -v23, v246 row_newbcast:2 row_mask:0xf bank_mask:0xf\n"
    "v_cvt_pk_f16_f32 v234, v244, v245\nv_cvt_pk_f16_f32 v226, v246, v247\nv_cvt_pk_f16_f32 v238, v248, v249\nv_cvt_pk_f16_f32 v230, v250, v251\nds_read_b32 v57, v56\n"
    "v_mov_b32_dpp v240, v18 row_newbcast:3 row_mask:0xf bank_mask:0xf\nv_mov_b32_dpp v241, v19 row_newbcast:3 row_mask:0xf bank_mask:0xf\n"
    "v_mul_f32_dpp v242, v16, v26 row_newbcast:3 row_mask:0xf bank_mask:0xf\nv_mul_f32_dpp v243, v17, v26 row_newbcast:3 row_mask:0xf bank_mask:0xf\n"
    "v_fmac_f32_dpp v240, v16, v25 row_newbcast:3 row_mask:0xf bank_mask:0xf\nv_fmac_f32_dpp v241, -v17, v25 row_newbcast:3 row_mask:0xf bank_mask:0xf\n"
    "v_fract_f32 v243, v243\nv_exp_f32 v242, v242\nv_fract_f32 v241, v241\nv_exp_f32 v240, v240\nv_cos_f32 v244, v243\nv_sin_f32 v245, v243\nv_cos_f32 v246, v241\n"
    "v_sin_f32 v247, v241\nv_mul_f32 v244, v242, v244\nv_mul_f32 v245, v242, v245\nv_mul_f32 v246, v240, v246\nv_mul_f32 v247, v240, v247\n"
    "v_mul_f32_dpp v248, v20, v244 row_newbcast:3 row_mask:0xf bank_mask:0xf\nv_mul_f32_dpp v249, v20, v245 row_newbcast:3 row_mask:0xf bank_mask:0xf\n"
    "v_mul_f32_dpp v250, v22, v246 row_newbcast:3 row_mask:0xf bank_mask:0xf\nv_mul_f32_dpp v251, v22, v247 row_newbcast:3 row_mask:0xf bank_mask:0xf\n"
    "v_fmac_f32_dpp v248, -v21, v245 row_newbcast:3 row_mask:0xf bank_mask:0xf\nv_fmac_f32_dpp v249, v21, v244 row_newbcast:3 row_mask:0xf bank_mask:0xf\n"
    "v_fmac_f32_dpp v250, v23, v247 row_newbcast:3 row_mask:0xf bank_mask:0xf\nv_fmac_f32_dpp v251, -v23, v246 row_newbcast:3 row_mask:0xf bank_mask:0xf\n"
    "v_cvt_pk_f16_f32 v235, v244, v245\nv_cvt_pk_f16_f32 v227, v246, v247\nv_cvt_pk_f16_f32 v239, v248, v249\nv_cvt_pk_f16_f32 v231, v250, v251\ns_waitcnt lgkmcnt(0)\n"
    "v_readfirstlane_b32 s44, v57\nv_mov_b32_dpp v16, v16 row_ror:12 row_mask:0xf bank_mask:0xf\nv_mov_b32_dpp v17, v17 row_ror:12 row_mask:0xf bank_mask:0xf\n"
    "v_mfma_f32_16x16x32_f16 v[40:43], v[224:227], v[232:235], v[40:43]\nv_mov_b32_dpp v18, v18 row_ror:12 row_mask:0xf bank_mask:0xf\n"
    "v_mov_b32_dpp v19, v19 row_ror:12 row_mask:0xf bank_mask:0xf\nv_mfma_f32_16x16x32_f16 v[44:47], v[224:227], v[236:239], v[44:47]\n"
    "v_mov_b32_dpp v20, v20 row_ror:12 row_mask:0xf bank_mask:0xf\nv_mov_b32_dpp v21, v21 row_ror:12 row_mask:0xf bank_mask:0xf\n"
    "v_mfma_f32_16x16x32_f16 v[48:51], v[228:231], v[232:235], v[48:51]\nv_mov_b32_dpp v22, v22 row_ror:12 row_mask:0xf bank_mask:0xf\n"
    "v_mov_b32_dpp v23, v23 row_ror:12 row_mask:0xf bank_mask:0xf\nv_mfma_f32_16x16x32_f16 v[52:55], v[228:231], v[236:239], v[52:55]\ns_sub_u32 s27, s27, 1\n"
    "s_cmp_eq_u32 s27, 0\ns_cbranch_scc1 .Lkx15\ns_cmp_lt_i32 s44, s10\ns_cbranch_scc1 .Lkgl13\ns_setpc_b64 s[94:95]\n.Lkx15:\ns_nop 4\nv_mul_f32 v40, v40, v24\n"
    "v_mul_f32 v41, v41, v24\nv_mul_f32 v42, v42, v24\nv_mul_f32 v43, v43, v24\nv_mul_f32 v44, v44, v24\nv_mul_f32 v45, v45, v24\nv_mul_f32 v46, v46, v24\n"
    "v_mul_f32 v47, v47, v24\nv_mul_f32 v48, v48, v24\nv_mul_f32 v49, v49, v24\nv_mul_f32 v50, v50, v24\nv_mul_f32 v51, v51, v24\nv_mul_f32 v52, v52, v24\n"
    "v_mul_f32 v53, v53, v24\nv_mul_f32 v54, v54, v24\nv_mul_f32 v55, v55, v24\nv_max3_f32 v240, abs(v40), abs(v41), abs(v42)\nv_max3_f32 v241, abs(v43), abs(v44), abs(v45)\n"
    "v_max3_f32 v240, v240, abs(v46), abs(v47)\nv_max3_f32 v241, v241, abs(v48), abs(v49)\nv_max3_f32 v240, v240, abs(v50), abs(v51)\n"
    "v_max3_f32 v241, v241, abs(v52), abs(v53)\nv_max3_f32 v240, v240, abs(v54), abs(v55)\nv_max_f32 v240, v240, v241\ns_nop 1\n"
    "v_max_f32_dpp v240, v240, v240 row_ror:1 row_mask:0xf bank_mask:0xf\ns_nop 1\nv_max_f32_dpp v240, v240, v240 row_ror:2 row_mask:0xf bank_mask:0xf\ns_nop 1\n"
    "v_max_f32_dpp v240, v240, v240 row_ror:4 row_mask:0xf bank_mask:0xf\ns_nop 1\nv_max_f32_dpp v240, v240, v240 row_ror:8 row_mask:0xf bank_mask:0xf\ns_cbranch_execz .Lhop\n"
    "v_mov_b32 v241, v240\ns_nop 1\nv_permlane16_swap_b32 v240, v241\nv_max_f32 v240, v240, v241\nv_mov_b32 v241, v240\ns_nop 1\nv_permlane32_swap_b32 v240, v241\n"
    "v_max_f32 v240, v240, v241\ns_mov_b64 exec, 1\nds_write_b32 v34, v240\ns_mov_b64 exec, -1\ns_setpc_b64 s[94:95]\n"
    ".Lgm14:\ns_waitcnt vmcnt(0)\nv_readfirstlane_b32 s11, v166\ns_mov_b32 s81, 0x3b000000\ns_mov_b32 s84, -1\ns_mov_b32 s85, 0\nv_mov_b32 v241, 0x3f803f80\n"
    "s_mul_i32 s11, s11, 0x9E3779B1\ns_add_u32 s11, s11, 0x7F4A7C15\nv_mov_b32 v160, v27\nv_xor_b32 v161, 32, v160\nv_xor_b32 v162, 64, v160\nv_xor_b32 v163, 0x60, v160\n"
    "v_mov_b32 v240, 0\nv_writelane_b32 v240, 2, 0\ns_mov_b32 s86, 0x100000\n.Ls18:\nds_read_b32 v243, v56 offset:0\ns_waitcnt lgkmcnt(0)\nv_cmp_gt_i32 vcc, 2, v243\n"
    "s_sub_u32 s86, s86, 1\ns_cbranch_scc1 .Ls19\ns_cbranch_vccz .Ls19\ns_sleep 1\ns_branch .Ls18\n.Ls19:\nds_read_b64_tr_b16 v[208:209], v176 offset:0\n"
    "ds_read_b64_tr_b16 v[210:211], v176 offset:256\nds_read_b64_tr_b16 v[212:213], v176 offset:3072\nds_read_b64_tr_b16 v[214:215], v176 offset:3328\n"
    "ds_read_b128 v[144:147], v160 offset:0\nds_read_b64_tr_b16 v[216:217], v176 offset:6144\nds_read_b64_tr_b16 v[218:219], v176 offset:6400\n"
    "ds_read_b128 v[148:151], v161 offset:0\nds_read_b64_tr_b16 v[220:221], v176 offset:9216\nds_read_b64_tr_b16 v[222:223], v176 offset:9472\n"
    "ds_read_b128 v[152:155], v162 offset:0\nds_read_b64_tr_b16 v[224:225], v176 offset:1024\nds_read_b64_tr_b16 v[226:227], v176 offset:1280\n"
    "ds_read_b128 v[156:159], v163 offset:0\nds_read_b64_tr_b16 v[228:229], v176 offset:4096\nds_read_b64_tr_b16 v[230:231], v176 offset:4352\ns_waitcnt lgkmcnt(11)\n"
    "v_dot2c_f32_bf16 v112, v241, v144\nv_dot2c_f32_bf16 v113, v144, v144\nv_dot2c_f32_bf16 v114, v241, v145\nv_dot2c_f32_bf16 v115, v145, v145\n"
    "v_dot2c_f32_bf16 v116, v241, v146\nv_dot2c_f32_bf16 v117, v146, v146\nv_dot2c_f32_bf16 v118, v241, v147\nv_dot2c_f32_bf16 v119, v147, v147\ns_waitcnt lgkmcnt(8)\n"
    "v_dot2c_f32_bf16 v112, v241, v148\nv_dot2c_f32_bf16 v113, v148, v148\nv_dot2c_f32_bf16 v114, v241, v149\nv_dot2c_f32_bf16 v115, v149, v149\n"
    "v_dot2c_f32_bf16 v116, v241, v150\nv_dot2c_f32_bf16 v117, v150, v150\nv_dot2c_f32_bf16 v118, v241, v151\nv_dot2c_f32_bf16 v119, v151, v151\ns_waitcnt lgkmcnt(5)\n"
    "v_dot2c_f32_bf16 v112, v241, v152\nv_dot2c_f32_bf16 v113, v152, v152\nv_dot2c_f32_bf16 v114, v241, v153\nv_dot2c_f32_bf16 v115, v153, v153\n"
    "v_dot2c_f32_bf16 v116, v241, v154\nv_dot2c_f32_bf16 v117, v154, v154\nv_dot2c_f32_bf16 v118, v241, v155\nv_dot2c_f32_bf16 v119, v155, v155\ns_waitcnt lgkmcnt(2)\n"
    "v_dot2c_f32_bf16 v112, v241, v156\nv_dot2c_f32_bf16 v113, v156, v156\nv_dot2c_f32_bf16 v114, v241, v157\nv_dot2c_f32_bf16 v115, v157, v157\n"
    "v_dot2c_f32_bf16 v116, v241, v158\nv_dot2c_f32_bf16 v117, v158, v158\nv_dot2c_f32_bf16 v118, v241, v159\nv_dot2c_f32_bf16 v119, v159, v159\n"
    "v_mfma_f32_32x32x16_bf16 v[64:79], v[208:211], v[144:147], v[64:79]\nds_read_b64_tr_b16 v[208:209], v176 offset:7168\nds_read_b64_tr_b16 v[210:211], v176 offset:7424\n"
    "ds_add_u32 v30, v240\nv_mfma_f32_32x32x16_bf16 v[64:79], v[212:215], v[148:151], v[64:79]\nds_read_b64_tr_b16 v[212:213], v176 offset:10240\n"
    "ds_read_b64_tr_b16 v[214:215], v176 offset:10496\nds_read_b32 v243, v56\nv_mfma_f32_32x32x16_bf16 v[64:79], v[216:219], v[152:155], v[64:79]\n"
    "ds_read_b64_tr_b16 v[216:217], v176 offset:2048\nds_read_b64_tr_b16 v[218:219], v176 offset:2304\nv_mfma_f32_32x32x16_bf16 v[64:79], v[220:223], v[156:159], v[64:79]\n"
    "ds_read_b64_tr_b16 v[220:221], v176 offset:5120\nds_read_b64_tr_b16 v[222:223], v176 offset:5376\nv_mfma_f32_32x32x16_bf16 v[80:95], v[224:227], v[144:147], v[80:95]\n"
    "ds_read_b64_tr_b16 v[224:225], v176 offset:8192\nds_read_b64_tr_b16 v[226:227], v176 offset:8448\ns_waitcnt lgkmcnt(6)\nv_readfirstlane_b32 s49, v243\n"
    "v_mfma_f32_32x32x16_bf16 v[80:95], v[228:231], v[148:151], v[80:95]\nds_read_b64_tr_b16 v[228:229], v176 offset:11264\nds_read_b64_tr_b16 v[230:231], v176 offset:11520\n"
    "s_cmp_lt_i32 s49, 3\ns_cbranch_scc1 .Lc20\n.Lc21:\nv_mfma_f32_32x32x16_bf16 v[80:95], v[208:211], v[152:155], v[80:95]\nds_read_b64_tr_b16 v[208:209], v177 offset:0\n"
    "ds_read_b64_tr_b16 v[210:211], v177 offset:256\nv_mfma_f32_32x32x16_bf16 v[80:95], v[212:215], v[156:159], v[80:95]\nds_read_b64_tr_b16 v[212:213], v177 offset:3072\n"
    "ds_read_b64_tr_b16 v[214:215], v177 offset:3328\ns_waitcnt lgkmcnt(6)\nv_mfma_f32_32x32x16_bf16 v[96:111], v[216:219], v[144:147], v[96:111]\ns_cmp_lt_i32 s49, 4\n"
    "s_cbranch_scc1 .Lc23\n.Lc24:\nds_read_b128 v[144:147], v160 offset:4096\nds_read_b64_tr_b16 v[216:217], v177 offset:6144\n"
    "ds_read_b64_tr_b16 v[218:219], v177 offset:6400\nv_mfma_f32_32x32x16_bf16 v[96:111], v[220:223], v[148:151], v[96:111]\nds_read_b128 v[148:151], v161 offset:4096\n"
    "ds_read_b64_tr_b16 v[220:221], v177 offset:9216\nds_read_b64_tr_b16 v[222:223], v177 offset:9472\nv_mfma_f32_32x32x16_bf16 v[96:111], v[224:227], v[152:155], v[96:111]\n"
    "ds_read_b128 v[152:155], v162 offset:4096\nds_read_b64_tr_b16 v[224:225], v177 offset:1024\nds_read_b64_tr_b16 v[226:227], v177 offset:1280\ns_waitcnt lgkmcnt(13)\n"
    "v_mfma_f32_32x32x16_bf16 v[96:111], v[228:231], v[156:159], v[96:111]\nds_read_b128 v[156:159], v163 offset:4096\nds_read_b64_tr_b16 v[228:229], v177 offset:4096\n"
    "ds_read_b64_tr_b16 v[230:231], v177 offset:4352\ns_waitcnt lgkmcnt(11)\nv_dot2c_f32_bf16 v112, v241, v144\nv_dot2c_f32_bf16 v113, v144, v144\n"
    "v_dot2c_f32_bf16 v114, v241, v145\nv_dot2c_f32_bf16 v115, v145, v145\nv_dot2c_f32_bf16 v116, v241, v146\nv_dot2c_f32_bf16 v117, v146, v146\n"
    "v_dot2c_f32_bf16 v118, v241, v147\nv_dot2c_f32_bf16 v119, v147, v147\ns_waitcnt lgkmcnt(8)\nv_dot2c_f32_bf16 v112, v241, v148\nv_dot2c_f32_bf16 v113, v148, v148\n"
    "v_dot2c_f32_bf16 v114, v241, v149\nv_dot2c_f32_bf16 v115, v149, v149\nv_dot2c_f32_bf16 v116, v241, v150\nv_dot2c_f32_bf16 v117, v150, v150\n"
    "v_dot2c_f32_bf16 v118, v241, v151\nv_dot2c_f32_bf16 v119, v151, v151\ns_waitcnt lgkmcnt(5)\nv_dot2c_f32_bf16 v112, v241, v152\nv_dot2c_f32_bf16 v113, v152, v152\n"
    "v_dot2c_f32_bf16 v114, v241, v153\nv_dot2c_f32_bf16 v115, v153, v153\nv_dot2c_f32_bf16 v116, v241, v154\nv_dot2c_f32_bf16 v117, v154, v154\n"
    "v_dot2c_f32_bf16 v118, v241, v155\nv_dot2c_f32_bf16 v119, v155, v155\ns_waitcnt lgkmcnt(2)\nv_dot2c_f32_bf16 v112, v241, v156\nv_dot2c_f32_bf16 v113, v156, v156\n"
    "v_dot2c_f32_bf16 v114, v241, v157\nv_dot2c_f32_bf16 v115, v157, v157\nv_dot2c_f32_bf16 v116, v241, v158\nv_dot2c_f32_bf16 v117, v158, v158\n"
    "v_dot2c_f32_bf16 v118, v241, v159\nv_dot2c_f32_bf16 v119, v159, v159\nv_mfma_f32_32x32x16_bf16 v[64:79], v[208:211], v[144:147], v[64:79]\n"
    "ds_read_b64_tr_b16 v[208:209], v177 offset:7168\nds_read_b64_tr_b16 v[210:211], v177 offset:7424\nds_add_u32 v30, v240\n"
    "v_mfma_f32_32x32x16_bf16 v[64:79], v[212:215], v[148:151], v[64:79]\nds_read_b64_tr_b16 v[212:213], v177 offset:10240\nds_read_b64_tr_b16 v[214:215], v177 offset:10496\n"
    "s_nop 0\ns_cbranch_execz .Lhop\nds_read_b32 v243, v56\nv_mfma_f32_32x32x16_bf16 v[64:79], v[216:219], v[152:155], v[64:79]\n"
    "ds_read_b64_tr_b16 v[216:217], v177 offset:2048\nds_read_b64_tr_b16 v[218:219], v177 offset:2304\nv_mfma_f32_32x32x16_bf16 v[64:79], v[220:223], v[156:159], v[64:79]\n"
    "ds_read_b64_tr_b16 v[220:221], v177 offset:5120\nds_read_b64_tr_b16 v[222:223], v177 offset:5376\nv_mfma_f32_32x32x16_bf16 v[80:95], v[224:227], v[144:147], v[80:95]\n"
    "ds_read_b64_tr_b16 v[224:225], v177 offset:8192\nds_read_b64_tr_b16 v[226:227], v177 offset:8448\ns_waitcnt lgkmcnt(6)\nv_readfirstlane_b32 s49, v243\n"
    "v_mfma_f32_32x32x16_bf16 v[80:95], v[228:231], v[148:151], v[80:95]\nds_read_b64_tr_b16 v[228:229], v177 offset:11264\nds_read_b64_tr_b16 v[230:231], v177 offset:11520\n"
    "s_cmp_lt_i32 s49, 5\ns_cbranch_scc1 .Lc26\n.Lc27:\nv_mfma_f32_32x32x16_bf16 v[80:95], v[208:211], v[152:155], v[80:95]\nds_read_b64_tr_b16 v[208:209], v178 offset:0\n"
    "ds_read_b64_tr_b16 v[210:211], v178 offset:256\nv_mfma_f32_32x32x16_bf16 v[80:95], v[212:215], v[156:159], v[80:95]\nds_read_b64_tr_b16 v[212:213], v178 offset:3072\n"
    "ds_read_b64_tr_b16 v[214:215], v178 offset:3328\ns_waitcnt lgkmcnt(6)\nv_mfma_f32_32x32x16_bf16 v[96:111], v[216:219], v[144:147], v[96:111]\ns_cmp_lt_i32 s49, 6\n"
    "s_cbranch_scc1 .Lc29\n.Lc30:\nds_read_b128 v[144:147], v160 offset:8192\nds_read_b64_tr_b16 v[216:217], v178 offset:6144\n"
    "ds_read_b64_tr_b16 v[218:219], v178 offset:6400\nv_mfma_f32_32x32x16_bf16 v[96:111], v[220:223], v[148:151], v[96:111]\nds_read_b128 v[148:151], v161 offset:8192\n"
    "ds_read_b64_tr_b16 v[220:221], v178 offset:9216\nds_read_b64_tr_b16 v[222:223], v178 offset:9472\nv_mfma_f32_32x32x16_bf16 v[96:111], v[224:227], v[152:155], v[96:111]\n"
    "ds_read_b128 v[152:155], v162 offset:8192\nds_read_b64_tr_b16 v[224:225], v178 offset:1024\nds_read_b64_tr_b16 v[226:227], v178 offset:1280\ns_waitcnt lgkmcnt(13)\n"
    "v_mfma_f32_32x32x16_bf16 v[96:111], v[228:231], v[156:159], v[96:111]\nds_read_b128 v[156:159], v163 offset:8192\nds_read_b64_tr_b16 v[228:229], v178 offset:4096\n"
    "ds_read_b64_tr_b16 v[230:231], v178 offset:4352\ns_waitcnt lgkmcnt(11)\nv_dot2c_f32_bf16 v112, v241, v144\nv_dot2c_f32_bf16 v113, v144, v144\n"
    "v_dot2c_f32_bf16 v114, v241, v145\nv_dot2c_f32_bf16 v115, v145, v145\nv_dot2c_f32_bf16 v116, v241, v146\nv_dot2c_f32_bf16 v117, v146, v146\n"
    "v_dot2c_f32_bf16 v118, v241, v147\nv_dot2c_f32_bf16 v119, v147, v147\ns_waitcnt lgkmcnt(8)\nv_dot2c_f32_bf16 v112, v241, v148\nv_dot2c_f32_bf16 v113, v148, v148\n"
    "v_dot2c_f32_bf16 v114, v241, v149\nv_dot2c_f32_bf16 v115, v149, v149\nv_dot2c_f32_bf16 v116, v241, v150\nv_dot2c_f32_bf16 v117, v150, v150\n"
    "v_dot2c_f32_bf16 v118, v241, v151\nv_dot2c_f32_bf16 v119, v151, v151\ns_waitcnt lgkmcnt(5)\nv_dot2c_f32_bf16 v112, v241, v152\nv_dot2c_f32_bf16 v113, v152, v152\n"
    "v_dot2c_f32_bf16 v114, v241, v153\nv_dot2c_f32_bf16 v115, v153, v153\nv_dot2c_f32_bf16 v116, v241, v154\nv_dot2c_f32_bf16 v117, v154, v154\n"
    "v_dot2c_f32_bf16 v118, v241, v155\nv_dot2c_f32_bf16 v119, v155, v155\ns_waitcnt lgkmcnt(2)\nv_dot2c_f32_bf16 v112, v241, v156\nv_dot2c_f32_bf16 v113, v156, v156\n"
    "v_dot2c_f32_bf16 v114, v241, v157\nv_dot2c_f32_bf16 v115, v157, v157\nv_dot2c_f32_bf16 v116, v241, v158\nv_dot2c_f32_bf16 v117, v158, v158\n"
    "v_dot2c_f32_bf16 v118, v241, v159\nv_dot2c_f32_bf16 v119, v159, v159\nv_mfma_f32_32x32x16_bf16 v[64:79], v[208:211], v[144:147], v[64:79]\n"
    "ds_read_b64_tr_b16 v[208:209], v178 offset:7168\nds_read_b64_tr_b16 v[210:211], v178 offset:7424\nds_add_u32 v30, v240\n"
    "v_mfma_f32_32x32x16_bf16 v[64:79], v[212:215], v[148:151], v[64:79]\nds_read_b64_tr_b16 v[212:213], v178 offset:10240\nds_read_b64_tr_b16 v[214:215], v178 offset:10496\n"
    "ds_read_b32 v243, v56\nv_mfma_f32_32x32x16_bf16 v[64:79], v[216:219], v[152:155], v[64:79]\nds_read_b64_tr_b16 v[216:217], v178 offset:2048\n"
    "ds_read_b64_tr_b16 v[218:219], v178 offset:2304\nv_mfma_f32_32x32x16_bf16 v[64:79], v[220:223], v[156:159], v[64:79]\nds_read_b64_tr_b16 v[220:221], v178 offset:5120\n"
    "ds_read_b64_tr_b16 v[222:223], v178 offset:5376\nv_mfma_f32_32x32x16_bf16 v[80:95], v[224:227], v[144:147], v[80:95]\nds_read_b64_tr_b16 v[224:225], v178 offset:8192\n"
    "ds_read_b64_tr_b16 v[226:227], v178 offset:8448\ns_waitcnt lgkmcnt(6)\nv_readfirstlane_b32 s49, v243\n"
    "v_mfma_f32_32x32x16_bf16 v[80:95], v[228:231], v[148:151], v[80:95]\nds_read_b64_tr_b16 v[228:229], v178 offset:11264\nds_read_b64_tr_b16 v[230:231], v178 offset:11520\n"
    "s_cmp_lt_i32 s49, 7\ns_cbranch_scc1 .Lc32\n.Lc33:\nv_mfma_f32_32x32x16_bf16 v[80:95], v[208:211], v[152:155], v[80:95]\nds_read_b64_tr_b16 v[208:209], v179 offset:0\n"
    "ds_read_b64_tr_b16 v[210:211], v179 offset:256\nv_mfma_f32_32x32x16_bf16 v[80:95], v[212:215], v[156:159], v[80:95]\nds_read_b64_tr_b16 v[212:213], v179 offset:3072\n"
    "ds_read_b64_tr_b16 v[214:215], v179 offset:3328\ns_waitcnt lgkmcnt(6)\nv_mfma_f32_32x32x16_bf16 v[96:111], v[216:219], v[144:147], v[96:111]\ns_cmp_lt_i32 s49, 8\n"
    "s_cbranch_scc1 .Lc35\n.Lc36:\nds_read_b128 v[144:147], v160 offset:0\nds_read_b64_tr_b16 v[216:217], v179 offset:6144\nds_read_b64_tr_b16 v[218:219], v179 offset:6400\n"
    "v_mfma_f32_32x32x16_bf16 v[96:111], v[220:223], v[148:151], v[96:111]\nds_read_b128 v[148:151], v161 offset:0\nds_read_b64_tr_b16 v[220:221], v179 offset:9216\n"
    "ds_read_b64_tr_b16 v[222:223], v179 offset:9472\nv_mfma_f32_32x32x16_bf16 v[96:111], v[224:227], v[152:155], v[96:111]\nds_read_b128 v[152:155], v162 offset:0\n"
    "ds_read_b64_tr_b16 v[224:225], v179 offset:1024\nds_read_b64_tr_b16 v[226:227], v179 offset:1280\ns_waitcnt lgkmcnt(13)\n"
    "v_mfma_f32_32x32x16_bf16 v[96:111], v[228:231], v[156:159], v[96:111]\nds_read_b128 v[156:159], v163 offset:0\nds_read_b64_tr_b16 v[228:229], v179 offset:4096\n"
    "ds_read_b64_tr_b16 v[230:231], v179 offset:4352\ns_waitcnt lgkmcnt(11)\nv_dot2c_f32_bf16 v112, v241, v144\nv_dot2c_f32_bf16 v113, v144, v144\n"
    "v_dot2c_f32_bf16 v114, v241, v145\nv_dot2c_f32_bf16 v115, v145, v145\nv_dot2c_f32_bf16 v116, v241, v146\nv_dot2c_f32_bf16 v117, v146, v146\n"
    "v_dot2c_f32_bf16 v118, v241, v147\nv_dot2c_f32_bf16 v119, v147, v147\ns_waitcnt lgkmcnt(8)\nv_dot2c_f32_bf16 v112, v241, v148\nv_dot2c_f32_bf16 v113, v148, v148\n"
    "v_dot2c_f32_bf16 v114, v241, v149\nv_dot2c_f32_bf16 v115, v149, v149\nv_dot2c_f32_bf16 v116, v241, v150\nv_dot2c_f32_bf16 v117, v150, v150\n"
    "v_dot2c_f32_bf16 v118, v241, v151\nv_dot2c_f32_bf16 v119, v151, v151\ns_waitcnt lgkmcnt(5)\nv_dot2c_f32_bf16 v112, v241, v152\nv_dot2c_f32_bf16 v113, v152, v152\n"
    "v_dot2c_f32_bf16 v114, v241, v153\nv_dot2c_f32_bf16 v115, v153, v153\nv_dot2c_f32_bf16 v116, v241, v154\nv_dot2c_f32_bf16 v117, v154, v154\n"
    "v_dot2c_f32_bf16 v118, v241, v155\nv_dot2c_f32_bf16 v119, v155, v155\ns_waitcnt lgkmcnt(2)\nv_dot2c_f32_bf16 v112, v241, v156\nv_dot2c_f32_bf16 v113, v156, v156\n"
    "v_dot2c_f32_bf16 v114, v241, v157\nv_dot2c_f32_bf16 v115, v157, v157\nv_dot2c_f32_bf16 v116, v241, v158\nv_dot2c_f32_bf16 v117, v158, v158\n"
    "v_dot2c_f32_bf16 v118, v241, v159\nv_dot2c_f32_bf16 v119, v159, v159\nv_mfma_f32_32x32x16_bf16 v[64:79], v[208:211], v[144:147], v[64:79]\n"
    "ds_read_b64_tr_b16 v[208:209], v179 offset:7168\nds_read_b64_tr_b16 v[210:211], v179 offset:7424\nds_add_u32 v30, v240\n"
    "v_mfma_f32_32x32x16_bf16 v[64:79], v[212:215], v[148:151], v[64:79]\nds_read_b64_tr_b16 v[212:213], v179 offset:10240\ns_nop 0\ns_cbranch_execz .Lhop\n"
    "ds_read_b64_tr_b16 v[214:215], v179 offset:10496\nds_read_b32 v243, v56\nv_mfma_f32_32x32x16_bf16 v[64:79], v[216:219], v[152:155], v[64:79]\n"
    "ds_read_b64_tr_b16 v[216:217], v179 offset:2048\nds_read_b64_tr_b16 v[218:219], v179 offset:2304\nv_mfma_f32_32x32x16_bf16 v[64:79], v[220:223], v[156:159], v[64:79]\n"
    "ds_read_b64_tr_b16 v[220:221], v179 offset:5120\nds_read_b64_tr_b16 v[222:223], v179 offset:5376\nv_mfma_f32_32x32x16_bf16 v[80:95], v[224:227], v[144:147], v[80:95]\n"
    "ds_read_b64_tr_b16 v[224:225], v179 offset:8192\nds_read_b64_tr_b16 v[226:227], v179 offset:8448\ns_waitcnt lgkmcnt(6)\nv_readfirstlane_b32 s49, v243\n"
    "v_mfma_f32_32x32x16_bf16 v[80:95], v[228:231], v[148:151], v[80:95]\nds_read_b64_tr_b16 v[228:229], v179 offset:11264\nds_read_b64_tr_b16 v[230:231], v179 offset:11520\n"
    "s_cmp_lt_i32 s49, 9\ns_cbranch_scc1 .Lc38\n.Lc39:\nv_mfma_f32_32x32x16_bf16 v[80:95], v[208:211], v[152:155], v[80:95]\nds_read_b64_tr_b16 v[208:209], v180 offset:0\n"
    "ds_read_b64_tr_b16 v[210:211], v180 offset:256\nv_mfma_f32_32x32x16_bf16 v[80:95], v[212:215], v[156:159], v[80:95]\nds_read_b64_tr_b16 v[212:213], v180 offset:3072\n"
    "ds_read_b64_tr_b16 v[214:215], v180 offset:3328\ns_waitcnt lgkmcnt(6)\nv_mfma_f32_32x32x16_bf16 v[96:111], v[216:219], v[144:147], v[96:111]\ns_cmp_lt_i32 s49, 10\n"
    "s_cbranch_scc1 .Lc41\n.Lc42:\nds_read_b128 v[144:147], v160 offset:4096\nds_read_b64_tr_b16 v[216:217], v180 offset:6144\n"
    "ds_read_b64_tr_b16 v[218:219], v180 offset:6400\nv_mfma_f32_32x32x16_bf16 v[96:111], v[220:223], v[148:151], v[96:111]\nds_read_b128 v[148:151], v161 offset:4096\n"
    "ds_read_b64_tr_b16 v[220:221], v180 offset:9216\nds_read_b64_tr_b16 v[222:223], v180 offset:9472\nv_mfma_f32_32x32x16_bf16 v[96:111], v[224:227], v[152:155], v[96:111]\n"
    "ds_read_b128 v[152:155], v162 offset:4096\nds_read_b64_tr_b16 v[224:225], v180 offset:1024\nds_read_b64_tr_b16 v[226:227], v180 offset:1280\ns_waitcnt lgkmcnt(13)\n"
    "v_mfma_f32_32x32x16_bf16 v[96:111], v[228:231], v[156:159], v[96:111]\nds_read_b128 v[156:159], v163 offset:4096\nds_read_b64_tr_b16 v[228:229], v180 offset:4096\n"
    "ds_read_b64_tr_b16 v[230:231], v180 offset:4352\ns_waitcnt lgkmcnt(11)\nv_dot2c_f32_bf16 v112, v241, v144\nv_dot2c_f32_bf16 v113, v144, v144\n"
    "v_dot2c_f32_bf16 v114, v241, v145\nv_dot2c_f32_bf16 v115, v145, v145\nv_dot2c_f32_bf16 v116, v241, v146\nv_dot2c_f32_bf16 v117, v146, v146\n"
    "v_dot2c_f32_bf16 v118, v241, v147\nv_dot2c_f32_bf16 v119, v147, v147\ns_waitcnt lgkmcnt(8)\nv_dot2c_f32_bf16 v112, v241, v148\nv_dot2c_f32_bf16 v113, v148, v148\n"
    "v_dot2c_f32_bf16 v114, v241, v149\nv_dot2c_f32_bf16 v115, v149, v149\nv_dot2c_f32_bf16 v116, v241, v150\nv_dot2c_f32_bf16 v117, v150, v150\n"
    "v_dot2c_f32_bf16 v118, v241, v151\nv_dot2c_f32_bf16 v119, v151, v151\ns_waitcnt lgkmcnt(5)\nv_dot2c_f32_bf16 v112, v241, v152\nv_dot2c_f32_bf16 v113, v152, v152\n"
    "v_dot2c_f32_bf16 v114, v241, v153\nv_dot2c_f32_bf16 v115, v153, v153\nv_dot2c_f32_bf16 v116, v241, v154\nv_dot2c_f32_bf16 v117, v154, v154\n"
    "v_dot2c_f32_bf16 v118, v241, v155\nv_dot2c_f32_bf16 v119, v155, v155\ns_waitcnt lgkmcnt(2)\nv_dot2c_f32_bf16 v112, v241, v156\nv_dot2c_f32_bf16 v113, v156, v156\n"
    "v_dot2c_f32_bf16 v114, v241, v157\nv_dot2c_f32_bf16 v115, v157, v157\nv_dot2c_f32_bf16 v116, v241, v158\nv_dot2c_f32_bf16 v117, v158, v158\n"
    "v_dot2c_f32_bf16 v118, v241, v159\nv_dot2c_f32_bf16 v119, v159, v159\nv_mfma_f32_32x32x16_bf16 v[64:79], v[208:211], v[144:147], v[64:79]\n"
    "ds_read_b64_tr_b16 v[208:209], v180 offset:7168\nds_read_b64_tr_b16 v[210:211], v180 offset:7424\nds_add_u32 v30, v240\n"
    "v_mfma_f32_32x32x16_bf16 v[64:79], v[212:215], v[148:151], v[64:79]\nds_read_b64_tr_b16 v[212:213], v180 offset:10240\nds_read_b64_tr_b16 v[214:215], v180 offset:10496\n"
    "ds_read_b32 v243, v56\nv_mfma_f32_32x32x16_bf16 v[64:79], v[216:219], v[152:155], v[64:79]\nds_read_b64_tr_b16 v[216:217], v180 offset:2048\n"
    "ds_read_b64_tr_b16 v[218:219], v180 offset:2304\nv_mfma_f32_32x32x16_bf16 v[64:79], v[220:223], v[156:159], v[64:79]\nds_read_b64_tr_b16 v[220:221], v180 offset:5120\n"
    "ds_read_b64_tr_b16 v[222:223], v180 offset:5376\nv_mfma_f32_32x32x16_bf16 v[80:95], v[224:227], v[144:147], v[80:95]\nds_read_b64_tr_b16 v[224:225], v180 offset:8192\n"
    "ds_read_b64_tr_b16 v[226:227], v180 offset:8448\ns_waitcnt lgkmcnt(6)\nv_readfirstlane_b32 s49, v243\n"
    "v_mfma_f32_32x32x16_bf16 v[80:95], v[228:231], v[148:151], v[80:95]\nds_read_b64_tr_b16 v[228:229], v180 offset:11264\nds_read_b64_tr_b16 v[230:231], v180 offset:11520\n"
    "s_cmp_lt_i32 s49, 11\ns_cbranch_scc1 .Lc44\n.Lc45:\nv_mfma_f32_32x32x16_bf16 v[80:95], v[208:211], v[152:155], v[80:95]\nds_read_b64_tr_b16 v[208:209], v181 offset:0\n"
    "ds_read_b64_tr_b16 v[210:211], v181 offset:256\nv_mfma_f32_32x32x16_bf16 v[80:95], v[212:215], v[156:159], v[80:95]\nds_read_b64_tr_b16 v[212:213], v181 offset:3072\n"
    "ds_read_b64_tr_b16 v[214:215], v181 offset:3328\ns_waitcnt lgkmcnt(6)\nv_mfma_f32_32x32x16_bf16 v[96:111], v[216:219], v[144:147], v[96:111]\ns_cmp_lt_i32 s49, 12\n"
    "s_cbranch_scc1 .Lc47\n.Lc48:\nds_read_b128 v[144:147], v160 offset:8192\nds_read_b64_tr_b16 v[216:217], v181 offset:6144\n"
    "ds_read_b64_tr_b16 v[218:219], v181 offset:6400\nv_mfma_f32_32x32x16_bf16 v[96:111], v[220:223], v[148:151], v[96:111]\nds_read_b128 v[148:151], v161 offset:8192\n"
    "ds_read_b64_tr_b16 v[220:221], v181 offset:9216\nds_read_b64_tr_b16 v[222:223], v181 offset:9472\nv_mfma_f32_32x32x16_bf16 v[96:111], v[224:227], v[152:155], v[96:111]\n"
    "ds_read_b128 v[152:155], v162 offset:8192\nds_read_b64_tr_b16 v[224:225], v181 offset:1024\nds_read_b64_tr_b16 v[226:227], v181 offset:1280\ns_waitcnt lgkmcnt(13)\n"
    "v_mfma_f32_32x32x16_bf16 v[96:111], v[228:231], v[156:159], v[96:111]\nds_read_b128 v[156:159], v163 offset:8192\nds_read_b64_tr_b16 v[228:229], v181 offset:4096\n"
    "ds_read_b64_tr_b16 v[230:231], v181 offset:4352\ns_waitcnt lgkmcnt(11)\nv_dot2c_f32_bf16 v112, v241, v144\nv_dot2c_f32_bf16 v113, v144, v144\n"
    "v_dot2c_f32_bf16 v114, v241, v145\nv_dot2c_f32_bf16 v115, v145, v145\nv_dot2c_f32_bf16 v116, v241, v146\nv_dot2c_f32_bf16 v117, v146, v146\n"
    "v_dot2c_f32_bf16 v118, v241, v147\nv_dot2c_f32_bf16 v119, v147, v147\ns_waitcnt lgkmcnt(8)\nv_dot2c_f32_bf16 v112, v241, v148\nv_dot2c_f32_bf16 v113, v148, v148\n"
    "v_dot2c_f32_bf16 v114, v241, v149\nv_dot2c_f32_bf16 v115, v149, v149\nv_dot2c_f32_bf16 v116, v241, v150\nv_dot2c_f32_bf16 v117, v150, v150\n"
    "v_dot2c_f32_bf16 v118, v241, v151\nv_dot2c_f32_bf16 v119, v151, v151\ns_waitcnt lgkmcnt(5)\nv_dot2c_f32_bf16 v112, v241, v152\nv_dot2c_f32_bf16 v113, v152, v152\n"
    "v_dot2c_f32_bf16 v114, v241, v153\nv_dot2c_f32_bf16 v115, v153, v153\nv_dot2c_f32_bf16 v116, v241, v154\nv_dot2c_f32_bf16 v117, v154, v154\n"
    "v_dot2c_f32_bf16 v118, v241, v155\nv_dot2c_f32_bf16 v119, v155, v155\ns_waitcnt lgkmcnt(2)\nv_dot2c_f32_bf16 v112, v241, v156\nv_dot2c_f32_bf16 v113, v156, v156\n"
    "v_dot2c_f32_bf16 v114, v241, v157\nv_dot2c_f32_bf16 v115, v157, v157\nv_dot2c_f32_bf16 v116, v241, v158\nv_dot2c_f32_bf16 v117, v158, v158\n"
    "v_dot2c_f32_bf16 v118, v241, v159\nv_dot2c_f32_bf16 v119, v159, v159\nv_mfma_f32_32x32x16_bf16 v[64:79], v[208:211], v[144:147], v[64:79]\n"
    "ds_read_b64_tr_b16 v[208:209], v181 offset:7168\nds_read_b64_tr_b16 v[210:211], v181 offset:7424\nds_add_u32 v30, v240\n"
    "v_mfma_f32_32x32x16_bf16 v[64:79], v[212:215], v[148:151], v[64:79]\ns_nop 0\ns_cbranch_execz .Lhop\nds_read_b64_tr_b16 v[212:213], v181 offset:10240\n"
    "ds_read_b64_tr_b16 v[214:215], v181 offset:10496\nds_read_b32 v243, v56\nv_mfma_f32_32x32x16_bf16 v[64:79], v[216:219], v[152:155], v[64:79]\n"
    "ds_read_b64_tr_b16 v[216:217], v181 offset:2048\nds_read_b64_tr_b16 v[218:219], v181 offset:2304\nv_mfma_f32_32x32x16_bf16 v[64:79], v[220:223], v[156:159], v[64:79]\n"
    "ds_read_b64_tr_b16 v[220:221], v181 offset:5120\nds_read_b64_tr_b16 v[222:223], v181 offset:5376\nv_mfma_f32_32x32x16_bf16 v[80:95], v[224:227], v[144:147], v[80:95]\n"
    "ds_read_b64_tr_b16 v[224:225], v181 offset:8192\nds_read_b64_tr_b16 v[226:227], v181 offset:8448\ns_waitcnt lgkmcnt(6)\nv_readfirstlane_b32 s49, v243\n"
    "v_mfma_f32_32x32x16_bf16 v[80:95], v[228:231], v[148:151], v[80:95]\nds_read_b64_tr_b16 v[228:229], v181 offset:11264\nds_read_b64_tr_b16 v[230:231], v181 offset:11520\n"
    "s_cmp_lt_i32 s49, 13\ns_cbranch_scc1 .Lc50\n.Lc51:\nv_mfma_f32_32x32x16_bf16 v[80:95], v[208:211], v[152:155], v[80:95]\nds_read_b64_tr_b16 v[208:209], v182 offset:0\n"
    "ds_read_b64_tr_b16 v[210:211], v182 offset:256\nv_mfma_f32_32x32x16_bf16 v[80:95], v[212:215], v[156:159], v[80:95]\nds_read_b64_tr_b16 v[212:213], v182 offset:3072\n"
    "ds_read_b64_tr_b16 v[214:215], v182 offset:3328\ns_waitcnt lgkmcnt(6)\nv_mfma_f32_32x32x16_bf16 v[96:111], v[216:219], v[144:147], v[96:111]\ns_cmp_lt_i32 s49, 14\n"
    "s_cbranch_scc1 .Lc53\n.Lc54:\nds_read_b128 v[144:147], v160 offset:0\nds_read_b64_tr_b16 v[216:217], v182 offset:6144\nds_read_b64_tr_b16 v[218:219], v182 offset:6400\n"
    "v_mfma_f32_32x32x16_bf16 v[96:111], v[220:223], v[148:151], v[96:111]\nds_read_b128 v[148:151], v161 offset:0\nds_read_b64_tr_b16 v[220:221], v182 offset:9216\n"
    "ds_read_b64_tr_b16 v[222:223], v182 offset:9472\nv_mfma_f32_32x32x16_bf16 v[96:111], v[224:227], v[152:155], v[96:111]\nds_read_b128 v[152:155], v162 offset:0\n"
    "ds_read_b64_tr_b16 v[224:225], v182 offset:1024\nds_read_b64_tr_b16 v[226:227], v182 offset:1280\ns_waitcnt lgkmcnt(13)\n"
    "v_mfma_f32_32x32x16_bf16 v[96:111], v[228:231], v[156:159], v[96:111]\nds_read_b128 v[156:159], v163 offset:0\nds_read_b64_tr_b16 v[228:229], v182 offset:4096\n"
    "ds_read_b64_tr_b16 v[230:231], v182 offset:4352\ns_waitcnt lgkmcnt(11)\nv_dot2c_f32_bf16 v112, v241, v144\nv_dot2c_f32_bf16 v113, v144, v144\n"
    "v_dot2c_f32_bf16 v114, v241, v145\nv_dot2c_f32_bf16 v115, v145, v145\nv_dot2c_f32_bf16 v116, v241, v146\nv_dot2c_f32_bf16 v117, v146, v146\n"
    "v_dot2c_f32_bf16 v118, v241, v147\nv_dot2c_f32_bf16 v119, v147, v147\ns_waitcnt lgkmcnt(8)\nv_dot2c_f32_bf16 v112, v241, v148\nv_dot2c_f32_bf16 v113, v148, v148\n"
    "v_dot2c_f32_bf16 v114, v241, v149\nv_dot2c_f32_bf16 v115, v149, v149\nv_dot2c_f32_bf16 v116, v241, v150\nv_dot2c_f32_bf16 v117, v150, v150\n"
    "v_dot2c_f32_bf16 v118, v241, v151\nv_dot2c_f32_bf16 v119, v151, v151\ns_waitcnt lgkmcnt(5)\nv_dot2c_f32_bf16 v112, v241, v152\nv_dot2c_f32_bf16 v113, v152, v152\n"
    "v_dot2c_f32_bf16 v114, v241, v153\nv_dot2c_f32_bf16 v115, v153, v153\nv_dot2c_f32_bf16 v116, v241, v154\nv_dot2c_f32_bf16 v117, v154, v154\n"
    "v_dot2c_f32_bf16 v118, v241, v155\nv_dot2c_f32_bf16 v119, v155, v155\ns_waitcnt lgkmcnt(2)\nv_dot2c_f32_bf16 v112, v241, v156\nv_dot2c_f32_bf16 v113, v156, v156\n"
    "v_dot2c_f32_bf16 v114, v241, v157\nv_dot2c_f32_bf16 v115, v157, v157\nv_dot2c_f32_bf16 v116, v241, v158\nv_dot2c_f32_bf16 v117, v158, v158\n"
    "v_dot2c_f32_bf16 v118, v241, v159\nv_dot2c_f32_bf16 v119, v159, v159\nv_mfma_f32_32x32x16_bf16 v[64:79], v[208:211], v[144:147], v[64:79]\n"
    "ds_read_b64_tr_b16 v[208:209], v182 offset:7168\nds_read_b64_tr_b16 v[210:211], v182 offset:7424\nds_add_u32 v30, v240\n"
    "v_mfma_f32_32x32x16_bf16 v[64:79], v[212:215], v[148:151], v[64:79]\nds_read_b64_tr_b16 v[212:213], v182 offset:10240\nds_read_b64_tr_b16 v[214:215], v182 offset:10496\n"
    "ds_read_b32 v243, v56\nv_mfma_f32_32x32x16_bf16 v[64:79], v[216:219], v[152:155], v[64:79]\nds_read_b64_tr_b16 v[216:217], v182 offset:2048\n"
    "ds_read_b64_tr_b16 v[218:219], v182 offset:2304\nv_mfma_f32_32x32x16_bf16 v[64:79], v[220:223], v[156:159], v[64:79]\nds_read_b64_tr_b16 v[220:221], v182 offset:5120\n"
    "ds_read_b64_tr_b16 v[222:223], v182 offset:5376\nv_mfma_f32_32x32x16_bf16 v[80:95], v[224:227], v[144:147], v[80:95]\nds_read_b64_tr_b16 v[224:225], v182 offset:8192\n"
    "ds_read_b64_tr_b16 v[226:227], v182 offset:8448\ns_waitcnt lgkmcnt(6)\nv_readfirstlane_b32 s49, v243\n"
    "v_mfma_f32_32x32x16_bf16 v[80:95], v[228:231], v[148:151], v[80:95]\nds_read_b64_tr_b16 v[228:229], v182 offset:11264\nds_read_b64_tr_b16 v[230:231], v182 offset:11520\n"
    "s_cmp_lt_i32 s49, 15\ns_cbranch_scc1 .Lc56\n.Lc57:\nv_mfma_f32_32x32x16_bf16 v[80:95], v[208:211], v[152:155], v[80:95]\nds_read_b64_tr_b16 v[208:209], v183 offset:0\n"
    "ds_read_b64_tr_b16 v[210:211], v183 offset:256\nv_mfma_f32_32x32x16_bf16 v[80:95], v[212:215], v[156:159], v[80:95]\nds_read_b64_tr_b16 v[212:213], v183 offset:3072\n"
    "ds_read_b64_tr_b16 v[214:215], v183 offset:3328\ns_waitcnt lgkmcnt(6)\nv_mfma_f32_32x32x16_bf16 v[96:111], v[216:219], v[144:147], v[96:111]\ns_cmp_lt_i32 s49, 16\n"
    "s_cbranch_scc1 .Lc59\n.Lc60:\nds_read_b128 v[144:147], v160 offset:4096\nds_read_b64_tr_b16 v[216:217], v183 offset:6144\n"
    "ds_read_b64_tr_b16 v[218:219], v183 offset:6400\nv_mfma_f32_32x32x16_bf16 v[96:111], v[220:223], v[148:151], v[96:111]\nds_read_b128 v[148:151], v161 offset:4096\n"
    "ds_read_b64_tr_b16 v[220:221], v183 offset:9216\nds_read_b64_tr_b16 v[222:223], v183 offset:9472\nv_mfma_f32_32x32x16_bf16 v[96:111], v[224:227], v[152:155], v[96:111]\n"
    "ds_read_b128 v[152:155], v162 offset:4096\nds_read_b64_tr_b16 v[224:225], v183 offset:1024\nds_read_b64_tr_b16 v[226:227], v183 offset:1280\ns_waitcnt lgkmcnt(13)\n"
    "v_mfma_f32_32x32x16_bf16 v[96:111], v[228:231], v[156:159], v[96:111]\nds_read_b128 v[156:159], v163 offset:4096\nds_read_b64_tr_b16 v[228:229], v183 offset:4096\n"
    "ds_read_b64_tr_b16 v[230:231], v183 offset:4352\ns_waitcnt lgkmcnt(11)\nv_dot2c_f32_bf16 v112, v241, v144\nv_dot2c_f32_bf16 v113, v144, v144\n"
    "v_dot2c_f32_bf16 v114, v241, v145\nv_dot2c_f32_bf16 v115, v145, v145\nv_dot2c_f32_bf16 v116, v241, v146\nv_dot2c_f32_bf16 v117, v146, v146\n"
    "v_dot2c_f32_bf16 v118, v241, v147\nv_dot2c_f32_bf16 v119, v147, v147\ns_waitcnt lgkmcnt(8)\nv_dot2c_f32_bf16 v112, v241, v148\nv_dot2c_f32_bf16 v113, v148, v148\n"
    "v_dot2c_f32_bf16 v114, v241, v149\nv_dot2c_f32_bf16 v115, v149, v149\nv_dot2c_f32_bf16 v116, v241, v150\nv_dot2c_f32_bf16 v117, v150, v150\n"
    "v_dot2c_f32_bf16 v118, v241, v151\nv_dot2c_f32_bf16 v119, v151, v151\ns_waitcnt lgkmcnt(5)\nv_dot2c_f32_bf16 v112, v241, v152\nv_dot2c_f32_bf16 v113, v152, v152\n"
    "v_dot2c_f32_bf16 v114, v241, v153\nv_dot2c_f32_bf16 v115, v153, v153\nv_dot2c_f32_bf16 v116, v241, v154\nv_dot2c_f32_bf16 v117, v154, v154\n"
    "v_dot2c_f32_bf16 v118, v241, v155\nv_dot2c_f32_bf16 v119, v155, v155\ns_waitcnt lgkmcnt(2)\nv_dot2c_f32_bf16 v112, v241, v156\nv_dot2c_f32_bf16 v113, v156, v156\n"
    "v_dot2c_f32_bf16 v114, v241, v157\nv_dot2c_f32_bf16 v115, v157, v157\nv_dot2c_f32_bf16 v116, v241, v158\nv_dot2c_f32_bf16 v117, v158, v158\n"
    "v_dot2c_f32_bf16 v118, v241, v159\nv_dot2c_f32_bf16 v119, v159, v159\nv_mfma_f32_32x32x16_bf16 v[64:79], v[208:211], v[144:147], v[64:79]\n"
    "ds_read_b64_tr_b16 v[208:209], v183 offset:7168\nds_read_b64_tr_b16 v[210:211], v183 offset:7424\nds_add_u32 v30, v240\ns_nop 0\ns_cbranch_execz .Lhop\n"
    "v_mfma_f32_32x32x16_bf16 v[64:79], v[212:215], v[148:151], v[64:79]\nds_read_b64_tr_b16 v[212:213], v183 offset:10240\nds_read_b64_tr_b16 v[214:215], v183 offset:10496\n"
    "v_add_f32 v112, v112, v114\nv_add_f32 v113, v113, v115\nv_add_f32 v116, v116, v118\nv_add_f32 v117, v117, v119\n"
    "v_mfma_f32_32x32x16_bf16 v[64:79], v[216:219], v[152:155], v[64:79]\nds_read_b64_tr_b16 v[216:217], v183 offset:2048\nds_read_b64_tr_b16 v[218:219], v183 offset:2304\n"
    "v_add_f32 v112, v112, v116\nv_add_f32 v113, v113, v117\nv_mfma_f32_32x32x16_bf16 v[64:79], v[220:223], v[156:159], v[64:79]\nds_read_b32 v243, v28\n"
    "ds_read_b32 v124, v38 offset:0\nds_read_b32 v128, v38 offset:128\nds_read_b32 v132, v38 offset:256\nds_read_b64_tr_b16 v[220:221], v183 offset:5120\n"
    "ds_read_b64_tr_b16 v[222:223], v183 offset:5376\nv_permlane32_swap_b32 v112, v113\nv_add_f32 v138, v112, v113\nv_add_f32 v139, v112, v113\n"
    "v_mfma_f32_32x32x16_bf16 v[80:95], v[224:227], v[144:147], v[80:95]\nds_read_b64_tr_b16 v[224:225], v183 offset:8192\nds_read_b64_tr_b16 v[226:227], v183 offset:8448\n"
    "v_permlane32_swap_b32 v138, v139\nv_mul_f32 v138, s81, v138\nv_mul_f32 v112, v138, v138\nv_cvt_pk_bf16_f32 v138, -v138, -v138\ns_waitcnt lgkmcnt(12)\n"
    "v_mfma_f32_32x32x16_bf16 v[80:95], v[228:231], v[148:151], v[80:95]\nds_read_b64_tr_b16 v[228:229], v183 offset:11264\nds_read_b64_tr_b16 v[230:231], v183 offset:11520\n"
    "v_cndmask_b32 v120, 0, v138, s[84:85]\nv_fma_f32 v139, v139, s81, -v112\nv_max_f32 v139, 0, v139\nv_cvt_pk_bf16_f32 v139, 0, v139\n"
    "v_mfma_f32_32x32x16_bf16 v[80:95], v[208:211], v[152:155], v[80:95]\ns_waitcnt lgkmcnt(6)\nv_cmp_gt_i32 vcc, v29, v243\nv_add_f32 v139, 0x3727c5ac, v139\n"
    "v_cvt_pk_bf16_f32 v139, 0, v139\nv_sqrt_f32 v139, v139\nv_mfma_f32_32x32x16_bf16 v[80:95], v[212:215], v[156:159], v[80:95]\nv_cvt_pk_bf16_f32 v139, 0, v139\n"
    "v_rcp_f32 v136, v139\nv_mov_b64 v[232:233], 0\nv_mov_b64 v[234:235], 0\ns_cbranch_vccnz .Lt62\n.Lt63:\n"
    "v_mfma_f32_32x32x16_bf16 v[64:79], v[124:127], v[120:123], v[64:79]\nv_mov_b32 v137, v136\nv_mov_b64 v[236:237], 0\nv_mov_b64 v[238:239], 0\nv_mov_b64 v[244:245], 0\n"
    "v_mov_b64 v[246:247], 0\nv_mfma_f32_32x32x16_bf16 v[80:95], v[128:131], v[120:123], v[80:95]\nv_mov_b64 v[248:249], 0\nv_mov_b64 v[250:251], 0\n"
    "s_mov_b32 s44, 0x00ff00ff\ns_mov_b32 s45, 0xff00ff00\ns_mov_b32 s46, s44\ns_mov_b32 s47, s44\ns_waitcnt lgkmcnt(0)\n"
    "v_mfma_f32_32x32x16_bf16 v[96:111], v[216:219], v[144:147], v[96:111]\nv_mul_f32 v64, v64, v136\nv_mul_f32 v65, v65, v136\nv_mul_f32 v66, v66, v136\n"
    "v_mul_f32 v67, v67, v136\nv_cvt_pk_bf16_f32 v64, v64, v65\nv_cvt_pk_bf16_f32 v65, v66, v67\nds_write_b64 v37, v[64:65] offset:0\n"
    "v_mfma_f32_32x32x16_bf16 v[96:111], v[220:223], v[148:151], v[96:111]\nv_mul_f32 v68, v68, v136\nv_mul_f32 v69, v69, v136\nv_mul_f32 v70, v70, v136\n"
    "v_mul_f32 v71, v71, v136\nv_cvt_pk_bf16_f32 v68, v68, v69\nv_cvt_pk_bf16_f32 v69, v70, v71\nds_write_b64 v37, v[68:69] offset:16\n"
    "v_mfma_f32_32x32x16_bf16 v[96:111], v[224:227], v[152:155], v[96:111]\nv_mul_f32 v72, v72, v136\nv_mul_f32 v73, v73, v136\nv_mul_f32 v74, v74, v136\n"
    "v_mul_f32 v75, v75, v136\nv_cvt_pk_bf16_f32 v72, v72, v73\nv_cvt_pk_bf16_f32 v73, v74, v75\nds_write_b64 v37, v[72:73] offset:32\n"
    "v_mfma_f32_32x32x16_bf16 v[96:111], v[228:231], v[156:159], v[96:111]\nv_mul_f32 v76, v76, v136\nv_mul_f32 v77, v77, v136\nv_mul_f32 v78, v78, v136\n"
    "v_mul_f32 v79, v79, v136\nv_cvt_pk_bf16_f32 v76, v76, v77\nv_cvt_pk_bf16_f32 v77, v78, v79\nds_write_b64 v37, v[76:77] offset:48\n"
    "v_mfma_f32_32x32x16_bf16 v[96:111], v[132:135], v[120:123], v[96:111]\nv_pk_mul_f32 v[80:81], v[80:81], v[136:137]\nv_pk_mul_f32 v[82:83], v[82:83], v[136:137]\n"
    "v_cvt_pk_bf16_f32 v80, v80, v81\nv_cvt_pk_bf16_f32 v81, v82, v83\nds_write_b64 v37, v[80:81] offset:64\nv_pk_mul_f32 v[84:85], v[84:85], v[136:137]\n"
    "v_pk_mul_f32 v[86:87], v[86:87], v[136:137]\nv_cvt_pk_bf16_f32 v84, v84, v85\nv_cvt_pk_bf16_f32 v85, v86, v87\nds_write_b64 v37, v[84:85] offset:80\n"
    "v_pk_mul_f32 v[88:89], v[88:89], v[136:137]\nv_pk_mul_f32 v[90:91], v[90:91], v[136:137]\nv_cvt_pk_bf16_f32 v88, v88, v89\nv_cvt_pk_bf16_f32 v89, v90, v91\n"
    "ds_write_b64 v37, v[88:89] offset:96\nv_pk_mul_f32 v[92:93], v[92:93], v[136:137]\nv_pk_mul_f32 v[94:95], v[94:95], v[136:137]\nv_cvt_pk_bf16_f32 v92, v92, v93\n"
    "v_cvt_pk_bf16_f32 v93, v94, v95\nds_write_b64 v37, v[92:93] offset:112\nv_pk_mul_f32 v[96:97], v[96:97], v[136:137]\nv_pk_mul_f32 v[98:99], v[98:99], v[136:137]\n"
    "v_cvt_pk_bf16_f32 v96, v96, v97\nv_cvt_pk_bf16_f32 v97, v98, v99\nds_write_b64 v37, v[96:97] offset:128\nv_pk_mul_f32 v[100:101], v[100:101], v[136:137]\n"
    "v_pk_mul_f32 v[102:103], v[102:103], v[136:137]\nv_cvt_pk_bf16_f32 v100, v100, v101\nv_cvt_pk_bf16_f32 v101, v102, v103\nds_write_b64 v37, v[100:101] offset:144\n"
    "v_pk_mul_f32 v[104:105], v[104:105], v[136:137]\nv_pk_mul_f32 v[106:107], v[106:107], v[136:137]\nv_cvt_pk_bf16_f32 v104, v104, v105\nv_cvt_pk_bf16_f32 v105, v106, v107\n"
    "ds_write_b64 v37, v[104:105] offset:160\nv_pk_mul_f32 v[108:109], v[108:109], v[136:137]\nv_pk_mul_f32 v[110:111], v[110:111], v[136:137]\n"
    "v_cvt_pk_bf16_f32 v108, v108, v109\nv_cvt_pk_bf16_f32 v109, v110, v111\nds_write_b64 v37, v[108:109] offset:176\ns_branch .Latt12\n.Lc20:\ns_mov_b32 s86, 0x100000\n"
    ".Lc22:\nds_read_b32 v243, v56\ns_waitcnt lgkmcnt(0)\nv_readfirstlane_b32 s49, v243\ns_sub_u32 s86, s86, 1\ns_cbranch_scc1 .Lc21\ns_cmp_ge_i32 s49, 3\n"
    "s_cbranch_scc1 .Lc21\ns_branch .Lc22\n.Lc23:\ns_mov_b32 s86, 0x100000\n.Lc25:\nds_read_b32 v243, v56\ns_waitcnt lgkmcnt(0)\nv_readfirstlane_b32 s49, v243\n"
    "s_sub_u32 s86, s86, 1\ns_cbranch_scc1 .Lc24\ns_cmp_ge_i32 s49, 4\ns_cbranch_scc1 .Lc24\ns_branch .Lc25\n.Lc26:\ns_mov_b32 s86, 0x100000\n.Lc28:\nds_read_b32 v243, v56\n"
    "s_waitcnt lgkmcnt(0)\nv_readfirstlane_b32 s49, v243\ns_sub_u32 s86, s86, 1\ns_cbranch_execz .Lhop\ns_cbranch_scc1 .Lc27\ns_cmp_ge_i32 s49, 5\ns_cbranch_scc1 .Lc27\n"
    "s_branch .Lc28\n.Lc29:\ns_mov_b32 s86, 0x100000\n.Lc31:\nds_read_b32 v243, v56\ns_waitcnt lgkmcnt(0)\nv_readfirstlane_b32 s49, v243\ns_sub_u32 s86, s86, 1\n"
    "s_cbranch_scc1 .Lc30\ns_cmp_ge_i32 s49, 6\ns_cbranch_scc1 .Lc30\ns_branch .Lc31\n.Lc32:\ns_mov_b32 s86, 0x100000\n.Lc34:\nds_read_b32 v243, v56\ns_waitcnt lgkmcnt(0)\n"
    "v_readfirstlane_b32 s49, v243\ns_sub_u32 s86, s86, 1\ns_cbranch_scc1 .Lc33\ns_cmp_ge_i32 s49, 7\ns_cbranch_scc1 .Lc33\ns_branch .Lc34\n.Lc35:\ns_mov_b32 s86, 0x100000\n"
    ".Lc37:\nds_read_b32 v243, v56\ns_waitcnt lgkmcnt(0)\nv_readfirstlane_b32 s49, v243\ns_sub_u32 s86, s86, 1\ns_cbranch_scc1 .Lc36\ns_cmp_ge_i32 s49, 8\n"
    "s_cbranch_scc1 .Lc36\ns_branch .Lc37\n.Lc38:\ns_mov_b32 s86, 0x100000\n.Lc40:\nds_read_b32 v243, v56\ns_waitcnt lgkmcnt(0)\nv_readfirstlane_b32 s49, v243\n"
    "s_sub_u32 s86, s86, 1\ns_cbranch_scc1 .Lc39\ns_cmp_ge_i32 s49, 9\ns_cbranch_scc1 .Lc39\ns_branch .Lc40\n.Lc41:\ns_mov_b32 s86, 0x100000\n.Lc43:\nds_read_b32 v243, v56\n"
    "s_waitcnt lgkmcnt(0)\nv_readfirstlane_b32 s49, v243\ns_sub_u32 s86, s86, 1\ns_cbranch_scc1 .Lc42\ns_cmp_ge_i32 s49, 10\ns_cbranch_scc1 .Lc42\ns_branch .Lc43\n.Lc44:\n"
    "s_mov_b32 s86, 0x100000\n.Lc46:\nds_read_b32 v243, v56\ns_waitcnt lgkmcnt(0)\nv_readfirstlane_b32 s49, v243\ns_sub_u32 s86, s86, 1\ns_cbranch_scc1 .Lc45\n"
    "s_cmp_ge_i32 s49, 11\ns_cbranch_scc1 .Lc45\ns_branch .Lc46\n.Lc47:\ns_mov_b32 s86, 0x100000\n.Lc49:\nds_read_b32 v243, v56\ns_waitcnt lgkmcnt(0)\n"
    "v_readfirstlane_b32 s49, v243\ns_sub_u32 s86, s86, 1\ns_cbranch_scc1 .Lc48\ns_cmp_ge_i32 s49, 12\ns_cbranch_scc1 .Lc48\ns_branch .Lc49\n.Lc50:\ns_mov_b32 s86, 0x100000\n"
    ".Lc52:\nds_read_b32 v243, v56\ns_waitcnt lgkmcnt(0)\nv_readfirstlane_b32 s49, v243\ns_sub_u32 s86, s86, 1\ns_cbranch_scc1 .Lc51\ns_cmp_ge_i32 s49, 13\n"
    "s_cbranch_scc1 .Lc51\ns_branch .Lc52\n.Lc53:\ns_mov_b32 s86, 0x100000\n.Lc55:\nds_read_b32 v243, v56\ns_waitcnt lgkmcnt(0)\nv_readfirstlane_b32 s49, v243\n"
    "s_sub_u32 s86, s86, 1\ns_cbranch_scc1 .Lc54\ns_cmp_ge_i32 s49, 14\ns_cbranch_scc1 .Lc54\ns_branch .Lc55\n.Lc56:\ns_mov_b32 s86, 0x100000\n.Lc58:\nds_read_b32 v243, v56\n"
    "s_waitcnt lgkmcnt(0)\nv_readfirstlane_b32 s49, v243\ns_sub_u32 s86, s86, 1\ns_cbranch_scc1 .Lc57\ns_cmp_ge_i32 s49, 15\ns_cbranch_scc1 .Lc57\ns_branch .Lc58\n.Lc59:\n"
    "s_mov_b32 s86, 0x100000\n.Lc61:\nds_read_b32 v243, v56\ns_waitcnt lgkmcnt(0)\nv_readfirstlane_b32 s49, v243\ns_sub_u32 s86, s86, 1\ns_cbranch_scc1 .Lc60\n"
    "s_cmp_ge_i32 s49, 16\ns_cbranch_scc1 .Lc60\ns_branch .Lc61\n.Lt62:\ns_mov_b32 s86, 0x100000\n.Ls64:\nds_read_b32 v243, v28 offset:0\ns_waitcnt lgkmcnt(0)\n"
    "v_cmp_gt_i32 vcc, v29, v243\ns_sub_u32 s86, s86, 1\ns_cbranch_scc1 .Ls65\ns_cbranch_vccz .Ls65\ns_branch .Ls64\n.Ls65:\nds_read_b32 v124, v38 offset:0\n"
    "ds_read_b32 v128, v38 offset:128\nds_read_b32 v132, v38 offset:256\ns_waitcnt lgkmcnt(0)\ns_branch .Lt63\n"
    ".Lhpre66:\nv_and_b32 v224, 127, v0\ns_getpc_b64 s[44:45]\ns_add_u32 s44, s44, g_tabs@rel32@lo+4\ns_addc_u32 s45, s45, g_tabs@rel32@hi+12\nv_lshlrev_b32 v224, 4, v224\n"
    "global_load_dwordx4 v[48:51], v224, s[44:45] offset:2064\nglobal_load_dwordx4 v[52:55], v224, s[44:45]\nglobal_load_dwordx2 v[62:63], v224, s[44:45] offset:16\n"
    "s_lshl_b32 s43, s22, 17\ns_lshl_b32 s42, s13, 15\ns_add_u32 s44, s43, s42\ns_add_u32 s44, s44, 0xe01000\nv_and_b32 v231, 63, v0\nv_lshlrev_b32 v59, 4, v231\n"
    "s_add_u32 s68, s4, s44\ns_addc_u32 s69, s5, 0\ns_add_u32 s70, s68, 8192\ns_addc_u32 s71, s69, 0\ns_add_u32 s72, s68, 16384\ns_addc_u32 s73, s69, 0\n"
    "s_add_u32 s74, s68, 24576\ns_addc_u32 s75, s69, 0\nv_bfe_u32 v251, v0, 5, 1\nv_and_b32 v250, 31, v0\ns_lshl_b32 s43, s13, 11\ns_add_u32 s42, s43, 0x1d000\n"
    "v_lshl_add_u32 v40, v251, 8, s42\nv_lshl_add_u32 v40, v250, 2, v40\nv_and_b32 v249, 63, v0\nv_lshl_add_u32 v41, v249, 4, s42\ns_mov_b32 s41, 0x1f600\n"
    "v_lshl_add_u32 v42, v249, 4, s41\nv_bfe_u32 v248, v0, 3, 3\nv_and_b32 v247, 7, v0\ns_lshl_b32 s40, s22, 19\ns_lshl_b32 s39, s13, 17\ns_lshl_b32 s38, s21, 7\n"
    "s_add_u32 s37, s12, s40\ns_add_u32 s37, s37, s39\ns_add_u32 s37, s37, s38\ns_add_u32 s37, s37, 0x200000\nv_lshl_add_u32 v43, v248, 13, s37\n"
    "v_lshl_add_u32 v43, v247, 4, v43\ns_add_u32 s36, s12, s40\ns_add_u32 s36, s36, s39\ns_add_u32 s36, s36, s38\ns_add_u32 s36, s36, 0x210000\n"
    "v_lshl_add_u32 v44, v248, 13, s36\nv_lshl_add_u32 v44, v247, 4, v44\ns_lshl_b32 s35, s13, 8\ns_add_u32 s34, s35, 0x27600\nv_lshl_add_u32 v45, v249, 2, s34\n"
    "s_add_u32 s33, s43, 0x1d400\nv_lshl_add_u32 v46, v251, 8, s33\nv_lshl_add_u32 v46, v250, 2, v46\nv_and_b32 v251, 31, v0\nv_bfe_u32 v250, v0, 5, 1\n"
    "s_lshl_b32 s43, s21, 15\ns_lshl_b32 s42, s22, 8\ns_lshl_b32 s41, s13, 6\ns_add_u32 s40, s43, s42\ns_add_u32 s40, s40, s41\nv_lshl_add_u32 v47, v251, 10, s40\n"
    "v_lshl_add_u32 v47, v250, 4, v47\nv_and_b32 v249, 63, v0\ns_lshr_b32 s39, s13, 1\ns_lshl_b32 s38, s39, 13\ns_and_b32 s37, s13, 1\ns_lshl_b32 s36, s37, 11\n"
    "s_add_u32 s35, s38, s36\ns_add_u32 s35, s35, 0x1f600\nv_lshl_add_u32 v60, v249, 4, s35\nv_and_b32 v248, 1, v0\ns_lshl_b32 s34, s39, 3\ns_add_u32 s33, s34, 0x27fc0\n"
    "v_lshl_add_u32 v61, v248, 2, s33\ns_cbranch_execz .Lhop\ns_add_u32 s32, s34, 0x27d80\nv_mov_b32 v252, s32\nv_and_b32 v247, 0x7f, v0\ns_mul_i32 s31, s39, 0x2100\n"
    "s_add_u32 s30, s31, 0x18000\nv_lshl_add_u32 v253, v247, 4, s30\ns_lshr_b32 s43, s13, 1\ns_mul_i32 s42, s43, 0x600\ns_add_u32 s41, s42, 0x1c200\nv_mov_b32 v254, s41\n"
    "s_lshl_b32 s40, s13, 2\ns_add_u32 s39, s40, 0x27f80\nv_mov_b32 v25, s39\nv_mov_b64 v[232:233], 0\nv_mov_b64 v[234:235], 0\nv_mov_b64 v[236:237], 0\n"
    "v_mov_b64 v[238:239], 0\nv_mov_b64 v[244:245], 0\nv_mov_b64 v[246:247], 0\nv_mov_b64 v[248:249], 0\nv_mov_b64 v[250:251], 0\ns_setpc_b64 s[94:95]\n.Lhcon67_0:\n"
    "s_add_u32 s43, s12, 0x400000\nv_lshl_add_u32 v16, v0, 4, s43\ns_lshl_b32 s42, s18, 13\ns_add_u32 s41, s12, s42\ns_add_u32 s41, s41, 0x200000\n"
    "v_lshl_add_u32 v17, v0, 4, s41\nv_lshrrev_b32 v231, 3, v0\nv_mul_u32_u24 v230, 0x50, v231\nv_and_b32 v229, 7, v0\nv_add_u32 v18, 0x1d550, v230\n"
    "v_lshl_add_u32 v18, v229, 3, v18\nv_bfe_u32 v228, v0, 4, 3\nv_xor_b32 v227, v0, v228\ns_mov_b32 s40, 0x19400\nv_lshl_add_u32 v19, v227, 3, s40\nv_bfe_u32 v226, v0, 4, 1\n"
    "v_xor_b32 v225, v0, v226\ns_mov_b32 s39, 0x17c00\nv_lshl_add_u32 v20, v225, 3, s39\ns_lshr_b32 s43, s12, 23\ns_add_u32 s42, s43, 3\nv_lshrrev_b32 v231, s42, v0\n"
    "s_lshr_b32 s41, s12, 22\ns_sub_u32 s40, 15, s41\nv_lshlrev_b32 v230, s40, v231\ns_lshr_b32 s39, s12, 20\ns_add_u32 s38, s39, 7\nv_and_b32 v229, s38, v0\n"
    "s_lshl_b32 s37, s18, 7\ns_add_u32 s36, s37, 0x600000\ns_lshl_b32 s35, s24, 18\ns_lshl_b32 s34, s25, 8\ns_add_u32 s33, s35, s34\ns_add_u32 s33, s33, 0x1200000\n"
    "s_cmp_lt_u32 s12, 1\ns_cselect_b32 s32, s36, s33\nv_add_u32 v21, s32, v230\nv_lshl_add_u32 v21, v229, 4, v21\nv_and_b32 v231, 63, v0\nv_lshlrev_b32 v230, 4, v231\n"
    "v_mov_b32 v229, 0xfc0\ns_lshr_b32 s43, s13, 1\ns_mul_i32 s42, s43, 26\nv_add_u32 v228, s42, v231\nv_cmp_gt_u32 vcc, 64, v228\ns_nop 1\n"
    "v_cndmask_b32 v227, v229, v230, vcc\ns_lshr_b32 s41, s3, 2\ns_mul_i32 s40, s41, 0x1950\ns_mul_i32 s39, s13, 0x150\ns_add_u32 s38, s40, s39\ns_add_u32 s38, s38, 0x1d000\n"
    "s_mul_i32 s37, s41, 0x3180\ns_and_b32 s36, s13, 1\ns_mul_i32 s35, s36, 0xfc0\ns_add_u32 s34, s37, s35\ns_add_u32 s34, s34, 0x20300\ns_cmp_lt_u32 s13, 2\n"
    "s_cselect_b32 s33, s38, s34\nv_add_u32 v22, s33, v227\nv_and_b32 v226, 31, v0\nv_sub_u32 v225, 0x3ff, v226\nv_bfe_u32 v224, v0, 5, 1\nv_lshl_add_u32 v175, v224, 3, v225\n"
    "v_and_b32 v174, 3, v175\nv_mul_u32_u24 v173, 0x840, v174\nv_lshrrev_b32 v172, 2, v175\ns_mul_i32 s32, s43, 0x2100\ns_add_u32 s31, s32, 0x17800\nv_add_u32 v23, s31, v173\n"
    "v_lshl_add_u32 v23, v172, 3, v23\ns_lshr_b32 s43, s12, 23\ns_add_u32 s42, s43, 4\nv_mov_b32 v26, s42\ns_setpc_b64 s[94:95]\n.Lhcon67_1:\nv_and_b32 v231, 63, v0\n"
    "v_add_u32 v230, 3, v231\nv_lshrrev_b32 v229, 2, v230\nv_mul_u32_u24 v228, 0x50, v229\nv_and_b32 v227, 3, v230\ns_lshr_b32 s43, s13, 1\ns_mul_i32 s42, s43, 0x1950\n"
    "s_add_u32 s41, s42, 0x1d500\nv_add_u32 v10, s41, v228\nv_lshl_add_u32 v10, v227, 4, v10\nv_add_u32 v226, 2, v231\nv_lshrrev_b32 v225, 2, v226\n"
    "v_mul_u32_u24 v224, 0x50, v225\nv_and_b32 v175, 3, v226\ns_add_u32 s40, s42, 0x1d508\nv_add_u32 v11, s40, v224\nv_lshl_add_u32 v11, v175, 4, v11\n"
    "v_bfe_u32 v174, v0, 2, 4\nv_mul_u32_u24 v173, 0x50, v174\nv_and_b32 v172, 3, v0\ns_add_u32 s39, s42, 0x1d550\nv_add_u32 v12, s39, v173\nv_lshl_add_u32 v12, v172, 4, v12\n"
    "s_and_b32 s38, s13, 1\nv_add_u32 v171, 1, v231\nv_lshrrev_b32 v170, 5, v171\nv_mul_u32_u24 v169, 0x240, v170\nv_and_b32 v168, 31, v171\ns_mul_i32 s37, s43, 0x3180\n"
    "s_add_u32 s36, s37, 0x20540\nv_add_u32 v167, s36, v169\nv_lshl_add_u32 v167, v168, 4, v167\nv_lshrrev_b32 v166, 6, v171\nv_mul_u32_u24 v165, 0x480, v166\n"
    "v_and_b32 v164, 63, v171\ns_add_u32 s35, s37, 0x22280\nv_add_u32 v163, s35, v165\nv_lshl_add_u32 v163, v164, 4, v163\ns_cmp_lt_u32 s38, 1\ns_cselect_b64 vcc, -1, 0\n"
    "v_cndmask_b32 v13, v163, v167, vcc\ns_and_b32 s43, s13, 1\nv_and_b32 v231, 63, v0\nv_add_u32 v230, 2, v231\nv_lshrrev_b32 v229, 5, v230\nv_mul_u32_u24 v228, 0x240, v229\n"
    "v_and_b32 v227, 31, v230\ns_lshr_b32 s42, s13, 1\ns_mul_i32 s41, s42, 0x3180\ns_add_u32 s40, s41, 0x20540\nv_add_u32 v226, s40, v228\nv_lshl_add_u32 v226, v227, 4, v226\n"
    "s_add_u32 s39, s41, 0x22b80\nv_lshl_add_u32 v225, v231, 4, s39\ns_cmp_lt_u32 s43, 1\ns_cselect_b64 vcc, -1, 0\nv_cndmask_b32 v14, v225, v226, vcc\n"
    "v_add_u32 v224, 1, v231\nv_lshrrev_b32 v175, 5, v224\nv_mul_u32_u24 v174, 0x240, v175\nv_and_b32 v173, 31, v224\nv_add_u32 v172, s40, v174\n"
    "v_lshl_add_u32 v172, v173, 4, v172\nv_lshrrev_b32 v171, 6, v224\nv_mul_u32_u24 v170, 0x480, v171\nv_and_b32 v169, 63, v224\ns_add_u32 s38, s41, 0x22280\n"
    "v_add_u32 v168, s38, v170\nv_lshl_add_u32 v168, v169, 4, v168\ns_cmp_lt_u32 s43, 1\ns_cselect_b64 vcc, -1, 0\nv_cndmask_b32 v167, v168, v172, vcc\n"
    "s_mov_b32 s37, 0xfffffb40\ns_cmp_lt_u32 s43, 1\ns_cselect_b32 s36, s37, 0x1f00\ns_add_u32 s35, s41, s36\ns_add_u32 s35, s35, 0x20300\nv_mov_b32 v166, s35\n"
    "v_cmp_gt_u32 vcc, 64, v224\ns_nop 1\nv_cndmask_b32 v15, v166, v167, vcc\ns_add_u32 s34, s41, 0x1fe40\nv_mov_b32 v165, s34\nv_cmp_gt_u32 vcc, 64, v230\ns_nop 1\n"
    "v_cndmask_b32 v35, v165, v226, vcc\ns_lshl_b32 s33, s18, 13\ns_cbranch_execz .Lhop\ns_lshl_b32 s32, s13, 11\ns_add_u32 s31, s33, s32\ns_add_u32 s31, s31, 0x800000\n"
    "v_lshl_add_u32 v37, v231, 4, s31\nv_lshrrev_b32 v231, 5, v0\nv_and_b32 v230, 3, v0\ns_lshl_b32 s43, s18, 6\ns_add_u32 s42, s43, 0xd80000\n"
    "v_lshl_add_u32 v38, v231, 14, s42\nv_lshl_add_u32 v38, v230, 4, v38\ns_setpc_b64 s[94:95]\n.Lhcon67_2:\ns_lshr_b32 s43, s13, 1\ns_lshl_b32 s42, s43, 3\n"
    "s_add_u32 s41, s42, 0x27fb0\nv_mov_b32 v24, s41\nv_add_u32 v231, 0xffffff86, v0\nv_lshrrev_b32 v230, 3, v231\nv_mul_u32_u24 v229, 0x50, v230\nv_and_b32 v228, 7, v231\n"
    "v_add_u32 v30, 0x1d004, v229\nv_lshl_add_u32 v30, v228, 3, v30\nv_add_u32 v227, 0xffffff87, v0\nv_lshrrev_b32 v226, 3, v227\nv_mul_u32_u24 v225, 0x50, v226\n"
    "v_and_b32 v224, 7, v227\nv_add_u32 v31, 0x1d000, v225\nv_lshl_add_u32 v31, v224, 3, v31\ns_and_b32 s40, s13, 1\nv_bfe_u32 v175, v0, 4, 1\n"
    "v_mul_u32_u24 v174, 0x480, v175\nv_mov_b32 v173, 0\ns_cmp_lt_u32 s40, 1\ns_cselect_b64 vcc, -1, 0\nv_cndmask_b32 v172, v173, v174, vcc\nv_bfe_u32 v171, v0, 1, 1\n"
    "v_mul_u32_u24 v170, 0x240, v171\nv_bfe_u32 v169, v0, 5, 1\nv_bfe_u32 v168, v0, 2, 2\nv_and_b32 v167, 1, v0\ns_mul_i32 s39, s43, 0x3180\ns_add_u32 s38, s39, 0x20300\n"
    "v_add_u32 v27, s38, v172\nv_add_u32 v27, v27, v170\nv_lshl_add_u32 v27, v169, 7, v27\nv_lshl_add_u32 v27, v168, 4, v27\nv_lshl_add_u32 v27, v167, 3, v27\n"
    "v_and_b32 v166, 3, v0\ns_add_u32 s37, s39, 0x22280\nv_lshl_add_u32 v29, v169, 8, s37\nv_lshl_add_u32 v29, v168, 5, v29\nv_lshl_add_u32 v29, v166, 3, v29\n"
    "v_add_u32 v28, s37, v174\nv_lshl_add_u32 v28, v169, 8, v28\nv_lshl_add_u32 v28, v168, 5, v28\nv_lshl_add_u32 v28, v166, 3, v28\nv_and_b32 v165, 31, v0\n"
    "v_bfe_u32 v164, v0, 3, 1\nv_xor_b32 v163, v169, v164\ns_mul_i32 s36, s43, 0x2800\ns_add_u32 s35, s36, 0x18000\nv_lshl_add_u32 v32, v165, 4, s35\n"
    "v_lshl_add_u32 v32, v163, 3, v32\nv_xor_b32 v162, v169, v168\ns_add_u32 s34, s36, 0x18c00\nv_lshl_add_u32 v33, v165, 5, s34\nv_lshl_add_u32 v33, v162, 3, v33\n"
    "v_bfe_u32 v161, v0, 4, 2\nv_xor_b32 v160, v0, v161\ns_mov_b32 s33, 0x18400\nv_lshl_add_u32 v34, v160, 3, s33\nv_xor_b32 v36, 16, v33\ns_bitcmp0_b32 s13, 0\n"
    "s_cselect_b32 s30, 256, 0\nv_add_u32 v39, s30, v23\ns_setpc_b64 s[94:95]\n"
    ".Llstb7:\nv_mov_b32 v72, 1\nds_write_b32 v56, v72 offset:32\ns_mov_b32 s58, 2\ns_mov_b32 s57, 98304\ns_add_u32 s50, s50, 1\ns_and_b32 s50, s50, 7\n"
    "s_sub_u32 s62, s61, 16384\ns_max_i32 s62, s62, 0\ns_add_u32 s62, s8, s62\ns_addc_u32 s63, s9, 0\nv_and_b32 v251, 63, v0\nv_lshlrev_b32 v71, 4, v251\n"
    "v_bfe_u32 v250, v0, 2, 4\nv_and_b32 v249, 3, v0\ns_mov_b32 s43, 0x1002000\nv_lshl_add_u32 v57, v250, 7, s43\nv_lshl_add_u32 v57, v249, 4, v57\ns_lshl_b32 s42, s19, 7\n"
    "s_lshl_b32 s41, s20, 2\ns_add_u32 s40, s42, s41\ns_add_u32 s40, s40, 0x1004000\nv_mov_b32 v58, s40\ns_lshl_b32 s39, s19, 17\ns_lshl_b32 s38, s20, 11\n"
    "s_add_u32 s37, s39, s38\nv_lshl_add_u32 v79, v251, 4, s37\nv_mov_b32 v80, 0x1000000\ns_lshl_b32 s36, s25, 7\ns_add_u32 s35, s36, 0xe04000\n"
    "v_lshl_add_u32 v188, v250, 15, s35\nv_lshl_add_u32 v188, v249, 12, v188\ns_lshl_b32 s34, s18, 13\ns_add_u32 s33, s34, 0x201000\nv_lshl_add_u32 v186, v251, 4, s33\n"
    "v_bfe_u32 v248, v0, 3, 1\nv_and_b32 v247, 7, v0\ns_lshl_b32 s32, s2, 7\ns_add_u32 s31, s32, 0x400000\nv_lshl_add_u32 v187, v248, 23, s31\n"
    "v_lshl_add_u32 v187, v247, 4, v187\nv_mov_b64 v[180:181], -1\nv_mov_b64 v[182:183], -1\nglobal_load_dword v78, v80, s[4:5] sc1\ns_mov_b32 m0, 146432\n"
    "v_xor_b32 v72, 32, v71\nglobal_load_lds_dwordx4 v72, s[62:63] offset:2048\nv_xor_b32 v73, 48, v71\nglobal_load_lds_dwordx4 v73, s[62:63] offset:3072\n"
    "s_mov_b32 m0, 150528\nv_xor_b32 v72, 4160, v71\nglobal_load_lds_dwordx4 v72, s[62:63] offset:0\nv_xor_b32 v73, 4176, v71\n"
    "global_load_lds_dwordx4 v73, s[62:63] offset:1024\nv_xor_b32 v72, 4192, v71\nglobal_load_lds_dwordx4 v72, s[62:63] offset:2048\nv_xor_b32 v73, 4208, v71\n"
    "global_load_lds_dwordx4 v73, s[62:63] offset:3072\ns_mov_b32 m0, 154624\nv_xor_b32 v72, 8320, v71\nglobal_load_lds_dwordx4 v72, s[62:63] offset:0\n"
    "v_xor_b32 v73, 8336, v71\nglobal_load_lds_dwordx4 v73, s[62:63] offset:1024\nv_xor_b32 v72, 8352, v71\nglobal_load_lds_dwordx4 v72, s[62:63] offset:2048\n"
    "v_xor_b32 v73, 8368, v71\nglobal_load_lds_dwordx4 v73, s[62:63] offset:3072\ns_mov_b32 m0, 158720\nv_xor_b32 v72, 12480, v71\n"
    "global_load_lds_dwordx4 v72, s[62:63] offset:0\nv_xor_b32 v73, 12496, v71\nglobal_load_lds_dwordx4 v73, s[62:63] offset:1024\nv_xor_b32 v72, 12512, v71\n"
    "global_load_lds_dwordx4 v72, s[62:63] offset:2048\nv_xor_b32 v73, 12528, v71\nglobal_load_lds_dwordx4 v73, s[62:63] offset:3072\ns_call_b64 s[94:95], .Lhpre66\n"
    "s_setprio 2\n.Ll68:\nds_read_b128 v[74:77], v56 offset:96\ns_mul_i32 s59, s50, 196608\ns_mul_i32 s56, s50, 12288\ns_add_u32 s52, s66, s59\ns_addc_u32 s53, s67, 0\n"
    "s_mov_b32 m0, s56\ns_cbranch_execz .Lhop\ns_nop 0\nglobal_load_lds_dwordx4 v64, s[52:53] offset:0\nglobal_load_lds_dwordx4 v64, s[52:53] offset:1024\n"
    "global_load_lds_dwordx4 v64, s[52:53] offset:2048\ns_add_u32 s56, s56, 3072\ns_mov_b32 m0, s56\ns_add_u32 s52, s52, 49152\ns_addc_u32 s53, s53, 0\n"
    "global_load_lds_dwordx4 v64, s[52:53] offset:0\nglobal_load_lds_dwordx4 v64, s[52:53] offset:1024\nglobal_load_lds_dwordx4 v64, s[52:53] offset:2048\n"
    "s_add_u32 s56, s56, 3072\ns_mov_b32 m0, s56\ns_add_u32 s52, s52, 49152\ns_addc_u32 s53, s53, 0\nglobal_load_lds_dwordx4 v64, s[52:53] offset:0\n"
    "global_load_lds_dwordx4 v64, s[52:53] offset:1024\nglobal_load_lds_dwordx4 v64, s[52:53] offset:2048\ns_add_u32 s56, s56, 3072\ns_lshl_b32 s59, s50, 7\n"
    "s_add_u32 s54, s48, s59\ns_addc_u32 s55, s49, 0\ns_add_u32 s57, s57, 4096\ns_cmp_eq_u32 s57, 110592\ns_cselect_b32 s57, 98304, s57\ns_sub_u32 s61, s58, 4\n"
    "s_waitcnt lgkmcnt(0)\nv_min3_i32 v74, v74, v75, v76\nv_min_i32 v74, v74, v77\nv_cmp_gt_i32 vcc, s61, v74\ns_add_u32 s59, s58, -1\ns_cmp_lg_u32 s58, 2\n"
    "s_cbranch_scc1 .Ll72\ns_mov_b32 s59, s58\ns_waitcnt vmcnt(23)\n.Ll72:\nv_mov_b32 v70, s59\ns_mov_b64 exec, 1\ns_waitcnt vmcnt(25)\nds_max_i32 v56, v70\n"
    "s_mov_b64 exec, -1\ns_mov_b32 m0, s56\ns_add_u32 s52, s52, 49152\ns_addc_u32 s53, s53, 0\nglobal_load_lds_dwordx4 v64, s[52:53] offset:0\n"
    "global_load_lds_dwordx4 v64, s[52:53] offset:1024\nglobal_load_lds_dwordx4 v64, s[52:53] offset:2048\ns_add_u32 s56, s56, 3072\ns_cbranch_vccnz .Ll69\n.Ll70:\n"
    "s_add_u32 m0, s57, 0\ns_nop 0\nglobal_load_lds_dwordx4 v65, s[54:55] offset:0\nglobal_load_lds_dwordx4 v66, s[54:55] offset:1024\n"
    "global_load_lds_dwordx4 v67, s[54:55] offset:2048\nglobal_load_lds_dwordx4 v68, s[54:55] offset:3072\ns_add_u32 m0, s57, 12288\ns_add_u32 s54, s54, 32768\n"
    "s_addc_u32 s55, s55, 0\nglobal_load_lds_dwordx4 v65, s[54:55] offset:0\nglobal_load_lds_dwordx4 v66, s[54:55] offset:1024\n"
    "global_load_lds_dwordx4 v67, s[54:55] offset:2048\nglobal_load_lds_dwordx4 v68, s[54:55] offset:3072\ns_add_u32 m0, s57, 24576\ns_add_u32 s54, s54, 32768\n"
    "s_addc_u32 s55, s55, 0\nglobal_load_lds_dwordx4 v65, s[54:55] offset:0\nglobal_load_lds_dwordx4 v66, s[54:55] offset:1024\n"
    "global_load_lds_dwordx4 v67, s[54:55] offset:2048\nglobal_load_lds_dwordx4 v68, s[54:55] offset:3072\ns_add_u32 s59, s58, 0\nv_mov_b32 v70, s59\ns_mov_b64 exec, 1\n"
    "s_waitcnt vmcnt(24)\nds_max_i32 v56, v70\ns_mov_b64 exec, -1\ns_add_u32 m0, s57, 36864\ns_add_u32 s54, s54, 32768\ns_addc_u32 s55, s55, 0\n"
    "global_load_lds_dwordx4 v65, s[54:55] offset:0\nglobal_load_lds_dwordx4 v66, s[54:55] offset:1024\nglobal_load_lds_dwordx4 v67, s[54:55] offset:2048\n"
    "global_load_lds_dwordx4 v68, s[54:55] offset:3072\ns_cmp_lg_u32 s58, 12\ns_cbranch_scc1 .Ll73\nglobal_load_dwordx4 v[84:87], v57, s[4:5] sc1\n.Ll73:\n"
    "s_add_u32 s50, s50, 1\ns_and_b32 s50, s50, 7\ns_add_u32 s58, s58, 2\ns_cmp_lt_u32 s58, 16\ns_cbranch_scc1 .Ll68\nv_readfirstlane_b32 s11, v78\n"
    "s_mul_i32 s11, s11, 0x9E3779B1\ns_add_u32 s11, s11, 0x7F4A7C15\nv_xor_b32 v84, s11, v84\nv_xor_b32 v85, s11, v85\nv_xor_b32 v86, s11, v86\nv_xor_b32 v87, s11, v87\n"
    "v_or3_b32 v78, v84, v85, v86\nv_or_b32 v78, v78, v87\nv_cmp_ne_u32 vcc, 0, v78\ns_cmp_eq_u64 vcc, 0\ns_cselect_b32 s48, 18, 17\nv_mov_b32 v73, s48\ns_mov_b64 exec, 1\n"
    "ds_write_b32 v56, v73 offset:4\ns_mov_b64 exec, -1\ns_add_u32 s59, s58, -1\nv_mov_b32 v70, s59\ns_mov_b64 exec, 1\ns_waitcnt vmcnt(16)\nds_max_i32 v56, v70\n"
    "s_mov_b64 exec, -1\nglobal_store_dwordx4 v79, v[180:183], s[4:5] offset:0 sc1\nglobal_store_dwordx4 v79, v[180:183], s[4:5] offset:1024 sc1\nv_add_u32 v79, 0x8000, v79\n"
    "global_store_dwordx4 v79, v[180:183], s[4:5] offset:0 sc1\nglobal_store_dwordx4 v79, v[180:183], s[4:5] offset:1024 sc1\nv_add_u32 v79, 0x8000, v79\n"
    "global_store_dwordx4 v79, v[180:183], s[4:5] offset:0 sc1\nglobal_store_dwordx4 v79, v[180:183], s[4:5] offset:1024 sc1\nv_add_u32 v79, 0x8000, v79\n"
    "global_store_dwordx4 v79, v[180:183], s[4:5] offset:0 sc1\nglobal_store_dwordx4 v79, v[180:183], s[4:5] offset:1024 sc1\nv_add_u32 v79, 0xffffc000, v188\n"
    "v_mov_b32 v240, s11\ns_cbranch_vccnz .Ll73x\nglobal_load_dword v255, v79, s[4:5] sc1\ns_add_u32 s59, s58, 0\nv_mov_b32 v70, s59\ns_mov_b64 exec, 1\ns_waitcnt vmcnt(9)\n"
    "ds_max_i32 v56, v70\ns_mov_b64 exec, -1\ns_setprio 0\ns_waitcnt vmcnt(1)\n.Ll73y:\ns_mov_b64 exec, 1\nglobal_store_dword v58, v240, s[4:5] sc1\ns_mov_b64 exec, -1\n"
    "global_store_dwordx4 v186, v[180:183], s[4:5] offset:-4096 sc1\nglobal_store_dwordx4 v186, v[180:183], s[4:5] offset:-3072 sc1\n"
    "global_store_dwordx4 v186, v[180:183], s[4:5] offset:-2048 sc1\nglobal_store_dwordx4 v186, v[180:183], s[4:5] offset:-1024 sc1\n"
    "global_store_dwordx4 v186, v[180:183], s[4:5] offset:0 sc1\nglobal_store_dwordx4 v186, v[180:183], s[4:5] offset:1024 sc1\n"
    "global_store_dwordx4 v186, v[180:183], s[4:5] offset:2048 sc1\nglobal_store_dwordx4 v186, v[180:183], s[4:5] offset:3072 sc1\n"
    "global_store_dwordx4 v187, v[180:183], s[4:5] sc1\ns_branch .Lafl11\n.Ll69:\ns_mov_b32 s60, 0x100000\n.Ll69x:\nds_read_b128 v[74:77], v56 offset:96\n"
    "s_waitcnt lgkmcnt(0)\nv_min3_i32 v74, v74, v75, v76\nv_min_i32 v74, v74, v77\nv_cmp_gt_i32 vcc, s61, v74\ns_cbranch_vccz .Ll70\ns_sub_u32 s60, s60, 1\n"
    "s_cbranch_scc1 .Ll70\nv_mov_b32 v70, s58\ns_mov_b64 exec, 1\ns_waitcnt vmcnt(12)\nds_max_i32 v56, v70\ns_mov_b64 exec, -1\ns_sleep 1\ns_branch .Ll69x\n"
    ".Lhalo10:\nv_and_b32 v251, 0x7f, v0\ns_lshl_b32 s43, s18, 11\ns_add_u32 s42, s43, 0xe00000\nv_lshl_add_u32 v57, v251, 4, s42\ns_lshr_b32 s41, s2, 4\n"
    "s_lshl_b32 s40, s41, 7\ns_and_b32 s39, s2, 15\ns_lshl_b32 s38, s39, 2\ns_cbranch_execz .Lhop\ns_add_u32 s37, s40, s38\ns_add_u32 s37, s37, 0x1002000\n"
    "v_mov_b32 v177, s37\nv_mov_b32 v178, 0x27f98\nv_bfe_u32 v251, v0, 5, 1\nv_mul_u32_u24 v250, 0xc00, v251\nv_bfe_u32 v249, v0, 4, 1\nv_bfe_u32 v248, v0, 2, 2\n"
    "v_and_b32 v247, 3, v0\ns_lshl_b32 s43, s13, 10\ns_add_u32 s42, s43, 0xfffffc00\nv_add_u32 v180, s42, v250\nv_lshl_add_u32 v180, v249, 9, v180\n"
    "v_lshl_add_u32 v180, v248, 6, v180\nv_lshl_add_u32 v180, v247, 3, v180\nv_lshl_add_u32 v180, v249, 5, v180\nv_and_b32 v246, 15, v0\nv_bfe_u32 v245, v0, 4, 2\n"
    "v_xor_b32 v244, v245, v246\ns_mov_b32 s41, 0x23c00\nv_lshl_add_u32 v181, v246, 10, s41\nv_lshl_add_u32 v181, v244, 4, v181\ns_lshl_b32 s40, s13, 7\n"
    "s_add_u32 s39, s40, 0x27b80\nv_lshl_add_u32 v58, v245, 4, s39\nv_mul_u32_u24 v243, 0xd0, v246\ns_lshl_b32 s38, s13, 6\ns_add_u32 s37, s38, 0x17fc0\n"
    "v_add_u32 v184, s37, v243\nv_lshl_add_u32 v184, v245, 3, v184\ns_mov_b32 s36, 0x27f80\nv_lshl_add_u32 v185, v247, 2, s36\ns_add_u32 s44, s23, 0\ns_and_b32 s44, s44, 7\n"
    "s_mul_i32 s45, s44, 12288\ns_lshl_b32 s44, s44, 7\nv_add_u32 v192, s45, v180\nv_xor_b32 v208, s44, v181\nv_xor_b32 v193, 32, v192\nv_xor_b32 v209, 64, v208\n"
    "s_add_u32 s44, s23, 1\ns_and_b32 s44, s44, 7\ns_mul_i32 s45, s44, 12288\ns_lshl_b32 s44, s44, 7\nv_add_u32 v194, s45, v180\nv_xor_b32 v210, s44, v181\n"
    "v_xor_b32 v195, 32, v194\nv_xor_b32 v211, 64, v210\ns_add_u32 s44, s23, 2\ns_and_b32 s44, s44, 7\ns_mul_i32 s45, s44, 12288\ns_lshl_b32 s44, s44, 7\n"
    "v_add_u32 v196, s45, v180\nv_xor_b32 v212, s44, v181\nv_xor_b32 v197, 32, v196\nv_xor_b32 v213, 64, v212\ns_add_u32 s44, s23, 3\ns_and_b32 s44, s44, 7\n"
    "s_mul_i32 s45, s44, 12288\ns_lshl_b32 s44, s44, 7\nv_add_u32 v198, s45, v180\nv_xor_b32 v214, s44, v181\nv_xor_b32 v199, 32, v198\nv_xor_b32 v215, 64, v214\n"
    "s_add_u32 s44, s23, 4\ns_and_b32 s44, s44, 7\ns_mul_i32 s45, s44, 12288\ns_lshl_b32 s44, s44, 7\nv_add_u32 v200, s45, v180\nv_xor_b32 v216, s44, v181\n"
    "v_xor_b32 v201, 32, v200\nv_xor_b32 v217, 64, v216\ns_add_u32 s44, s23, 5\ns_and_b32 s44, s44, 7\ns_mul_i32 s45, s44, 12288\ns_lshl_b32 s44, s44, 7\n"
    "v_add_u32 v202, s45, v180\nv_xor_b32 v218, s44, v181\nv_xor_b32 v203, 32, v202\nv_xor_b32 v219, 64, v218\ns_add_u32 s44, s23, 6\ns_and_b32 s44, s44, 7\n"
    "s_mul_i32 s45, s44, 12288\ns_lshl_b32 s44, s44, 7\nv_add_u32 v204, s45, v180\nv_xor_b32 v220, s44, v181\nv_xor_b32 v205, 32, v204\nv_xor_b32 v221, 64, v220\n"
    "s_add_u32 s44, s23, 7\ns_and_b32 s44, s44, 7\ns_mul_i32 s45, s44, 12288\ns_lshl_b32 s44, s44, 7\nv_add_u32 v206, s45, v180\nv_xor_b32 v222, s44, v181\n"
    "v_xor_b32 v207, 32, v206\nv_xor_b32 v223, 64, v222\ns_cmp_eq_u32 s13, 1\ns_cbranch_scc1 .Lh74\ns_waitcnt vmcnt(0)\nv_lshl_or_b32 v168, v169, 16, v168\n"
    "v_lshl_or_b32 v169, v171, 16, v170\nv_lshl_or_b32 v170, v173, 16, v172\nv_lshl_or_b32 v171, v175, 16, v174\nv_readfirstlane_b32 s11, v166\n"
    "global_store_dwordx4 v57, v[168:171], s[4:5] sc1\ns_mul_i32 s11, s11, 0x9E3779B1\ns_add_u32 s11, s11, 0x7F4A7C15\n.Lh74:\ns_call_b64 s[94:95], .Lhpre66\n"
    "s_mul_i32 s88, s13, 12\ns_add_u32 s88, s88, -8\ns_lshl_b32 s90, s88, 10\ns_add_u32 s90, s92, s90\ns_addc_u32 s91, s93, 0\ns_sub_u32 s89, 12, 1\ns_mov_b64 exec, 0\n"
    "s_swappc_b64 s[94:95], s[90:91]\n.Lz208:\nv_mov_b32 v176, 1\nv_mov_b32 v179, s11\ns_mov_b64 exec, 1\ns_waitcnt vmcnt(0)\nds_write_b32 v25, v176 offset:16\n"
    "s_mov_b64 exec, -1\ns_cmp_lg_u32 s13, 3\ns_cbranch_scc1 .Lh75\ns_mov_b32 s86, 0x100000\n.Ls76:\nds_read_b32 v176, v178 offset:0\ns_waitcnt lgkmcnt(0)\n"
    "v_cmp_gt_i32 vcc, 1, v176\ns_sub_u32 s86, s86, 1\ns_cbranch_scc1 .Ls77\ns_cbranch_vccz .Ls77\ns_sleep 1\ns_branch .Ls76\n.Ls77:\ns_mov_b64 exec, 1\n"
    "global_store_dword v177, v179, s[4:5] sc1\ns_mov_b64 exec, -1\n.Lh75:\ns_mov_b32 s48, 0\ns_cmp_lt_i32 s48, 3\ns_cbranch_scc1 .Lc78\n.Lc79:\nds_read_b128 v[64:67], v208\n"
    "ds_read_b128 v[68:71], v209\nds_read_b128 v[72:75], v210\nds_read_b128 v[76:79], v211\nds_read_b64_tr_b16 v[128:129], v192 offset:0\n"
    "ds_read_b64_tr_b16 v[130:131], v192 offset:256\nds_read_b64_tr_b16 v[132:133], v193 offset:0\nds_read_b64_tr_b16 v[134:135], v193 offset:256\n"
    "ds_read_b64_tr_b16 v[136:137], v192 offset:6144\nds_read_b64_tr_b16 v[138:139], v192 offset:6400\nds_read_b64_tr_b16 v[140:141], v193 offset:6144\n"
    "ds_read_b64_tr_b16 v[142:143], v193 offset:6400\ns_waitcnt lgkmcnt(0)\ns_setprio 3\nv_mfma_f32_16x16x32_bf16 v[152:155], v[64:67], v[64:67], 0\n"
    "v_mfma_f32_16x16x32_bf16 v[152:155], v[68:71], v[68:71], v[152:155]\nv_mfma_f32_16x16x32_bf16 v[152:155], v[72:75], v[72:75], v[152:155]\n"
    "v_mfma_f32_16x16x32_bf16 v[152:155], v[76:79], v[76:79], v[152:155]\nv_mfma_f32_16x16x32_bf16 v[144:147], v[128:131], v[64:67], 0\n"
    "v_mfma_f32_16x16x32_bf16 v[148:151], v[132:135], v[64:67], 0\nv_mfma_f32_16x16x32_bf16 v[144:147], v[136:139], v[68:71], v[144:147]\n"
    "v_mfma_f32_16x16x32_bf16 v[148:151], v[140:143], v[68:71], v[148:151]\ns_setprio 0\ns_cmp_lt_i32 s48, 3\ns_cbranch_scc1 .Lc81\n.Lc82:\nds_read_b128 v[80:83], v212\n"
    "ds_read_b128 v[84:87], v213\nds_read_b128 v[88:91], v214\nds_read_b128 v[92:95], v215\nds_read_b64_tr_b16 v[128:129], v194 offset:0\n"
    "ds_read_b64_tr_b16 v[130:131], v194 offset:256\nds_read_b64_tr_b16 v[132:133], v195 offset:0\nds_read_b64_tr_b16 v[134:135], v195 offset:256\n"
    "ds_read_b64_tr_b16 v[136:137], v194 offset:6144\nds_read_b64_tr_b16 v[138:139], v194 offset:6400\nds_read_b64_tr_b16 v[140:141], v195 offset:6144\n"
    "ds_read_b64_tr_b16 v[142:143], v195 offset:6400\ns_waitcnt lgkmcnt(0)\ns_setprio 3\nv_mfma_f32_16x16x32_bf16 v[152:155], v[80:83], v[80:83], v[152:155]\n"
    "v_mfma_f32_16x16x32_bf16 v[152:155], v[84:87], v[84:87], v[152:155]\ns_cbranch_execz .Lhop\nv_mfma_f32_16x16x32_bf16 v[152:155], v[88:91], v[88:91], v[152:155]\n"
    "v_mfma_f32_16x16x32_bf16 v[152:155], v[92:95], v[92:95], v[152:155]\nv_mfma_f32_16x16x32_bf16 v[144:147], v[128:131], v[72:75], v[144:147]\n"
    "v_mfma_f32_16x16x32_bf16 v[148:151], v[132:135], v[72:75], v[148:151]\nv_mfma_f32_16x16x32_bf16 v[144:147], v[136:139], v[76:79], v[144:147]\n"
    "v_mfma_f32_16x16x32_bf16 v[148:151], v[140:143], v[76:79], v[148:151]\ns_setprio 0\ns_cmp_lt_i32 s48, 5\ns_cbranch_scc1 .Lc84\n.Lc85:\nds_read_b128 v[96:99], v216\n"
    "ds_read_b128 v[100:103], v217\nds_read_b128 v[104:107], v218\nds_read_b128 v[108:111], v219\nds_read_b64_tr_b16 v[128:129], v196 offset:0\n"
    "ds_read_b64_tr_b16 v[130:131], v196 offset:256\nds_read_b64_tr_b16 v[132:133], v197 offset:0\nds_read_b64_tr_b16 v[134:135], v197 offset:256\n"
    "ds_read_b64_tr_b16 v[136:137], v196 offset:6144\nds_read_b64_tr_b16 v[138:139], v196 offset:6400\nds_read_b64_tr_b16 v[140:141], v197 offset:6144\n"
    "ds_read_b64_tr_b16 v[142:143], v197 offset:6400\ns_waitcnt lgkmcnt(0)\ns_setprio 3\nv_mfma_f32_16x16x32_bf16 v[152:155], v[96:99], v[96:99], v[152:155]\n"
    "v_mfma_f32_16x16x32_bf16 v[152:155], v[100:103], v[100:103], v[152:155]\nv_mfma_f32_16x16x32_bf16 v[152:155], v[104:107], v[104:107], v[152:155]\n"
    "v_mfma_f32_16x16x32_bf16 v[152:155], v[108:111], v[108:111], v[152:155]\nv_mfma_f32_16x16x32_bf16 v[144:147], v[128:131], v[80:83], v[144:147]\n"
    "v_mfma_f32_16x16x32_bf16 v[148:151], v[132:135], v[80:83], v[148:151]\nv_mfma_f32_16x16x32_bf16 v[144:147], v[136:139], v[84:87], v[144:147]\n"
    "v_mfma_f32_16x16x32_bf16 v[148:151], v[140:143], v[84:87], v[148:151]\ns_setprio 0\ns_cmp_lt_i32 s48, 7\ns_cbranch_scc1 .Lc87\n.Lc88:\nds_read_b128 v[112:115], v220\n"
    "ds_read_b128 v[116:119], v221\nds_read_b128 v[120:123], v222\nds_read_b128 v[124:127], v223\nds_read_b64_tr_b16 v[128:129], v198 offset:0\n"
    "ds_read_b64_tr_b16 v[130:131], v198 offset:256\nds_read_b64_tr_b16 v[132:133], v199 offset:0\nds_read_b64_tr_b16 v[134:135], v199 offset:256\n"
    "ds_read_b64_tr_b16 v[136:137], v198 offset:6144\nds_read_b64_tr_b16 v[138:139], v198 offset:6400\nds_read_b64_tr_b16 v[140:141], v199 offset:6144\n"
    "ds_read_b64_tr_b16 v[142:143], v199 offset:6400\ns_waitcnt lgkmcnt(0)\ns_setprio 3\nv_mfma_f32_16x16x32_bf16 v[152:155], v[112:115], v[112:115], v[152:155]\n"
    "v_mfma_f32_16x16x32_bf16 v[152:155], v[116:119], v[116:119], v[152:155]\nv_mfma_f32_16x16x32_bf16 v[152:155], v[120:123], v[120:123], v[152:155]\n"
    "v_mfma_f32_16x16x32_bf16 v[152:155], v[124:127], v[124:127], v[152:155]\nv_mfma_f32_16x16x32_bf16 v[144:147], v[128:131], v[88:91], v[144:147]\n"
    "v_mfma_f32_16x16x32_bf16 v[148:151], v[132:135], v[88:91], v[148:151]\nv_mfma_f32_16x16x32_bf16 v[144:147], v[136:139], v[92:95], v[144:147]\n"
    "v_mfma_f32_16x16x32_bf16 v[148:151], v[140:143], v[92:95], v[148:151]\ns_setprio 0\ns_cmp_lt_i32 s48, 9\ns_cbranch_scc1 .Lc90\n.Lc91:\n"
    "ds_read_b64_tr_b16 v[128:129], v200 offset:0\nds_read_b64_tr_b16 v[130:131], v200 offset:256\nds_read_b64_tr_b16 v[132:133], v201 offset:0\n"
    "ds_read_b64_tr_b16 v[134:135], v201 offset:256\nds_read_b64_tr_b16 v[136:137], v200 offset:6144\nds_read_b64_tr_b16 v[138:139], v200 offset:6400\n"
    "ds_read_b64_tr_b16 v[140:141], v201 offset:6144\nds_read_b64_tr_b16 v[142:143], v201 offset:6400\ns_waitcnt lgkmcnt(0)\ns_setprio 3\n"
    "v_mfma_f32_16x16x32_bf16 v[144:147], v[128:131], v[96:99], v[144:147]\nv_mfma_f32_16x16x32_bf16 v[148:151], v[132:135], v[96:99], v[148:151]\n"
    "v_mfma_f32_16x16x32_bf16 v[144:147], v[136:139], v[100:103], v[144:147]\nv_mfma_f32_16x16x32_bf16 v[148:151], v[140:143], v[100:103], v[148:151]\ns_setprio 0\n"
    "s_mov_b32 s44, 0xaaaaaaaa\ns_mov_b32 s45, s44\ns_mov_b32 s46, 0xcccccccc\ns_mov_b32 s47, s46\nv_cndmask_b32 v156, v152, v153, s[44:45]\n"
    "v_cndmask_b32 v157, v154, v155, s[44:45]\ns_mov_b32 s44, 0x00f0000f\ns_mov_b32 s45, 0xf0000f00\nv_cndmask_b32 v156, v156, v157, s[46:47]\ns_mov_b64 s[46:47], 0xffff\n"
    "v_cndmask_b32 v156, 0, v156, s[44:45]\nv_cndmask_b32 v157, 0, v153, s[46:47]\ns_mov_b32 s44, 0x3b000000\ns_nop 0\nv_permlane32_swap_b32 v156, v157\n"
    "v_add_f32 v158, v156, v157\nv_add_f32 v159, v156, v157\ns_nop 1\nv_permlane16_swap_b32 v158, v159\nv_add_f32 v156, v158, v159\nv_add_f32 v157, v158, v159\ns_nop 1\n"
    "v_permlane32_swap_b32 v156, v157\nv_mul_f32 v157, s44, v157\nv_mul_f32 v158, v157, v157\nv_fma_f32 v158, v156, s44, -v158\nv_max_f32 v158, 0, v158\n"
    "v_cvt_pk_bf16_f32 v158, 0, v158\nv_add_f32 v158, 0x3727c5ac, v158\nv_cvt_pk_bf16_f32 v158, 0, v158\nv_sqrt_f32 v158, v158\nv_cvt_pk_bf16_f32 v157, 0, -v157\n"
    "v_cvt_pk_bf16_f32 v158, 0, v158\nv_rcp_f32 v158, v158\nv_mov_b32 v159, 1\ns_mov_b32 s46, 0xffff0000\ns_mov_b32 s44, 0x00020002\ns_mov_b32 s45, s44\ns_cmp_lt_i32 s48, 11\n"
    "s_cbranch_scc1 .Lc93\n.Lc94:\nds_read_b64_tr_b16 v[128:129], v202 offset:0\nds_read_b64_tr_b16 v[130:131], v202 offset:256\nds_read_b64_tr_b16 v[132:133], v203 offset:0\n"
    "ds_read_b64_tr_b16 v[134:135], v203 offset:256\nds_read_b64_tr_b16 v[136:137], v202 offset:6144\nds_read_b64_tr_b16 v[138:139], v202 offset:6400\n"
    "ds_read_b64_tr_b16 v[140:141], v203 offset:6144\nds_read_b64_tr_b16 v[142:143], v203 offset:6400\ns_waitcnt lgkmcnt(0)\ns_setprio 3\n"
    "v_mfma_f32_16x16x32_bf16 v[144:147], v[128:131], v[104:107], v[144:147]\nv_mfma_f32_16x16x32_bf16 v[148:151], v[132:135], v[104:107], v[148:151]\n"
    "v_mfma_f32_16x16x32_bf16 v[144:147], v[136:139], v[108:111], v[144:147]\nv_mfma_f32_16x16x32_bf16 v[148:151], v[140:143], v[108:111], v[148:151]\ns_setprio 0\n"
    "s_cmp_lt_i32 s48, 13\ns_cbranch_scc1 .Lc96\n.Lc97:\nds_read_b64_tr_b16 v[128:129], v204 offset:0\nds_read_b64_tr_b16 v[130:131], v204 offset:256\n"
    "ds_read_b64_tr_b16 v[132:133], v205 offset:0\nds_read_b64_tr_b16 v[134:135], v205 offset:256\nds_read_b64_tr_b16 v[136:137], v204 offset:6144\n"
    "ds_read_b64_tr_b16 v[138:139], v204 offset:6400\nds_read_b64_tr_b16 v[140:141], v205 offset:6144\nds_read_b64_tr_b16 v[142:143], v205 offset:6400\ns_waitcnt lgkmcnt(0)\n"
    "s_setprio 3\nv_mfma_f32_16x16x32_bf16 v[144:147], v[128:131], v[112:115], v[144:147]\nv_mfma_f32_16x16x32_bf16 v[148:151], v[132:135], v[112:115], v[148:151]\n"
    "v_mfma_f32_16x16x32_bf16 v[144:147], v[136:139], v[116:119], v[144:147]\nv_mfma_f32_16x16x32_bf16 v[148:151], v[140:143], v[116:119], v[148:151]\ns_setprio 0\n"
    "s_cmp_lt_i32 s48, 15\ns_cbranch_scc1 .Lc99\n.Lc100:\nds_read_b32 v156, v56 offset:4\nds_read_b64_tr_b16 v[128:129], v206 offset:0\n"
    "ds_read_b64_tr_b16 v[130:131], v206 offset:256\ns_cbranch_execz .Lhop\nds_read_b64_tr_b16 v[132:133], v207 offset:0\nds_read_b64_tr_b16 v[134:135], v207 offset:256\n"
    "ds_read_b64_tr_b16 v[136:137], v206 offset:6144\nds_read_b64_tr_b16 v[138:139], v206 offset:6400\nds_read_b64_tr_b16 v[140:141], v207 offset:6144\n"
    "ds_read_b64_tr_b16 v[142:143], v207 offset:6400\ns_waitcnt lgkmcnt(0)\ns_setprio 3\nv_mfma_f32_16x16x32_bf16 v[144:147], v[128:131], v[120:123], v[144:147]\n"
    "v_mfma_f32_16x16x32_bf16 v[148:151], v[132:135], v[120:123], v[148:151]\nv_mfma_f32_16x16x32_bf16 v[144:147], v[136:139], v[124:127], v[144:147]\n"
    "v_mfma_f32_16x16x32_bf16 v[148:151], v[140:143], v[124:127], v[148:151]\ns_setprio 3\nv_readfirstlane_b32 s48, v156\ns_nop 4\nv_and_b32 v160, s46, v144\n"
    "v_and_b32 v161, s46, v145\nv_and_b32 v162, s46, v146\nv_and_b32 v163, s46, v147\nv_and_b32 v164, s46, v148\nv_and_b32 v165, s46, v149\nv_and_b32 v166, s46, v150\n"
    "v_and_b32 v167, s46, v151\nv_sub_f32 v168, v144, v160\nv_sub_f32 v169, v145, v161\nv_sub_f32 v170, v146, v162\nv_sub_f32 v171, v147, v163\nv_sub_f32 v172, v148, v164\n"
    "v_sub_f32 v173, v149, v165\nv_sub_f32 v174, v150, v166\nv_sub_f32 v175, v151, v167\nv_cvt_pk_bf16_f32 v160, v160, v168\nv_cvt_pk_bf16_f32 v161, v161, v169\n"
    "v_cvt_pk_bf16_f32 v162, v162, v170\nv_cvt_pk_bf16_f32 v163, v163, v171\nv_cvt_pk_bf16_f32 v164, v164, v172\nv_cvt_pk_bf16_f32 v165, v165, v173\n"
    "v_cvt_pk_bf16_f32 v166, v166, v174\nv_cvt_pk_bf16_f32 v167, v167, v175\ns_mov_b64 exec, s[44:45]\nds_write_b128 v58, v[160:163]\nds_write_b128 v58, v[164:167] offset:64\n"
    "ds_write_b32 v25, v159 offset:32\ns_mov_b64 exec, -1\ns_setprio 0\n"
    ".Lafl11:\nglobal_load_dwordx4 v[224:227], v47, s[8:9] offset:0\nglobal_load_dwordx4 v[228:231], v47, s[8:9] offset:32\ns_mov_b32 s44, 0x00ff00ff\n"
    "s_mov_b32 s45, 0xff00ff00\ns_mov_b32 s46, s44\ns_mov_b32 s47, s44\ns_sub_u32 s26, s48, 17\ns_cmp_eq_u32 s26, 0\ns_cbranch_scc1 .La102\n"
    "global_load_dwordx4 v[64:67], v59, s[68:69] offset:-4096 sc1\nglobal_load_dwordx4 v[68:71], v59, s[68:69] offset:-3072 sc1\n"
    "global_load_dwordx4 v[72:75], v59, s[68:69] offset:-2048 sc1\nglobal_load_dwordx4 v[76:79], v59, s[68:69] offset:-1024 sc1\n"
    "global_load_dwordx4 v[80:83], v59, s[68:69] offset:0 sc1\nglobal_load_dwordx4 v[84:87], v59, s[68:69] offset:1024 sc1\n"
    "global_load_dwordx4 v[88:91], v59, s[68:69] offset:2048 sc1\nglobal_load_dwordx4 v[92:95], v59, s[68:69] offset:3072 sc1\n"
    "global_load_dwordx4 v[96:99], v59, s[70:71] offset:-4096 sc1\nglobal_load_dwordx4 v[100:103], v59, s[70:71] offset:-3072 sc1\n"
    "global_load_dwordx4 v[104:107], v59, s[70:71] offset:-2048 sc1\nglobal_load_dwordx4 v[108:111], v59, s[70:71] offset:-1024 sc1\n"
    "global_load_dwordx4 v[112:115], v59, s[70:71] offset:0 sc1\nglobal_load_dwordx4 v[116:119], v59, s[70:71] offset:1024 sc1\n"
    "global_load_dwordx4 v[120:123], v59, s[70:71] offset:2048 sc1\nglobal_load_dwordx4 v[124:127], v59, s[70:71] offset:3072 sc1\n.La104:\ns_cmp_lt_u32 s13, 2\n"
    "s_cbranch_scc1 .La105\nv_fmac_f32_dpp v144, v144, v157 row_newbcast:1 row_mask:0xf bank_mask:0xf\n"
    "v_fmac_f32_dpp v145, v145, v157 row_newbcast:1 row_mask:0xf bank_mask:0xf\nv_fmac_f32_dpp v146, v146, v157 row_newbcast:1 row_mask:0xf bank_mask:0xf\n"
    "v_fmac_f32_dpp v147, v147, v157 row_newbcast:1 row_mask:0xf bank_mask:0xf\nv_fmac_f32_dpp v148, v148, v157 row_newbcast:1 row_mask:0xf bank_mask:0xf\n"
    "v_fmac_f32_dpp v149, v149, v157 row_newbcast:1 row_mask:0xf bank_mask:0xf\nv_fmac_f32_dpp v150, v150, v157 row_newbcast:1 row_mask:0xf bank_mask:0xf\n"
    "v_fmac_f32_dpp v151, v151, v157 row_newbcast:1 row_mask:0xf bank_mask:0xf\nv_mul_f32 v144, v144, v158\nv_mul_f32 v145, v145, v158\nv_mul_f32 v146, v146, v158\n"
    "v_mul_f32 v147, v147, v158\nv_mul_f32 v148, v148, v158\nv_mul_f32 v149, v149, v158\nv_mul_f32 v150, v150, v158\nv_mul_f32 v151, v151, v158\n"
    "v_cvt_pk_bf16_f32 v144, v144, v145\nv_cvt_pk_bf16_f32 v145, v146, v147\nv_cvt_pk_bf16_f32 v146, v148, v149\nv_cvt_pk_bf16_f32 v147, v150, v151\ns_mov_b32 s86, 0x100000\n"
    ".Ls107:\nds_read_b32 v240, v185 offset:96\ns_waitcnt lgkmcnt(0)\nv_cmp_gt_i32 vcc, 16, v240\ns_sub_u32 s86, s86, 1\ns_cbranch_scc1 .Ls108\ns_cbranch_vccz .Ls108\n"
    "s_sleep 1\ns_branch .Ls107\n.Ls108:\nds_write2_b64 v184, v[144:145], v[146:147] offset1:4\ns_branch .La106\n.Lc78:\ns_mov_b32 s86, 0x100000\n.Lc80:\n"
    "ds_read_b32 v156, v56\ns_waitcnt lgkmcnt(0)\nv_readfirstlane_b32 s48, v156\ns_sub_u32 s86, s86, 1\ns_cbranch_scc1 .Lc79\ns_cmp_ge_i32 s48, 3\ns_cbranch_scc1 .Lc79\n"
    "s_sleep 1\ns_branch .Lc80\n.Lc81:\ns_mov_b32 s86, 0x100000\n.Lc83:\nds_read_b32 v176, v56\ns_waitcnt lgkmcnt(0)\nv_readfirstlane_b32 s48, v176\ns_sub_u32 s86, s86, 1\n"
    "s_cbranch_scc1 .Lc82\ns_cmp_ge_i32 s48, 3\ns_cbranch_scc1 .Lc82\ns_sleep 1\ns_branch .Lc83\n.Lc84:\ns_mov_b32 s86, 0x100000\n.Lc86:\nds_read_b32 v176, v56\n"
    "s_waitcnt lgkmcnt(0)\nv_readfirstlane_b32 s48, v176\ns_sub_u32 s86, s86, 1\ns_cbranch_scc1 .Lc85\ns_cmp_ge_i32 s48, 5\ns_cbranch_scc1 .Lc85\ns_sleep 1\ns_branch .Lc86\n"
    ".Lc87:\ns_mov_b32 s86, 0x100000\n.Lc89:\nds_read_b32 v176, v56\ns_waitcnt lgkmcnt(0)\nv_readfirstlane_b32 s48, v176\ns_sub_u32 s86, s86, 1\ns_cbranch_scc1 .Lc88\n"
    "s_cmp_ge_i32 s48, 7\ns_cbranch_scc1 .Lc88\ns_sleep 1\ns_branch .Lc89\n.Lc90:\ns_mov_b32 s86, 0x100000\n.Lc92:\nds_read_b32 v176, v56\ns_waitcnt lgkmcnt(0)\n"
    "v_readfirstlane_b32 s48, v176\ns_sub_u32 s86, s86, 1\ns_cbranch_scc1 .Lc91\ns_cmp_ge_i32 s48, 9\ns_cbranch_scc1 .Lc91\ns_sleep 1\ns_branch .Lc92\n.Lc93:\n"
    "s_mov_b32 s86, 0x100000\n.Lc95:\nds_read_b32 v176, v56\ns_waitcnt lgkmcnt(0)\nv_readfirstlane_b32 s48, v176\ns_sub_u32 s86, s86, 1\ns_cbranch_scc1 .Lc94\n"
    "s_cmp_ge_i32 s48, 11\ns_cbranch_scc1 .Lc94\ns_sleep 1\ns_branch .Lc95\n.Lc96:\ns_mov_b32 s86, 0x100000\n.Lc98:\nds_read_b32 v176, v56\ns_waitcnt lgkmcnt(0)\n"
    "v_readfirstlane_b32 s48, v176\ns_sub_u32 s86, s86, 1\ns_cbranch_scc1 .Lc97\ns_cmp_ge_i32 s48, 13\ns_cbranch_scc1 .Lc97\ns_sleep 1\ns_branch .Lc98\n.Lc99:\n"
    "s_mov_b32 s86, 0x100000\n.Lc101:\nds_read_b32 v176, v56\ns_waitcnt lgkmcnt(0)\nv_readfirstlane_b32 s48, v176\ns_sub_u32 s86, s86, 1\ns_cbranch_scc1 .Lc100\n"
    "s_cmp_ge_i32 s48, 15\ns_cbranch_scc1 .Lc100\ns_sleep 1\ns_branch .Lc101\ns_cbranch_execz .Lhop\n.La105:\ns_xor_b32 s48, s26, 1\ns_or_b32 s48, s48, s13\n"
    "s_cbranch_scc1 .La106\nglobal_load_dword v255, v188, s[4:5] sc1\n.La106:\n"
    ".Latt12:\ns_setprio 1\ns_waitcnt lgkmcnt(0)\ns_barrier\nds_read_b128 v[192:195], v6\nds_read_b128 v[196:199], v6 offset:208\ns_mov_b64 exec, s[44:45]\n"
    "ds_read_b128 v[232:235], v7\nds_read_b128 v[236:239], v7 offset:208\ns_mov_b64 exec, -1\nds_read_b64_tr_b16 v[200:201], v8 offset:0\n"
    "ds_read_b64_tr_b16 v[202:203], v8 offset:32\nds_read_b64_tr_b16 v[204:205], v8 offset:208\nds_read_b64_tr_b16 v[206:207], v8 offset:240\ns_mov_b32 s48, 0x3eb8aa3b\n"
    "v_mov_b32 v220, 0x3f803f80\nv_mov_b32 v221, 0x3f803f80\nv_mov_b32 v222, 0x3f803f80\nv_mov_b32 v223, 0x3f803f80\ns_waitcnt lgkmcnt(5)\n"
    "v_mfma_f32_16x16x32_bf16 v[208:211], v[192:195], v[232:235], v[2:5]\ns_waitcnt lgkmcnt(4)\nv_mfma_f32_16x16x32_bf16 v[212:215], v[196:199], v[236:239], v[2:5]\ns_nop 5\n"
    "v_max3_f32 v216, v208, v209, v210\ns_nop 0\nv_max3_f32 v217, v212, v213, v214\nv_max_f32 v216, v216, v211\nv_max_f32 v217, v217, v215\ns_nop 1\n"
    "v_permlane16_swap_b32 v216, v217\nv_max_f32 v218, v216, v217\nv_max_f32 v217, v216, v217\ns_nop 1\nv_permlane32_swap_b32 v218, v217\nv_max_f32 v218, v218, v217\n"
    "v_mul_f32 v216, 0xbeb8aa3b, v218\nv_mul_f32 v217, 0xbeb8aa3b, v218\ns_nop 1\nv_permlane16_swap_b32 v216, v217\n"
    "v_pk_fma_f32 v[208:209], v[208:209], s[48:49], v[216:217] op_sel:[0,0,0] op_sel_hi:[1,0,0]\n"
    "v_pk_fma_f32 v[210:211], v[210:211], s[48:49], v[216:217] op_sel:[0,0,0] op_sel_hi:[1,0,0]\n"
    "v_pk_fma_f32 v[212:213], v[212:213], s[48:49], v[216:217] op_sel:[0,0,1] op_sel_hi:[1,0,1]\n"
    "v_pk_fma_f32 v[214:215], v[214:215], s[48:49], v[216:217] op_sel:[0,0,1] op_sel_hi:[1,0,1]\nv_exp_f32 v208, v208\nv_exp_f32 v209, v209\nv_exp_f32 v210, v210\n"
    "v_exp_f32 v211, v211\nv_exp_f32 v212, v212\nv_exp_f32 v213, v213\nv_exp_f32 v214, v214\nv_exp_f32 v215, v215\ns_mov_b64 exec, s[46:47]\n"
    "v_cvt_pk_bf16_f32 v244, v208, v209\nv_cvt_pk_bf16_f32 v245, v210, v211\nv_cvt_pk_bf16_f32 v248, v212, v213\nv_cvt_pk_bf16_f32 v249, v214, v215\ns_not_b64 exec, exec\n"
    "v_cvt_pk_bf16_f32 v246, v208, v209\nv_cvt_pk_bf16_f32 v247, v210, v211\nv_cvt_pk_bf16_f32 v250, v212, v213\nv_cvt_pk_bf16_f32 v251, v214, v215\ns_mov_b64 exec, -1\n"
    "s_waitcnt lgkmcnt(0)\nv_mfma_f32_16x16x32_bf16 v[208:211], v[220:223], v[244:247], 0\nv_mfma_f32_16x16x32_bf16 v[212:215], v[220:223], v[248:251], 0\n"
    "v_mfma_f32_16x16x32_bf16 v[192:195], v[200:203], v[244:247], 0\nv_mfma_f32_16x16x32_bf16 v[196:199], v[204:207], v[248:251], 0\ns_nop 4\nv_rcp_f32 v216, v208\n"
    "v_rcp_f32 v217, v212\ns_nop 0\nv_pk_mul_f32 v[192:193], v[192:193], v[216:217] op_sel:[0,0] op_sel_hi:[1,0]\n"
    "v_pk_mul_f32 v[194:195], v[194:195], v[216:217] op_sel:[0,0] op_sel_hi:[1,0]\nv_pk_mul_f32 v[196:197], v[196:197], v[216:217] op_sel:[0,1] op_sel_hi:[1,1]\n"
    "v_pk_mul_f32 v[198:199], v[198:199], v[216:217] op_sel:[0,1] op_sel_hi:[1,1]\nv_cvt_pk_bf16_f32 v200, v192, v193\nv_cvt_pk_bf16_f32 v201, v194, v195\n"
    "v_cvt_pk_bf16_f32 v202, v196, v197\nv_cvt_pk_bf16_f32 v203, v198, v199\ns_nop 0\nv_permlane16_swap_b32 v200, v202\nv_permlane16_swap_b32 v201, v203\n"
    "global_store_dwordx4 v9, v[200:203], s[4:5] sc1\ns_setprio 0\ns_cmp_gt_u32 s3, 3\ns_cbranch_scc1 .Lhb110\n"
    "s_cmp_lg_u32 s12, 0\ns_cbranch_scc1 .Lb109\ns_cmp_eq_u32 s27, 0\ns_cbranch_scc1 .Lb118\ns_mov_b32 s10, 64\ns_call_b64 s[94:95], .Lkgl13\n.Lb118:\n"
    "ds_write_b128 v33, v[40:43] offset:0\nds_write_b128 v33, v[44:47] offset:1024\nds_write_b128 v33, v[48:51] offset:2048\nds_write_b128 v33, v[52:55] offset:3072\n"
    "v_mov_b32 v240, v35\nv_mov_b32 v241, v39\nglobal_load_dwordx4 v[252:255], v39, s[4:5] sc1\ns_waitcnt lgkmcnt(0)\ns_barrier\ns_call_b64 s[94:95], .Lgsa119\n"
    "s_add_u32 s43, s12, 0x400000\nv_lshl_add_u32 v16, v0, 4, s43\ns_lshl_b32 s42, s18, 13\ns_add_u32 s41, s12, s42\ns_add_u32 s41, s41, 0x200000\n"
    "v_lshl_add_u32 v17, v0, 4, s41\nv_lshrrev_b32 v127, 3, v0\nv_mul_u32_u24 v126, 0x50, v127\nv_and_b32 v125, 7, v0\nv_add_u32 v18, 0x1d550, v126\n"
    "v_lshl_add_u32 v18, v125, 3, v18\nv_bfe_u32 v124, v0, 4, 3\nv_xor_b32 v123, v0, v124\ns_mov_b32 s40, 0x19400\nv_lshl_add_u32 v19, v123, 3, s40\nv_bfe_u32 v122, v0, 4, 1\n"
    "v_xor_b32 v121, v0, v122\ns_mov_b32 s39, 0x17c00\nv_lshl_add_u32 v20, v121, 3, s39\ns_lshr_b32 s43, s12, 23\ns_add_u32 s42, s43, 3\nv_lshrrev_b32 v127, s42, v0\n"
    "s_lshr_b32 s41, s12, 22\ns_sub_u32 s40, 15, s41\nv_lshlrev_b32 v126, s40, v127\ns_lshr_b32 s39, s12, 20\ns_add_u32 s38, s39, 7\nv_and_b32 v125, s38, v0\n"
    "s_lshl_b32 s37, s18, 7\ns_add_u32 s36, s37, 0x600000\ns_lshl_b32 s35, s24, 18\ns_lshl_b32 s34, s25, 8\ns_add_u32 s33, s35, s34\ns_add_u32 s33, s33, 0x1200000\n"
    "s_cmp_lt_u32 s12, 1\ns_cselect_b32 s32, s36, s33\nv_add_u32 v21, s32, v126\nv_lshl_add_u32 v21, v125, 4, v21\nv_and_b32 v127, 63, v0\nv_lshlrev_b32 v126, 4, v127\n"
    "v_mov_b32 v125, 0xfc0\ns_lshr_b32 s43, s13, 1\ns_mul_i32 s42, s43, 26\nv_add_u32 v124, s42, v127\nv_cmp_gt_u32 vcc, 64, v124\ns_nop 1\n"
    "v_cndmask_b32 v123, v125, v126, vcc\ns_lshr_b32 s41, s3, 2\ns_mul_i32 s40, s41, 0x1950\ns_mul_i32 s39, s13, 0x150\ns_add_u32 s38, s40, s39\ns_add_u32 s38, s38, 0x1d000\n"
    "s_mul_i32 s37, s41, 0x3180\ns_and_b32 s36, s13, 1\ns_mul_i32 s35, s36, 0xfc0\ns_add_u32 s34, s37, s35\ns_add_u32 s34, s34, 0x20300\ns_cmp_lt_u32 s13, 2\n"
    "s_cselect_b32 s33, s38, s34\nv_add_u32 v22, s33, v123\nv_and_b32 v122, 31, v0\n.Lg115:\ns_waitcnt vmcnt(0)\nv_xor_b32 v252, s11, v252\nv_xor_b32 v253, s11, v253\n"
    "v_xor_b32 v254, s11, v254\nv_xor_b32 v255, s11, v255\nv_or3_b32 v224, v252, v253, v254\nv_or_b32 v224, v224, v255\nv_cmp_ne_u32 vcc, 0, v224\ns_cbranch_vccz .Lg116\n"
    "s_sub_u32 s80, s80, 1\ns_cbranch_scc1 .Lg116\ns_sleep 2\nglobal_load_dwordx4 v[252:255], v241, s[4:5] sc1\ns_cbranch_execz .Lhop\ns_branch .Lg115\n.Lg116:\n"
    "s_mov_b32 s80, 0x10000\ns_mov_b32 s86, 0x100000\n.Ls120:\nds_read_b32 v244, v58 offset:64\ns_waitcnt lgkmcnt(0)\nv_cmp_gt_i32 vcc, 2, v244\ns_sub_u32 s86, s86, 1\n"
    "s_cbranch_scc1 .Ls121\ns_cbranch_vccz .Ls121\ns_sleep 1\ns_branch .Ls120\n.Ls121:\ns_sleep 6\ns_branch .Lgst112\n.Lgsa119:\ns_lshl_b32 s43, s21, 15\n"
    "s_add_u32 s42, s12, s43\nv_lshl_add_u32 v192, v0, 4, s42\ns_lshl_b32 s41, s13, 10\ns_add_u32 s51, s41, 0x1f600\ns_mov_b32 s40, 0x1f600\nv_lshl_add_u32 v57, v0, 4, s40\n"
    "v_and_b32 v239, 3, v0\ns_mov_b32 s39, 0x27f80\nv_lshl_add_u32 v58, v239, 2, s39\nv_and_b32 v238, 0x7f, v0\ns_lshr_b32 s38, s13, 1\ns_mul_i32 s37, s38, 0x2100\n"
    "s_add_u32 s36, s37, 0x18000\nv_lshl_add_u32 v59, v238, 4, s36\nv_and_b32 v237, 31, v0\ns_mov_b32 s35, 0x27600\nv_lshl_add_u32 v39, v237, 2, s35\ns_lshl_b32 s34, s22, 13\n"
    "s_lshl_b32 s33, s21, 7\ns_add_u32 s32, s12, s34\ns_add_u32 s32, s32, s33\ns_add_u32 s32, s32, 0x400000\nv_lshl_add_u32 v56, v237, 2, s32\nv_add_u32 v193, 4096, v192\n"
    "v_add_u32 v194, 8192, v192\nv_add_u32 v195, 12288, v192\nv_add_u32 v196, 16384, v192\nv_add_u32 v197, 20480, v192\nv_add_u32 v198, 24576, v192\n"
    "v_add_u32 v199, 28672, v192\ns_mov_b32 s80, 0x10000\ns_setpc_b64 s[94:95]\n.Lb109:\ns_call_b64 s[94:95], .Lgsa119\ns_barrier\nv_alignbit_b32 v228, v11, v10, 16\n"
    "v_alignbit_b32 v229, v12, v11, 16\nv_alignbit_b32 v230, v13, v12, 16\nv_alignbit_b32 v231, v14, v13, 16\nds_write_b128 v59, v[10:13]\nv_mov_b32 v232, v11\n"
    "v_mov_b32 v233, v12\nv_mov_b32 v234, v13\nv_mov_b32 v235, v14\nds_write_b128 v59, v[228:231] offset:2112\nv_alignbit_b32 v236, v12, v11, 16\n"
    "v_alignbit_b32 v237, v13, v12, 16\nv_alignbit_b32 v238, v14, v13, 16\nv_alignbit_b32 v239, v15, v14, 16\nds_write_b128 v59, v[232:235] offset:4224\n"
    "ds_write_b128 v59, v[236:239] offset:6336\ns_waitcnt vmcnt(0)\ns_sleep 7\n.Lgst112:\ns_add_u32 m0, s51, 0\ns_nop 2\nglobal_load_lds_dwordx4 v192, s[4:5] sc1\n"
    "s_add_u32 m0, s51, 4096\ns_nop 2\nglobal_load_lds_dwordx4 v193, s[4:5] sc1\ns_add_u32 m0, s51, 8192\ns_nop 2\nglobal_load_lds_dwordx4 v194, s[4:5] sc1\n"
    "s_add_u32 m0, s51, 12288\ns_nop 2\nglobal_load_lds_dwordx4 v195, s[4:5] sc1\ns_add_u32 m0, s51, 16384\ns_nop 2\nglobal_load_lds_dwordx4 v196, s[4:5] sc1\n"
    "s_add_u32 m0, s51, 20480\ns_nop 2\nglobal_load_lds_dwordx4 v197, s[4:5] sc1\ns_add_u32 m0, s51, 24576\ns_nop 2\nglobal_load_lds_dwordx4 v198, s[4:5] sc1\n"
    "s_add_u32 m0, s51, 28672\ns_nop 2\nglobal_load_lds_dwordx4 v199, s[4:5] sc1\ns_cmp_lg_u32 s12, 0\ns_cbranch_scc1 .Lb118y\nv_sub_u32 v121, 0x3ff, v122\n"
    "v_bfe_u32 v120, v0, 5, 1\nv_lshl_add_u32 v119, v120, 3, v121\nv_and_b32 v118, 3, v119\nv_mul_u32_u24 v117, 0x840, v118\nv_lshrrev_b32 v116, 2, v119\n"
    "s_mul_i32 s32, s43, 0x2100\ns_add_u32 s31, s32, 0x17800\nv_add_u32 v23, s31, v117\nv_lshl_add_u32 v23, v116, 3, v23\ns_lshl_b32 s43, s13, 2\ns_add_u32 s42, s43, 0x27f80\n"
    "v_mov_b32 v25, s42\ns_lshr_b32 s41, s12, 23\ns_add_u32 s40, s41, 4\nv_mov_b32 v26, s40\nv_and_b32 v127, 31, v0\nv_mul_u32_u24 v126, 0x50, v127\nv_bfe_u32 v125, v0, 5, 1\n"
    "s_lshr_b32 s39, s13, 1\ns_mul_i32 s38, s39, 0x1950\ns_and_b32 s37, s13, 1\ns_mul_i32 s36, s37, 0xa00\ns_add_u32 s35, s38, s36\ns_add_u32 s35, s35, 0x1d000\n"
    "v_add_u32 v27, s35, v126\nv_lshl_add_u32 v27, v125, 4, v27\nv_bfe_u32 v124, v0, 1, 3\nv_xor_b32 v123, v125, v124\ns_mul_i32 s34, s39, 0x2800\ns_lshl_b32 s33, s37, 11\n"
    "s_add_u32 s32, s34, s33\ns_add_u32 s32, s32, 0x19400\nv_lshl_add_u32 v28, v127, 6, s32\nv_lshl_add_u32 v28, v123, 3, v28\nv_xor_b32 v29, 16, v28\nv_xor_b32 v30, 32, v28\n"
    "v_xor_b32 v31, 48, v28\ns_lshr_b32 s43, s12, 23\ns_lshl_b32 s42, s43, 13\ns_lshr_b32 s41, s18, 4\ns_lshl_b32 s40, s41, 7\ns_and_b32 s39, s18, 15\ns_lshl_b32 s38, s39, 2\n"
    "s_add_u32 s37, s42, s40\ns_add_u32 s37, s37, s38\ns_add_u32 s37, s37, 0x1008000\nv_mov_b32 v32, s37\nv_bfe_u32 v127, v0, 2, 4\nv_and_b32 v126, 3, v0\n"
    "s_mov_b32 s36, 0x1008000\nv_lshl_add_u32 v125, v127, 7, s36\nv_lshl_add_u32 v125, v126, 4, v125\nv_and_b32 v124, 7, v0\ns_lshl_b32 s35, s24, 3\n"
    "v_add_u32 v123, s35, v124\nv_lshrrev_b32 v122, 2, v123\nv_and_b32 v121, 3, v123\ns_mov_b32 s34, 0x100a000\nv_lshl_add_u32 v120, v122, 7, s34\n"
    "v_lshl_add_u32 v120, v121, 4, v120\ns_cmp_lt_u32 s12, 1\ns_cselect_b64 vcc, -1, 0\nv_cndmask_b32 v33, v120, v125, vcc\nv_and_b32 v119, 63, v0\ns_lshl_b32 s33, s18, 13\n"
    "s_lshl_b32 s32, s13, 11\ns_add_u32 s31, s33, s32\ns_add_u32 s31, s31, 0x600000\nv_lshl_add_u32 v40, v119, 4, s31\nv_bfe_u32 v127, v0, 4, 1\nv_and_b32 v126, 15, v0\n"
    "s_lshl_b32 s43, s18, 10\ns_lshl_b32 s42, s13, 8\ns_add_u32 s41, s43, s42\ns_add_u32 s41, s41, 0xd00000\nv_lshl_add_u32 v41, v127, 19, s41\n"
    "v_lshl_add_u32 v41, v126, 4, v41\ns_cbranch_execz .Lhop\nv_lshrrev_b32 v125, 5, v0\nv_and_b32 v124, 3, v0\ns_lshl_b32 s40, s18, 6\ns_add_u32 s39, s40, 0xd80000\n"
    "v_lshl_add_u32 v24, v125, 14, s39\nv_lshl_add_u32 v24, v124, 4, v24\nv_mov_b64 v[52:53], -1\nv_mov_b64 v[54:55], -1\ns_branch .Lb118x\n.Lb118y:\n"
    "s_add_u32 s43, s12, 0x400000\nv_lshl_add_u32 v16, v0, 4, s43\ns_lshl_b32 s42, s18, 13\ns_add_u32 s41, s12, s42\ns_add_u32 s41, s41, 0x200000\n"
    "v_lshl_add_u32 v17, v0, 4, s41\ns_lshr_b32 s43, s12, 23\ns_add_u32 s42, s43, 3\nv_lshrrev_b32 v127, s42, v0\ns_lshr_b32 s41, s12, 22\ns_sub_u32 s40, 15, s41\n"
    "v_lshlrev_b32 v126, s40, v127\ns_lshr_b32 s39, s12, 20\ns_add_u32 s38, s39, 7\nv_and_b32 v125, s38, v0\ns_lshl_b32 s37, s18, 7\ns_add_u32 s36, s37, 0x600000\n"
    "s_lshl_b32 s35, s24, 18\ns_lshl_b32 s34, s25, 8\ns_add_u32 s33, s35, s34\ns_add_u32 s33, s33, 0x1200000\ns_cmp_lt_u32 s12, 1\ns_cselect_b32 s32, s36, s33\n"
    "v_add_u32 v21, s32, v126\nv_lshl_add_u32 v21, v125, 4, v21\ns_add_u32 s31, s43, 4\nv_mov_b32 v26, s31\ns_lshr_b32 s43, s12, 23\ns_lshl_b32 s42, s43, 13\n"
    "s_lshr_b32 s41, s18, 4\ns_lshl_b32 s40, s41, 7\ns_and_b32 s39, s18, 15\ns_lshl_b32 s38, s39, 2\ns_add_u32 s37, s42, s40\ns_add_u32 s37, s37, s38\n"
    "s_add_u32 s37, s37, 0x1008000\nv_mov_b32 v32, s37\nv_bfe_u32 v127, v0, 2, 4\nv_and_b32 v126, 3, v0\ns_mov_b32 s36, 0x1008000\nv_lshl_add_u32 v125, v127, 7, s36\n"
    "v_lshl_add_u32 v125, v126, 4, v125\nv_and_b32 v124, 7, v0\ns_lshl_b32 s35, s24, 3\nv_add_u32 v123, s35, v124\nv_lshrrev_b32 v122, 2, v123\nv_and_b32 v121, 3, v123\n"
    "s_mov_b32 s34, 0x100a000\nv_lshl_add_u32 v120, v122, 7, s34\nv_lshl_add_u32 v120, v121, 4, v120\ns_cmp_lt_u32 s12, 1\ns_cselect_b64 vcc, -1, 0\n"
    "v_cndmask_b32 v33, v120, v125, vcc\ns_mov_b32 s33, 0x100e000\nv_lshl_add_u32 v240, v127, 7, s33\nv_lshl_add_u32 v240, v126, 4, v240\nv_and_b32 v119, 63, v0\n"
    "s_lshl_b32 s32, s18, 13\ns_lshl_b32 s31, s13, 11\ns_add_u32 s30, s32, s31\ns_add_u32 s30, s30, 0x1200000\nv_lshl_add_u32 v40, v119, 4, s30\nv_mov_b64 v[52:53], -1\n"
    "v_mov_b64 v[54:55], -1\n.Lb118x:\ns_setprio 3\ns_waitcnt vmcnt(7)\n.Lr122:\nds_read_b128 v[208:211], v57 offset:0\ns_waitcnt lgkmcnt(0)\nv_max3_u32 v244, v208, v209, 0\n"
    "v_max3_u32 v244, v210, v211, v244\nv_cmp_eq_u32 vcc, -1, v244\ns_cbranch_vccnz .Lw123\n.Lw123x:\ns_barrier\ns_waitcnt vmcnt(6)\n.Lr124:\n"
    "ds_read_b128 v[212:215], v57 offset:4096\ns_waitcnt lgkmcnt(0)\nv_max3_u32 v244, v212, v213, 0\nv_max3_u32 v244, v214, v215, v244\nv_cmp_eq_u32 vcc, -1, v244\n"
    "s_cbranch_vccnz .Lw125\n.Lw125x:\ns_barrier\ns_waitcnt vmcnt(5)\n.Lr126:\nds_read_b128 v[216:219], v57 offset:8192\ns_waitcnt vmcnt(4)\n"
    "ds_read_b128 v[220:223], v57 offset:12288\ns_waitcnt lgkmcnt(1)\nv_max3_u32 v244, v216, v217, 0\nv_max3_u32 v244, v218, v219, v244\ns_waitcnt lgkmcnt(0)\n"
    "v_max3_u32 v244, v220, v221, v244\nv_max3_u32 v244, v222, v223, v244\nv_cmp_eq_u32 vcc, -1, v244\ns_cbranch_vccnz .Lw127\n.Lw127x:\ns_barrier\ns_waitcnt vmcnt(3)\n"
    ".Lr128:\nds_read_b128 v[224:227], v57 offset:16384\ns_waitcnt vmcnt(2)\nds_read_b128 v[228:231], v57 offset:20480\ns_waitcnt lgkmcnt(1)\nv_max3_u32 v244, v224, v225, 0\n"
    "v_max3_u32 v244, v226, v227, v244\ns_waitcnt lgkmcnt(0)\nv_max3_u32 v244, v228, v229, v244\nv_max3_u32 v244, v230, v231, v244\nv_cmp_eq_u32 vcc, -1, v244\n"
    "s_cbranch_vccnz .Lw129\n.Lw129x:\ns_barrier\ns_waitcnt vmcnt(0)\ns_cmp_lg_u32 s13, 1\ns_cbranch_scc1 .Lw129z\nglobal_load_dwordx4 v[252:255], v240, s[4:5] sc1\n.Lw129z:\n"
    "global_store_dwordx4 v40, v[52:55], s[4:5] sc1\nglobal_store_dwordx4 v40, v[52:55], s[4:5] offset:1024 sc1\ns_cmp_lg_u32 s12, 0\ns_cbranch_scc1 .Lw129y\n"
    "global_store_dwordx4 v41, v[52:55], s[4:5] sc1\n.Lw129y:\n.Lr130:\nds_read_b128 v[232:235], v57 offset:24576\nds_read_b128 v[236:239], v57 offset:28672\n"
    "s_waitcnt lgkmcnt(1)\nv_max3_u32 v244, v232, v233, 0\nv_max3_u32 v244, v234, v235, v244\ns_waitcnt lgkmcnt(0)\nv_max3_u32 v244, v236, v237, v244\n"
    "v_max3_u32 v244, v238, v239, v244\nv_cmp_eq_u32 vcc, -1, v244\ns_cbranch_vccnz .Lw131\n.Lw131x:\ns_setprio 0\ns_barrier\n"
    "s_add_u32 s58, s4, 8192\ns_addc_u32 s59, s5, 0\ns_add_u32 s60, s4, 16384\ns_addc_u32 s61, s5, 0\ns_add_u32 s62, s4, 24576\ns_addc_u32 s63, s5, 0\n"
    "s_mov_b32 s45, 0x3c000000\ns_cmp_lg_u32 s13, 0\ns_cbranch_scc1 .Lg136\ns_waitcnt vmcnt(0)\ns_setprio 3\ns_mov_b32 exec_hi, 0\ns_barrier\n"
    "ds_read2_b32 v[44:45], v39 offset0:0 offset1:32\nds_read2_b32 v[46:47], v39 offset0:64 offset1:96\nds_read2_b32 v[48:49], v39 offset0:128 offset1:160\n"
    "ds_read2_b32 v[50:51], v39 offset0:192 offset1:224\ns_waitcnt lgkmcnt(0)\nv_pk_add_f32 v[44:45], v[44:45], v[46:47]\nv_pk_add_f32 v[48:49], v[48:49], v[50:51]\n"
    "v_pk_add_f32 v[40:41], v[44:45], v[48:49]\nv_mul_f32 v40, s45, v40\nv_mul_f32 v42, v40, v40\nv_fma_f32 v41, v41, s45, -v42\nv_max_f32 v41, 0, v41\nv_sqrt_f32 v41, v41\n"
    "s_nop 0\nv_cvt_pk_bf16_f32 v40, v40, v41\nglobal_store_dword v56, v40, s[4:5] sc1\ns_mov_b32 exec_hi, -1\ns_setprio 0\ns_branch .Lln114\n.Lg136:\ns_cmp_eq_u32 s13, 1\n"
    "s_cbranch_scc1 .Lg135\n.Lg134:\ns_waitcnt vmcnt(0)\ns_barrier\ns_branch .Lln114\n.Lg135:\ns_waitcnt vmcnt(2)\ns_cmp_lg_u32 s12, 0\ns_cbranch_scc1 .Lg137\n"
    "s_mov_b32 s80, 0x10000\n.Lg133:\nv_xor_b32 v252, s11, v252\nv_xor_b32 v253, s11, v253\nv_xor_b32 v254, s11, v254\nv_xor_b32 v255, s11, v255\n"
    "v_or3_b32 v224, v252, v253, v254\nv_or_b32 v224, v224, v255\nv_cmp_ne_u32 vcc, 0, v224\ns_cbranch_vccz .Lg134\ns_sub_u32 s80, s80, 1\ns_cbranch_scc1 .Lg134\n"
    "s_cbranch_execz .Lhop\ns_sleep 2\nglobal_load_dwordx4 v[252:255], v240, s[4:5] sc1\ns_waitcnt vmcnt(0)\ns_branch .Lg133\n.Lg137:\ns_sub_u32 s87, s100, 300\n"
    "v_subrev_u32 v252, s87, v252\nv_subrev_u32 v253, s87, v253\nv_subrev_u32 v254, s87, v254\nv_subrev_u32 v255, s87, v255\nv_max3_u32 v252, v252, v253, v254\n"
    "v_max_u32 v252, v252, v255\ns_nop 1\nv_max_u32_dpp v252, v252, v252 row_ror:1 row_mask:0xf bank_mask:0xf\ns_nop 1\n"
    "v_max_u32_dpp v252, v252, v252 row_ror:2 row_mask:0xf bank_mask:0xf\ns_nop 1\nv_max_u32_dpp v252, v252, v252 row_ror:4 row_mask:0xf bank_mask:0xf\ns_nop 1\n"
    "v_max_u32_dpp v252, v252, v252 row_ror:8 row_mask:0xf bank_mask:0xf\nv_mov_b32 v253, v252\ns_nop 1\nv_permlane16_swap_b32 v252, v253\nv_max_u32 v252, v252, v253\n"
    "v_mov_b32 v253, v252\ns_nop 1\nv_permlane32_swap_b32 v252, v253\nv_max_u32 v252, v252, v253\ns_nop 0\nv_readfirstlane_b32 s87, v252\ns_min_u32 s87, s87, 400\n"
    "s_max_u32 s87, s87, 300\ns_sub_u32 s87, s87, 300\ns_add_u32 s78, s78, 5200\ns_waitcnt vmcnt(0)\ns_barrier\ns_memrealtime s[100:101]\ns_memtime s[76:77]\n"
    "s_waitcnt lgkmcnt(0)\ns_sub_u32 s76, s76, s78\ns_max_i32 s76, s76, 0\ns_mul_i32 s76, s76, 49\ns_lshr_b32 s76, s76, 10\ns_sub_u32 s87, s87, s76\ns_max_i32 s87, s87, 0\n"
    "s_waitcnt lgkmcnt(0)\ns_add_u32 s87, s87, s100\ns_add_u32 s87, s87, 30\n.Lj138:\ns_memrealtime s[100:101]\ns_waitcnt lgkmcnt(0)\ns_sub_u32 s101, s87, s100\n"
    "s_cmp_le_i32 s101, 0\ns_cbranch_scc0 .Lj138\ns_branch .Lln114\n"
    ".Lhb110:\ns_cmp_lg_u32 s12, 0\ns_cbranch_scc1 .Lh139\ns_setprio 1\ns_cmp_eq_u32 s13, 0\ns_cbranch_scc1 .Lh139y\n.Lh139x:\ns_cmp_eq_u32 s26, 0\ns_waitcnt lgkmcnt(0)\n"
    "s_barrier\ns_cbranch_scc1 .Lh140\n.Lh141:\nds_read_b64 v[216:217], v252\nds_read_b128 v[192:195], v60 offset:0\nds_read_b128 v[196:199], v60 offset:1024\n"
    "ds_read_b128 v[200:203], v60 offset:4096\nds_read_b128 v[204:207], v60 offset:5120\nv_mov_b32 v220, 0\nv_mov_b32 v221, 2\ns_mov_b64 exec, 1\n"
    "ds_write_b32 v25, v221 offset:64\ns_mov_b64 exec, -1\nds_write_b16 v254, v220 offset:1400\ns_waitcnt vmcnt(17)\n"
    "global_load_dwordx4 v[128:131], v59, s[72:73] offset:-4096 sc1\nv_add_u32_sdwa v240, v254, v48 dst_sel:DWORD dst_unused:UNUSED_PAD src0_sel:DWORD src1_sel:WORD_0\n"
    "v_add_u32_sdwa v241, v254, v48 dst_sel:DWORD dst_unused:UNUSED_PAD src0_sel:DWORD src1_sel:WORD_1\n"
    "v_add_u32_sdwa v242, v254, v49 dst_sel:DWORD dst_unused:UNUSED_PAD src0_sel:DWORD src1_sel:WORD_0\n"
    "v_add_u32_sdwa v243, v254, v49 dst_sel:DWORD dst_unused:UNUSED_PAD src0_sel:DWORD src1_sel:WORD_1\n"
    "v_add_u32_sdwa v244, v254, v50 dst_sel:DWORD dst_unused:UNUSED_PAD src0_sel:DWORD src1_sel:WORD_0\nglobal_load_dwordx4 v[132:135], v59, s[72:73] offset:-3072 sc1\n"
    "v_add_u32_sdwa v245, v254, v50 dst_sel:DWORD dst_unused:UNUSED_PAD src0_sel:DWORD src1_sel:WORD_1\n"
    "v_add_u32_sdwa v246, v254, v51 dst_sel:DWORD dst_unused:UNUSED_PAD src0_sel:DWORD src1_sel:WORD_0\n"
    "v_add_u32_sdwa v247, v254, v51 dst_sel:DWORD dst_unused:UNUSED_PAD src0_sel:DWORD src1_sel:WORD_1\ns_waitcnt lgkmcnt(6)\nv_mov_b32 v218, 3\nv_add_f32 v216, v216, v217\n"
    "global_load_dwordx4 v[136:139], v59, s[72:73] offset:-2048 sc1\nv_lshrrev_b32 v216, 23, v216\nv_sub_u32 v216, 0x10c, v216\nv_min_u32 v216, 0xfc, v216\n"
    "v_sub_u32 v217, 0xfe, v216\nv_lshlrev_b32 v216, 23, v216\nglobal_load_dwordx4 v[140:143], v59, s[72:73] offset:-1024 sc1\nv_lshlrev_b32 v217, 23, v217\n"
    "s_waitcnt lgkmcnt(2)\nv_pk_add_f32 v[208:209], v[192:193], v[200:201]\nv_pk_add_f32 v[210:211], v[194:195], v[202:203]\nv_pk_add_f32 v[212:213], v[196:197], v[204:205]\n"
    "v_pk_add_f32 v[214:215], v[198:199], v[206:207]\nglobal_load_dwordx4 v[144:147], v59, s[72:73] offset:0 sc1\n"
    "v_pk_mul_f32 v[208:209], v[208:209], v[216:217] op_sel_hi:[1,0]\nv_pk_mul_f32 v[210:211], v[210:211], v[216:217] op_sel_hi:[1,0]\n"
    "v_pk_mul_f32 v[212:213], v[212:213], v[216:217] op_sel_hi:[1,0]\nv_pk_mul_f32 v[214:215], v[214:215], v[216:217] op_sel_hi:[1,0]\nv_cvt_pk_f16_f32 v208, v208, v209\n"
    "global_load_dwordx4 v[148:151], v59, s[72:73] offset:1024 sc1\nv_cvt_pk_f16_f32 v209, v210, v211\nv_cvt_pk_f16_f32 v210, v212, v213\nv_cvt_pk_f16_f32 v211, v214, v215\n"
    "ds_write_b16 v240, v208\nds_write_b16_d16_hi v241, v208\nglobal_load_dwordx4 v[152:155], v59, s[72:73] offset:2048 sc1\nds_write_b16 v242, v209\n"
    "ds_write_b16_d16_hi v243, v209\nds_write_b16 v244, v210\nds_write_b16_d16_hi v245, v210\nds_write_b16 v246, v211\n"
    "global_load_dwordx4 v[156:159], v59, s[72:73] offset:3072 sc1\nds_write_b16_d16_hi v247, v211\ns_mov_b64 exec, 1\nds_write_b32 v25, v218 offset:64\n"
    "ds_write_b32 v252, v217 offset:32\ns_mov_b64 exec, -1\nv_add_u32_sdwa v232, v254, v52 dst_sel:DWORD dst_unused:UNUSED_PAD src0_sel:DWORD src1_sel:WORD_0\n"
    "v_add_u32_sdwa v233, v254, v52 dst_sel:DWORD dst_unused:UNUSED_PAD src0_sel:DWORD src1_sel:WORD_1\n"
    "v_add_u32_sdwa v234, v254, v53 dst_sel:DWORD dst_unused:UNUSED_PAD src0_sel:DWORD src1_sel:WORD_0\n"
    "v_add_u32_sdwa v235, v254, v53 dst_sel:DWORD dst_unused:UNUSED_PAD src0_sel:DWORD src1_sel:WORD_1\nglobal_load_dwordx4 v[160:163], v59, s[74:75] offset:-4096 sc1\n"
    "v_add_u32_sdwa v236, v254, v54 dst_sel:DWORD dst_unused:UNUSED_PAD src0_sel:DWORD src1_sel:WORD_0\n"
    "v_add_u32_sdwa v237, v254, v54 dst_sel:DWORD dst_unused:UNUSED_PAD src0_sel:DWORD src1_sel:WORD_1\n"
    "v_add_u32_sdwa v238, v254, v55 dst_sel:DWORD dst_unused:UNUSED_PAD src0_sel:DWORD src1_sel:WORD_0\n"
    "v_add_u32_sdwa v239, v254, v55 dst_sel:DWORD dst_unused:UNUSED_PAD src0_sel:DWORD src1_sel:WORD_1\n"
    "v_add_u32_sdwa v248, v254, v62 dst_sel:DWORD dst_unused:UNUSED_PAD src0_sel:DWORD src1_sel:WORD_0\nglobal_load_dwordx4 v[164:167], v59, s[74:75] offset:-3072 sc1\n"
    "v_add_u32_sdwa v249, v254, v62 dst_sel:DWORD dst_unused:UNUSED_PAD src0_sel:DWORD src1_sel:WORD_1\n"
    "v_add_u32_sdwa v250, v254, v63 dst_sel:DWORD dst_unused:UNUSED_PAD src0_sel:DWORD src1_sel:WORD_0\ns_mov_b32 s86, 0x100000\n.Ls148:\nds_read_b32 v216, v61 offset:0\n"
    "s_waitcnt lgkmcnt(0)\nv_cmp_gt_i32 vcc, 3, v216\ns_sub_u32 s86, s86, 1\ns_cbranch_scc1 .Ls149\ns_cbranch_vccz .Ls149\ns_branch .Ls148\n.Ls149:\nds_read_u16 v200, v232\n"
    "ds_read_u16 v201, v233\nglobal_load_dwordx4 v[168:171], v59, s[74:75] offset:-2048 sc1\nds_read_u16 v202, v234\nds_read_u16 v203, v235\nds_read_u16 v204, v236\n"
    "ds_read_u16 v205, v237\nds_read_u16 v206, v238\nglobal_load_dwordx4 v[172:175], v59, s[74:75] offset:-1024 sc1\nds_read_u16 v207, v239\nds_read_u16 v208, v248\n"
    "ds_read_u16 v209, v249\nds_read_u16 v210, v250\ns_waitcnt lgkmcnt(0)\nv_lshl_or_b32 v192, v201, 16, v200\nglobal_load_dwordx4 v[176:179], v59, s[74:75] offset:0 sc1\n"
    "v_lshl_or_b32 v193, v203, 16, v202\ns_cbranch_execz .Lhop\nv_lshl_or_b32 v194, v205, 16, v204\nv_lshl_or_b32 v195, v207, 16, v206\nv_lshl_or_b32 v196, v209, 16, v208\n"
    "v_mov_b32 v197, v210\nglobal_load_dwordx4 v[180:183], v59, s[74:75] offset:1024 sc1\nv_alignbit_b32 v212, v193, v192, 16\nv_alignbit_b32 v213, v194, v193, 16\n"
    "v_alignbit_b32 v214, v195, v194, 16\nv_alignbit_b32 v215, v196, v195, 16\nds_write_b128 v253, v[192:195]\nglobal_load_dwordx4 v[184:187], v59, s[74:75] offset:2048 sc1\n"
    "v_mov_b32 v216, v193\nv_mov_b32 v217, v194\nv_mov_b32 v218, v195\nv_mov_b32 v219, v196\nds_write_b128 v253, v[212:215] offset:2112\n"
    "global_load_dwordx4 v[188:191], v59, s[74:75] offset:3072 sc1\nv_alignbit_b32 v220, v194, v193, 16\nv_alignbit_b32 v221, v195, v194, 16\n"
    "v_alignbit_b32 v222, v196, v195, 16\nv_alignbit_b32 v223, v197, v196, 16\nds_write_b128 v253, v[216:219] offset:4224\nds_write_b128 v253, v[220:223] offset:6336\n"
    "s_waitcnt vmcnt(16)\nv_permlane32_swap_b32 v224, v226\nv_permlane32_swap_b32 v225, v227\nv_permlane32_swap_b32 v228, v230\nv_permlane32_swap_b32 v229, v231\n"
    "v_mov_b64 v[240:241], v[224:225]\nv_mov_b64 v[242:243], v[226:227]\nv_mov_b64 v[244:245], v[228:229]\nv_mov_b64 v[246:247], v[230:231]\nv_mov_b64 v[252:253], -1\n"
    "v_mov_b64 v[254:255], -1\ns_branch .Lhwo113\n.Lh139y:\nv_mov_b32 v240, s11\nv_add_u32 v58, 0x2000, v58\ns_mov_b64 exec, 1\ns_waitcnt vmcnt(19)\n"
    "global_store_dword v58, v240, s[4:5] sc1\ns_mov_b64 exec, -1\ns_branch .Lh139x\n.Lh139:\ns_barrier\nglobal_load_dword v240, v47, s[4:5] offset:0 sc1\n"
    "global_load_dword v241, v47, s[4:5] offset:128 sc1\nglobal_load_dword v242, v47, s[4:5] offset:512 sc1\nglobal_load_dword v243, v47, s[4:5] offset:640 sc1\n"
    "global_load_dword v244, v47, s[4:5] offset:1024 sc1\nglobal_load_dword v245, v47, s[4:5] offset:1152 sc1\nglobal_load_dword v246, v47, s[4:5] offset:1536 sc1\n"
    "global_load_dword v247, v47, s[4:5] offset:1664 sc1\nglobal_load_dwordx4 v[128:131], v59, s[72:73] offset:-4096 sc1\nv_bfe_u32 v223, v0, 5, 1\nv_and_b32 v222, 31, v0\n"
    "s_lshl_b32 s43, s13, 11\nglobal_load_dwordx4 v[132:135], v59, s[72:73] offset:-3072 sc1\ns_add_u32 s42, s43, 0x1d000\nv_lshl_add_u32 v40, v223, 8, s42\n"
    "v_lshl_add_u32 v40, v222, 2, v40\nv_and_b32 v221, 63, v0\nglobal_load_dwordx4 v[136:139], v59, s[72:73] offset:-2048 sc1\nv_lshl_add_u32 v41, v221, 4, s42\n"
    "s_mov_b32 s41, 0x1f600\nv_lshl_add_u32 v42, v221, 4, s41\nv_bfe_u32 v220, v0, 3, 3\nglobal_load_dwordx4 v[140:143], v59, s[72:73] offset:-1024 sc1\n"
    "v_and_b32 v219, 7, v0\ns_lshl_b32 s40, s22, 19\ns_lshl_b32 s39, s13, 17\ns_lshl_b32 s38, s21, 7\nglobal_load_dwordx4 v[144:147], v59, s[72:73] offset:0 sc1\n"
    "s_add_u32 s37, s12, s40\ns_add_u32 s37, s37, s39\ns_add_u32 s37, s37, s38\nglobal_load_dwordx4 v[148:151], v59, s[72:73] offset:1024 sc1\ns_add_u32 s37, s37, 0x200000\n"
    "v_lshl_add_u32 v43, v220, 13, s37\nv_lshl_add_u32 v43, v219, 4, v43\ns_add_u32 s36, s12, s40\nglobal_load_dwordx4 v[152:155], v59, s[72:73] offset:2048 sc1\n"
    "s_add_u32 s36, s36, s39\ns_add_u32 s36, s36, s38\ns_add_u32 s36, s36, 0x210000\nv_lshl_add_u32 v44, v220, 13, s36\n"
    "global_load_dwordx4 v[156:159], v59, s[72:73] offset:3072 sc1\nv_lshl_add_u32 v44, v219, 4, v44\ns_lshl_b32 s35, s13, 8\ns_add_u32 s34, s35, 0x27600\n"
    "v_lshl_add_u32 v45, v221, 2, s34\nglobal_load_dwordx4 v[160:163], v59, s[74:75] offset:-4096 sc1\ns_add_u32 s33, s43, 0x1d400\nv_lshl_add_u32 v46, v223, 8, s33\n"
    "v_lshl_add_u32 v46, v222, 2, v46\nglobal_load_dwordx4 v[164:167], v59, s[74:75] offset:-3072 sc1\ns_add_u32 s32, s12, 0x400000\nv_lshl_add_u32 v16, v0, 4, s32\n"
    "s_lshl_b32 s31, s18, 13\ns_add_u32 s30, s12, s31\nglobal_load_dwordx4 v[168:171], v59, s[74:75] offset:-2048 sc1\ns_add_u32 s30, s30, 0x200000\n"
    "v_lshl_add_u32 v17, v0, 4, s30\ns_lshr_b32 s43, s12, 23\ns_add_u32 s42, s43, 3\nglobal_load_dwordx4 v[172:175], v59, s[74:75] offset:-1024 sc1\n"
    "v_lshrrev_b32 v223, s42, v0\ns_lshr_b32 s41, s12, 22\ns_sub_u32 s40, 15, s41\nv_lshlrev_b32 v222, s40, v223\nglobal_load_dwordx4 v[176:179], v59, s[74:75] offset:0 sc1\n"
    "s_lshr_b32 s39, s12, 20\ns_add_u32 s38, s39, 7\nv_and_b32 v221, s38, v0\nglobal_load_dwordx4 v[180:183], v59, s[74:75] offset:1024 sc1\ns_lshl_b32 s37, s18, 7\n"
    "s_add_u32 s36, s37, 0x600000\ns_lshl_b32 s35, s24, 18\ns_lshl_b32 s34, s25, 8\nglobal_load_dwordx4 v[184:187], v59, s[74:75] offset:2048 sc1\ns_add_u32 s33, s35, s34\n"
    "s_add_u32 s33, s33, 0x1200000\ns_cmp_lt_u32 s12, 1\ns_cselect_b32 s32, s36, s33\nglobal_load_dwordx4 v[188:191], v59, s[74:75] offset:3072 sc1\nv_add_u32 v21, s32, v222\n"
    "v_lshl_add_u32 v21, v221, 4, v21\ns_add_u32 s31, s43, 4\nv_mov_b32 v26, s31\ns_waitcnt vmcnt(16)\ns_mov_b32 s80, 0x10000\nv_max3_u32 v232, v240, v241, v242\n"
    "v_max3_u32 v233, v243, v244, v245\nv_max3_u32 v232, v232, v246, v247\nv_max_u32 v232, v232, v233\nv_cmp_eq_u32 vcc, -1, v232\ns_cbranch_vccnz .Lw147\n"
    ".Lhwo113:\nv_lshlrev_b32 v192, 16, v240\nv_and_b32 v193, 0xffff0000, v240\nv_lshlrev_b32 v194, 16, v241\nv_and_b32 v195, 0xffff0000, v241\nv_lshlrev_b32 v196, 16, v242\n"
    "v_and_b32 v197, 0xffff0000, v242\nv_lshlrev_b32 v198, 16, v243\nv_and_b32 v199, 0xffff0000, v243\nv_lshlrev_b32 v200, 16, v244\nv_and_b32 v201, 0xffff0000, v244\n"
    "v_lshlrev_b32 v202, 16, v245\nv_and_b32 v203, 0xffff0000, v245\nv_lshlrev_b32 v204, 16, v246\nv_and_b32 v205, 0xffff0000, v246\nv_lshlrev_b32 v206, 16, v247\n"
    "v_and_b32 v207, 0xffff0000, v247\ns_add_u32 s58, s4, 8192\ns_addc_u32 s59, s5, 0\ns_add_u32 s60, s4, 16384\ns_addc_u32 s61, s5, 0\ns_add_u32 s62, s4, 24576\n"
    "s_addc_u32 s63, s5, 0\ns_add_u32 s54, s4, 2097152\ns_addc_u32 s55, s5, 0\ns_mov_b32 s48, 0x3f803f80\ns_waitcnt lgkmcnt(0)\ns_setprio 2\ns_barrier\n"
    "ds_read_b128 v[208:211], v42 offset:0\nds_read_b128 v[212:215], v42 offset:1024\ns_nop 0\ns_cbranch_execz .Lhop\nds_read_b128 v[216:219], v42 offset:2048\n"
    "ds_read_b128 v[220:223], v42 offset:3072\ns_waitcnt lgkmcnt(3)\nv_mfma_f32_32x32x16_bf16 v[192:207], v[64:67], v[208:211], v[192:207]\ns_barrier\n"
    "ds_read_b128 v[224:227], v42 offset:4096\ns_waitcnt lgkmcnt(3)\nv_mfma_f32_32x32x16_bf16 v[192:207], v[68:71], v[212:215], v[192:207]\n"
    "ds_read_b128 v[228:231], v42 offset:5120\ns_waitcnt lgkmcnt(3)\nv_mfma_f32_32x32x16_bf16 v[192:207], v[72:75], v[216:219], v[192:207]\n"
    "ds_read_b128 v[232:235], v42 offset:6144\ns_waitcnt lgkmcnt(3)\nv_mfma_f32_32x32x16_bf16 v[192:207], v[76:79], v[220:223], v[192:207]\n"
    "ds_read_b128 v[208:211], v42 offset:7168\ns_waitcnt lgkmcnt(3)\nv_mfma_f32_32x32x16_bf16 v[192:207], v[80:83], v[224:227], v[192:207]\ns_barrier\n"
    "ds_read_b128 v[212:215], v42 offset:8192\ns_waitcnt lgkmcnt(3)\nv_mfma_f32_32x32x16_bf16 v[192:207], v[84:87], v[228:231], v[192:207]\n"
    "ds_read_b128 v[216:219], v42 offset:9216\ns_waitcnt lgkmcnt(3)\nv_mfma_f32_32x32x16_bf16 v[192:207], v[88:91], v[232:235], v[192:207]\n"
    "ds_read_b128 v[220:223], v42 offset:10240\ns_waitcnt lgkmcnt(3)\nv_mfma_f32_32x32x16_bf16 v[192:207], v[92:95], v[208:211], v[192:207]\n"
    "ds_read_b128 v[224:227], v42 offset:11264\ns_waitcnt lgkmcnt(3)\nv_mfma_f32_32x32x16_bf16 v[192:207], v[96:99], v[212:215], v[192:207]\n"
    "ds_read_b128 v[228:231], v42 offset:12288\ns_waitcnt lgkmcnt(3)\nv_mfma_f32_32x32x16_bf16 v[192:207], v[100:103], v[216:219], v[192:207]\n"
    "ds_read_b128 v[232:235], v42 offset:13312\ns_waitcnt lgkmcnt(3)\nv_mfma_f32_32x32x16_bf16 v[192:207], v[104:107], v[220:223], v[192:207]\n"
    "ds_read_b128 v[208:211], v42 offset:14336\ns_waitcnt lgkmcnt(3)\nv_mfma_f32_32x32x16_bf16 v[192:207], v[108:111], v[224:227], v[192:207]\n"
    "ds_read_b128 v[212:215], v42 offset:15360\ns_waitcnt lgkmcnt(3)\nv_mfma_f32_32x32x16_bf16 v[192:207], v[112:115], v[228:231], v[192:207]\ns_barrier\n"
    "ds_read_b128 v[216:219], v42 offset:16384\nds_read_b128 v[220:223], v42 offset:17408\ns_waitcnt lgkmcnt(4)\n"
    "v_mfma_f32_32x32x16_bf16 v[192:207], v[116:119], v[232:235], v[192:207]\nds_read_b128 v[224:227], v42 offset:18432\ns_waitcnt lgkmcnt(4)\n"
    "v_mfma_f32_32x32x16_bf16 v[192:207], v[120:123], v[208:211], v[192:207]\nds_read_b128 v[228:231], v42 offset:19456\ns_waitcnt lgkmcnt(4)\n"
    "v_mfma_f32_32x32x16_bf16 v[192:207], v[124:127], v[212:215], v[192:207]\nds_read_b128 v[232:235], v42 offset:20480\ns_waitcnt vmcnt(8) lgkmcnt(4)\n"
    "v_mfma_f32_32x32x16_bf16 v[192:207], v[128:131], v[216:219], v[192:207]\nds_read_b128 v[208:211], v42 offset:21504\nds_read_b128 v[212:215], v42 offset:22528\n"
    "s_waitcnt lgkmcnt(5)\nv_mfma_f32_32x32x16_bf16 v[192:207], v[132:135], v[220:223], v[192:207]\nds_read_b128 v[216:219], v42 offset:23552\ns_waitcnt lgkmcnt(5)\n"
    "v_mfma_f32_32x32x16_bf16 v[192:207], v[136:139], v[224:227], v[192:207]\ns_barrier\nds_read_b128 v[220:223], v42 offset:24576\ns_waitcnt lgkmcnt(5)\n"
    "v_mfma_f32_32x32x16_bf16 v[192:207], v[140:143], v[228:231], v[192:207]\nds_read_b128 v[224:227], v42 offset:25600\ns_waitcnt lgkmcnt(5)\n"
    "v_mfma_f32_32x32x16_bf16 v[192:207], v[144:147], v[232:235], v[192:207]\nds_read_b128 v[228:231], v42 offset:26624\nds_read_b128 v[232:235], v42 offset:27648\n"
    "s_waitcnt lgkmcnt(6)\nv_mfma_f32_32x32x16_bf16 v[192:207], v[148:151], v[208:211], v[192:207]\nds_read_b128 v[208:211], v42 offset:28672\ns_waitcnt lgkmcnt(6)\n"
    "v_mfma_f32_32x32x16_bf16 v[192:207], v[152:155], v[212:215], v[192:207]\nds_read_b128 v[212:215], v42 offset:29696\ns_waitcnt lgkmcnt(6)\n"
    "v_mfma_f32_32x32x16_bf16 v[192:207], v[156:159], v[216:219], v[192:207]\nds_read_b128 v[216:219], v42 offset:30720\ns_waitcnt vmcnt(0) lgkmcnt(6)\n"
    "v_mfma_f32_32x32x16_bf16 v[192:207], v[160:163], v[220:223], v[192:207]\nds_read_b128 v[220:223], v42 offset:31744\ns_waitcnt lgkmcnt(6)\n"
    "v_mfma_f32_32x32x16_bf16 v[192:207], v[164:167], v[224:227], v[192:207]\ns_waitcnt lgkmcnt(5)\nv_mfma_f32_32x32x16_bf16 v[192:207], v[168:171], v[228:231], v[192:207]\n"
    "s_waitcnt lgkmcnt(4)\nv_mfma_f32_32x32x16_bf16 v[192:207], v[172:175], v[232:235], v[192:207]\ns_waitcnt lgkmcnt(3)\n"
    "v_mfma_f32_32x32x16_bf16 v[192:207], v[176:179], v[208:211], v[192:207]\ns_waitcnt lgkmcnt(2)\nv_mfma_f32_32x32x16_bf16 v[192:207], v[180:183], v[212:215], v[192:207]\n"
    "s_waitcnt lgkmcnt(1)\nv_mfma_f32_32x32x16_bf16 v[192:207], v[184:187], v[216:219], v[192:207]\ns_waitcnt lgkmcnt(0)\n"
    "v_mfma_f32_32x32x16_bf16 v[192:207], v[188:191], v[220:223], v[192:207]\ns_nop 11\nv_cvt_pk_bf16_f32 v208, v192, v193\nv_cvt_pk_bf16_f32 v209, v194, v195\n"
    "v_cvt_pk_bf16_f32 v210, v196, v197\nv_cvt_pk_bf16_f32 v211, v198, v199\nv_cvt_pk_bf16_f32 v212, v200, v201\nv_cvt_pk_bf16_f32 v213, v202, v203\n"
    "v_cvt_pk_bf16_f32 v214, v204, v205\nv_cvt_pk_bf16_f32 v215, v206, v207\nv_dot2_f32_bf16 v56, v208, s48, 0\nv_dot2_f32_bf16 v57, v208, v208, 0\n"
    "v_dot2_f32_bf16 v58, v209, s48, 0\nv_dot2_f32_bf16 v59, v209, v209, 0\nv_dot2_f32_bf16 v56, v210, s48, v56\nv_dot2_f32_bf16 v57, v210, v210, v57\n"
    "v_dot2_f32_bf16 v58, v211, s48, v58\nv_dot2_f32_bf16 v59, v211, v211, v59\nv_dot2_f32_bf16 v56, v212, s48, v56\nv_dot2_f32_bf16 v57, v212, v212, v57\n"
    "v_dot2_f32_bf16 v58, v213, s48, v58\nv_dot2_f32_bf16 v59, v213, v213, v59\nv_dot2_f32_bf16 v56, v214, s48, v56\nv_dot2_f32_bf16 v57, v214, v214, v57\n"
    "v_dot2_f32_bf16 v58, v215, s48, v58\nv_dot2_f32_bf16 v59, v215, v215, v59\nds_write2_b32 v40, v208, v209 offset1:32\ns_nop 1\nv_pk_add_f32 v[56:57], v[56:57], v[58:59]\n"
    "s_nop 1\nv_permlane32_swap_b32 v56, v57\nv_add_f32 v56, v56, v57\nds_write_b32 v45, v56\nds_write2_b32 v40, v210, v211 offset0:128 offset1:160\n"
    "ds_read_b128 v[216:219], v41\nds_write2_b32 v46, v212, v213 offset1:32\ns_waitcnt lgkmcnt(3)\ns_barrier\nds_write2_b32 v46, v214, v215 offset0:128 offset1:160\n"
    "ds_read_b128 v[220:223], v41 offset:1024\ns_setprio 0\ns_waitcnt lgkmcnt(3)\nglobal_store_dwordx4 v43, v[216:219], s[4:5] sc1\ns_waitcnt lgkmcnt(0)\n"
    "global_store_dwordx4 v44, v[220:223], s[4:5] sc1\ns_cmp_lg_u32 s12, 0\ns_cbranch_scc1 .Lz150\ns_call_b64 s[94:95], .Lhcon67_1\ns_call_b64 s[94:95], .Lhcon67_0\n"
    "s_call_b64 s[94:95], .Lhcon67_2\ns_sleep 2\n.Lz150:\nds_read_b64 v[128:129], v23 offset:896\nds_read_b64 v[130:131], v23 offset:904\n"
    "ds_read_b64 v[132:133], v23 offset:864\nds_read_b64 v[134:135], v23 offset:872\nds_read_b64 v[136:137], v23 offset:832\ns_nop 0\ns_cbranch_execz .Lhop\n"
    "ds_read_b64 v[138:139], v23 offset:840\nds_read_b64 v[140:141], v23 offset:800\nds_read_b64 v[142:143], v23 offset:808\nds_read_b64 v[144:145], v23 offset:704\n"
    "ds_read_b64 v[146:147], v23 offset:712\nds_read_b64 v[148:149], v23 offset:672\nds_read_b64 v[150:151], v23 offset:680\nds_read_b64 v[152:153], v23 offset:640\n"
    "ds_read_b64 v[154:155], v23 offset:648\nds_read_b64 v[156:157], v23 offset:608\nds_read_b64 v[158:159], v23 offset:616\nds_read_b64 v[160:161], v39 offset:512\n"
    "ds_read_b64 v[162:163], v39 offset:520\nds_read_b64 v[164:165], v39 offset:480\nds_read_b64 v[166:167], v39 offset:488\nds_read_b64 v[168:169], v39 offset:448\n"
    "ds_read_b64 v[170:171], v39 offset:456\nds_read_b64 v[172:173], v39 offset:416\nds_read_b64 v[174:175], v39 offset:424\nds_read_b64 v[176:177], v39 offset:320\n"
    "ds_read_b64 v[178:179], v39 offset:328\nds_read_b64 v[180:181], v39 offset:288\nds_read_b64 v[182:183], v39 offset:296\nds_read_b64 v[184:185], v39 offset:256\n"
    "ds_read_b64 v[186:187], v39 offset:264\nds_read_b64 v[188:189], v39 offset:224\nds_read_b64 v[190:191], v39 offset:232\n"
    ".Lln114:\ns_cmp_gt_u32 s3, 3\ns_waitcnt lgkmcnt(0)\ns_barrier\nglobal_load_dwordx4 v[240:243], v17, s[4:5] sc1\nglobal_load_dwordx4 v[208:211], v16, s[4:5] sc1\n"
    "global_load_dwordx4 v[212:215], v16, s[58:59] sc1\nglobal_load_dwordx4 v[216:219], v16, s[60:61] sc1\nglobal_load_dwordx4 v[220:223], v16, s[62:63] sc1\n"
    "s_cbranch_scc1 .Llc152\nds_read_b128 v[10:13], v59\nds_read_b32 v14, v59 offset:4236\nds_read_u16 v15, v59 offset:6350\nds_read_b64 v[64:65], v23 offset:2016\n"
    "ds_read_b64 v[66:67], v23 offset:2024\nds_read_b64 v[68:69], v23 offset:1984\nds_read_b64 v[70:71], v23 offset:1992\nds_read_b64 v[72:73], v23 offset:1952\n"
    "ds_read_b64 v[74:75], v23 offset:1960\nds_read_b64 v[76:77], v23 offset:1920\nds_read_b64 v[78:79], v23 offset:1928\nds_read_b64 v[80:81], v23 offset:1888\n"
    "ds_read_b64 v[82:83], v23 offset:1896\nds_read_b64 v[84:85], v23 offset:1856\nds_read_b64 v[86:87], v23 offset:1864\nds_read_b64 v[88:89], v23 offset:1824\n"
    "ds_read_b64 v[90:91], v23 offset:1832\nds_read_b64 v[92:93], v23 offset:1792\nds_read_b64 v[94:95], v23 offset:1800\nds_read_b64 v[96:97], v23 offset:1760\n"
    "ds_read_b64 v[98:99], v23 offset:1768\nds_read_b64 v[100:101], v23 offset:1728\nds_read_b64 v[102:103], v23 offset:1736\nds_read_b64 v[104:105], v23 offset:1696\n"
    "ds_read_b64 v[106:107], v23 offset:1704\nds_read_b64 v[108:109], v23 offset:1664\nds_read_b64 v[110:111], v23 offset:1672\nds_read_b64 v[112:113], v23 offset:1632\n"
    "ds_read_b64 v[114:115], v23 offset:1640\nds_read_b64 v[116:117], v23 offset:1600\nds_read_b64 v[118:119], v23 offset:1608\nds_read_b64 v[120:121], v23 offset:1568\n"
    "ds_read_b64 v[122:123], v23 offset:1576\nds_read_b64 v[124:125], v23 offset:1536\nds_read_b64 v[126:127], v23 offset:1544\nds_read_b64 v[128:129], v23 offset:1504\n"
    "ds_read_b64 v[130:131], v23 offset:1512\nds_read_b64 v[132:133], v23 offset:1472\nds_read_b64 v[134:135], v23 offset:1480\nds_read_b64 v[136:137], v23 offset:1440\n"
    "ds_read_b64 v[138:139], v23 offset:1448\nds_read_b64 v[140:141], v23 offset:1408\nds_read_b64 v[142:143], v23 offset:1416\nds_read_b64 v[144:145], v23 offset:1376\n"
    "ds_read_b64 v[146:147], v23 offset:1384\nds_read_b64 v[148:149], v23 offset:1344\nds_read_b64 v[150:151], v23 offset:1352\nds_read_b64 v[152:153], v23 offset:1312\n"
    "ds_read_b64 v[154:155], v23 offset:1320\nds_read_b64 v[156:157], v23 offset:1280\nds_read_b64 v[158:159], v23 offset:1288\nds_read_b64 v[160:161], v23 offset:1248\n"
    "ds_read_b64 v[162:163], v23 offset:1256\nds_read_b64 v[164:165], v23 offset:1216\nds_read_b64 v[166:167], v23 offset:1224\nds_read_b64 v[168:169], v23 offset:1184\n"
    "ds_read_b64 v[170:171], v23 offset:1192\nds_read_b64 v[172:173], v23 offset:1152\nds_read_b64 v[174:175], v23 offset:1160\nds_read_b64 v[176:177], v23 offset:1120\n"
    "ds_read_b64 v[178:179], v23 offset:1128\nds_read_b64 v[180:181], v23 offset:1088\nds_read_b64 v[182:183], v23 offset:1096\nds_read_b64 v[184:185], v23 offset:1056\n"
    "ds_read_b64 v[186:187], v23 offset:1064\nds_read_b64 v[188:189], v23 offset:1024\nds_read_b64 v[190:191], v23 offset:1032\nds_read_b64 v[192:193], v23 offset:992\n"
    "ds_read_b64 v[194:195], v23 offset:1000\nds_read_b64 v[196:197], v23 offset:960\nds_read_b64 v[198:199], v23 offset:968\nds_read_b64 v[200:201], v23 offset:928\n"
    "ds_read_b64 v[202:203], v23 offset:936\n.Llc152:\nv_mov_b64 v[236:237], 0\nv_mov_b64 v[238:239], 0\ns_cmp_lg_u32 s12, 0\ns_cbranch_scc1 .Ln153\n"
    "s_load_dword s17, s[16:17], 0\nv_mov_b32 v232, 115200\nds_read_u16 v233, v232 offset:1024\nds_read_u16 v234, v232 offset:2560\nds_read_b32 v235, v232 offset:48032\n"
    "ds_read_b32 v232, v232 offset:48040\ns_waitcnt lgkmcnt(0)\nv_fma_mix_f32 v233, v233, -v235, 0 op_sel_hi:[1,0,0]\nv_fma_mix_f32 v234, v234, -v232, 0 op_sel_hi:[1,0,0]\n"
    "s_lshl_b32 s16, s17, 16\ns_and_b32 s17, s17, 0xffff0000\nv_readfirstlane_b32 s14, v235\nv_readfirstlane_b32 s15, v232\nv_readfirstlane_b32 s28, v233\n"
    "v_readfirstlane_b32 s29, v234\ns_lshr_b32 s26, s14, 16\ns_lshr_b32 s27, s15, 16\n.Ln153:\nds_write_b128 v22, v[236:239]\ns_mov_b32 s44, 0x3f80\n"
    "s_mov_b32 s46, 0x3e800000\ns_mov_b32 s48, 0x3727c5ac\ns_mov_b32 s80, 0x10000\n.Ln155:\ns_waitcnt vmcnt(4)\ns_cbranch_execz .Lhop\nv_lshlrev_b32 v56, 16, v240\n"
    "v_lshlrev_b32 v57, 16, v241\nv_lshlrev_b32 v58, 16, v242\nv_lshlrev_b32 v59, 16, v243\nv_and_b32 v60, 0xffff0000, v240\nv_and_b32 v61, 0xffff0000, v241\n"
    "v_and_b32 v62, 0xffff0000, v242\nv_and_b32 v63, 0xffff0000, v243\nv_max3_u32 v232, v240, v241, v242\nv_max_u32 v232, v232, v243\nv_cmp_eq_u32 s[50:51], -1, v232\n"
    "s_waitcnt vmcnt(3)\nv_dot2_f32_bf16 v224, v208, s44, 0\nv_dot2_f32_bf16 v228, v208, v208, 0\nv_dot2_f32_bf16 v225, v209, s44, 0\nv_dot2_f32_bf16 v229, v209, v209, 0\n"
    "v_dot2_f32_bf16 v226, v210, s44, 0\nv_dot2_f32_bf16 v230, v210, v210, 0\nv_dot2_f32_bf16 v227, v211, s44, 0\nv_dot2_f32_bf16 v231, v211, v211, 0\ns_waitcnt vmcnt(2)\n"
    "v_dot2_f32_bf16 v224, v212, s44, v224\nv_dot2_f32_bf16 v228, v212, v212, v228\nv_dot2_f32_bf16 v225, v213, s44, v225\nv_dot2_f32_bf16 v229, v213, v213, v229\n"
    "v_dot2_f32_bf16 v226, v214, s44, v226\nv_dot2_f32_bf16 v230, v214, v214, v230\nv_dot2_f32_bf16 v227, v215, s44, v227\nv_dot2_f32_bf16 v231, v215, v215, v231\n"
    "s_waitcnt vmcnt(1)\nv_dot2_f32_bf16 v224, v216, s44, v224\nv_dot2_f32_bf16 v228, v216, v216, v228\nv_dot2_f32_bf16 v225, v217, s44, v225\n"
    "v_dot2_f32_bf16 v229, v217, v217, v229\nv_dot2_f32_bf16 v226, v218, s44, v226\nv_dot2_f32_bf16 v230, v218, v218, v230\nv_dot2_f32_bf16 v227, v219, s44, v227\n"
    "v_dot2_f32_bf16 v231, v219, v219, v231\ns_waitcnt vmcnt(0)\nv_dot2_f32_bf16 v224, v220, s44, v224\nv_dot2_f32_bf16 v228, v220, v220, v228\n"
    "v_dot2_f32_bf16 v225, v221, s44, v225\nv_dot2_f32_bf16 v229, v221, v221, v229\nv_dot2_f32_bf16 v226, v222, s44, v226\nv_dot2_f32_bf16 v230, v222, v222, v230\n"
    "v_dot2_f32_bf16 v227, v223, s44, v227\nv_dot2_f32_bf16 v231, v223, v223, v231\nv_pk_mul_f32 v[224:225], v[224:225], s[46:47] op_sel_hi:[1,0]\ns_nop 0\n"
    "v_pk_mul_f32 v[226:227], v[226:227], s[46:47] op_sel_hi:[1,0]\nv_cmp_u_f32 vcc, v228, v229\nv_cmp_u_f32 s[52:53], v230, v231\n"
    "v_pk_fma_f32 v[232:233], v[224:225], v[224:225], s[48:49] op_sel_hi:[1,1,0] neg_lo:[0,0,1] neg_hi:[0,0,1]\n"
    "v_pk_fma_f32 v[234:235], v[226:227], v[226:227], s[48:49] op_sel_hi:[1,1,0] neg_lo:[0,0,1] neg_hi:[0,0,1]\n"
    "v_pk_fma_f32 v[228:229], v[228:229], s[46:47], v[232:233] op_sel_hi:[1,0,1] neg_lo:[0,0,1] neg_hi:[0,0,1]\n"
    "v_pk_fma_f32 v[230:231], v[230:231], s[46:47], v[234:235] op_sel_hi:[1,0,1] neg_lo:[0,0,1] neg_hi:[0,0,1]\nv_max_f32 v228, s48, v228\nv_max_f32 v229, s48, v229\n"
    "v_max_f32 v230, s48, v230\nv_max_f32 v231, s48, v231\nv_rsq_f32 v228, v228\nv_rsq_f32 v229, v229\nv_rsq_f32 v230, v230\nv_rsq_f32 v231, v231\n"
    "v_pk_add_f32 v[244:245], v[56:57], v[224:225] neg_lo:[0,1] neg_hi:[0,1]\nv_pk_add_f32 v[246:247], v[58:59], v[226:227] neg_lo:[0,1] neg_hi:[0,1]\n"
    "v_pk_add_f32 v[248:249], v[60:61], v[224:225] neg_lo:[0,1] neg_hi:[0,1]\nv_pk_add_f32 v[250:251], v[62:63], v[226:227] neg_lo:[0,1] neg_hi:[0,1]\n"
    "s_or_b64 vcc, vcc, s[50:51]\nv_pk_mul_f32 v[244:245], v[244:245], v[228:229]\nv_pk_mul_f32 v[246:247], v[246:247], v[230:231]\n"
    "v_pk_mul_f32 v[248:249], v[248:249], v[228:229]\nv_pk_mul_f32 v[250:251], v[250:251], v[230:231]\ns_or_b64 vcc, vcc, s[52:53]\nv_cvt_pk_f16_f32 v232, v244, v245\n"
    "v_cvt_pk_f16_f32 v233, v246, v247\nv_cvt_pk_f16_f32 v234, v248, v249\nds_write_b64 v18, v[232:233]\nv_cvt_pk_f16_f32 v235, v250, v251\n"
    "ds_write_b64 v18, v[234:235] offset:6480\ns_cbranch_scc1 .Llw158\n.Llx159:\ns_cmp_gt_u32 s3, 3\ns_cbranch_scc1 .Lhcv156\n"
    "s_waitcnt lgkmcnt(0)\ns_barrier\ns_memrealtime s[100:101]\nds_read_b128 v[220:223], v27 offset:1392\nds_read_b128 v[224:227], v27 offset:1360\n"
    "ds_read_b128 v[228:231], v27 offset:1312\nv_fmac_f32 v56, s16, v244\nv_fmac_f32 v57, s16, v245\nv_fmac_f32 v58, s16, v246\nv_fmac_f32 v59, s16, v247\n"
    "v_fmac_f32 v60, s17, v248\nv_fmac_f32 v61, s17, v249\nv_fmac_f32 v62, s17, v250\nv_fmac_f32 v63, s17, v251\ns_waitcnt lgkmcnt(2)\n"
    "v_mfma_f32_32x32x16_f16 v[204:219], v[64:67], v[220:223], 0\nds_read_b128 v[232:235], v27 offset:1280\nds_read_b128 v[236:239], v27 offset:1232\ns_cmp_lg_u32 s13, 0\n"
    "s_cbranch_scc1 .Lz160\ns_waitcnt lgkmcnt(0)\nv_mov_b32 v40, s100\ns_mov_b64 exec, 1\nglobal_store_dword v32, v40, s[4:5] sc1\ns_mov_b64 exec, -1\ns_cmp_lg_u32 s2, 0\n"
    "s_cbranch_scc1 .Lz160\nv_mov_b32 v41, 16777216\nv_mov_b32 v42, s11\ns_mov_b64 exec, 1\nglobal_store_dword v41, v42, s[4:5] sc1\ns_mov_b64 exec, -1\n.Lz160:\n"
    "s_waitcnt lgkmcnt(3)\nv_mfma_f32_32x32x16_f16 v[204:219], v[68:71], v[224:227], v[204:219]\nds_read_b128 v[240:243], v27 offset:1200\n"
    "ds_read_b128 v[220:223], v27 offset:1152\ns_waitcnt lgkmcnt(4)\nv_mfma_f32_32x32x16_f16 v[204:219], v[72:75], v[228:231], v[204:219]\n"
    "ds_read_b128 v[224:227], v27 offset:1120\nds_read_b128 v[228:231], v27 offset:1072\ns_waitcnt lgkmcnt(5)\n"
    "v_mfma_f32_32x32x16_f16 v[204:219], v[76:79], v[232:235], v[204:219]\nds_read_b128 v[232:235], v27 offset:1040\ns_waitcnt lgkmcnt(5)\n"
    "v_mfma_f32_32x32x16_f16 v[204:219], v[80:83], v[236:239], v[204:219]\nds_read_b128 v[236:239], v27 offset:992\ns_waitcnt lgkmcnt(5)\n"
    "v_mfma_f32_32x32x16_f16 v[204:219], v[84:87], v[240:243], v[204:219]\nds_read_b128 v[240:243], v27 offset:960\ns_waitcnt lgkmcnt(5)\n"
    "v_mfma_f32_32x32x16_f16 v[204:219], v[88:91], v[220:223], v[204:219]\nds_read_b128 v[220:223], v27 offset:912\ns_waitcnt lgkmcnt(5)\n"
    "v_mfma_f32_32x32x16_f16 v[204:219], v[92:95], v[224:227], v[204:219]\nds_read_b128 v[224:227], v27 offset:880\ns_waitcnt lgkmcnt(5)\n"
    "v_mfma_f32_32x32x16_f16 v[204:219], v[96:99], v[228:231], v[204:219]\nds_read_b128 v[228:231], v27 offset:832\ns_waitcnt lgkmcnt(5)\n"
    "v_mfma_f32_32x32x16_f16 v[204:219], v[100:103], v[232:235], v[204:219]\nds_read_b128 v[232:235], v27 offset:800\ns_waitcnt lgkmcnt(5)\n"
    "v_mfma_f32_32x32x16_f16 v[204:219], v[104:107], v[236:239], v[204:219]\nds_read_b128 v[236:239], v27 offset:752\ns_waitcnt lgkmcnt(5)\n"
    "v_mfma_f32_32x32x16_f16 v[204:219], v[108:111], v[240:243], v[204:219]\nds_read_b128 v[240:243], v27 offset:720\ns_waitcnt lgkmcnt(5)\n"
    "v_mfma_f32_32x32x16_f16 v[204:219], v[112:115], v[220:223], v[204:219]\nds_read_b128 v[220:223], v27 offset:672\ns_waitcnt lgkmcnt(5)\n"
    "v_mfma_f32_32x32x16_f16 v[204:219], v[116:119], v[224:227], v[204:219]\nds_read_b128 v[224:227], v27 offset:640\ns_waitcnt lgkmcnt(5)\ns_nop 0\ns_cbranch_execz .Lhop\n"
    "v_mfma_f32_32x32x16_f16 v[204:219], v[120:123], v[228:231], v[204:219]\nds_read_b128 v[228:231], v27 offset:592\ns_waitcnt lgkmcnt(5)\n"
    "v_mfma_f32_32x32x16_f16 v[204:219], v[124:127], v[232:235], v[204:219]\nds_read_b128 v[232:235], v27 offset:560\ns_waitcnt lgkmcnt(5)\n"
    "v_mfma_f32_32x32x16_f16 v[204:219], v[128:131], v[236:239], v[204:219]\nds_read_b128 v[236:239], v27 offset:512\ns_waitcnt lgkmcnt(5)\n"
    "v_mfma_f32_32x32x16_f16 v[204:219], v[132:135], v[240:243], v[204:219]\nds_read_b128 v[240:243], v27 offset:480\ns_waitcnt lgkmcnt(5)\n"
    "v_mfma_f32_32x32x16_f16 v[204:219], v[136:139], v[220:223], v[204:219]\nds_read_b128 v[220:223], v27 offset:432\ns_waitcnt lgkmcnt(5)\n"
    "v_mfma_f32_32x32x16_f16 v[204:219], v[140:143], v[224:227], v[204:219]\nds_read_b128 v[224:227], v27 offset:400\ns_waitcnt lgkmcnt(5)\n"
    "v_mfma_f32_32x32x16_f16 v[204:219], v[144:147], v[228:231], v[204:219]\nds_read_b128 v[228:231], v27 offset:352\ns_waitcnt lgkmcnt(5)\n"
    "v_mfma_f32_32x32x16_f16 v[204:219], v[148:151], v[232:235], v[204:219]\nds_read_b128 v[232:235], v27 offset:320\ns_waitcnt lgkmcnt(5)\n"
    "v_mfma_f32_32x32x16_f16 v[204:219], v[152:155], v[236:239], v[204:219]\nds_read_b128 v[236:239], v27 offset:272\ns_waitcnt lgkmcnt(5)\n"
    "v_mfma_f32_32x32x16_f16 v[204:219], v[156:159], v[240:243], v[204:219]\nds_read_b128 v[240:243], v27 offset:240\ns_waitcnt lgkmcnt(5)\n"
    "v_mfma_f32_32x32x16_f16 v[204:219], v[160:163], v[220:223], v[204:219]\nds_read_b128 v[220:223], v27 offset:192\ns_waitcnt lgkmcnt(5)\n"
    "v_mfma_f32_32x32x16_f16 v[204:219], v[164:167], v[224:227], v[204:219]\nds_read_b128 v[224:227], v27 offset:160\ns_waitcnt lgkmcnt(5)\n"
    "v_mfma_f32_32x32x16_f16 v[204:219], v[168:171], v[228:231], v[204:219]\nds_read_b128 v[228:231], v27 offset:112\ns_waitcnt lgkmcnt(5)\n"
    "v_mfma_f32_32x32x16_f16 v[204:219], v[172:175], v[232:235], v[204:219]\nds_read_b128 v[232:235], v27 offset:80\ns_waitcnt lgkmcnt(5)\n"
    "v_mfma_f32_32x32x16_f16 v[204:219], v[176:179], v[236:239], v[204:219]\nds_read_b128 v[236:239], v27 offset:32\ns_waitcnt lgkmcnt(5)\n"
    "v_mfma_f32_32x32x16_f16 v[204:219], v[180:183], v[240:243], v[204:219]\ns_waitcnt lgkmcnt(4)\nv_mfma_f32_32x32x16_f16 v[204:219], v[184:187], v[220:223], v[204:219]\n"
    "s_waitcnt lgkmcnt(3)\nv_mfma_f32_32x32x16_f16 v[204:219], v[188:191], v[224:227], v[204:219]\ns_waitcnt lgkmcnt(2)\n"
    "v_mfma_f32_32x32x16_f16 v[204:219], v[192:195], v[228:231], v[204:219]\ns_waitcnt lgkmcnt(1)\nv_mfma_f32_32x32x16_f16 v[204:219], v[196:199], v[232:235], v[204:219]\n"
    "s_waitcnt lgkmcnt(0)\nv_mfma_f32_32x32x16_f16 v[204:219], v[200:203], v[236:239], v[204:219]\ns_nop 11\nv_cvt_pk_bf16_f32 v204, v204, v205\n"
    "v_cvt_pk_bf16_f32 v205, v206, v207\nds_write_b64 v28, v[204:205] offset:0\nv_cvt_pk_bf16_f32 v208, v208, v209\nv_cvt_pk_bf16_f32 v209, v210, v211\n"
    "ds_write_b64 v29, v[208:209] offset:0\nv_cvt_pk_bf16_f32 v212, v212, v213\nv_cvt_pk_bf16_f32 v213, v214, v215\nds_write_b64 v30, v[212:213] offset:0\n"
    "v_cvt_pk_bf16_f32 v216, v216, v217\nv_cvt_pk_bf16_f32 v217, v218, v219\nds_write_b64 v31, v[216:217] offset:0\ns_cmp_lg_u32 s12, 0\ns_cbranch_scc1 .Lz162\n"
    "v_bfe_u32 v95, v0, 5, 1\nv_and_b32 v94, 31, v0\nv_lshlrev_b32 v39, 9, v95\nv_lshl_add_u32 v39, v94, 2, v39\nv_and_b32 v93, 63, v0\ns_lshl_b32 s43, s24, 15\n"
    "s_and_b32 s42, s25, 3\ns_lshl_b32 s41, s42, 13\ns_lshl_b32 s40, s13, 11\ns_add_u32 s39, s43, s41\ns_add_u32 s39, s39, s40\ns_add_u32 s39, s39, 0xd00000\n"
    "v_lshl_add_u32 v34, v93, 2, s39\ns_lshl_b32 s38, s19, 17\ns_lshl_b32 s37, s13, 15\ns_add_u32 s82, s38, s37\ns_add_u32 s82, s82, 0x600800\ns_lshl_b32 s36, s23, 8\n"
    "s_add_u32 s35, s43, s41\ns_add_u32 s35, s35, s40\ns_add_u32 s35, s35, s36\ns_add_u32 s35, s35, 0xd00000\nv_lshl_add_u32 v35, v93, 2, s35\ns_lshr_b32 s34, s18, 4\n"
    "s_lshl_b32 s33, s34, 7\ns_and_b32 s32, s18, 15\ns_lshl_b32 s31, s32, 2\ns_add_u32 s30, s33, s31\ns_add_u32 s30, s30, 0x100e000\nv_mov_b32 v141, s30\nv_mov_b32 v142, 5\n"
    "v_bfe_u32 v95, v0, 2, 4\nv_and_b32 v94, 3, v0\ns_mov_b32 s43, 0x100e000\nv_lshl_add_u32 v143, v95, 7, s43\nv_lshl_add_u32 v143, v94, 4, v143\ns_add_u32 s82, s4, s82\n"
    "s_addc_u32 s83, s5, 0\ns_and_b32 s86, s23, 7\ns_add_u32 s86, s86, 0\ns_and_b32 s86, s86, 7\ns_lshl_b32 s44, s86, 12\ns_add_u32 s52, s82, s44\ns_addc_u32 s53, s83, 0\n"
    "s_and_b32 s86, s23, 7\ns_add_u32 s86, s86, 1\ns_and_b32 s86, s86, 7\ns_lshl_b32 s45, s86, 12\ns_add_u32 s54, s82, s45\ns_addc_u32 s55, s83, 0\ns_and_b32 s86, s23, 7\n"
    "s_add_u32 s86, s86, 2\ns_and_b32 s86, s86, 7\ns_lshl_b32 s46, s86, 12\ns_add_u32 s56, s82, s46\ns_addc_u32 s57, s83, 0\ns_and_b32 s86, s23, 7\ns_add_u32 s86, s86, 3\n"
    "s_and_b32 s86, s86, 7\ns_lshl_b32 s47, s86, 12\ns_add_u32 s58, s82, s47\ns_addc_u32 s59, s83, 0\ns_and_b32 s86, s23, 7\ns_add_u32 s86, s86, 4\ns_and_b32 s86, s86, 7\n"
    "s_lshl_b32 s48, s86, 12\ns_add_u32 s60, s82, s48\ns_addc_u32 s61, s83, 0\ns_and_b32 s86, s23, 7\ns_add_u32 s86, s86, 5\ns_and_b32 s86, s86, 7\ns_lshl_b32 s49, s86, 12\n"
    "s_add_u32 s62, s82, s49\ns_addc_u32 s63, s83, 0\ns_and_b32 s86, s23, 7\ns_add_u32 s86, s86, 6\ns_and_b32 s86, s86, 7\ns_lshl_b32 s50, s86, 12\ns_add_u32 s64, s82, s50\n"
    "s_addc_u32 s65, s83, 0\ns_and_b32 s86, s23, 7\ns_add_u32 s86, s86, 7\ns_and_b32 s86, s86, 7\ns_lshl_b32 s51, s86, 12\ns_add_u32 s66, s82, s51\ns_addc_u32 s67, s83, 0\n"
    "s_mov_b32 s81, 0x3b000000\ns_mov_b32 s84, -1\ns_mov_b32 s85, 0\nv_mov_b32 v241, 0x3f803f80\nv_mov_b64 v[120:121], 0\nv_mov_b64 v[122:123], 0\nv_mov_b64 v[124:125], 0\n"
    "v_mov_b64 v[126:127], 0\nv_mov_b64 v[128:129], 0\nv_mov_b64 v[130:131], 0\nv_mov_b64 v[132:133], 0\nv_mov_b64 v[134:135], 0\nv_mad_u32_u24 v242, s44, 3, v36\n"
    "s_mov_b32 s80, 0x10000\nds_read_b64_tr_b16 v[208:209], v242 offset:0\nds_read_b64_tr_b16 v[210:211], v242 offset:256\nds_read_b64_tr_b16 v[212:213], v242 offset:3072\n"
    "ds_read_b64_tr_b16 v[214:215], v242 offset:3328\nds_read_b64_tr_b16 v[216:217], v242 offset:6144\nds_read_b64_tr_b16 v[218:219], v242 offset:6400\n"
    "ds_read_b64_tr_b16 v[220:221], v242 offset:9216\nds_read_b64_tr_b16 v[222:223], v242 offset:9472\nds_read_b64_tr_b16 v[224:225], v242 offset:1024\n"
    "ds_read_b64_tr_b16 v[226:227], v242 offset:1280\ns_cbranch_execz .Lhop\nds_read_b64_tr_b16 v[228:229], v242 offset:4096\nds_read_b64_tr_b16 v[230:231], v242 offset:4352\n"
    "ds_read_b32 v124, v38 offset:0\nds_read_b32 v128, v38 offset:128\nds_read_b32 v132, v38 offset:256\n.Lz162:\ns_cmp_lt_u32 s3, 2\ns_waitcnt lgkmcnt(0)\ns_barrier\n"
    "s_memrealtime s[88:89]\nds_read_b64 v[44:45], v19\nds_read_b64 v[46:47], v19 offset:10240\ns_cbranch_scc1 .Le164\nds_read_b64 v[48:49], v20\n"
    "ds_read_b64 v[50:51], v20 offset:10240\ns_waitcnt lgkmcnt(0)\nv_dot2c_f32_bf16 v56, s26, v48\nv_dot2c_f32_bf16 v57, s14, v48\nv_dot2c_f32_bf16 v58, s26, v49\n"
    "v_dot2c_f32_bf16 v59, s14, v49\nv_dot2c_f32_bf16 v60, s27, v50\nv_dot2c_f32_bf16 v61, s15, v50\nv_dot2c_f32_bf16 v62, s27, v51\nv_dot2c_f32_bf16 v63, s15, v51\n"
    "s_branch .Lz161\n.Le164:\ns_cmp_lg_u32 s13, 0\ns_cbranch_scc1 .Lz161\nglobal_load_dwordx4 v[40:43], v33, s[4:5] sc1\n.Lz161:\ns_waitcnt lgkmcnt(0)\n"
    "v_dot2c_f32_bf16 v56, s26, v44\nv_dot2c_f32_bf16 v57, s14, v44\nv_dot2c_f32_bf16 v58, s26, v45\nv_dot2c_f32_bf16 v59, s14, v45\nv_dot2c_f32_bf16 v60, s27, v46\n"
    "v_dot2c_f32_bf16 v61, s15, v46\nv_dot2c_f32_bf16 v62, s27, v47\nv_dot2c_f32_bf16 v63, s15, v47\nv_cvt_pk_bf16_f32 v52, v56, v60\nv_cvt_pk_bf16_f32 v53, v57, v61\n"
    "v_cvt_pk_bf16_f32 v54, v58, v62\nv_cvt_pk_bf16_f32 v55, v59, v63\nglobal_store_dwordx4 v21, v[52:55], s[4:5] sc1\ns_cmp_lg_u32 s12, 0\ns_cbranch_scc1 .Lz163x\n"
    "s_mov_b32 exec_lo, 0xf0000000\ns_mov_b32 exec_hi, 0xf0000000\nglobal_store_dwordx4 v24, v[52:55], s[4:5] sc1\ns_mov_b64 exec, -1\ns_cmp_lg_u32 s3, 0\n"
    "s_cbranch_scc1 .Lqg166\ns_waitcnt vmcnt(2)\ns_branch .Lz163y\n.Lz163x:\ns_cmp_lg_u32 s3, 0\ns_cbranch_scc1 .Lz163\ns_waitcnt vmcnt(1)\n.Lz163y:\n"
    "s_sub_u32 s87, s100, 300\nv_subrev_u32 v40, s87, v40\nv_subrev_u32 v41, s87, v41\nv_subrev_u32 v42, s87, v42\nv_subrev_u32 v43, s87, v43\nv_max3_u32 v40, v40, v41, v42\n"
    "v_max_u32 v40, v40, v43\ns_nop 1\nv_max_u32_dpp v40, v40, v40 row_ror:1 row_mask:0xf bank_mask:0xf\ns_nop 1\n"
    "v_max_u32_dpp v40, v40, v40 row_ror:2 row_mask:0xf bank_mask:0xf\ns_nop 1\nv_max_u32_dpp v40, v40, v40 row_ror:4 row_mask:0xf bank_mask:0xf\ns_nop 1\n"
    "v_max_u32_dpp v40, v40, v40 row_ror:8 row_mask:0xf bank_mask:0xf\nv_mov_b32 v41, v40\ns_nop 1\nv_permlane16_swap_b32 v40, v41\nv_max_u32 v40, v40, v41\n"
    "v_mov_b32 v41, v40\ns_nop 1\nv_permlane32_swap_b32 v40, v41\nv_max_u32 v40, v40, v41\ns_nop 0\nv_readfirstlane_b32 s87, v40\ns_min_u32 s87, s87, 370\n"
    "s_max_u32 s87, s87, 300\ns_sub_u32 s87, s87, 300\ns_mov_b32 s100, s88\n.Lz163:\ns_cmp_eq_u32 s12, 0\ns_cbranch_scc1 .Lqg166\n"
    ".Lfin165:\ns_lshl_b32 s43, s18, 13\ns_add_u32 s42, s43, 0x1200000\nv_lshl_add_u32 v16, v0, 4, s42\nv_lshrrev_b32 v239, 3, v0\nv_and_b32 v238, 7, v0\n"
    "s_lshl_b32 s41, s25, 16\ns_lshl_b32 s40, s24, 7\ns_add_u32 s39, s41, s40\nv_lshl_add_u32 v17, v239, 10, s39\nv_lshl_add_u32 v17, v238, 4, v17\ns_lshl_b32 s38, s3, 4\n"
    "v_lshl_add_u32 v18, v0, 4, s38\nv_mul_u32_u24 v237, 0x410, v238\nv_lshl_add_u32 v22, v239, 2, v237\ns_mov_b32 s80, 0x10000\ns_cmp_lg_u32 s3, 0\ns_cbranch_scc1 .Lz170\n"
    "s_waitcnt lgkmcnt(0)\ns_add_u32 s87, s87, s100\ns_add_u32 s87, s87, 25\n.Lj171:\ns_memrealtime s[100:101]\ns_waitcnt lgkmcnt(0)\ns_sub_u32 s101, s87, s100\n"
    "s_cmp_le_i32 s101, 0\ns_cbranch_scc0 .Lj171\n.Lz170:\ns_barrier\n.Lf168:\nglobal_load_dwordx4 v[44:47], v16, s[4:5] sc1\ns_waitcnt vmcnt(0)\nds_write_b128 v18, v[44:47]\n"
    "v_max3_u32 v48, v44, v45, v46\nv_max_u32 v48, v48, v47\nv_cmp_eq_u32 vcc, -1, v48\ns_waitcnt lgkmcnt(0)\ns_cbranch_vccnz .Lf169\n.Lf169x:\ns_barrier\n"
    "ds_read2st64_b32 v[44:45], v22 offset1:1\nds_read2st64_b32 v[46:47], v22 offset0:2 offset1:3\ns_waitcnt lgkmcnt(0)\nglobal_store_dwordx4 v17, v[44:47], s[6:7] sc1\n"
    "s_endpgm\n.Lf169:\ns_sub_u32 s80, s80, 1\ns_cbranch_scc0 .Lf168\ns_branch .Lf169x\n"
    ".Lhcv156:\ns_setprio 0\ns_bitcmp1_b32 s13, 0\ns_cbranch_scc1 .Lk173\ns_cmp_lg_u32 s12, 0\ns_waitcnt lgkmcnt(0)\ns_barrier\nds_read_b64 v[192:193], v11 offset:0\n"
    "ds_read_b128 v[194:197], v10 offset:0\nds_read_b64 v[198:199], v12 offset:0\nds_read_b64 v[200:201], v11 offset:1280\nds_read_b128 v[202:205], v10 offset:1280\n"
    "ds_read_b64 v[206:207], v12 offset:1280\nds_read_b64 v[208:209], v11 offset:2560\nds_read_b128 v[210:213], v10 offset:2560\nds_read_b64 v[214:215], v12 offset:2560\n"
    "s_cbranch_scc1 .Lz176\nglobal_store_dwordx4 v37, v[252:255], s[4:5] offset:0 sc1\nglobal_store_dwordx4 v37, v[252:255], s[4:5] offset:1024 sc1\n"
    "global_store_dwordx4 v37, v[252:255], s[54:55] offset:0 sc1\nglobal_store_dwordx4 v37, v[252:255], s[54:55] offset:1024 sc1\n.Lz176:\ns_waitcnt lgkmcnt(6)\n"
    "v_alignbit_b32 v216, v194, v193, 16\nv_alignbit_b32 v217, v195, v194, 16\nv_alignbit_b32 v218, v196, v195, 16\nv_alignbit_b32 v219, v197, v196, 16\n"
    "v_alignbit_b32 v220, v198, v197, 16\nv_pk_add_f16 v224, v193, v216\nv_pk_add_f16 v225, v194, v217\nv_pk_add_f16 v226, v195, v218\nv_pk_add_f16 v227, v196, v219\n"
    "v_pk_add_f16 v228, v197, v220\nv_pk_add_f16 v236, v216, v225\nv_pk_add_f16 v237, v217, v226\nv_pk_add_f16 v238, v218, v227\nv_pk_add_f16 v239, v219, v228\n"
    "ds_write_b64 v13, v[236:237] offset:4040\nv_pk_add_f16 v232, v224, v195\nv_pk_add_f16 v233, v225, v196\nv_pk_add_f16 v234, v226, v197\nv_pk_add_f16 v235, v227, v198\n"
    "ds_write_b64 v14, v[238:239] offset:4032\nv_pk_add_f16 v232, v232, v225\nv_pk_add_f16 v233, v233, v226\nv_pk_add_f16 v234, v234, v227\nv_pk_add_f16 v235, v235, v228\n"
    "ds_write_b128 v13, v[232:235] offset:0\ns_cbranch_execz .Lhop\ns_mov_b64 exec, 1\nds_write_b32 v25, v26 offset:48\ns_mov_b64 exec, -1\ns_waitcnt lgkmcnt(6)\n"
    "v_alignbit_b32 v216, v202, v201, 16\nv_alignbit_b32 v217, v203, v202, 16\nv_alignbit_b32 v218, v204, v203, 16\nv_alignbit_b32 v219, v205, v204, 16\n"
    "v_alignbit_b32 v220, v206, v205, 16\nv_pk_add_f16 v224, v201, v216\nv_pk_add_f16 v225, v202, v217\nv_pk_add_f16 v226, v203, v218\nv_pk_add_f16 v227, v204, v219\n"
    "v_pk_add_f16 v228, v205, v220\nv_pk_add_f16 v236, v216, v225\nv_pk_add_f16 v237, v217, v226\nv_pk_add_f16 v238, v218, v227\nv_pk_add_f16 v239, v219, v228\n"
    "ds_write_b64 v13, v[236:237] offset:5192\nv_pk_add_f16 v232, v224, v203\nv_pk_add_f16 v233, v225, v204\nv_pk_add_f16 v234, v226, v205\nv_pk_add_f16 v235, v227, v206\n"
    "ds_write_b64 v14, v[238:239] offset:5184\nv_pk_add_f16 v232, v232, v225\nv_pk_add_f16 v233, v233, v226\nv_pk_add_f16 v234, v234, v227\nv_pk_add_f16 v235, v235, v228\n"
    "ds_write_b128 v13, v[232:235] offset:1152\ns_waitcnt lgkmcnt(6)\nv_alignbit_b32 v216, v210, v209, 16\nv_alignbit_b32 v217, v211, v210, 16\n"
    "v_alignbit_b32 v218, v212, v211, 16\nv_alignbit_b32 v219, v213, v212, 16\nv_alignbit_b32 v220, v214, v213, 16\nv_pk_add_f16 v224, v209, v216\n"
    "v_pk_add_f16 v225, v210, v217\nv_pk_add_f16 v226, v211, v218\nv_pk_add_f16 v227, v212, v219\nv_pk_add_f16 v228, v213, v220\nv_pk_add_f16 v236, v216, v225\n"
    "v_pk_add_f16 v237, v217, v226\nv_pk_add_f16 v238, v218, v227\nv_pk_add_f16 v239, v219, v228\nds_write_b64 v15, v[236:237] offset:6344\nv_pk_add_f16 v232, v224, v211\n"
    "v_pk_add_f16 v233, v225, v212\nv_pk_add_f16 v234, v226, v213\nv_pk_add_f16 v235, v227, v214\nds_write_b64 v35, v[238:239] offset:6336\nv_pk_add_f16 v232, v232, v225\n"
    "v_pk_add_f16 v233, v233, v226\nv_pk_add_f16 v234, v234, v227\nv_pk_add_f16 v235, v235, v228\nds_write_b128 v15, v[232:235] offset:2304\ns_setprio 0\n"
    "ds_read_b64_tr_b16 v[40:41], v27 offset:1984\nds_read_b64_tr_b16 v[42:43], v27 offset:2048\nds_read_b64_tr_b16 v[44:45], v27 offset:1728\n"
    "ds_read_b64_tr_b16 v[46:47], v27 offset:1792\nds_read_b64_tr_b16 v[48:49], v27 offset:1408\nds_read_b64_tr_b16 v[50:51], v27 offset:1472\n"
    "ds_read_b64_tr_b16 v[52:53], v27 offset:1152\nds_read_b64_tr_b16 v[54:55], v27 offset:1216\nds_read_b64_tr_b16 v[240:241], v27 offset:832\n"
    "ds_read_b64_tr_b16 v[242:243], v27 offset:896\ns_waitcnt lgkmcnt(8)\nv_mfma_f32_32x32x16_f16 v[192:207], v[40:43], v[128:131], 0\n"
    "ds_read_b64_tr_b16 v[40:41], v27 offset:576\nds_read_b64_tr_b16 v[42:43], v27 offset:640\ns_waitcnt lgkmcnt(8)\n"
    "v_mfma_f32_32x32x16_f16 v[192:207], v[44:47], v[132:135], v[192:207]\nds_read_b64_tr_b16 v[44:45], v27 offset:6016\nds_read_b64_tr_b16 v[46:47], v27 offset:6080\n"
    "v_fmac_f32 v56, s16, v244\nv_fmac_f32 v57, s16, v245\ns_waitcnt lgkmcnt(8)\nv_mfma_f32_32x32x16_f16 v[192:207], v[48:51], v[136:139], v[192:207]\n"
    "ds_read_b64_tr_b16 v[48:49], v27 offset:5760\nds_read_b64_tr_b16 v[50:51], v27 offset:5824\nv_fmac_f32 v58, s16, v246\nv_fmac_f32 v59, s16, v247\ns_waitcnt lgkmcnt(8)\n"
    "v_mfma_f32_32x32x16_f16 v[192:207], v[52:55], v[140:143], v[192:207]\nds_read_b64_tr_b16 v[52:53], v27 offset:5440\nds_read_b64_tr_b16 v[54:55], v27 offset:5504\n"
    "v_fmac_f32 v60, s17, v248\nv_fmac_f32 v61, s17, v249\ns_waitcnt lgkmcnt(8)\nv_mfma_f32_32x32x16_f16 v[192:207], v[240:243], v[160:163], v[192:207]\n"
    "ds_read_b64_tr_b16 v[240:241], v27 offset:5184\nds_read_b64_tr_b16 v[242:243], v27 offset:5248\nv_fmac_f32 v62, s17, v250\nv_fmac_f32 v63, s17, v251\n"
    "s_waitcnt lgkmcnt(8)\nv_mfma_f32_32x32x16_f16 v[192:207], v[40:43], v[164:167], v[192:207]\nds_read_b64_tr_b16 v[40:41], v27 offset:4864\n"
    "ds_read_b64_tr_b16 v[42:43], v27 offset:4928\ns_waitcnt lgkmcnt(8)\nv_mfma_f32_32x32x16_f16 v[192:207], v[44:47], v[144:147], v[192:207]\n"
    "ds_read_b64_tr_b16 v[44:45], v27 offset:4608\nds_read_b64_tr_b16 v[46:47], v27 offset:4672\ns_waitcnt lgkmcnt(8)\n"
    "v_mfma_f32_32x32x16_f16 v[192:207], v[48:51], v[148:151], v[192:207]\nds_read_b32 v244, v30\nds_read_b64 v[246:247], v31\nds_read_b32 v245, v30 offset:6480\n"
    "ds_read_b64 v[248:249], v31 offset:6480\ns_waitcnt lgkmcnt(10)\nv_mfma_f32_32x32x16_f16 v[192:207], v[52:55], v[152:155], v[192:207]\ns_waitcnt lgkmcnt(8)\n"
    "v_mfma_f32_32x32x16_f16 v[192:207], v[240:243], v[156:159], v[192:207]\ns_waitcnt lgkmcnt(6)\nv_mfma_f32_32x32x16_f16 v[192:207], v[40:43], v[176:179], v[192:207]\n"
    "s_waitcnt lgkmcnt(0)\nv_fma_mix_f32 v56, v244, s28, v56 op_sel:[1,0,0] op_sel_hi:[1,0,0]\nv_fma_mix_f32 v57, v246, s28, v57 op_sel:[0,0,0] op_sel_hi:[1,0,0]\n"
    "v_fma_mix_f32 v58, v246, s28, v58 op_sel:[1,0,0] op_sel_hi:[1,0,0]\ns_waitcnt lgkmcnt(4)\nv_mfma_f32_32x32x16_f16 v[192:207], v[44:47], v[180:183], v[192:207]\n"
    "v_fma_mix_f32 v59, v247, s28, v59 op_sel:[0,0,0] op_sel_hi:[1,0,0]\nv_fma_mix_f32 v60, v245, s29, v60 op_sel:[1,0,0] op_sel_hi:[1,0,0]\n"
    "v_fma_mix_f32 v61, v248, s29, v61 op_sel:[0,0,0] op_sel_hi:[1,0,0]\nv_fma_mix_f32 v62, v248, s29, v62 op_sel:[1,0,0] op_sel_hi:[1,0,0]\n"
    "v_fma_mix_f32 v63, v249, s29, v63 op_sel:[0,0,0] op_sel_hi:[1,0,0]\ns_nop 6\nv_cvt_pk_bf16_f32 v192, v192, v193\nv_cvt_pk_bf16_f32 v193, v194, v195\n"
    "ds_write_b64 v32, v[192:193] offset:1024\nv_cvt_pk_bf16_f32 v196, v196, v197\nv_cvt_pk_bf16_f32 v197, v198, v199\nds_write_b64 v32, v[196:197] offset:1536\n"
    "v_cvt_pk_bf16_f32 v200, v200, v201\nv_cvt_pk_bf16_f32 v201, v202, v203\nds_write_b64 v32, v[200:201] offset:2048\nv_cvt_pk_bf16_f32 v204, v204, v205\n"
    "v_cvt_pk_bf16_f32 v205, v206, v207\nds_write_b64 v32, v[204:205] offset:2560\ns_branch .Lk174\n.Lk173:\ns_cmp_lg_u32 s12, 0\ns_waitcnt lgkmcnt(0)\ns_barrier\n"
    "ds_read_b64 v[192:193], v11 offset:0\ns_cbranch_execz .Lhop\nds_read_b128 v[194:197], v10 offset:0\nds_read_b64 v[198:199], v12 offset:0\n"
    "ds_read_b64 v[200:201], v11 offset:1280\nds_read_b128 v[202:205], v10 offset:1280\nds_read_b64 v[206:207], v12 offset:1280\ns_cbranch_scc1 .Lz177\n"
    "global_store_dwordx4 v37, v[252:255], s[4:5] offset:0 sc1\nglobal_store_dwordx4 v37, v[252:255], s[4:5] offset:1024 sc1\n"
    "global_store_dwordx4 v37, v[252:255], s[54:55] offset:0 sc1\nglobal_store_dwordx4 v37, v[252:255], s[54:55] offset:1024 sc1\n.Lz177:\ns_waitcnt lgkmcnt(3)\n"
    "v_alignbit_b32 v216, v193, v192, 16\nv_alignbit_b32 v217, v194, v193, 16\nv_alignbit_b32 v218, v195, v194, 16\nv_alignbit_b32 v219, v196, v195, 16\n"
    "v_alignbit_b32 v220, v197, v196, 16\nv_alignbit_b32 v221, v198, v197, 16\nv_alignbit_b32 v222, v199, v198, 16\nv_pk_add_f16 v224, v192, v216\n"
    "v_pk_add_f16 v225, v193, v217\nv_pk_add_f16 v226, v194, v218\nv_pk_add_f16 v227, v195, v219\nv_pk_add_f16 v228, v196, v220\nv_pk_add_f16 v229, v197, v221\n"
    "v_pk_add_f16 v230, v198, v222\nv_pk_add_f16 v223, v226, v227\nv_pk_add_f16 v231, v228, v229\nv_pk_add_f16 v232, v224, v196\nv_pk_add_f16 v233, v225, v197\n"
    "v_pk_add_f16 v234, v226, v198\nv_pk_add_f16 v235, v227, v199\nv_pk_add_f16 v208, v225, v223\nv_pk_add_f16 v209, v223, v228\nv_pk_add_f16 v210, v227, v231\n"
    "v_pk_add_f16 v211, v231, v230\nv_pk_add_f16 v236, v208, v216\nv_pk_add_f16 v237, v209, v217\nv_pk_add_f16 v238, v210, v218\nv_pk_add_f16 v239, v211, v219\n"
    "ds_write_b128 v14, v[236:239] offset:0\nv_pk_add_f16 v232, v232, v208\nv_pk_add_f16 v233, v233, v209\nv_pk_add_f16 v234, v234, v210\nv_pk_add_f16 v235, v235, v211\n"
    "ds_write_b128 v13, v[232:235] offset:0\ns_waitcnt lgkmcnt(2)\nv_alignbit_b32 v216, v201, v200, 16\nv_alignbit_b32 v217, v202, v201, 16\n"
    "v_alignbit_b32 v218, v203, v202, 16\nv_alignbit_b32 v219, v204, v203, 16\nv_alignbit_b32 v220, v205, v204, 16\nv_alignbit_b32 v221, v206, v205, 16\n"
    "v_alignbit_b32 v222, v207, v206, 16\nv_pk_add_f16 v224, v200, v216\nv_pk_add_f16 v225, v201, v217\nv_pk_add_f16 v226, v202, v218\nv_pk_add_f16 v227, v203, v219\n"
    "v_pk_add_f16 v228, v204, v220\nv_pk_add_f16 v229, v205, v221\nv_pk_add_f16 v230, v206, v222\nv_pk_add_f16 v223, v226, v227\nv_pk_add_f16 v231, v228, v229\n"
    "v_pk_add_f16 v232, v224, v204\nv_pk_add_f16 v233, v225, v205\nv_pk_add_f16 v234, v226, v206\nv_pk_add_f16 v235, v227, v207\nv_pk_add_f16 v208, v225, v223\n"
    "v_pk_add_f16 v209, v223, v228\nv_pk_add_f16 v210, v227, v231\nv_pk_add_f16 v211, v231, v230\nv_pk_add_f16 v236, v208, v216\nv_pk_add_f16 v237, v209, v217\n"
    "v_pk_add_f16 v238, v210, v218\nv_pk_add_f16 v239, v211, v219\nds_write_b128 v14, v[236:239] offset:1152\nv_pk_add_f16 v232, v232, v208\nv_pk_add_f16 v233, v233, v209\n"
    "v_pk_add_f16 v234, v234, v210\nv_pk_add_f16 v235, v235, v211\nds_write_b128 v15, v[232:235] offset:1152\nv_fmac_f32 v56, s16, v244\nv_fmac_f32 v57, s16, v245\n"
    "v_fmac_f32 v58, s16, v246\nv_fmac_f32 v59, s16, v247\nv_fmac_f32 v60, s17, v248\nv_fmac_f32 v61, s17, v249\nv_fmac_f32 v62, s17, v250\nv_fmac_f32 v63, s17, v251\n"
    "ds_read_b32 v244, v30\nds_read_b64 v[246:247], v31\nds_read_b32 v245, v30 offset:6480\nds_read_b64 v[248:249], v31 offset:6480\ns_setprio 0\nds_read_b32 v250, v24\n"
    "ds_read_b64_tr_b16 v[40:41], v28 offset:512\nds_read_b64_tr_b16 v[42:43], v28 offset:640\nds_read_b64_tr_b16 v[44:45], v28 offset:0\n"
    "ds_read_b64_tr_b16 v[46:47], v28 offset:128\nds_read_b64_tr_b16 v[48:49], v29 offset:512\nds_read_b64_tr_b16 v[50:51], v29 offset:640\n"
    "ds_read_b64_tr_b16 v[52:53], v29 offset:0\nds_read_b64_tr_b16 v[54:55], v29 offset:128\nds_read_b64_tr_b16 v[240:241], v28 offset:2816\n"
    "ds_read_b64_tr_b16 v[242:243], v28 offset:2944\ns_waitcnt lgkmcnt(8)\nv_mfma_f32_32x32x16_f16 v[224:239], v[40:43], v[160:163], 0\nv_cmp_gt_i32 vcc, v26, v250\n"
    "ds_read_b64_tr_b16 v[40:41], v28 offset:2304\nds_read_b64_tr_b16 v[42:43], v28 offset:2432\ns_waitcnt lgkmcnt(8)\n"
    "v_mfma_f32_32x32x16_f16 v[224:239], v[44:47], v[164:167], v[224:239]\nds_read_b64_tr_b16 v[44:45], v29 offset:2816\nds_read_b64_tr_b16 v[46:47], v29 offset:2944\n"
    "s_waitcnt lgkmcnt(8)\nv_mfma_f32_32x32x16_f16 v[208:223], v[48:51], v[168:171], 0\nds_read_b64_tr_b16 v[48:49], v29 offset:2304\n"
    "ds_read_b64_tr_b16 v[50:51], v29 offset:2432\ns_waitcnt lgkmcnt(8)\nv_mfma_f32_32x32x16_f16 v[208:223], v[52:55], v[172:175], v[208:223]\ns_cbranch_vccnz .Lw178\n"
    ".Lr179:\nds_read_b64_tr_b16 v[52:53], v27 offset:832\nds_read_b64_tr_b16 v[54:55], v27 offset:896\ns_waitcnt lgkmcnt(8)\n"
    "v_mfma_f32_32x32x16_f16 v[224:239], v[240:243], v[176:179], v[224:239]\nds_read_b64_tr_b16 v[240:241], v27 offset:576\nds_read_b64_tr_b16 v[242:243], v27 offset:640\n"
    "v_fma_mix_f32 v56, v244, s28, v56 op_sel:[1,0,0] op_sel_hi:[1,0,0]\nv_fma_mix_f32 v57, v246, s28, v57 op_sel:[0,0,0] op_sel_hi:[1,0,0]\ns_waitcnt lgkmcnt(8)\n"
    "v_mfma_f32_32x32x16_f16 v[224:239], v[40:43], v[180:183], v[224:239]\nds_read_b64_tr_b16 v[40:41], v27 offset:256\nds_read_b64_tr_b16 v[42:43], v27 offset:320\n"
    "v_fma_mix_f32 v58, v246, s28, v58 op_sel:[1,0,0] op_sel_hi:[1,0,0]\nv_fma_mix_f32 v59, v247, s28, v59 op_sel:[0,0,0] op_sel_hi:[1,0,0]\ns_waitcnt lgkmcnt(8)\n"
    "v_mfma_f32_32x32x16_f16 v[208:223], v[44:47], v[184:187], v[208:223]\nds_read_b64_tr_b16 v[44:45], v27 offset:0\nds_read_b64_tr_b16 v[46:47], v27 offset:64\n"
    "s_cbranch_execz .Lhop\nv_fma_mix_f32 v60, v245, s29, v60 op_sel:[1,0,0] op_sel_hi:[1,0,0]\nv_fma_mix_f32 v61, v248, s29, v61 op_sel:[0,0,0] op_sel_hi:[1,0,0]\n"
    "s_waitcnt lgkmcnt(8)\nv_mfma_f32_32x32x16_f16 v[208:223], v[48:51], v[188:191], v[208:223]\nds_read_b64_tr_b16 v[48:49], v27 offset:4864\n"
    "ds_read_b64_tr_b16 v[50:51], v27 offset:4928\nv_fma_mix_f32 v62, v248, s29, v62 op_sel:[1,0,0] op_sel_hi:[1,0,0]\n"
    "v_fma_mix_f32 v63, v249, s29, v63 op_sel:[0,0,0] op_sel_hi:[1,0,0]\ns_waitcnt lgkmcnt(8)\nv_mfma_f32_32x32x16_f16 v[192:207], v[52:55], v[128:131], 0\n"
    "ds_read_b64_tr_b16 v[52:53], v27 offset:4608\nds_read_b64_tr_b16 v[54:55], v27 offset:4672\ns_waitcnt lgkmcnt(8)\n"
    "v_mfma_f32_32x32x16_f16 v[192:207], v[240:243], v[132:135], v[192:207]\nds_read_b64_tr_b16 v[240:241], v27 offset:4288\nds_read_b64_tr_b16 v[242:243], v27 offset:4352\n"
    "v_cvt_pk_bf16_f32 v224, v224, v225\nv_cvt_pk_bf16_f32 v225, v226, v227\nds_write_b64 v33, v[224:225] offset:0\ns_waitcnt lgkmcnt(9)\n"
    "v_mfma_f32_32x32x16_f16 v[192:207], v[40:43], v[136:139], v[192:207]\nds_read_b64_tr_b16 v[40:41], v27 offset:4032\nds_read_b64_tr_b16 v[42:43], v27 offset:4096\n"
    "v_cvt_pk_bf16_f32 v228, v228, v229\nv_cvt_pk_bf16_f32 v229, v230, v231\nds_write_b64 v36, v[228:229] offset:0\ns_waitcnt lgkmcnt(10)\n"
    "v_mfma_f32_32x32x16_f16 v[192:207], v[44:47], v[140:143], v[192:207]\nv_add_f32 v232, v232, v216\nv_add_f32 v233, v233, v217\nv_add_f32 v234, v234, v218\n"
    "v_add_f32 v235, v235, v219\ns_waitcnt lgkmcnt(8)\nv_mfma_f32_32x32x16_f16 v[192:207], v[48:51], v[144:147], v[192:207]\nv_add_f32 v236, v236, v220\n"
    "v_add_f32 v237, v237, v221\nv_add_f32 v238, v238, v222\nv_add_f32 v239, v239, v223\ns_waitcnt lgkmcnt(6)\n"
    "v_mfma_f32_32x32x16_f16 v[192:207], v[52:55], v[148:151], v[192:207]\nv_cvt_pk_bf16_f32 v232, v232, v233\nv_cvt_pk_bf16_f32 v233, v234, v235\n"
    "ds_write_b64 v33, v[232:233] offset:1024\ns_waitcnt lgkmcnt(5)\nv_mfma_f32_32x32x16_f16 v[192:207], v[240:243], v[152:155], v[192:207]\n"
    "v_cvt_pk_bf16_f32 v236, v236, v237\nv_cvt_pk_bf16_f32 v237, v238, v239\nds_write_b64 v36, v[236:237] offset:1024\ns_waitcnt lgkmcnt(3)\n"
    "v_mfma_f32_32x32x16_f16 v[192:207], v[40:43], v[156:159], v[192:207]\ns_nop 11\nv_cvt_pk_bf16_f32 v192, v192, v193\nv_cvt_pk_bf16_f32 v193, v194, v195\n"
    "ds_write_b64 v32, v[192:193] offset:0\nv_cvt_pk_bf16_f32 v196, v196, v197\nv_cvt_pk_bf16_f32 v197, v198, v199\nds_write_b64 v32, v[196:197] offset:512\n.Lk174:\n"
    "s_setprio 1\ns_waitcnt vmcnt(0) lgkmcnt(0)\ns_barrier\nds_read_b64 v[208:209], v19\nds_read_b64 v[210:211], v19 offset:10240\nds_read_b64 v[212:213], v20\n"
    "ds_read_b64 v[214:215], v20 offset:10240\nds_read_b64 v[216:217], v34\nds_read_b64 v[218:219], v34 offset:10240\ns_cmp_lg_u32 s12, 0\ns_waitcnt lgkmcnt(4)\n"
    "v_dot2c_f32_bf16 v56, s26, v208\nv_dot2c_f32_bf16 v57, s14, v208\nv_dot2c_f32_bf16 v58, s26, v209\nv_dot2c_f32_bf16 v59, s14, v209\nv_dot2c_f32_bf16 v60, s27, v210\n"
    "v_dot2c_f32_bf16 v61, s15, v210\nv_dot2c_f32_bf16 v62, s27, v211\nv_dot2c_f32_bf16 v63, s15, v211\ns_waitcnt lgkmcnt(2)\nv_dot2c_f32_bf16 v56, s26, v212\n"
    "v_dot2c_f32_bf16 v57, s14, v212\nv_dot2c_f32_bf16 v58, s26, v213\nv_dot2c_f32_bf16 v59, s14, v213\nv_dot2c_f32_bf16 v60, s27, v214\nv_dot2c_f32_bf16 v61, s15, v214\n"
    "v_dot2c_f32_bf16 v62, s27, v215\nv_dot2c_f32_bf16 v63, s15, v215\ns_waitcnt lgkmcnt(0)\nv_dot2c_f32_bf16 v56, s26, v216\nv_dot2c_f32_bf16 v57, s14, v216\n"
    "v_dot2c_f32_bf16 v58, s26, v217\nv_dot2c_f32_bf16 v59, s14, v217\nv_dot2c_f32_bf16 v60, s27, v218\nv_dot2c_f32_bf16 v61, s15, v218\nv_dot2c_f32_bf16 v62, s27, v219\n"
    "v_dot2c_f32_bf16 v63, s15, v219\nv_cvt_pk_bf16_f32 v52, v56, v60\nv_cvt_pk_bf16_f32 v53, v57, v61\nv_cvt_pk_bf16_f32 v54, v58, v62\nv_cvt_pk_bf16_f32 v55, v59, v63\n"
    "global_store_dwordx4 v21, v[52:55], s[4:5] sc1\ns_setprio 0\ns_cbranch_scc1 .Lfin165\ns_mov_b32 exec_lo, 0xf0000000\ns_mov_b32 exec_hi, 0xf0000000\n"
    "global_store_dwordx4 v38, v[52:55], s[4:5] sc1\ns_mov_b64 exec, -1\n"
    "s_max_u32 s43, s19, 1\ns_lshl_b32 s42, s43, 14\ns_lshl_b32 s41, s13, 12\ns_add_u32 s46, s42, s41\ns_add_u32 s46, s46, 0xd7c000\nv_and_b32 v223, 63, v0\n"
    "v_lshlrev_b32 v224, 2, v223\ns_add_u32 s48, s4, s46\ns_addc_u32 s49, s5, 0\ns_setprio 3\ns_barrier\nglobal_load_dword v192, v224, s[48:49] offset:0 sc1\n"
    "global_load_dword v193, v224, s[48:49] offset:256 sc1\nglobal_load_dword v194, v224, s[48:49] offset:512 sc1\nglobal_load_dword v195, v224, s[48:49] offset:768 sc1\n"
    "global_load_dword v196, v224, s[48:49] offset:1024 sc1\nglobal_load_dword v197, v224, s[48:49] offset:1280 sc1\nglobal_load_dword v198, v224, s[48:49] offset:1536 sc1\n"
    "global_load_dword v199, v224, s[48:49] offset:1792 sc1\nglobal_load_dword v200, v224, s[48:49] offset:2048 sc1\nglobal_load_dword v201, v224, s[48:49] offset:2304 sc1\n"
    "global_load_dword v202, v224, s[48:49] offset:2560 sc1\nglobal_load_dword v203, v224, s[48:49] offset:2816 sc1\nglobal_load_dword v204, v224, s[48:49] offset:3072 sc1\n"
    "global_load_dword v205, v224, s[48:49] offset:3328 sc1\nglobal_load_dword v206, v224, s[48:49] offset:3584 sc1\nglobal_load_dword v207, v224, s[48:49] offset:3840 sc1\n"
    "s_setprio 0\ns_mov_b32 s12, 8388608\nv_add_u32 v9, 8388608, v9\nv_bfe_u32 v223, v0, 3, 1\nv_bfe_u32 v222, v0, 4, 2\nv_bfe_u32 v221, v0, 2, 1\nv_and_b32 v220, 3, v0\n"
    "s_mul_i32 s43, s13, 0x6000\ns_add_u32 s42, s43, 0x400\nv_lshl_add_u32 v229, v223, 9, s42\nv_lshl_add_u32 v229, v222, 7, v229\nv_lshl_add_u32 v229, v221, 6, v229\n"
    "v_lshl_add_u32 v229, v220, 3, v229\nv_lshl_add_u32 v229, v223, 5, v229\nv_lshl_add_u32 v219, v223, 9, s42\nv_lshl_add_u32 v219, v222, 7, v219\n"
    "v_lshl_add_u32 v219, v221, 6, v219\nv_lshl_add_u32 v219, v220, 3, v219\nv_lshl_add_u32 v219, v223, 5, v219\nv_xor_b32 v230, 32, v219\ns_lshr_b32 s41, s13, 1\n"
    "s_lshl_b32 s40, s41, 7\ns_and_b32 s39, s13, 1\ns_lshl_b32 s38, s39, 6\ns_add_u32 s37, s40, s38\ns_add_u32 s37, s37, 0x27c80\nv_lshl_add_u32 v231, v222, 4, s37\n"
    "ds_read_b128 v[40:43], v231\nds_read_b64_tr_b16 v[128:129], v229 offset:0\nds_read_b64_tr_b16 v[130:131], v229 offset:3072\ns_nop 0\ns_cbranch_execz .Lhop\n"
    "ds_read_b64_tr_b16 v[132:133], v229 offset:6144\nds_read_b64_tr_b16 v[134:135], v229 offset:9216\nds_read_b64_tr_b16 v[136:137], v229 offset:12288\n"
    "ds_read_b64_tr_b16 v[138:139], v229 offset:15360\nds_read_b64_tr_b16 v[140:141], v229 offset:18432\nds_read_b64_tr_b16 v[142:143], v229 offset:21504\n"
    "ds_read_b64_tr_b16 v[144:145], v230 offset:0\nds_read_b64_tr_b16 v[146:147], v230 offset:3072\nds_read_b64_tr_b16 v[148:149], v230 offset:6144\n"
    "ds_read_b64_tr_b16 v[150:151], v230 offset:9216\nds_read_b64_tr_b16 v[152:153], v230 offset:12288\nds_read_b64_tr_b16 v[154:155], v230 offset:15360\n"
    "ds_read_b64_tr_b16 v[156:157], v230 offset:18432\nds_read_b64_tr_b16 v[158:159], v230 offset:21504\nds_read_b64_tr_b16 v[160:161], v229 offset:1024\n"
    "ds_read_b64_tr_b16 v[162:163], v229 offset:4096\nds_read_b64_tr_b16 v[164:165], v229 offset:7168\nds_read_b64_tr_b16 v[166:167], v229 offset:10240\n"
    "ds_read_b64_tr_b16 v[168:169], v229 offset:13312\nds_read_b64_tr_b16 v[170:171], v229 offset:16384\nds_read_b64_tr_b16 v[172:173], v229 offset:19456\n"
    "ds_read_b64_tr_b16 v[174:175], v229 offset:22528\nds_read_b64_tr_b16 v[176:177], v230 offset:1024\nds_read_b64_tr_b16 v[178:179], v230 offset:4096\n"
    "ds_read_b64_tr_b16 v[180:181], v230 offset:7168\nds_read_b64_tr_b16 v[182:183], v230 offset:10240\nds_read_b64_tr_b16 v[184:185], v230 offset:13312\n"
    "ds_read_b64_tr_b16 v[186:187], v230 offset:16384\nds_read_b64_tr_b16 v[188:189], v230 offset:19456\nds_read_b64_tr_b16 v[190:191], v230 offset:22528\n"
    "v_and_b32 v223, 63, v0\ns_lshl_b32 s43, s13, 12\ns_add_u32 s42, s43, 0x23000\nv_lshl_add_u32 v225, v223, 4, s42\ns_mov_b32 s80, 0x10000\ns_waitcnt lgkmcnt(0)\n.Lq182:\n"
    "s_setprio 3\ns_waitcnt vmcnt(0)\nv_mfma_f32_16x16x32_bf16 v[208:211], v[128:131], v[192:195], 0\nv_mfma_f32_16x16x32_bf16 v[212:215], v[144:147], v[192:195], 0\n"
    "v_mfma_f32_16x16x32_bf16 v[216:219], v[160:163], v[192:195], 0\nv_mfma_f32_16x16x32_bf16 v[220:223], v[176:179], v[192:195], 0\n"
    "v_mfma_f32_16x16x32_bf16 v[208:211], v[132:135], v[196:199], v[208:211]\nv_mfma_f32_16x16x32_bf16 v[212:215], v[148:151], v[196:199], v[212:215]\n"
    "v_mfma_f32_16x16x32_bf16 v[216:219], v[164:167], v[196:199], v[216:219]\nv_mfma_f32_16x16x32_bf16 v[220:223], v[180:183], v[196:199], v[220:223]\n"
    "v_mfma_f32_16x16x32_bf16 v[208:211], v[136:139], v[200:203], v[208:211]\nv_mfma_f32_16x16x32_bf16 v[212:215], v[152:155], v[200:203], v[212:215]\n"
    "v_mfma_f32_16x16x32_bf16 v[216:219], v[168:171], v[200:203], v[216:219]\nv_mfma_f32_16x16x32_bf16 v[220:223], v[184:187], v[200:203], v[220:223]\n"
    "v_mfma_f32_16x16x32_bf16 v[208:211], v[140:143], v[204:207], v[208:211]\nv_mfma_f32_16x16x32_bf16 v[212:215], v[156:159], v[204:207], v[212:215]\n"
    "v_mfma_f32_16x16x32_bf16 v[216:219], v[172:175], v[204:207], v[216:219]\nv_mfma_f32_16x16x32_bf16 v[220:223], v[188:191], v[204:207], v[220:223]\ns_setprio 0\n"
    "v_mov_b64 v[48:49], 0\nv_mov_b64 v[50:51], 0\ns_nop 1\nv_cmp_u_f32 vcc, v208, v208\nds_write_b128 v225, v[208:211] offset:0\nds_write_b128 v225, v[212:215] offset:1024\n"
    "ds_write_b128 v225, v[216:219] offset:2048\nds_write_b128 v225, v[220:223] offset:3072\ns_cbranch_vccnz .Lq183\n.Lq183x:\ns_lshl_b32 s43, s22, 17\n"
    "s_lshl_b32 s42, s13, 15\ns_add_u32 s44, s43, s42\ns_add_u32 s44, s44, 0xe01000\nv_and_b32 v167, 63, v0\nv_lshlrev_b32 v59, 4, v167\ns_add_u32 s68, s4, s44\n"
    "s_addc_u32 s69, s5, 0\ns_add_u32 s70, s68, 8192\ns_addc_u32 s71, s69, 0\ns_add_u32 s72, s68, 16384\ns_addc_u32 s73, s69, 0\ns_add_u32 s74, s68, 24576\n"
    "s_addc_u32 s75, s69, 0\nv_and_b32 v159, 63, v0\ns_lshl_b32 s43, s13, 10\ns_add_u32 s42, s43, 0x23000\nv_lshl_add_u32 v226, v159, 4, s42\nv_and_b32 v158, 15, v0\n"
    "s_lshl_b32 s41, s13, 7\ns_add_u32 s40, s41, 0x27d80\nv_lshl_add_u32 v227, v158, 3, s40\ns_mov_b32 s39, 0x27d80\nv_lshl_add_u32 v228, v158, 3, s39\n"
    "v_mul_u32_u24 v157, 0xd0, v158\nv_bfe_u32 v156, v0, 4, 2\ns_lshr_b32 s38, s13, 1\ns_lshl_b32 s37, s38, 6\ns_and_b32 s36, s13, 1\ns_lshl_b32 s35, s36, 5\n"
    "s_add_u32 s34, s37, s35\ns_add_u32 s34, s34, 0x18040\nv_add_u32 v240, s34, v157\nv_lshl_add_u32 v240, v156, 3, v240\nv_and_b32 v155, 3, v0\ns_mov_b32 s33, 0x27f80\n"
    "v_lshl_add_u32 v241, v155, 2, s33\ns_mov_b32 s47, 0x3b000000\nv_mov_b32 v242, 3\nv_mov_b64 v[232:233], 0\nv_mov_b64 v[234:235], 0\nv_mov_b64 v[236:237], 0\n"
    "v_mov_b64 v[238:239], 0\nv_mov_b64 v[244:245], 0\nv_mov_b64 v[246:247], 0\nv_mov_b64 v[248:249], 0\nv_mov_b64 v[250:251], 0\ns_waitcnt lgkmcnt(0)\n"
    "v_lshlrev_b32 v44, 16, v40\nv_lshlrev_b32 v45, 16, v41\nv_lshlrev_b32 v46, 16, v42\nv_lshlrev_b32 v47, 16, v43\nv_and_b32 v40, 0xffff0000, v40\n"
    "v_and_b32 v41, 0xffff0000, v41\nv_and_b32 v42, 0xffff0000, v42\nv_and_b32 v43, 0xffff0000, v43\nv_add_f32 v40, v40, v44\nv_add_f32 v41, v41, v45\n"
    "v_add_f32 v42, v42, v46\nv_add_f32 v43, v43, v47\nv_lshlrev_b32 v52, 16, v192\nv_and_b32 v53, 0xffff0000, v192\nv_add_f32 v48, v48, v52\nv_add_f32 v50, v50, v53\n"
    "v_fmac_f32 v49, v52, v52\nv_fmac_f32 v51, v53, v53\nv_lshlrev_b32 v52, 16, v193\nv_and_b32 v53, 0xffff0000, v193\nv_add_f32 v48, v48, v52\nv_add_f32 v50, v50, v53\n"
    "v_fmac_f32 v49, v52, v52\nv_fmac_f32 v51, v53, v53\nv_lshlrev_b32 v52, 16, v194\nv_and_b32 v53, 0xffff0000, v194\nv_add_f32 v48, v48, v52\nv_add_f32 v50, v50, v53\n"
    "v_fmac_f32 v49, v52, v52\nv_fmac_f32 v51, v53, v53\nv_lshlrev_b32 v52, 16, v195\nv_and_b32 v53, 0xffff0000, v195\nv_add_f32 v48, v48, v52\nv_add_f32 v50, v50, v53\n"
    "v_fmac_f32 v49, v52, v52\nv_fmac_f32 v51, v53, v53\nv_lshlrev_b32 v52, 16, v196\nv_and_b32 v53, 0xffff0000, v196\nv_add_f32 v48, v48, v52\nv_add_f32 v50, v50, v53\n"
    "v_fmac_f32 v49, v52, v52\nv_fmac_f32 v51, v53, v53\nv_lshlrev_b32 v52, 16, v197\nv_and_b32 v53, 0xffff0000, v197\nv_add_f32 v48, v48, v52\nv_add_f32 v50, v50, v53\n"
    "v_fmac_f32 v49, v52, v52\nv_fmac_f32 v51, v53, v53\nv_lshlrev_b32 v52, 16, v198\nv_and_b32 v53, 0xffff0000, v198\nv_add_f32 v48, v48, v52\nv_add_f32 v50, v50, v53\n"
    "v_fmac_f32 v49, v52, v52\nv_fmac_f32 v51, v53, v53\nv_lshlrev_b32 v52, 16, v199\nv_and_b32 v53, 0xffff0000, v199\nv_add_f32 v48, v48, v52\nv_add_f32 v50, v50, v53\n"
    "v_fmac_f32 v49, v52, v52\nv_fmac_f32 v51, v53, v53\nv_lshlrev_b32 v52, 16, v200\ns_cbranch_execz .Lhop\nv_and_b32 v53, 0xffff0000, v200\nv_add_f32 v48, v48, v52\n"
    "v_add_f32 v50, v50, v53\nv_fmac_f32 v49, v52, v52\nv_fmac_f32 v51, v53, v53\nv_lshlrev_b32 v52, 16, v201\nv_and_b32 v53, 0xffff0000, v201\nv_add_f32 v48, v48, v52\n"
    "v_add_f32 v50, v50, v53\nv_fmac_f32 v49, v52, v52\nv_fmac_f32 v51, v53, v53\nv_lshlrev_b32 v52, 16, v202\nv_and_b32 v53, 0xffff0000, v202\nv_add_f32 v48, v48, v52\n"
    "v_add_f32 v50, v50, v53\nv_fmac_f32 v49, v52, v52\nv_fmac_f32 v51, v53, v53\nv_lshlrev_b32 v52, 16, v203\nv_and_b32 v53, 0xffff0000, v203\nv_add_f32 v48, v48, v52\n"
    "v_add_f32 v50, v50, v53\nv_fmac_f32 v49, v52, v52\nv_fmac_f32 v51, v53, v53\nv_lshlrev_b32 v52, 16, v204\nv_and_b32 v53, 0xffff0000, v204\nv_add_f32 v48, v48, v52\n"
    "v_add_f32 v50, v50, v53\nv_fmac_f32 v49, v52, v52\nv_fmac_f32 v51, v53, v53\nv_lshlrev_b32 v52, 16, v205\nv_and_b32 v53, 0xffff0000, v205\nv_add_f32 v48, v48, v52\n"
    "v_add_f32 v50, v50, v53\nv_fmac_f32 v49, v52, v52\nv_fmac_f32 v51, v53, v53\nv_lshlrev_b32 v52, 16, v206\nv_and_b32 v53, 0xffff0000, v206\nv_add_f32 v48, v48, v52\n"
    "v_add_f32 v50, v50, v53\nv_fmac_f32 v49, v52, v52\nv_fmac_f32 v51, v53, v53\nv_lshlrev_b32 v52, 16, v207\nv_and_b32 v53, 0xffff0000, v207\nv_add_f32 v48, v48, v52\n"
    "v_add_f32 v50, v50, v53\nv_fmac_f32 v49, v52, v52\nv_fmac_f32 v51, v53, v53\nv_add_f32 v48, v48, v50\nv_add_f32 v49, v49, v51\ns_nop 1\nv_permlane16_swap_b32 v48, v49\n"
    "v_add_f32 v50, v48, v49\nv_add_f32 v49, v48, v49\ns_nop 1\nv_permlane32_swap_b32 v50, v49\nv_add_f32 v50, v50, v49\nv_mov_b32 v48, v50\nv_mov_b32 v49, v50\ns_nop 1\n"
    "v_permlane16_swap_b32 v48, v49\ns_mov_b64 exec, 0xffff\nds_write_b64 v227, v[48:49]\ns_mov_b64 exec, 1\nds_write_b32 v25, v242 offset:16\ns_mov_b64 exec, -1\n"
    "s_mov_b32 s86, 0x100000\n.Ls184:\nds_read_b32 v48, v241 offset:16\ns_waitcnt lgkmcnt(0)\nv_cmp_gt_i32 vcc, 3, v48\ns_sub_u32 s86, s86, 1\ns_cbranch_scc1 .Ls185\n"
    "s_cbranch_vccz .Ls185\ns_sleep 1\ns_branch .Ls184\n.Ls185:\nds_read_b64 v[48:49], v228 offset:0\nds_read_b64 v[50:51], v228 offset:128\n"
    "ds_read_b64 v[52:53], v228 offset:256\nds_read_b64 v[54:55], v228 offset:384\nds_read_b128 v[208:211], v226 offset:0\nds_read_b128 v[212:215], v226 offset:4096\n"
    "ds_read_b128 v[216:219], v226 offset:8192\nds_read_b128 v[220:223], v226 offset:12288\ns_waitcnt lgkmcnt(4)\nv_add_f32 v48, v48, v50\nv_add_f32 v49, v49, v51\n"
    "v_add_f32 v52, v52, v54\nv_add_f32 v53, v53, v55\nv_add_f32 v48, v48, v52\nv_add_f32 v49, v49, v53\nv_mul_f32 v48, s47, v48\nv_mul_f32 v50, v48, v48\n"
    "v_fma_f32 v49, v49, s47, -v50\nv_max_f32 v49, 0, v49\nv_cvt_pk_bf16_f32 v49, 0, v49\nv_add_f32 v49, 0x3727c5ac, v49\nv_cvt_pk_bf16_f32 v49, 0, v49\nv_sqrt_f32 v49, v49\n"
    "v_cvt_pk_bf16_f32 v48, 0, -v48\nv_cvt_pk_bf16_f32 v49, 0, v49\nv_rcp_f32 v49, v49\ns_waitcnt lgkmcnt(0)\nv_add_f32 v208, v208, v216\nv_add_f32 v209, v209, v217\n"
    "v_add_f32 v210, v210, v218\nv_add_f32 v211, v211, v219\nv_add_f32 v212, v212, v220\nv_add_f32 v213, v213, v221\nv_add_f32 v214, v214, v222\nv_add_f32 v215, v215, v223\n"
    "v_fmac_f32 v208, v48, v40\nv_fmac_f32 v209, v48, v41\nv_fmac_f32 v210, v48, v42\nv_fmac_f32 v211, v48, v43\nv_add_f32 v208, v208, v212\nv_add_f32 v209, v209, v213\n"
    "v_add_f32 v210, v210, v214\nv_add_f32 v211, v211, v215\nv_mul_f32 v208, v208, v49\nv_mul_f32 v209, v209, v49\nv_mul_f32 v210, v210, v49\nv_mul_f32 v211, v211, v49\n"
    "v_cvt_pk_bf16_f32 v44, v208, v209\nv_cvt_pk_bf16_f32 v45, v210, v211\nds_write_b64 v240, v[44:45]\ns_setprio 0\nv_bfe_u32 v223, v0, 5, 1\nv_and_b32 v222, 31, v0\n"
    "s_lshl_b32 s43, s21, 15\ns_lshl_b32 s42, s22, 13\ns_lshl_b32 s41, s13, 11\ns_add_u32 s40, s43, s42\ns_add_u32 s40, s40, s41\ns_add_u32 s40, s40, 0x600000\n"
    "v_lshl_add_u32 v47, v223, 8, s40\nv_lshl_add_u32 v47, v222, 2, v47\ns_mov_b32 s44, 0x00ff00ff\ns_mov_b32 s45, 0xff00ff00\ns_mov_b32 s46, s44\ns_mov_b32 s47, s44\n"
    "s_branch .Latt12\n"
    ".Lqg166:\ns_mov_b32 s12, 8388608\nv_add_u32 v9, 8388608, v9\ns_cmp_lg_u32 s3, 0\ns_cbranch_scc1 .Lz186\ns_waitcnt lgkmcnt(0)\ns_add_u32 s87, s87, s100\n"
    "s_add_u32 s87, s87, 32\n.Lj187:\ns_memrealtime s[100:101]\ns_waitcnt lgkmcnt(0)\ns_sub_u32 s101, s87, s100\ns_cmp_le_i32 s101, 0\ns_cbranch_scc0 .Lj187\n.Lz186:\n"
    "s_barrier\nglobal_load_dword v144, v39, s[52:53] offset:-2048 sc1\nglobal_load_dword v145, v39, s[52:53] offset:-1920 sc1\n"
    "global_load_dword v146, v39, s[52:53] offset:-1792 sc1\nglobal_load_dword v147, v39, s[52:53] offset:-1664 sc1\nglobal_load_dword v148, v39, s[52:53] offset:-1024 sc1\n"
    "global_load_dword v149, v39, s[52:53] offset:-896 sc1\nglobal_load_dword v150, v39, s[52:53] offset:-768 sc1\nglobal_load_dword v151, v39, s[52:53] offset:-640 sc1\n"
    "global_load_dword v152, v39, s[52:53] offset:0 sc1\nglobal_load_dword v153, v39, s[52:53] offset:128 sc1\nglobal_load_dword v154, v39, s[52:53] offset:256 sc1\n"
    "global_load_dword v155, v39, s[52:53] offset:384 sc1\nglobal_load_dword v156, v39, s[52:53] offset:1024 sc1\nglobal_load_dword v157, v39, s[52:53] offset:1152 sc1\n"
    "global_load_dword v158, v39, s[52:53] offset:1280 sc1\nglobal_load_dword v159, v39, s[52:53] offset:1408 sc1\nglobal_load_dword v160, v39, s[54:55] offset:-2048 sc1\n"
    "global_load_dword v161, v39, s[54:55] offset:-1920 sc1\nglobal_load_dword v162, v39, s[54:55] offset:-1792 sc1\nglobal_load_dword v163, v39, s[54:55] offset:-1664 sc1\n"
    "global_load_dword v164, v39, s[54:55] offset:-1024 sc1\nglobal_load_dword v165, v39, s[54:55] offset:-896 sc1\nglobal_load_dword v166, v39, s[54:55] offset:-768 sc1\n"
    "global_load_dword v167, v39, s[54:55] offset:-640 sc1\nglobal_load_dword v168, v39, s[54:55] offset:0 sc1\nglobal_load_dword v169, v39, s[54:55] offset:128 sc1\n"
    "global_load_dword v170, v39, s[54:55] offset:256 sc1\nglobal_load_dword v171, v39, s[54:55] offset:384 sc1\nglobal_load_dword v172, v39, s[54:55] offset:1024 sc1\n"
    "global_load_dword v173, v39, s[54:55] offset:1152 sc1\nglobal_load_dword v174, v39, s[54:55] offset:1280 sc1\ns_nop 0\ns_cbranch_execz .Lhop\n"
    "global_load_dword v175, v39, s[54:55] offset:1408 sc1\nglobal_load_dword v176, v39, s[56:57] offset:-2048 sc1\nglobal_load_dword v177, v39, s[56:57] offset:-1920 sc1\n"
    "global_load_dword v178, v39, s[56:57] offset:-1792 sc1\nglobal_load_dword v179, v39, s[56:57] offset:-1664 sc1\nglobal_load_dword v180, v39, s[56:57] offset:-1024 sc1\n"
    "global_load_dword v181, v39, s[56:57] offset:-896 sc1\nglobal_load_dword v182, v39, s[56:57] offset:-768 sc1\nglobal_load_dword v183, v39, s[56:57] offset:-640 sc1\n"
    "global_load_dword v184, v39, s[56:57] offset:0 sc1\nglobal_load_dword v185, v39, s[56:57] offset:128 sc1\nglobal_load_dword v186, v39, s[56:57] offset:256 sc1\n"
    "global_load_dword v187, v39, s[56:57] offset:384 sc1\nglobal_load_dword v188, v39, s[56:57] offset:1024 sc1\nglobal_load_dword v189, v39, s[56:57] offset:1152 sc1\n"
    "global_load_dword v190, v39, s[56:57] offset:1280 sc1\nglobal_load_dword v191, v39, s[56:57] offset:1408 sc1\n.Ld188:\ns_waitcnt vmcnt(44)\n"
    "v_dot2_f32_bf16 v112, v144, v241, 0\nv_dot2_f32_bf16 v113, v144, v144, 0\nv_dot2_f32_bf16 v114, v145, v241, 0\nv_dot2_f32_bf16 v115, v145, v145, 0\n"
    "v_dot2_f32_bf16 v116, v146, v241, 0\nv_dot2_f32_bf16 v117, v146, v146, 0\nv_dot2_f32_bf16 v118, v147, v241, 0\nv_dot2_f32_bf16 v119, v147, v147, 0\ns_waitcnt vmcnt(40)\n"
    "v_dot2_f32_bf16 v112, v148, v241, v112\nv_dot2_f32_bf16 v113, v148, v148, v113\nv_dot2_f32_bf16 v114, v149, v241, v114\nv_dot2_f32_bf16 v115, v149, v149, v115\n"
    "v_dot2_f32_bf16 v116, v150, v241, v116\nv_dot2_f32_bf16 v117, v150, v150, v117\nv_dot2_f32_bf16 v118, v151, v241, v118\nv_dot2_f32_bf16 v119, v151, v151, v119\n"
    "s_waitcnt vmcnt(36)\nv_dot2_f32_bf16 v112, v152, v241, v112\nv_dot2_f32_bf16 v113, v152, v152, v113\nv_dot2_f32_bf16 v114, v153, v241, v114\n"
    "v_dot2_f32_bf16 v115, v153, v153, v115\nv_dot2_f32_bf16 v116, v154, v241, v116\nv_dot2_f32_bf16 v117, v154, v154, v117\nv_dot2_f32_bf16 v118, v155, v241, v118\n"
    "v_dot2_f32_bf16 v119, v155, v155, v119\ns_waitcnt vmcnt(32)\nv_dot2_f32_bf16 v112, v156, v241, v112\nv_dot2_f32_bf16 v113, v156, v156, v113\n"
    "v_dot2_f32_bf16 v114, v157, v241, v114\nv_dot2_f32_bf16 v115, v157, v157, v115\nv_dot2_f32_bf16 v116, v158, v241, v116\nv_dot2_f32_bf16 v117, v158, v158, v117\n"
    "v_dot2_f32_bf16 v118, v159, v241, v118\nv_dot2_f32_bf16 v119, v159, v159, v119\nv_mfma_f32_32x32x16_bf16 v[64:79], v[208:211], v[144:147], 0\n"
    "ds_read_b64_tr_b16 v[208:209], v242 offset:7168\nds_read_b64_tr_b16 v[210:211], v242 offset:7424\nv_add_f32 v112, v112, v114\nv_add_f32 v113, v113, v115\n"
    "global_load_dword v192, v39, s[58:59] offset:-2048 sc1\nv_mfma_f32_32x32x16_bf16 v[64:79], v[212:215], v[148:151], v[64:79]\n"
    "ds_read_b64_tr_b16 v[212:213], v242 offset:10240\nds_read_b64_tr_b16 v[214:215], v242 offset:10496\nv_add_f32 v116, v116, v118\nv_add_f32 v117, v117, v119\n"
    "global_load_dword v193, v39, s[58:59] offset:-1920 sc1\nv_mfma_f32_32x32x16_bf16 v[64:79], v[216:219], v[152:155], v[64:79]\n"
    "ds_read_b64_tr_b16 v[216:217], v242 offset:2048\nds_read_b64_tr_b16 v[218:219], v242 offset:2304\nv_add_f32 v112, v112, v116\nv_add_f32 v113, v113, v117\n"
    "global_load_dword v194, v39, s[58:59] offset:-1792 sc1\ns_waitcnt lgkmcnt(6)\nv_mfma_f32_32x32x16_bf16 v[64:79], v[220:223], v[156:159], v[64:79]\n"
    "ds_read_b64_tr_b16 v[220:221], v242 offset:5120\nds_read_b64_tr_b16 v[222:223], v242 offset:5376\nv_cmp_u_f32 vcc, v112, v113\nv_permlane32_swap_b32 v112, v113\n"
    "v_add_f32 v112, v112, v113\nglobal_load_dword v195, v39, s[58:59] offset:-1664 sc1\nv_mfma_f32_32x32x16_bf16 v[80:95], v[224:227], v[144:147], 0\n"
    "ds_read_b64_tr_b16 v[224:225], v242 offset:8192\nds_read_b64_tr_b16 v[226:227], v242 offset:8448\nglobal_load_dword v196, v39, s[58:59] offset:-1024 sc1\n"
    "global_load_dword v197, v39, s[58:59] offset:-896 sc1\nv_mad_u32_u24 v240, s45, 3, v36\nv_mfma_f32_32x32x16_bf16 v[80:95], v[228:231], v[148:151], v[80:95]\n"
    "ds_read_b64_tr_b16 v[228:229], v242 offset:11264\nds_read_b64_tr_b16 v[230:231], v242 offset:11520\ns_cbranch_vccnz .Ld189\n.Ld190:\n"
    "global_store_dword v35, v112, s[4:5] sc1\ns_memrealtime s[100:101]\nglobal_load_dword v198, v39, s[58:59] offset:-768 sc1\ns_waitcnt lgkmcnt(6)\n"
    "v_mfma_f32_32x32x16_bf16 v[80:95], v[208:211], v[152:155], v[80:95]\nds_read_b64_tr_b16 v[208:209], v240 offset:0\nds_read_b64_tr_b16 v[210:211], v240 offset:256\n"
    "global_load_dword v199, v39, s[58:59] offset:-640 sc1\nglobal_load_dword v200, v39, s[58:59] offset:0 sc1\nglobal_load_dword v201, v39, s[58:59] offset:128 sc1\n"
    "v_mfma_f32_32x32x16_bf16 v[80:95], v[212:215], v[156:159], v[80:95]\nds_read_b64_tr_b16 v[212:213], v240 offset:3072\nds_read_b64_tr_b16 v[214:215], v240 offset:3328\n"
    "global_load_dword v202, v39, s[58:59] offset:256 sc1\nglobal_load_dword v203, v39, s[58:59] offset:384 sc1\nglobal_load_dword v204, v39, s[58:59] offset:1024 sc1\n"
    "v_mfma_f32_32x32x16_bf16 v[96:111], v[216:219], v[144:147], 0\nds_read_b64_tr_b16 v[216:217], v240 offset:6144\nds_read_b64_tr_b16 v[218:219], v240 offset:6400\n"
    "global_load_dword v205, v39, s[58:59] offset:1152 sc1\ns_waitcnt vmcnt(31)\nv_max3_u32 v244, v160, v161, v162\nv_max3_u32 v243, v163, v164, v165\ns_waitcnt lgkmcnt(6)\n"
    "v_mfma_f32_32x32x16_bf16 v[96:111], v[220:223], v[148:151], v[96:111]\nds_read_b64_tr_b16 v[220:221], v240 offset:9216\nds_read_b64_tr_b16 v[222:223], v240 offset:9472\n"
    "global_load_dword v206, v39, s[58:59] offset:1280 sc1\nv_max3_u32 v244, v244, v166, v167\nv_max3_u32 v243, v243, v168, v169\nv_max3_u32 v244, v244, v170, v171\n"
    "v_mfma_f32_32x32x16_bf16 v[96:111], v[224:227], v[152:155], v[96:111]\nds_read_b64_tr_b16 v[224:225], v240 offset:1024\nds_read_b64_tr_b16 v[226:227], v240 offset:1280\n"
    "v_max3_u32 v243, v243, v172, v173\nv_max3_u32 v244, v244, v174, v175\nv_max_u32 v243, v243, v244\nv_cmp_eq_u32 vcc, -1, v243\n"
    "v_mfma_f32_32x32x16_bf16 v[96:111], v[228:231], v[156:159], v[96:111]\nds_read_b64_tr_b16 v[228:229], v240 offset:4096\nds_read_b64_tr_b16 v[230:231], v240 offset:4352\n"
    "global_load_dword v207, v39, s[58:59] offset:1408 sc1\ns_cbranch_vccnz .Lx193\n.Lx194:\ns_waitcnt lgkmcnt(6)\n"
    "v_mfma_f32_32x32x16_bf16 v[64:79], v[208:211], v[160:163], v[64:79]\nds_read_b64_tr_b16 v[208:209], v240 offset:7168\nds_read_b64_tr_b16 v[210:211], v240 offset:7424\n"
    "global_load_dword v144, v39, s[60:61] offset:-2048 sc1\nglobal_load_dword v145, v39, s[60:61] offset:-1920 sc1\ns_nop 0\ns_cbranch_execz .Lhop\n"
    "v_mfma_f32_32x32x16_bf16 v[64:79], v[212:215], v[164:167], v[64:79]\nds_read_b64_tr_b16 v[212:213], v240 offset:10240\nds_read_b64_tr_b16 v[214:215], v240 offset:10496\n"
    "global_load_dword v146, v39, s[60:61] offset:-1792 sc1\nglobal_load_dword v147, v39, s[60:61] offset:-1664 sc1\n"
    "v_mfma_f32_32x32x16_bf16 v[64:79], v[216:219], v[168:171], v[64:79]\nds_read_b64_tr_b16 v[216:217], v240 offset:2048\nds_read_b64_tr_b16 v[218:219], v240 offset:2304\n"
    "global_load_dword v148, v39, s[60:61] offset:-1024 sc1\nglobal_load_dword v149, v39, s[60:61] offset:-896 sc1\ns_waitcnt lgkmcnt(6)\n"
    "v_mfma_f32_32x32x16_bf16 v[64:79], v[220:223], v[172:175], v[64:79]\nds_read_b64_tr_b16 v[220:221], v240 offset:5120\nds_read_b64_tr_b16 v[222:223], v240 offset:5376\n"
    "global_load_dword v150, v39, s[60:61] offset:-768 sc1\nglobal_load_dword v151, v39, s[60:61] offset:-640 sc1\n"
    "v_mfma_f32_32x32x16_bf16 v[80:95], v[224:227], v[160:163], v[80:95]\nds_read_b64_tr_b16 v[224:225], v240 offset:8192\nds_read_b64_tr_b16 v[226:227], v240 offset:8448\n"
    "global_load_dword v152, v39, s[60:61] offset:0 sc1\nglobal_load_dword v153, v39, s[60:61] offset:128 sc1\nv_mad_u32_u24 v242, s46, 3, v36\n"
    "v_mfma_f32_32x32x16_bf16 v[80:95], v[228:231], v[164:167], v[80:95]\nds_read_b64_tr_b16 v[228:229], v240 offset:11264\nds_read_b64_tr_b16 v[230:231], v240 offset:11520\n"
    "global_load_dword v154, v39, s[60:61] offset:256 sc1\nglobal_load_dword v155, v39, s[60:61] offset:384 sc1\ns_waitcnt lgkmcnt(6)\n"
    "v_mfma_f32_32x32x16_bf16 v[80:95], v[208:211], v[168:171], v[80:95]\nds_read_b64_tr_b16 v[208:209], v242 offset:0\nds_read_b64_tr_b16 v[210:211], v242 offset:256\n"
    "global_load_dword v156, v39, s[60:61] offset:1024 sc1\nglobal_load_dword v157, v39, s[60:61] offset:1152 sc1\n"
    "v_mfma_f32_32x32x16_bf16 v[80:95], v[212:215], v[172:175], v[80:95]\nds_read_b64_tr_b16 v[212:213], v242 offset:3072\nds_read_b64_tr_b16 v[214:215], v242 offset:3328\n"
    "global_load_dword v158, v39, s[60:61] offset:1280 sc1\nglobal_load_dword v159, v39, s[60:61] offset:1408 sc1\n"
    "v_mfma_f32_32x32x16_bf16 v[96:111], v[216:219], v[160:163], v[96:111]\nds_read_b64_tr_b16 v[216:217], v242 offset:6144\nds_read_b64_tr_b16 v[218:219], v242 offset:6400\n"
    "s_waitcnt vmcnt(33)\nv_max3_u32 v244, v176, v177, v178\nv_max3_u32 v243, v179, v180, v181\ns_waitcnt lgkmcnt(6)\n"
    "v_mfma_f32_32x32x16_bf16 v[96:111], v[220:223], v[164:167], v[96:111]\nds_read_b64_tr_b16 v[220:221], v242 offset:9216\nds_read_b64_tr_b16 v[222:223], v242 offset:9472\n"
    "v_max3_u32 v244, v244, v182, v183\nv_max3_u32 v243, v243, v184, v185\nv_max3_u32 v244, v244, v186, v187\n"
    "v_mfma_f32_32x32x16_bf16 v[96:111], v[224:227], v[168:171], v[96:111]\nds_read_b64_tr_b16 v[224:225], v242 offset:1024\nds_read_b64_tr_b16 v[226:227], v242 offset:1280\n"
    "v_max3_u32 v243, v243, v188, v189\nv_max3_u32 v244, v244, v190, v191\nv_max_u32 v243, v243, v244\nv_cmp_eq_u32 vcc, -1, v243\n"
    "v_mfma_f32_32x32x16_bf16 v[96:111], v[228:231], v[172:175], v[96:111]\nds_read_b64_tr_b16 v[228:229], v242 offset:4096\nds_read_b64_tr_b16 v[230:231], v242 offset:4352\n"
    "s_cbranch_vccnz .Lx195\n.Lx196:\ns_waitcnt lgkmcnt(6)\nv_mfma_f32_32x32x16_bf16 v[64:79], v[208:211], v[176:179], v[64:79]\n"
    "ds_read_b64_tr_b16 v[208:209], v242 offset:7168\nds_read_b64_tr_b16 v[210:211], v242 offset:7424\nglobal_load_dword v160, v39, s[62:63] offset:-2048 sc1\n"
    "global_load_dword v161, v39, s[62:63] offset:-1920 sc1\nv_mfma_f32_32x32x16_bf16 v[64:79], v[212:215], v[180:183], v[64:79]\n"
    "ds_read_b64_tr_b16 v[212:213], v242 offset:10240\nds_read_b64_tr_b16 v[214:215], v242 offset:10496\nglobal_load_dword v162, v39, s[62:63] offset:-1792 sc1\n"
    "global_load_dword v163, v39, s[62:63] offset:-1664 sc1\nv_mfma_f32_32x32x16_bf16 v[64:79], v[216:219], v[184:187], v[64:79]\n"
    "ds_read_b64_tr_b16 v[216:217], v242 offset:2048\nds_read_b64_tr_b16 v[218:219], v242 offset:2304\nglobal_load_dword v164, v39, s[62:63] offset:-1024 sc1\n"
    "global_load_dword v165, v39, s[62:63] offset:-896 sc1\ns_waitcnt lgkmcnt(6)\nv_mfma_f32_32x32x16_bf16 v[64:79], v[220:223], v[188:191], v[64:79]\n"
    "ds_read_b64_tr_b16 v[220:221], v242 offset:5120\nds_read_b64_tr_b16 v[222:223], v242 offset:5376\nglobal_load_dword v166, v39, s[62:63] offset:-768 sc1\n"
    "global_load_dword v167, v39, s[62:63] offset:-640 sc1\nv_mfma_f32_32x32x16_bf16 v[80:95], v[224:227], v[176:179], v[80:95]\n"
    "ds_read_b64_tr_b16 v[224:225], v242 offset:8192\nds_read_b64_tr_b16 v[226:227], v242 offset:8448\nglobal_load_dword v168, v39, s[62:63] offset:0 sc1\n"
    "global_load_dword v169, v39, s[62:63] offset:128 sc1\nv_mad_u32_u24 v240, s47, 3, v36\nv_mfma_f32_32x32x16_bf16 v[80:95], v[228:231], v[180:183], v[80:95]\n"
    "ds_read_b64_tr_b16 v[228:229], v242 offset:11264\nds_read_b64_tr_b16 v[230:231], v242 offset:11520\nglobal_load_dword v170, v39, s[62:63] offset:256 sc1\n"
    "global_load_dword v171, v39, s[62:63] offset:384 sc1\ns_waitcnt lgkmcnt(6)\nv_mfma_f32_32x32x16_bf16 v[80:95], v[208:211], v[184:187], v[80:95]\n"
    "ds_read_b64_tr_b16 v[208:209], v240 offset:0\nds_read_b64_tr_b16 v[210:211], v240 offset:256\nglobal_load_dword v172, v39, s[62:63] offset:1024 sc1\n"
    "global_load_dword v173, v39, s[62:63] offset:1152 sc1\nv_mfma_f32_32x32x16_bf16 v[80:95], v[212:215], v[188:191], v[80:95]\n"
    "ds_read_b64_tr_b16 v[212:213], v240 offset:3072\nds_read_b64_tr_b16 v[214:215], v240 offset:3328\nglobal_load_dword v174, v39, s[62:63] offset:1280 sc1\n"
    "global_load_dword v175, v39, s[62:63] offset:1408 sc1\nv_mfma_f32_32x32x16_bf16 v[96:111], v[216:219], v[176:179], v[96:111]\n"
    "ds_read_b64_tr_b16 v[216:217], v240 offset:6144\nds_read_b64_tr_b16 v[218:219], v240 offset:6400\ns_waitcnt vmcnt(32)\nv_max3_u32 v244, v192, v193, v194\n"
    "v_max3_u32 v243, v195, v196, v197\ns_waitcnt lgkmcnt(6)\nv_mfma_f32_32x32x16_bf16 v[96:111], v[220:223], v[180:183], v[96:111]\n"
    "ds_read_b64_tr_b16 v[220:221], v240 offset:9216\nds_read_b64_tr_b16 v[222:223], v240 offset:9472\nv_max3_u32 v244, v244, v198, v199\nv_max3_u32 v243, v243, v200, v201\n"
    "v_max3_u32 v244, v244, v202, v203\nv_mfma_f32_32x32x16_bf16 v[96:111], v[224:227], v[184:187], v[96:111]\nds_read_b64_tr_b16 v[224:225], v240 offset:1024\n"
    "ds_read_b64_tr_b16 v[226:227], v240 offset:1280\nv_max3_u32 v243, v243, v204, v205\nv_max3_u32 v244, v244, v206, v207\nv_max_u32 v243, v243, v244\n"
    "v_cmp_eq_u32 vcc, -1, v243\nv_mfma_f32_32x32x16_bf16 v[96:111], v[228:231], v[188:191], v[96:111]\nds_read_b64_tr_b16 v[228:229], v240 offset:4096\n"
    "ds_read_b64_tr_b16 v[230:231], v240 offset:4352\ns_cbranch_vccnz .Lx197\n.Lx198:\ns_waitcnt lgkmcnt(6)\n"
    "v_mfma_f32_32x32x16_bf16 v[64:79], v[208:211], v[192:195], v[64:79]\nds_read_b64_tr_b16 v[208:209], v240 offset:7168\nds_read_b64_tr_b16 v[210:211], v240 offset:7424\n"
    "global_load_dword v176, v39, s[64:65] offset:-2048 sc1\ns_nop 0\ns_cbranch_execz .Lhop\nglobal_load_dword v177, v39, s[64:65] offset:-1920 sc1\n"
    "v_mfma_f32_32x32x16_bf16 v[64:79], v[212:215], v[196:199], v[64:79]\nds_read_b64_tr_b16 v[212:213], v240 offset:10240\nds_read_b64_tr_b16 v[214:215], v240 offset:10496\n"
    "global_load_dword v178, v39, s[64:65] offset:-1792 sc1\nglobal_load_dword v179, v39, s[64:65] offset:-1664 sc1\n"
    "v_mfma_f32_32x32x16_bf16 v[64:79], v[216:219], v[200:203], v[64:79]\nds_read_b64_tr_b16 v[216:217], v240 offset:2048\nds_read_b64_tr_b16 v[218:219], v240 offset:2304\n"
    "global_load_dword v180, v39, s[64:65] offset:-1024 sc1\nglobal_load_dword v181, v39, s[64:65] offset:-896 sc1\ns_waitcnt lgkmcnt(6)\n"
    "v_mfma_f32_32x32x16_bf16 v[64:79], v[220:223], v[204:207], v[64:79]\nds_read_b64_tr_b16 v[220:221], v240 offset:5120\nds_read_b64_tr_b16 v[222:223], v240 offset:5376\n"
    "global_load_dword v182, v39, s[64:65] offset:-768 sc1\nglobal_load_dword v183, v39, s[64:65] offset:-640 sc1\n"
    "v_mfma_f32_32x32x16_bf16 v[80:95], v[224:227], v[192:195], v[80:95]\nds_read_b64_tr_b16 v[224:225], v240 offset:8192\nds_read_b64_tr_b16 v[226:227], v240 offset:8448\n"
    "global_load_dword v184, v39, s[64:65] offset:0 sc1\nglobal_load_dword v185, v39, s[64:65] offset:128 sc1\nv_mad_u32_u24 v242, s48, 3, v36\n"
    "v_mfma_f32_32x32x16_bf16 v[80:95], v[228:231], v[196:199], v[80:95]\nds_read_b64_tr_b16 v[228:229], v240 offset:11264\nds_read_b64_tr_b16 v[230:231], v240 offset:11520\n"
    "global_load_dword v186, v39, s[64:65] offset:256 sc1\nglobal_load_dword v187, v39, s[64:65] offset:384 sc1\ns_waitcnt lgkmcnt(6)\n"
    "v_mfma_f32_32x32x16_bf16 v[80:95], v[208:211], v[200:203], v[80:95]\nds_read_b64_tr_b16 v[208:209], v242 offset:0\nds_read_b64_tr_b16 v[210:211], v242 offset:256\n"
    "global_load_dword v188, v39, s[64:65] offset:1024 sc1\nglobal_load_dword v189, v39, s[64:65] offset:1152 sc1\n"
    "v_mfma_f32_32x32x16_bf16 v[80:95], v[212:215], v[204:207], v[80:95]\nds_read_b64_tr_b16 v[212:213], v242 offset:3072\nds_read_b64_tr_b16 v[214:215], v242 offset:3328\n"
    "global_load_dword v190, v39, s[64:65] offset:1280 sc1\nglobal_load_dword v191, v39, s[64:65] offset:1408 sc1\n"
    "v_mfma_f32_32x32x16_bf16 v[96:111], v[216:219], v[192:195], v[96:111]\nds_read_b64_tr_b16 v[216:217], v242 offset:6144\nds_read_b64_tr_b16 v[218:219], v242 offset:6400\n"
    "s_waitcnt vmcnt(32)\nv_max3_u32 v244, v144, v145, v146\nv_max3_u32 v243, v147, v148, v149\ns_waitcnt lgkmcnt(6)\n"
    "v_mfma_f32_32x32x16_bf16 v[96:111], v[220:223], v[196:199], v[96:111]\nds_read_b64_tr_b16 v[220:221], v242 offset:9216\nds_read_b64_tr_b16 v[222:223], v242 offset:9472\n"
    "v_max3_u32 v244, v244, v150, v151\nv_max3_u32 v243, v243, v152, v153\nv_max3_u32 v244, v244, v154, v155\n"
    "v_mfma_f32_32x32x16_bf16 v[96:111], v[224:227], v[200:203], v[96:111]\nds_read_b64_tr_b16 v[224:225], v242 offset:1024\nds_read_b64_tr_b16 v[226:227], v242 offset:1280\n"
    "v_max3_u32 v243, v243, v156, v157\nv_max3_u32 v244, v244, v158, v159\nv_max_u32 v243, v243, v244\nv_cmp_eq_u32 vcc, -1, v243\n"
    "v_mfma_f32_32x32x16_bf16 v[96:111], v[228:231], v[204:207], v[96:111]\nds_read_b64_tr_b16 v[228:229], v242 offset:4096\nds_read_b64_tr_b16 v[230:231], v242 offset:4352\n"
    "s_cbranch_vccnz .Lx199\n.Lx200:\ns_waitcnt lgkmcnt(6)\nv_mfma_f32_32x32x16_bf16 v[64:79], v[208:211], v[144:147], v[64:79]\n"
    "ds_read_b64_tr_b16 v[208:209], v242 offset:7168\nds_read_b64_tr_b16 v[210:211], v242 offset:7424\nglobal_load_dword v192, v39, s[66:67] offset:-2048 sc1\n"
    "global_load_dword v193, v39, s[66:67] offset:-1920 sc1\nv_mfma_f32_32x32x16_bf16 v[64:79], v[212:215], v[148:151], v[64:79]\n"
    "ds_read_b64_tr_b16 v[212:213], v242 offset:10240\nds_read_b64_tr_b16 v[214:215], v242 offset:10496\nglobal_load_dword v194, v39, s[66:67] offset:-1792 sc1\n"
    "global_load_dword v195, v39, s[66:67] offset:-1664 sc1\nv_mfma_f32_32x32x16_bf16 v[64:79], v[216:219], v[152:155], v[64:79]\n"
    "ds_read_b64_tr_b16 v[216:217], v242 offset:2048\nds_read_b64_tr_b16 v[218:219], v242 offset:2304\nglobal_load_dword v196, v39, s[66:67] offset:-1024 sc1\n"
    "global_load_dword v197, v39, s[66:67] offset:-896 sc1\ns_waitcnt lgkmcnt(6)\nv_mfma_f32_32x32x16_bf16 v[64:79], v[220:223], v[156:159], v[64:79]\n"
    "ds_read_b64_tr_b16 v[220:221], v242 offset:5120\nds_read_b64_tr_b16 v[222:223], v242 offset:5376\nglobal_load_dword v198, v39, s[66:67] offset:-768 sc1\n"
    "global_load_dword v199, v39, s[66:67] offset:-640 sc1\nv_mfma_f32_32x32x16_bf16 v[80:95], v[224:227], v[144:147], v[80:95]\n"
    "ds_read_b64_tr_b16 v[224:225], v242 offset:8192\nds_read_b64_tr_b16 v[226:227], v242 offset:8448\nglobal_load_dword v200, v39, s[66:67] offset:0 sc1\n"
    "global_load_dword v201, v39, s[66:67] offset:128 sc1\nv_mad_u32_u24 v240, s49, 3, v36\nv_mfma_f32_32x32x16_bf16 v[80:95], v[228:231], v[148:151], v[80:95]\n"
    "ds_read_b64_tr_b16 v[228:229], v242 offset:11264\nds_read_b64_tr_b16 v[230:231], v242 offset:11520\nglobal_load_dword v202, v39, s[66:67] offset:256 sc1\n"
    "global_load_dword v203, v39, s[66:67] offset:384 sc1\ns_waitcnt lgkmcnt(6)\nv_mfma_f32_32x32x16_bf16 v[80:95], v[208:211], v[152:155], v[80:95]\n"
    "ds_read_b64_tr_b16 v[208:209], v240 offset:0\nds_read_b64_tr_b16 v[210:211], v240 offset:256\nglobal_load_dword v204, v39, s[66:67] offset:1024 sc1\n"
    "global_load_dword v205, v39, s[66:67] offset:1152 sc1\nv_mfma_f32_32x32x16_bf16 v[80:95], v[212:215], v[156:159], v[80:95]\n"
    "ds_read_b64_tr_b16 v[212:213], v240 offset:3072\nds_read_b64_tr_b16 v[214:215], v240 offset:3328\nglobal_load_dword v206, v39, s[66:67] offset:1280 sc1\n"
    "global_load_dword v207, v39, s[66:67] offset:1408 sc1\nv_mfma_f32_32x32x16_bf16 v[96:111], v[216:219], v[144:147], v[96:111]\n"
    "ds_read_b64_tr_b16 v[216:217], v240 offset:6144\nds_read_b64_tr_b16 v[218:219], v240 offset:6400\ns_waitcnt vmcnt(32)\nv_max3_u32 v244, v160, v161, v162\n"
    "v_max3_u32 v243, v163, v164, v165\ns_waitcnt lgkmcnt(6)\nv_mfma_f32_32x32x16_bf16 v[96:111], v[220:223], v[148:151], v[96:111]\n"
    "ds_read_b64_tr_b16 v[220:221], v240 offset:9216\nds_read_b64_tr_b16 v[222:223], v240 offset:9472\nv_max3_u32 v244, v244, v166, v167\nv_max3_u32 v243, v243, v168, v169\n"
    "v_max3_u32 v244, v244, v170, v171\nv_mfma_f32_32x32x16_bf16 v[96:111], v[224:227], v[152:155], v[96:111]\nds_read_b64_tr_b16 v[224:225], v240 offset:1024\n"
    "ds_read_b64_tr_b16 v[226:227], v240 offset:1280\nv_max3_u32 v243, v243, v172, v173\nv_max3_u32 v244, v244, v174, v175\nv_max_u32 v243, v243, v244\n"
    "v_cmp_eq_u32 vcc, -1, v243\nv_mfma_f32_32x32x16_bf16 v[96:111], v[228:231], v[156:159], v[96:111]\nds_read_b64_tr_b16 v[228:229], v240 offset:4096\n"
    "ds_read_b64_tr_b16 v[230:231], v240 offset:4352\ns_cbranch_vccnz .Lx201\n.Lx202:\nglobal_load_dword v112, v34, s[4:5] offset:0 sc1\n"
    "global_load_dword v113, v34, s[4:5] offset:256 sc1\ns_waitcnt lgkmcnt(6)\nv_mfma_f32_32x32x16_bf16 v[64:79], v[208:211], v[160:163], v[64:79]\ns_nop 0\n"
    "s_cbranch_execz .Lhop\nds_read_b64_tr_b16 v[208:209], v240 offset:7168\nds_read_b64_tr_b16 v[210:211], v240 offset:7424\n"
    "global_load_dword v114, v34, s[4:5] offset:512 sc1\nglobal_load_dword v115, v34, s[4:5] offset:768 sc1\n"
    "v_mfma_f32_32x32x16_bf16 v[64:79], v[212:215], v[164:167], v[64:79]\nds_read_b64_tr_b16 v[212:213], v240 offset:10240\nds_read_b64_tr_b16 v[214:215], v240 offset:10496\n"
    "global_load_dword v116, v34, s[4:5] offset:1024 sc1\nglobal_load_dword v117, v34, s[4:5] offset:1280 sc1\n"
    "v_mfma_f32_32x32x16_bf16 v[64:79], v[216:219], v[168:171], v[64:79]\nds_read_b64_tr_b16 v[216:217], v240 offset:2048\nds_read_b64_tr_b16 v[218:219], v240 offset:2304\n"
    "v_mad_u32_u24 v242, s50, 3, v36\nglobal_load_dword v118, v34, s[4:5] offset:1536 sc1\nglobal_load_dword v119, v34, s[4:5] offset:1792 sc1\ns_waitcnt lgkmcnt(6)\n"
    "v_mfma_f32_32x32x16_bf16 v[64:79], v[220:223], v[172:175], v[64:79]\nds_read_b64_tr_b16 v[220:221], v240 offset:5120\nds_read_b64_tr_b16 v[222:223], v240 offset:5376\n"
    "s_waitcnt vmcnt(24)\nv_max3_u32 v244, v176, v177, v178\nv_max3_u32 v243, v179, v180, v181\nv_mfma_f32_32x32x16_bf16 v[80:95], v[224:227], v[160:163], v[80:95]\n"
    "ds_read_b64_tr_b16 v[224:225], v240 offset:8192\nds_read_b64_tr_b16 v[226:227], v240 offset:8448\nv_max3_u32 v244, v244, v182, v183\nv_max3_u32 v243, v243, v184, v185\n"
    "v_max3_u32 v244, v244, v186, v187\nv_mfma_f32_32x32x16_bf16 v[80:95], v[228:231], v[164:167], v[80:95]\nds_read_b64_tr_b16 v[228:229], v240 offset:11264\n"
    "ds_read_b64_tr_b16 v[230:231], v240 offset:11520\nv_max3_u32 v243, v243, v188, v189\nv_max3_u32 v244, v244, v190, v191\nv_max_u32 v243, v243, v244\n"
    "v_cmp_eq_u32 vcc, -1, v243\ns_waitcnt lgkmcnt(6)\nv_mfma_f32_32x32x16_bf16 v[80:95], v[208:211], v[168:171], v[80:95]\nds_read_b64_tr_b16 v[208:209], v242 offset:0\n"
    "ds_read_b64_tr_b16 v[210:211], v242 offset:256\ns_cbranch_vccnz .Lx203\n.Lx204:\nv_mfma_f32_32x32x16_bf16 v[80:95], v[212:215], v[172:175], v[80:95]\n"
    "ds_read_b64_tr_b16 v[212:213], v242 offset:3072\nds_read_b64_tr_b16 v[214:215], v242 offset:3328\nv_mad_u32_u24 v240, s51, 3, v36\n"
    "v_mfma_f32_32x32x16_bf16 v[96:111], v[216:219], v[160:163], v[96:111]\nds_read_b64_tr_b16 v[216:217], v242 offset:6144\nds_read_b64_tr_b16 v[218:219], v242 offset:6400\n"
    "s_waitcnt vmcnt(8)\nv_max3_u32 v244, v192, v193, v194\nv_max3_u32 v243, v195, v196, v197\ns_waitcnt lgkmcnt(6)\n"
    "v_mfma_f32_32x32x16_bf16 v[96:111], v[220:223], v[164:167], v[96:111]\nds_read_b64_tr_b16 v[220:221], v242 offset:9216\nds_read_b64_tr_b16 v[222:223], v242 offset:9472\n"
    "v_max3_u32 v244, v244, v198, v199\nv_max3_u32 v243, v243, v200, v201\nv_max3_u32 v244, v244, v202, v203\n"
    "v_mfma_f32_32x32x16_bf16 v[96:111], v[224:227], v[168:171], v[96:111]\nds_read_b64_tr_b16 v[224:225], v240 offset:0\nds_read_b64_tr_b16 v[226:227], v240 offset:256\n"
    "v_max3_u32 v243, v243, v204, v205\nv_max3_u32 v244, v244, v206, v207\nv_max_u32 v243, v243, v244\nv_cmp_eq_u32 vcc, -1, v243\n"
    "v_mfma_f32_32x32x16_bf16 v[96:111], v[228:231], v[172:175], v[96:111]\nds_read_b64_tr_b16 v[228:229], v240 offset:3072\nds_read_b64_tr_b16 v[230:231], v240 offset:3328\n"
    "s_cbranch_vccnz .Lx205\n.Lx206:\ns_waitcnt lgkmcnt(6)\nv_mfma_f32_32x32x16_bf16 v[64:79], v[208:211], v[176:179], v[64:79]\n"
    "ds_read_b64_tr_b16 v[208:209], v240 offset:6144\nds_read_b64_tr_b16 v[210:211], v240 offset:6400\nv_mfma_f32_32x32x16_bf16 v[64:79], v[212:215], v[180:183], v[64:79]\n"
    "ds_read_b64_tr_b16 v[212:213], v240 offset:9216\nds_read_b64_tr_b16 v[214:215], v240 offset:9472\ns_waitcnt vmcnt(0)\nv_add_f32 v138, v112, v113\n"
    "v_add_f32 v139, v114, v115\nv_add_f32 v244, v116, v117\nv_mfma_f32_32x32x16_bf16 v[64:79], v[216:219], v[184:187], v[64:79]\n"
    "ds_read_b64_tr_b16 v[216:217], v242 offset:1024\nds_read_b64_tr_b16 v[218:219], v242 offset:1280\nv_add_f32 v243, v118, v119\nv_add_f32 v138, v138, v139\n"
    "v_add_f32 v244, v244, v243\nv_add_f32 v139, v138, v244\ns_waitcnt lgkmcnt(6)\nv_mfma_f32_32x32x16_bf16 v[64:79], v[220:223], v[188:191], v[64:79]\n"
    "ds_read_b64_tr_b16 v[220:221], v242 offset:4096\nds_read_b64_tr_b16 v[222:223], v242 offset:4352\nv_add_f32 v138, v138, v244\nv_cmp_u_f32 vcc, v138, v138\n"
    "v_mfma_f32_32x32x16_bf16 v[64:79], v[224:227], v[192:195], v[64:79]\nds_read_b64_tr_b16 v[224:225], v242 offset:7168\nds_read_b64_tr_b16 v[226:227], v242 offset:7424\n"
    "s_cbranch_vccnz .Lp191\n.Lp192:\nv_permlane32_swap_b32 v138, v139\nv_mul_f32 v138, s81, v138\nv_mul_f32 v112, v138, v138\n"
    "v_mfma_f32_32x32x16_bf16 v[64:79], v[228:231], v[196:199], v[64:79]\nds_read_b64_tr_b16 v[228:229], v242 offset:10240\nds_read_b64_tr_b16 v[230:231], v242 offset:10496\n"
    "s_memrealtime s[100:101]\ns_memtime s[78:79]\nv_cvt_pk_bf16_f32 v138, -v138, -v138\nv_fma_f32 v139, v139, s81, -v112\nv_cndmask_b32 v120, 0, v138, s[84:85]\n"
    "s_waitcnt lgkmcnt(6)\nv_mfma_f32_32x32x16_bf16 v[64:79], v[208:211], v[200:203], v[64:79]\nds_read_b64_tr_b16 v[208:209], v240 offset:1024\n"
    "ds_read_b64_tr_b16 v[210:211], v240 offset:1280\nv_max_f32 v139, 0, v139\nv_cvt_pk_bf16_f32 v139, 0, v139\nv_add_f32 v139, 0x3727c5ac, v139\n"
    "v_mfma_f32_32x32x16_bf16 v[64:79], v[212:215], v[204:207], v[64:79]\nds_read_b64_tr_b16 v[212:213], v240 offset:4096\nds_read_b64_tr_b16 v[214:215], v240 offset:4352\n"
    "v_mfma_f32_32x32x16_bf16 v[64:79], v[124:127], v[120:123], v[64:79]\nv_cvt_pk_bf16_f32 v139, 0, v139\nv_sqrt_f32 v139, v139\nv_mov_b64 v[232:233], 0\n"
    "v_mov_b64 v[234:235], 0\nv_cvt_pk_bf16_f32 v139, 0, v139\nv_rcp_f32 v136, v139\nv_mfma_f32_32x32x16_bf16 v[80:95], v[128:131], v[120:123], v[80:95]\n"
    "v_mov_b32 v137, v136\nv_mov_b64 v[236:237], 0\nv_mov_b64 v[238:239], 0\nv_mov_b64 v[244:245], 0\nv_mov_b64 v[246:247], 0\n"
    "v_mfma_f32_32x32x16_bf16 v[80:95], v[216:219], v[176:179], v[80:95]\nds_read_b64_tr_b16 v[216:217], v240 offset:7168\nds_read_b64_tr_b16 v[218:219], v240 offset:7424\n"
    "v_mul_f32 v64, v64, v136\nv_mul_f32 v65, v65, v136\nv_mul_f32 v66, v66, v136\ns_waitcnt lgkmcnt(6)\nv_mfma_f32_32x32x16_bf16 v[80:95], v[220:223], v[180:183], v[80:95]\n"
    "ds_read_b64_tr_b16 v[220:221], v240 offset:10240\nds_read_b64_tr_b16 v[222:223], v240 offset:10496\nv_mul_f32 v67, v67, v136\nv_cvt_pk_bf16_f32 v64, v64, v65\n"
    "v_cvt_pk_bf16_f32 v65, v66, v67\nv_mfma_f32_32x32x16_bf16 v[80:95], v[224:227], v[184:187], v[80:95]\nds_read_b64_tr_b16 v[224:225], v242 offset:2048\n"
    "ds_read_b64_tr_b16 v[226:227], v242 offset:2304\nds_write_b64 v37, v[64:65] offset:0\nv_mul_f32 v68, v68, v136\nv_mul_f32 v69, v69, v136\n"
    "v_mfma_f32_32x32x16_bf16 v[80:95], v[228:231], v[188:191], v[80:95]\ns_cbranch_execz .Lhop\nds_read_b64_tr_b16 v[228:229], v242 offset:5120\n"
    "ds_read_b64_tr_b16 v[230:231], v242 offset:5376\nv_mul_f32 v70, v70, v136\nv_mul_f32 v71, v71, v136\nv_cvt_pk_bf16_f32 v68, v68, v69\ns_waitcnt lgkmcnt(7)\n"
    "v_mfma_f32_32x32x16_bf16 v[80:95], v[208:211], v[192:195], v[80:95]\nds_read_b64_tr_b16 v[208:209], v242 offset:8192\nds_read_b64_tr_b16 v[210:211], v242 offset:8448\n"
    "v_cvt_pk_bf16_f32 v69, v70, v71\nds_write_b64 v37, v[68:69] offset:16\nv_mul_f32 v72, v72, v136\nv_mfma_f32_32x32x16_bf16 v[80:95], v[212:215], v[196:199], v[80:95]\n"
    "ds_read_b64_tr_b16 v[212:213], v242 offset:11264\nds_read_b64_tr_b16 v[214:215], v242 offset:11520\nv_mul_f32 v73, v73, v136\nv_mul_f32 v74, v74, v136\n"
    "v_mul_f32 v75, v75, v136\nv_mfma_f32_32x32x16_bf16 v[80:95], v[216:219], v[200:203], v[80:95]\nds_read_b64_tr_b16 v[216:217], v240 offset:2048\n"
    "ds_read_b64_tr_b16 v[218:219], v240 offset:2304\nv_cvt_pk_bf16_f32 v72, v72, v73\nv_cvt_pk_bf16_f32 v73, v74, v75\nds_write_b64 v37, v[72:73] offset:32\n"
    "s_waitcnt lgkmcnt(8)\nv_mfma_f32_32x32x16_bf16 v[80:95], v[220:223], v[204:207], v[80:95]\nds_read_b64_tr_b16 v[220:221], v240 offset:5120\n"
    "ds_read_b64_tr_b16 v[222:223], v240 offset:5376\nv_mul_f32 v76, v76, v136\nv_mul_f32 v77, v77, v136\nv_mul_f32 v78, v78, v136\n"
    "v_mfma_f32_32x32x16_bf16 v[96:111], v[224:227], v[176:179], v[96:111]\nds_read_b64_tr_b16 v[224:225], v240 offset:8192\nds_read_b64_tr_b16 v[226:227], v240 offset:8448\n"
    "v_mul_f32 v79, v79, v136\nv_cvt_pk_bf16_f32 v76, v76, v77\nv_cvt_pk_bf16_f32 v77, v78, v79\nv_mfma_f32_32x32x16_bf16 v[96:111], v[132:135], v[120:123], v[96:111]\n"
    "ds_write_b64 v37, v[76:77] offset:48\ns_cmp_lg_u32 s13, 1\ns_cbranch_scc1 .Lz207\nv_mov_b32 v140, s100\ns_mov_b64 exec, 1\nglobal_store_dword v141, v140, s[4:5] sc1\n"
    "s_mov_b64 exec, -1\n.Lz207:\nv_mfma_f32_32x32x16_bf16 v[96:111], v[228:231], v[180:183], v[96:111]\nds_read_b64_tr_b16 v[228:229], v240 offset:11264\n"
    "ds_read_b64_tr_b16 v[230:231], v240 offset:11520\nv_mul_f32 v80, v80, v136\nv_mul_f32 v81, v81, v136\nv_mul_f32 v82, v82, v136\nv_mul_f32 v83, v83, v136\n"
    "v_cvt_pk_bf16_f32 v80, v80, v81\ns_waitcnt lgkmcnt(8)\nv_mfma_f32_32x32x16_bf16 v[96:111], v[208:211], v[184:187], v[96:111]\nv_cvt_pk_bf16_f32 v81, v82, v83\n"
    "ds_write_b64 v37, v[80:81] offset:64\nv_mul_f32 v84, v84, v136\nv_mul_f32 v85, v85, v136\nv_mul_f32 v86, v86, v136\n"
    "v_mfma_f32_32x32x16_bf16 v[96:111], v[212:215], v[188:191], v[96:111]\nv_mul_f32 v87, v87, v136\nv_cvt_pk_bf16_f32 v84, v84, v85\nv_cvt_pk_bf16_f32 v85, v86, v87\n"
    "ds_write_b64 v37, v[84:85] offset:80\nv_mul_f32 v88, v88, v136\nv_mfma_f32_32x32x16_bf16 v[96:111], v[216:219], v[192:195], v[96:111]\nv_mul_f32 v89, v89, v136\n"
    "v_mul_f32 v90, v90, v136\nv_mul_f32 v91, v91, v136\nv_cvt_pk_bf16_f32 v88, v88, v89\nv_cvt_pk_bf16_f32 v89, v90, v91\ns_waitcnt lgkmcnt(2)\n"
    "v_mfma_f32_32x32x16_bf16 v[96:111], v[220:223], v[196:199], v[96:111]\nds_write_b64 v37, v[88:89] offset:96\nv_mul_f32 v92, v92, v136\nv_mul_f32 v93, v93, v136\n"
    "v_mul_f32 v94, v94, v136\nv_mul_f32 v95, v95, v136\nv_mfma_f32_32x32x16_bf16 v[96:111], v[224:227], v[200:203], v[96:111]\nv_cvt_pk_bf16_f32 v92, v92, v93\n"
    "v_cvt_pk_bf16_f32 v93, v94, v95\nds_write_b64 v37, v[92:93] offset:112\nv_mfma_f32_32x32x16_bf16 v[96:111], v[228:231], v[204:207], v[96:111]\nv_mov_b64 v[248:249], 0\n"
    "v_mov_b64 v[250:251], 0\ns_mov_b32 s44, 0x00ff00ff\ns_mov_b32 s45, 0xff00ff00\ns_mov_b32 s46, s44\ns_mov_b32 s47, s44\ns_nop 5\n"
    "v_pk_mul_f32 v[96:97], v[96:97], v[136:137]\nv_pk_mul_f32 v[98:99], v[98:99], v[136:137]\nv_cvt_pk_bf16_f32 v96, v96, v97\nv_cvt_pk_bf16_f32 v97, v98, v99\n"
    "ds_write_b64 v37, v[96:97] offset:128\nv_pk_mul_f32 v[100:101], v[100:101], v[136:137]\nv_pk_mul_f32 v[102:103], v[102:103], v[136:137]\n"
    "v_cvt_pk_bf16_f32 v100, v100, v101\nv_cvt_pk_bf16_f32 v101, v102, v103\nds_write_b64 v37, v[100:101] offset:144\nv_pk_mul_f32 v[104:105], v[104:105], v[136:137]\n"
    "v_pk_mul_f32 v[106:107], v[106:107], v[136:137]\nv_cvt_pk_bf16_f32 v104, v104, v105\nv_cvt_pk_bf16_f32 v105, v106, v107\nds_write_b64 v37, v[104:105] offset:160\n"
    "v_pk_mul_f32 v[108:109], v[108:109], v[136:137]\nv_pk_mul_f32 v[110:111], v[110:111], v[136:137]\nv_cvt_pk_bf16_f32 v108, v108, v109\nv_cvt_pk_bf16_f32 v109, v110, v111\n"
    "ds_write_b64 v37, v[108:109] offset:176\ns_branch .Latt12\n"
    ".Ll73x:\ns_add_u32 s59, s58, 0\nv_mov_b32 v70, s59\ns_mov_b64 exec, 1\ns_waitcnt vmcnt(8)\nds_max_i32 v56, v70\ns_mov_b64 exec, -1\ns_setprio 0\ns_waitcnt vmcnt(0)\n"
    "s_branch .Ll73y\n.La102:\ns_waitcnt vmcnt(0)\ns_branch .La104\n.Lw123:\ns_sub_u32 s80, s80, 1\ns_cbranch_scc1 .Lw123x\ns_mov_b64 exec, vcc\ns_add_u32 m0, s51, 0\n"
    "s_nop 2\nglobal_load_lds_dwordx4 v192, s[4:5] sc1\ns_mov_b64 exec, -1\ns_waitcnt vmcnt(0)\ns_branch .Lr122\n.Lw125:\ns_sub_u32 s80, s80, 1\ns_cbranch_scc1 .Lw125x\n"
    "s_mov_b64 exec, vcc\ns_add_u32 m0, s51, 4096\ns_nop 2\nglobal_load_lds_dwordx4 v193, s[4:5] sc1\ns_mov_b64 exec, -1\ns_waitcnt vmcnt(0)\ns_branch .Lr124\n.Lw127:\n"
    "s_sub_u32 s80, s80, 1\ns_cbranch_scc1 .Lw127x\ns_mov_b64 exec, vcc\ns_add_u32 m0, s51, 8192\ns_nop 2\nglobal_load_lds_dwordx4 v194, s[4:5] sc1\ns_add_u32 m0, s51, 12288\n"
    "s_nop 2\nglobal_load_lds_dwordx4 v195, s[4:5] sc1\ns_mov_b64 exec, -1\ns_waitcnt vmcnt(0)\ns_branch .Lr126\n.Lw129:\ns_sub_u32 s80, s80, 1\ns_cbranch_scc1 .Lw129x\n"
    "s_mov_b64 exec, vcc\ns_add_u32 m0, s51, 16384\ns_nop 2\nglobal_load_lds_dwordx4 v196, s[4:5] sc1\ns_add_u32 m0, s51, 20480\ns_nop 2\n"
    "global_load_lds_dwordx4 v197, s[4:5] sc1\ns_mov_b64 exec, -1\ns_waitcnt vmcnt(0)\ns_branch .Lr128\n.Lw131:\ns_sub_u32 s80, s80, 1\ns_cbranch_scc1 .Lw131x\n"
    "s_mov_b64 exec, vcc\ns_add_u32 m0, s51, 24576\ns_nop 2\ns_nop 0\ns_cbranch_execz .Lhop\nglobal_load_lds_dwordx4 v198, s[4:5] sc1\ns_add_u32 m0, s51, 28672\ns_nop 2\n"
    "global_load_lds_dwordx4 v199, s[4:5] sc1\ns_mov_b64 exec, -1\ns_waitcnt vmcnt(0)\ns_branch .Lr130\n.Lh140:\ns_cmp_lg_u32 s13, 0\ns_cbranch_scc1 .Lh143\n"
    "s_mov_b32 s80, 0x10000\nv_mov_b32 v241, 18\n.Lh142:\nglobal_load_dwordx4 v[192:195], v57, s[4:5] sc1\ns_waitcnt vmcnt(0)\nv_xor_b32 v192, s11, v192\n"
    "v_xor_b32 v193, s11, v193\nv_xor_b32 v194, s11, v194\nv_xor_b32 v195, s11, v195\nv_or3_b32 v242, v192, v193, v194\nv_or_b32 v242, v242, v195\nv_cmp_ne_u32 vcc, 0, v242\n"
    "s_cbranch_vccz .Lh144\ns_sub_u32 s80, s80, 1\ns_cbranch_scc1 .Lh144\ns_sleep 4\ns_branch .Lh142\n.Lh144:\ns_mov_b64 exec, 1\nds_write_b32 v56, v241 offset:4\n"
    "s_mov_b64 exec, -1\n.Lh143:\ns_mov_b32 s86, 0x100000\n.Ls145:\nds_read_b32 v242, v56 offset:4\ns_waitcnt lgkmcnt(0)\nv_cmp_gt_i32 vcc, 18, v242\ns_sub_u32 s86, s86, 1\n"
    "s_cbranch_scc1 .Ls146\ns_cbranch_vccz .Ls146\ns_sleep 1\ns_branch .Ls145\n.Ls146:\nglobal_load_dwordx4 v[64:67], v59, s[68:69] offset:-4096 sc1\n"
    "global_load_dwordx4 v[68:71], v59, s[68:69] offset:-3072 sc1\nglobal_load_dwordx4 v[72:75], v59, s[68:69] offset:-2048 sc1\n"
    "global_load_dwordx4 v[76:79], v59, s[68:69] offset:-1024 sc1\nglobal_load_dwordx4 v[80:83], v59, s[68:69] offset:0 sc1\n"
    "global_load_dwordx4 v[84:87], v59, s[68:69] offset:1024 sc1\nglobal_load_dwordx4 v[88:91], v59, s[68:69] offset:2048 sc1\n"
    "global_load_dwordx4 v[92:95], v59, s[68:69] offset:3072 sc1\nglobal_load_dwordx4 v[96:99], v59, s[70:71] offset:-4096 sc1\n"
    "global_load_dwordx4 v[100:103], v59, s[70:71] offset:-3072 sc1\nglobal_load_dwordx4 v[104:107], v59, s[70:71] offset:-2048 sc1\n"
    "global_load_dwordx4 v[108:111], v59, s[70:71] offset:-1024 sc1\nglobal_load_dwordx4 v[112:115], v59, s[70:71] offset:0 sc1\n"
    "global_load_dwordx4 v[116:119], v59, s[70:71] offset:1024 sc1\nglobal_load_dwordx4 v[120:123], v59, s[70:71] offset:2048 sc1\n"
    "global_load_dwordx4 v[124:127], v59, s[70:71] offset:3072 sc1\ns_branch .Lh141\n.Lw147:\ns_sub_u32 s80, s80, 1\ns_cbranch_scc1 .Lhwo113\n"
    "global_load_dword v240, v47, s[4:5] offset:0 sc1\nglobal_load_dword v241, v47, s[4:5] offset:128 sc1\nglobal_load_dword v242, v47, s[4:5] offset:512 sc1\n"
    "global_load_dword v243, v47, s[4:5] offset:640 sc1\nglobal_load_dword v244, v47, s[4:5] offset:1024 sc1\nglobal_load_dword v245, v47, s[4:5] offset:1152 sc1\n"
    "global_load_dword v246, v47, s[4:5] offset:1536 sc1\nglobal_load_dword v247, v47, s[4:5] offset:1664 sc1\ns_waitcnt vmcnt(0)\nv_max3_u32 v232, v240, v241, v242\n"
    "v_max3_u32 v233, v243, v244, v245\nv_max3_u32 v232, v232, v246, v247\nv_max_u32 v232, v232, v233\nv_cmp_eq_u32 vcc, -1, v232\ns_cbranch_vccnz .Lw147\ns_branch .Lhwo113\n"
    ".Llw158:\nv_max3_u32 v232, v208, v209, v210\nv_max3_u32 v232, v232, v211, v212\nv_max3_u32 v232, v232, v213, v214\nv_max3_u32 v232, v232, v215, v216\n"
    "v_max3_u32 v232, v232, v217, v218\nv_max3_u32 v232, v232, v219, v220\nv_max3_u32 v232, v232, v221, v222\nv_max_u32 v232, v232, v223\nv_cmp_eq_u32 vcc, -1, v232\n"
    "s_or_b64 vcc, vcc, s[50:51]\ns_cbranch_scc0 .Llx159\ns_sub_u32 s80, s80, 1\ns_cbranch_scc1 .Llx159\ns_mov_b64 exec, vcc\nglobal_load_dwordx4 v[240:243], v17, s[4:5] sc1\n"
    "global_load_dwordx4 v[208:211], v16, s[4:5] sc1\nglobal_load_dwordx4 v[212:215], v16, s[58:59] sc1\nglobal_load_dwordx4 v[216:219], v16, s[60:61] sc1\n"
    "global_load_dwordx4 v[220:223], v16, s[62:63] sc1\ns_mov_b64 exec, -1\ns_branch .Ln155\n.Lw178:\ns_mov_b32 s86, 0x100000\n.Ls180:\nds_read_b32 v250, v24 offset:0\n"
    "s_waitcnt lgkmcnt(0)\nv_cmp_gt_i32 vcc, v26, v250\ns_sub_u32 s86, s86, 1\ns_cbranch_scc1 .Ls181\ns_cbranch_vccz .Ls181\ns_sleep 1\ns_branch .Ls180\n.Ls181:\n"
    "s_branch .Lr179\n.Lq183:\nv_max3_u32 v52, v192, v193, v194\nv_max3_u32 v52, v52, v195, v196\nv_max3_u32 v52, v52, v197, v198\nv_max3_u32 v52, v52, v199, v200\n"
    "v_max3_u32 v52, v52, v201, v202\nv_max3_u32 v52, v52, v203, v204\nv_max3_u32 v52, v52, v205, v206\nv_max_u32 v52, v52, v207\nv_cmp_eq_u32 vcc, -1, v52\n"
    "s_cbranch_vccz .Lq183x\ns_sub_u32 s80, s80, 1\ns_cbranch_scc1 .Lq183x\ns_setprio 3\ns_waitcnt lgkmcnt(0)\nglobal_load_dword v192, v224, s[48:49] offset:0 sc1\n"
    "global_load_dword v193, v224, s[48:49] offset:256 sc1\nglobal_load_dword v194, v224, s[48:49] offset:512 sc1\nglobal_load_dword v195, v224, s[48:49] offset:768 sc1\n"
    "global_load_dword v196, v224, s[48:49] offset:1024 sc1\nglobal_load_dword v197, v224, s[48:49] offset:1280 sc1\nglobal_load_dword v198, v224, s[48:49] offset:1536 sc1\n"
    "global_load_dword v199, v224, s[48:49] offset:1792 sc1\nglobal_load_dword v200, v224, s[48:49] offset:2048 sc1\nglobal_load_dword v201, v224, s[48:49] offset:2304 sc1\n"
    "global_load_dword v202, v224, s[48:49] offset:2560 sc1\nglobal_load_dword v203, v224, s[48:49] offset:2816 sc1\nglobal_load_dword v204, v224, s[48:49] offset:3072 sc1\n"
    "global_load_dword v205, v224, s[48:49] offset:3328 sc1\nglobal_load_dword v206, v224, s[48:49] offset:3584 sc1\nglobal_load_dword v207, v224, s[48:49] offset:3840 sc1\n"
    "s_branch .Lq182\n.Ld189:\nv_max3_u32 v244, v144, v145, v146\nv_max3_u32 v243, v147, v148, v149\nv_max3_u32 v244, v244, v150, v151\nv_max3_u32 v243, v243, v152, v153\n"
    "v_max3_u32 v244, v244, v154, v155\nv_max3_u32 v243, v243, v156, v157\nv_max3_u32 v244, v244, v158, v159\nv_max_u32 v243, v243, v244\nv_cmp_eq_u32 vcc, -1, v243\n"
    "s_cbranch_vccz .Ld190\ns_sub_u32 s80, s80, 1\ns_cbranch_scc1 .Ld190\ns_sleep 2\nglobal_load_dword v144, v39, s[52:53] offset:-2048 sc1\n"
    "global_load_dword v145, v39, s[52:53] offset:-1920 sc1\nglobal_load_dword v146, v39, s[52:53] offset:-1792 sc1\nglobal_load_dword v147, v39, s[52:53] offset:-1664 sc1\n"
    "global_load_dword v148, v39, s[52:53] offset:-1024 sc1\nglobal_load_dword v149, v39, s[52:53] offset:-896 sc1\nglobal_load_dword v150, v39, s[52:53] offset:-768 sc1\n"
    "global_load_dword v151, v39, s[52:53] offset:-640 sc1\nglobal_load_dword v152, v39, s[52:53] offset:0 sc1\nglobal_load_dword v153, v39, s[52:53] offset:128 sc1\n"
    "global_load_dword v154, v39, s[52:53] offset:256 sc1\nglobal_load_dword v155, v39, s[52:53] offset:384 sc1\nglobal_load_dword v156, v39, s[52:53] offset:1024 sc1\n"
    "global_load_dword v157, v39, s[52:53] offset:1152 sc1\ns_cbranch_execz .Lhop\nglobal_load_dword v158, v39, s[52:53] offset:1280 sc1\n"
    "global_load_dword v159, v39, s[52:53] offset:1408 sc1\nds_read_b64_tr_b16 v[208:209], v242 offset:0\nds_read_b64_tr_b16 v[210:211], v242 offset:256\n"
    "ds_read_b64_tr_b16 v[212:213], v242 offset:3072\nds_read_b64_tr_b16 v[214:215], v242 offset:3328\nds_read_b64_tr_b16 v[216:217], v242 offset:6144\n"
    "ds_read_b64_tr_b16 v[218:219], v242 offset:6400\nds_read_b64_tr_b16 v[220:221], v242 offset:9216\nds_read_b64_tr_b16 v[222:223], v242 offset:9472\n"
    "ds_read_b64_tr_b16 v[224:225], v242 offset:1024\nds_read_b64_tr_b16 v[226:227], v242 offset:1280\nds_read_b64_tr_b16 v[228:229], v242 offset:4096\n"
    "ds_read_b64_tr_b16 v[230:231], v242 offset:4352\ns_waitcnt vmcnt(0) lgkmcnt(0)\ns_branch .Ld188\n.Lp191:\nv_max3_u32 v243, v112, v113, v114\n"
    "v_max3_u32 v243, v243, v115, v116\nv_max3_u32 v243, v243, v117, v118\nv_max_u32 v243, v243, v119\nv_cmp_eq_u32 vcc, -1, v243\ns_cbranch_vccz .Lp192\n"
    "s_sub_u32 s80, s80, 1\ns_cbranch_scc1 .Lp192\nglobal_load_dword v112, v34, s[4:5] offset:0 sc1\nglobal_load_dword v113, v34, s[4:5] offset:256 sc1\n"
    "global_load_dword v114, v34, s[4:5] offset:512 sc1\nglobal_load_dword v115, v34, s[4:5] offset:768 sc1\nglobal_load_dword v116, v34, s[4:5] offset:1024 sc1\n"
    "global_load_dword v117, v34, s[4:5] offset:1280 sc1\nglobal_load_dword v118, v34, s[4:5] offset:1536 sc1\nglobal_load_dword v119, v34, s[4:5] offset:1792 sc1\n"
    "s_waitcnt vmcnt(0)\nv_add_f32 v138, v112, v113\nv_add_f32 v139, v114, v115\nv_add_f32 v244, v116, v117\nv_add_f32 v243, v118, v119\nv_add_f32 v138, v138, v139\n"
    "v_add_f32 v244, v244, v243\nv_add_f32 v139, v138, v244\nv_add_f32 v138, v138, v244\nv_cmp_u_f32 vcc, v138, v138\ns_nop 0\ns_branch .Lp191\n.Lx193:\n"
    "s_sub_u32 s80, s80, 1\ns_cbranch_scc1 .Lx194\ns_sleep 2\nglobal_load_dword v160, v39, s[54:55] offset:-2048 sc1\nglobal_load_dword v161, v39, s[54:55] offset:-1920 sc1\n"
    "global_load_dword v162, v39, s[54:55] offset:-1792 sc1\nglobal_load_dword v163, v39, s[54:55] offset:-1664 sc1\nglobal_load_dword v164, v39, s[54:55] offset:-1024 sc1\n"
    "global_load_dword v165, v39, s[54:55] offset:-896 sc1\nglobal_load_dword v166, v39, s[54:55] offset:-768 sc1\nglobal_load_dword v167, v39, s[54:55] offset:-640 sc1\n"
    "global_load_dword v168, v39, s[54:55] offset:0 sc1\nglobal_load_dword v169, v39, s[54:55] offset:128 sc1\nglobal_load_dword v170, v39, s[54:55] offset:256 sc1\n"
    "global_load_dword v171, v39, s[54:55] offset:384 sc1\nglobal_load_dword v172, v39, s[54:55] offset:1024 sc1\nglobal_load_dword v173, v39, s[54:55] offset:1152 sc1\n"
    "global_load_dword v174, v39, s[54:55] offset:1280 sc1\nglobal_load_dword v175, v39, s[54:55] offset:1408 sc1\ns_waitcnt vmcnt(0)\nv_max3_u32 v244, v160, v161, v162\n"
    "v_max3_u32 v243, v163, v164, v165\nv_max3_u32 v244, v244, v166, v167\nv_max3_u32 v243, v243, v168, v169\nv_max3_u32 v244, v244, v170, v171\n"
    "v_max3_u32 v243, v243, v172, v173\nv_max3_u32 v244, v244, v174, v175\nv_max_u32 v243, v243, v244\nv_cmp_eq_u32 vcc, -1, v243\ns_cbranch_vccnz .Lx193\ns_branch .Lx194\n"
    ".Lx195:\ns_sub_u32 s80, s80, 1\ns_cbranch_scc1 .Lx196\ns_sleep 2\nglobal_load_dword v176, v39, s[56:57] offset:-2048 sc1\n"
    "global_load_dword v177, v39, s[56:57] offset:-1920 sc1\nglobal_load_dword v178, v39, s[56:57] offset:-1792 sc1\nglobal_load_dword v179, v39, s[56:57] offset:-1664 sc1\n"
    "global_load_dword v180, v39, s[56:57] offset:-1024 sc1\nglobal_load_dword v181, v39, s[56:57] offset:-896 sc1\nglobal_load_dword v182, v39, s[56:57] offset:-768 sc1\n"
    "global_load_dword v183, v39, s[56:57] offset:-640 sc1\nglobal_load_dword v184, v39, s[56:57] offset:0 sc1\nglobal_load_dword v185, v39, s[56:57] offset:128 sc1\n"
    "global_load_dword v186, v39, s[56:57] offset:256 sc1\nglobal_load_dword v187, v39, s[56:57] offset:384 sc1\nglobal_load_dword v188, v39, s[56:57] offset:1024 sc1\n"
    "global_load_dword v189, v39, s[56:57] offset:1152 sc1\nglobal_load_dword v190, v39, s[56:57] offset:1280 sc1\nglobal_load_dword v191, v39, s[56:57] offset:1408 sc1\n"
    "s_waitcnt vmcnt(0)\nv_max3_u32 v244, v176, v177, v178\nv_max3_u32 v243, v179, v180, v181\nv_max3_u32 v244, v244, v182, v183\nv_max3_u32 v243, v243, v184, v185\n"
    "v_max3_u32 v244, v244, v186, v187\nv_max3_u32 v243, v243, v188, v189\nv_max3_u32 v244, v244, v190, v191\nv_max_u32 v243, v243, v244\nv_cmp_eq_u32 vcc, -1, v243\n"
    "s_cbranch_vccnz .Lx195\ns_branch .Lx196\n.Lx197:\ns_sub_u32 s80, s80, 1\ns_cbranch_scc1 .Lx198\ns_sleep 2\nglobal_load_dword v192, v39, s[58:59] offset:-2048 sc1\n"
    "global_load_dword v193, v39, s[58:59] offset:-1920 sc1\nglobal_load_dword v194, v39, s[58:59] offset:-1792 sc1\nglobal_load_dword v195, v39, s[58:59] offset:-1664 sc1\n"
    "global_load_dword v196, v39, s[58:59] offset:-1024 sc1\nglobal_load_dword v197, v39, s[58:59] offset:-896 sc1\nglobal_load_dword v198, v39, s[58:59] offset:-768 sc1\n"
    "global_load_dword v199, v39, s[58:59] offset:-640 sc1\nglobal_load_dword v200, v39, s[58:59] offset:0 sc1\nglobal_load_dword v201, v39, s[58:59] offset:128 sc1\n"
    "global_load_dword v202, v39, s[58:59] offset:256 sc1\nglobal_load_dword v203, v39, s[58:59] offset:384 sc1\nglobal_load_dword v204, v39, s[58:59] offset:1024 sc1\n"
    "global_load_dword v205, v39, s[58:59] offset:1152 sc1\nglobal_load_dword v206, v39, s[58:59] offset:1280 sc1\nglobal_load_dword v207, v39, s[58:59] offset:1408 sc1\n"
    "s_waitcnt vmcnt(0)\nv_max3_u32 v244, v192, v193, v194\nv_max3_u32 v243, v195, v196, v197\nv_max3_u32 v244, v244, v198, v199\nv_max3_u32 v243, v243, v200, v201\n"
    "v_max3_u32 v244, v244, v202, v203\nv_max3_u32 v243, v243, v204, v205\nv_max3_u32 v244, v244, v206, v207\nv_max_u32 v243, v243, v244\nv_cmp_eq_u32 vcc, -1, v243\n"
    "s_cbranch_vccnz .Lx197\ns_branch .Lx198\n.Lx199:\ns_sub_u32 s80, s80, 1\ns_cbranch_scc1 .Lx200\ns_sleep 2\nglobal_load_dword v144, v39, s[60:61] offset:-2048 sc1\n"
    "global_load_dword v145, v39, s[60:61] offset:-1920 sc1\nglobal_load_dword v146, v39, s[60:61] offset:-1792 sc1\nglobal_load_dword v147, v39, s[60:61] offset:-1664 sc1\n"
    "global_load_dword v148, v39, s[60:61] offset:-1024 sc1\nglobal_load_dword v149, v39, s[60:61] offset:-896 sc1\nglobal_load_dword v150, v39, s[60:61] offset:-768 sc1\n"
    "global_load_dword v151, v39, s[60:61] offset:-640 sc1\nglobal_load_dword v152, v39, s[60:61] offset:0 sc1\nglobal_load_dword v153, v39, s[60:61] offset:128 sc1\ns_nop 0\n"
    "s_cbranch_execz .Lhop\nglobal_load_dword v154, v39, s[60:61] offset:256 sc1\nglobal_load_dword v155, v39, s[60:61] offset:384 sc1\n"
    "global_load_dword v156, v39, s[60:61] offset:1024 sc1\nglobal_load_dword v157, v39, s[60:61] offset:1152 sc1\nglobal_load_dword v158, v39, s[60:61] offset:1280 sc1\n"
    "global_load_dword v159, v39, s[60:61] offset:1408 sc1\ns_waitcnt vmcnt(0)\nv_max3_u32 v244, v144, v145, v146\nv_max3_u32 v243, v147, v148, v149\n"
    "v_max3_u32 v244, v244, v150, v151\nv_max3_u32 v243, v243, v152, v153\nv_max3_u32 v244, v244, v154, v155\nv_max3_u32 v243, v243, v156, v157\n"
    "v_max3_u32 v244, v244, v158, v159\nv_max_u32 v243, v243, v244\nv_cmp_eq_u32 vcc, -1, v243\ns_cbranch_vccnz .Lx199\ns_branch .Lx200\n.Lx201:\ns_sub_u32 s80, s80, 1\n"
    "s_cbranch_scc1 .Lx202\ns_sleep 2\nglobal_load_dword v160, v39, s[62:63] offset:-2048 sc1\nglobal_load_dword v161, v39, s[62:63] offset:-1920 sc1\n"
    "global_load_dword v162, v39, s[62:63] offset:-1792 sc1\nglobal_load_dword v163, v39, s[62:63] offset:-1664 sc1\nglobal_load_dword v164, v39, s[62:63] offset:-1024 sc1\n"
    "global_load_dword v165, v39, s[62:63] offset:-896 sc1\nglobal_load_dword v166, v39, s[62:63] offset:-768 sc1\nglobal_load_dword v167, v39, s[62:63] offset:-640 sc1\n"
    "global_load_dword v168, v39, s[62:63] offset:0 sc1\nglobal_load_dword v169, v39, s[62:63] offset:128 sc1\nglobal_load_dword v170, v39, s[62:63] offset:256 sc1\n"
    "global_load_dword v171, v39, s[62:63] offset:384 sc1\nglobal_load_dword v172, v39, s[62:63] offset:1024 sc1\nglobal_load_dword v173, v39, s[62:63] offset:1152 sc1\n"
    "global_load_dword v174, v39, s[62:63] offset:1280 sc1\nglobal_load_dword v175, v39, s[62:63] offset:1408 sc1\ns_waitcnt vmcnt(0)\nv_max3_u32 v244, v160, v161, v162\n"
    "v_max3_u32 v243, v163, v164, v165\nv_max3_u32 v244, v244, v166, v167\nv_max3_u32 v243, v243, v168, v169\nv_max3_u32 v244, v244, v170, v171\n"
    "v_max3_u32 v243, v243, v172, v173\nv_max3_u32 v244, v244, v174, v175\nv_max_u32 v243, v243, v244\nv_cmp_eq_u32 vcc, -1, v243\ns_cbranch_vccnz .Lx201\ns_branch .Lx202\n"
    ".Lx203:\ns_sub_u32 s80, s80, 1\ns_cbranch_scc1 .Lx204\ns_sleep 2\nglobal_load_dword v176, v39, s[64:65] offset:-2048 sc1\n"
    "global_load_dword v177, v39, s[64:65] offset:-1920 sc1\nglobal_load_dword v178, v39, s[64:65] offset:-1792 sc1\nglobal_load_dword v179, v39, s[64:65] offset:-1664 sc1\n"
    "global_load_dword v180, v39, s[64:65] offset:-1024 sc1\nglobal_load_dword v181, v39, s[64:65] offset:-896 sc1\nglobal_load_dword v182, v39, s[64:65] offset:-768 sc1\n"
    "global_load_dword v183, v39, s[64:65] offset:-640 sc1\nglobal_load_dword v184, v39, s[64:65] offset:0 sc1\nglobal_load_dword v185, v39, s[64:65] offset:128 sc1\n"
    "global_load_dword v186, v39, s[64:65] offset:256 sc1\nglobal_load_dword v187, v39, s[64:65] offset:384 sc1\nglobal_load_dword v188, v39, s[64:65] offset:1024 sc1\n"
    "global_load_dword v189, v39, s[64:65] offset:1152 sc1\nglobal_load_dword v190, v39, s[64:65] offset:1280 sc1\nglobal_load_dword v191, v39, s[64:65] offset:1408 sc1\n"
    "s_waitcnt vmcnt(0)\nv_max3_u32 v244, v176, v177, v178\nv_max3_u32 v243, v179, v180, v181\nv_max3_u32 v244, v244, v182, v183\nv_max3_u32 v243, v243, v184, v185\n"
    "v_max3_u32 v244, v244, v186, v187\nv_max3_u32 v243, v243, v188, v189\nv_max3_u32 v244, v244, v190, v191\nv_max_u32 v243, v243, v244\nv_cmp_eq_u32 vcc, -1, v243\n"
    "s_cbranch_vccnz .Lx203\ns_branch .Lx204\n.Lx205:\ns_sub_u32 s80, s80, 1\ns_cbranch_scc1 .Lx206\ns_sleep 2\nglobal_load_dword v192, v39, s[66:67] offset:-2048 sc1\n"
    "global_load_dword v193, v39, s[66:67] offset:-1920 sc1\nglobal_load_dword v194, v39, s[66:67] offset:-1792 sc1\nglobal_load_dword v195, v39, s[66:67] offset:-1664 sc1\n"
    "global_load_dword v196, v39, s[66:67] offset:-1024 sc1\nglobal_load_dword v197, v39, s[66:67] offset:-896 sc1\nglobal_load_dword v198, v39, s[66:67] offset:-768 sc1\n"
    "global_load_dword v199, v39, s[66:67] offset:-640 sc1\nglobal_load_dword v200, v39, s[66:67] offset:0 sc1\nglobal_load_dword v201, v39, s[66:67] offset:128 sc1\n"
    "global_load_dword v202, v39, s[66:67] offset:256 sc1\nglobal_load_dword v203, v39, s[66:67] offset:384 sc1\nglobal_load_dword v204, v39, s[66:67] offset:1024 sc1\n"
    "global_load_dword v205, v39, s[66:67] offset:1152 sc1\nglobal_load_dword v206, v39, s[66:67] offset:1280 sc1\nglobal_load_dword v207, v39, s[66:67] offset:1408 sc1\n"
    "s_waitcnt vmcnt(0)\nv_max3_u32 v244, v192, v193, v194\nv_max3_u32 v243, v195, v196, v197\nv_max3_u32 v244, v244, v198, v199\nv_max3_u32 v243, v243, v200, v201\n"
    "v_max3_u32 v244, v244, v202, v203\nv_max3_u32 v243, v243, v204, v205\nv_max3_u32 v244, v244, v206, v207\nv_max_u32 v243, v243, v244\nv_cmp_eq_u32 vcc, -1, v243\n"
    "s_cbranch_vccnz .Lx205\ns_branch .Lx206\n.fill 62, 4, 0xBF800000\ns_cbranch_execz .Lhop\n.fill 255, 4, 0xBF800000\ns_cbranch_execz .Lhop\n.Lk_all_end:\n"
    :: "{s[0:1]}"(__builtin_amdgcn_kernarg_segment_ptr()), "{s2}"(blockIdx.x), "{v0}"(threadIdx.x), "{s3}"((uint32_t)(uintptr_t)(__attribute__((address_space(3))) char*)smem)
    : "v255", "s101", "vcc", "scc", "memory");
}

extern "C" void kernel_launch(void* const* d_in, const int* in_sizes, int n_in,
                              void* d_out, int out_size, void* d_ws, size_t ws_size,
                              hipStream_t stream) {
  Args a;
  a.x = (const u16*)d_in[0]; a.w = (const u16*)d_in[1]; a.wo = (const u16*)d_in[2];
  a.A_re = (const u16*)d_in[4]; a.A_im = (const u16*)d_in[5]; a.B_re = (const u16*)d_in[6]; a.B_im = (const u16*)d_in[7];
  a.C_re = (const u16*)d_in[8]; a.C_im = (const u16*)d_in[9]; a.log_dt = (const u16*)d_in[10]; a.Dskip = (const u16*)d_in[11];
  a.out = (u16*)d_out; a.ws = (char*)d_ws;
  k_all<<<256, 512, 0, stream>>>(a);
}
```

```cpp
#include <hip/hip_runtime.h>
#include <stdint.h>

typedef unsigned short u16;
constexpr int rnd_l(int v) { int e = 0; while ((v >> e) >= 256) ++e; if (!e) return v; const int h = 1 << (e - 1), q = v >> e, r = v & ((1 << e) - 1); return (q + ((r > h || (r == h && (q & 1))) ? 1 : 0)) << e; }
constexpr int slot_l(int v) { v = rnd_l(v); int e = 0; while ((v >> e) >= 256) ++e; return e * 128 + (v >> e); }
struct Tabs { u16 img[1032]; u16 prd[1024]; };
constexpr Tabs mk_tabs() {
  Tabs t{};
  for (int i = 0; i < 1032; ++i) {
    const int f = 1023 - i, o = f >> 3, a = (o - 74) / 12, m = f - 8 * (74 + 12 * a);
    int v = -1;
    if (f >= 32 && f - 32 <= 525) v = f - 32;
    if (o >= 74 && o < 122 && m < 64) v = a >= 2 ? 16 * (65 + m) - 8 * (a - 2) : (m < 62 ? 8 * (66 + m) + 4 * a : (m == 62 ? 1024 : -1));
    t.img[i] = (u16)(2 * (v < 0 ? 700 : slot_l(v)));
  }
  for (int w = 0; w < 1024; ++w) {
    const int tile = (w >> 9) * 2 + ((w >> 2) & 1), lane = (w >> 3) & 63;
    const int r = (lane >> 4) * 4 + (w & 3), C = (tile & 1) * 16 + (lane & 15);
    const int lr = 8 * C + (r < 4 ? r : (r == 4 ? 258 : 256 * (r - 4))) + 4 * (tile >> 1);
    const bool ok = r <= 12 && lr <= 2048 && rnd_l(lr) == lr;
    t.prd[w] = (u16)(2 * (ok ? slot_l(lr) : 704 + (lane & 63)));
  }
  return t;
}
extern "C" { extern __device__ __attribute__((aligned(16), used)) const Tabs g_tabs = mk_tabs(); }

struct Args { const u16 *x, *w, *wo, *A_re, *A_im, *B_re, *B_im, *C_re, *C_im, *log_dt, *Dskip; u16* out; char* ws; };

__global__ __launch_bounds__(512, 2) __attribute__((aligned(4096))) void k_all(const Args ar) {
  __shared__ __attribute__((aligned(128))) char smem[163840];
  asm volatile(
    ".Lk_all_beg:\ns_load_dwordx16 s[64:79], s[0:1], 0\ns_load_dwordx8 s[80:87], s[0:1], 0x40\ns_load_dwordx2 s[4:5], s[0:1], 0x60\nv_readfirstlane_b32 s3, v0\n"
    "s_lshr_b32 s25, s2, 3\ns_lshr_b32 s3, s3, 6\ns_cmp_lg_u32 s3, 4\ns_cbranch_scc1 .Loth1\ns_lshr_b32 s51, s2, 5\ns_and_b32 s60, s25, 3\nv_bfe_u32 v64, v0, 2, 4\n"
    "s_lshl1_add_u32 s50, s60, s51\nv_lshrrev_b32 v65, 4, v0\nv_mul_u32_u24 v64, 0xc00, v64\ns_and_b32 s50, s50, 7\nv_and_b32 v66, 2, v65\ns_and_b32 s61, s2, 1\n"
    "v_xor_b32 v66, v66, v0\ns_lshl3_add_u32 s51, s61, s51\nv_and_b32 v66, 3, v66\ns_lshl_b32 s51, s51, 6\nv_lshl_add_u32 v64, v66, 4, v64\ns_mul_i32 m0, s50, 12288\n"
    "v_add_u32 v64, s51, v64\ns_lshl_b32 s59, m0, 4\ns_mov_b32 s58, 3\ns_waitcnt lgkmcnt(0)\ns_add_u32 s52, s66, s59\n.Le5:\ns_addc_u32 s53, s67, 0\n"
    "global_load_lds_dwordx4 v64, s[52:53] offset:0\nglobal_load_lds_dwordx4 v64, s[52:53] offset:1024\nglobal_load_lds_dwordx4 v64, s[52:53] offset:2048\n"
    "s_add_u32 m0, m0, 3072\ns_add_u32 s59, s59, 49152\ns_sub_u32 s58, s58, 1\ns_cbranch_scc1 .Le5x\ns_add_u32 s52, s66, s59\ns_branch .Le5\n.Le5x:\n"
    "s_bfe_u32 s61, s2, 0x20001\nv_and_b32 v65, 3, v65\ns_lshl2_add_u32 s61, s61, s60\nv_xor_b32 v65, v65, v0\ns_lshl_b32 s61, s61, 17\nv_and_b32 v65, 7, v65\n"
    "s_add_u32 s48, s64, s61\nv_bfe_u32 v66, v0, 3, 3\ns_addc_u32 s49, s65, 0\nv_lshlrev_b32 v66, 10, v66\ns_lshl_b32 s59, s50, 7\nv_lshl_add_u32 v65, v65, 4, v66\n"
    "s_add_u32 s54, s48, s59\nv_xor_b32 v66, 64, v65\ns_addc_u32 s55, s49, 0\nv_add_u32 v67, 14336, v65\ns_mov_b32 m0, 98304\nv_add_u32 v68, 21504, v66\n"
    "v_add_u32 v66, 7168, v66\n.Le6:\nglobal_load_lds_dwordx4 v65, s[54:55] offset:0\nglobal_load_lds_dwordx4 v66, s[54:55] offset:1024\n"
    "global_load_lds_dwordx4 v67, s[54:55] offset:2048\nglobal_load_lds_dwordx4 v68, s[54:55] offset:3072\ns_add_u32 s54, s54, 32768\ns_addc_u32 s55, s55, 0\n"
    "s_add_u32 m0, m0, 12288\ns_cmp_lt_u32 m0, 147456\ns_cbranch_scc1 .Le6\ns_barrier\ns_branch .Lcom2\n.Lhop:\ns_sub_u32 s89, s89, 1\ns_cbranch_scc1 .Lhpx\n"
    "s_add_u32 s90, s90, 1024\ns_addc_u32 s91, s91, 0\ns_setpc_b64 s[90:91]\n.Lhpx:\ns_mov_b64 exec, -1\ns_setpc_b64 s[94:95]\n"
    ".Loth1:\ns_and_b32 s24, s2, 7\ns_lshl_b32 s44, s3, 2\ns_lshl_b32 s18, s24, 5\ns_add_u32 s18, s18, s25\ns_getpc_b64 s[92:93]\n.Lk_pc:\n"
    "s_sub_u32 s92, s92, .Lk_pc-.Lk_all_beg+4\ns_subb_u32 s93, s93, 0\nv_mov_b32 v70, 16777216\ns_cmp_gt_u32 s3, 3\ns_cbranch_scc1 .Loth3\nv_and_b32 v65, 63, v0\n"
    "s_lshl3_add_u32 s47, s44, s25\nv_lshl_add_u32 v64, v65, 2, s3\ns_sub_u32 s47, s47, 1984\nv_cmp_gt_u32 vcc, 16, v65\nv_lshl_add_u32 v65, v65, 7, s47\n"
    "s_lshl_b32 s47, s18, 9\nv_cndmask_b32 v64, v65, v64, vcc\ns_lshl_b32 s46, s18, 2\nv_min_u32 v64, (.Lk_all_end-.Lk_all_beg)/128-8, v64\nv_lshlrev_b32 v64, 7, v64\n"
    "s_mov_b64 exec, 0x3ffff\nglobal_load_dword v255, v64, s[92:93] offset:1024\ns_mov_b64 exec, -1\nv_lshlrev_b32 v64, 1, v0\nv_and_b32 v64, 0x1fc, v64\nv_mov_b32 v65, s46\n"
    "v_or_b32 v64, s47, v64\ns_waitcnt lgkmcnt(0)\nglobal_load_dword v160, v64, s[70:71]\nglobal_load_dword v161, v64, s[72:73]\nglobal_load_dword v162, v64, s[78:79]\n"
    "global_load_dword v163, v64, s[80:81]\nglobal_load_dword v164, v64, s[74:75]\nglobal_load_dword v165, v64, s[76:77]\nglobal_load_dword v167, v65, s[82:83]\n"
    "global_load_dword v166, v70, s[4:5] sc1\ns_branch .Lcom2\n.Loth3:\ns_cmp_eq_u32 s3, 5\ns_cbranch_scc1 .Loth4\ns_and_b32 s47, s18, 15\ns_lshl1_add_u32 s47, s47, s3\n"
    "s_lshl_b32 s47, s47, 14\ns_and_b32 s48, s18, 0xf0\nv_and_b32 v69, 32, v0\ns_lshl2_add_u32 s47, s48, s47\nv_and_b32 v71, 31, v0\ns_sub_u32 s47, s47, 94208\n"
    "v_lshl_add_u32 v69, v69, 8, s47\nv_lshl_add_u32 v69, v71, 1, v69\ns_waitcnt lgkmcnt(0)\nglobal_load_ushort v168, v69, s[68:69] offset:-4096\n"
    "global_load_ushort v169, v69, s[68:69] offset:-3072\nglobal_load_ushort v170, v69, s[68:69] offset:-2048\nglobal_load_ushort v171, v69, s[68:69] offset:-1024\n"
    "global_load_ushort v172, v69, s[68:69] offset:0\nglobal_load_ushort v173, v69, s[68:69] offset:1024\nglobal_load_ushort v174, v69, s[68:69] offset:2048\n"
    "global_load_ushort v175, v69, s[68:69] offset:3072\nglobal_load_dword v166, v70, s[4:5] sc1\ns_branch .Lcom2\n.Loth4:\nv_lshlrev_b32 v68, 4, v0\n"
    "v_mov_b32 v80, 0x3f803f80\nv_mov_b32 v81, 0x3f803f80\nv_mov_b32 v82, 0x3f803f80\nv_mov_b32 v83, 0x3f803f80\nv_add_u32 v68, 142336, v68\nv_mov_b64 v[84:85], 0\n"
    "v_mov_b64 v[86:87], 0\nds_write_b128 v68, v[80:83]\ns_mov_b64 exec, 0xff\nds_write_b128 v68, v[84:87] offset:16256\ns_mov_b64 exec, -1\ns_waitcnt lgkmcnt(0)\n"
    ".Lcom2:\ns_and_b32 s24, s2, 7\ns_lshr_b32 s25, s2, 3\ns_and_b32 s13, s3, 3\ns_lshl_b32 s43, s24, 5\ns_add_u32 s18, s43, s25\ns_lshr_b32 s42, s24, 1\n"
    "s_lshl_b32 s41, s42, 2\ns_and_b32 s40, s25, 3\ns_add_u32 s19, s41, s40\ns_and_b32 s39, s24, 1\ns_lshl_b32 s38, s39, 3\ns_lshr_b32 s37, s25, 2\ns_add_u32 s20, s38, s37\n"
    "s_lshl_b32 s36, s24, 3\ns_and_b32 s35, s25, 7\ns_add_u32 s21, s36, s35\ns_lshr_b32 s22, s25, 3\ns_lshl_b32 s34, s40, 1\ns_add_u32 s33, s37, s34\ns_and_b32 s23, s33, 7\n"
    "s_lshl_b32 s44, s18, 2\ns_mov_b64 s[6:7], s[86:87]\ns_mov_b64 s[8:9], s[64:65]\ns_mov_b32 s12, 0\nv_bfe_u32 v251, v0, 4, 2\nv_lshlrev_b32 v250, 2, v251\n"
    "s_add_u32 s43, s19, s3\ns_cmp_lt_u32 s43, 1\ns_cselect_b32 s42, 8, 0\nv_and_b32 v249, 7, v0\nv_add_u32 v248, 1, v249\nv_max_u32 v247, s42, v248\n"
    "v_sub_u32 v2, v250, v247\nv_add_u32 v246, 9, v249\nv_sub_u32 v40, v246, v247\nv_add_u32 v3, 1, v2\nv_add_u32 v4, 2, v2\nv_add_u32 v5, 3, v2\n"
    "v_cmp_lt_u32 s[36:37], v2, v40\ns_cbranch_execz .Lhop\nv_cmp_lt_u32 s[38:39], v3, v40\nv_cmp_lt_u32 s[40:41], v4, v40\nv_cmp_lt_u32 s[42:43], v5, v40\n"
    "v_mov_b32 v40, 0xf149f2ca\nv_cndmask_b32 v2, v40, 0, s[36:37]\nv_cndmask_b32 v3, v40, 0, s[38:39]\nv_cndmask_b32 v4, v40, 0, s[40:41]\n"
    "v_cndmask_b32 v5, v40, 0, s[42:43]\nv_and_b32 v251, 15, v0\nv_max_u32 v250, 1, v251\nv_mul_u32_u24 v249, 0x1a0, v250\nv_bfe_u32 v248, v0, 4, 2\ns_mul_i32 s43, s3, 0xd00\n"
    "s_add_u32 s42, s43, 0x18040\nv_add_u32 v6, s42, v249\nv_lshl_add_u32 v6, v248, 4, v6\nv_and_b32 v247, 7, v0\nv_mul_u32_u24 v246, 0x1a0, v247\n"
    "s_add_u32 s41, s43, 0x18d00\nv_add_u32 v7, s41, v246\nv_lshl_add_u32 v7, v248, 4, v7\nv_bfe_u32 v245, v0, 2, 2\nv_lshl_add_u32 v244, v248, 2, v245\n"
    "v_max_u32 v243, 1, v244\nv_mul_u32_u24 v242, 0x1a0, v243\nv_and_b32 v241, 3, v0\ns_add_u32 s40, s43, 0x18080\nv_add_u32 v8, s40, v242\nv_lshl_add_u32 v8, v241, 3, v8\n"
    "v_bfe_u32 v240, v0, 4, 1\ns_lshl_b32 s39, s19, 7\ns_lshl_b32 s38, s3, 4\ns_add_u32 s37, s39, s38\nv_add_u32 v239, s37, v240\nv_lshl_add_u32 v239, v247, 1, v239\n"
    "v_lshrrev_b32 v238, 5, v239\nv_bfe_u32 v237, v0, 3, 1\nv_bfe_u32 v236, v0, 5, 1\nv_and_b32 v235, 31, v239\ns_lshl_b32 s36, s20, 11\nv_lshl_add_u32 v9, v238, 15, s36\n"
    "v_lshl_add_u32 v9, v237, 10, v9\nv_lshl_add_u32 v9, v236, 9, v9\nv_lshl_add_u32 v9, v235, 4, v9\nv_mov_b32 v56, 163712\ns_add_u32 s16, s84, s44\ns_addc_u32 s17, s85, 0\n"
    "s_cmp_eq_u32 s3, 4\ns_cbranch_scc1 .Llstb7\ns_barrier\ns_cmp_gt_u32 s3, 3\ns_cbranch_scc1 .Lhalo10\n"
    "v_bfe_u32 v251, v0, 5, 1\nv_bfe_u32 v250, v0, 2, 2\nv_bfe_u32 v249, v0, 4, 1\nv_xor_b32 v248, v249, v251\nv_and_b32 v247, 3, v0\nv_lshlrev_b32 v36, 9, v251\n"
    "v_lshl_add_u32 v36, v250, 6, v36\nv_lshl_add_u32 v36, v248, 5, v36\nv_lshl_add_u32 v36, v247, 3, v36\nv_and_b32 v246, 31, v0\nv_bfe_u32 v245, v0, 1, 3\n"
    "v_xor_b32 v244, v251, v245\ns_mul_i32 s43, s13, 0x3000\ns_add_u32 s42, s43, 0x18000\nv_lshl_add_u32 v27, v246, 7, s42\nv_lshl_add_u32 v27, v244, 4, v27\n"
    "v_mul_u32_u24 v243, 0xd0, v246\ns_mul_i32 s41, s13, 0x1a00\ns_add_u32 s40, s41, 0x18d00\nv_add_u32 v37, s40, v243\nv_lshl_add_u32 v37, v251, 3, v37\n"
    "s_mov_b32 s39, 0x27c00\nv_lshl_add_u32 v38, v246, 2, s39\ns_mov_b32 s38, 0x27fe0\nv_lshl_add_u32 v242, v247, 2, s38\nv_and_b32 v241, 4, v0\nv_lshlrev_b32 v240, 4, v241\n"
    "v_sub_u32 v28, v242, v240\nv_mov_b32 v239, 16\nv_cmp_gt_u32 vcc, 1, v241\ns_nop 1\nv_cndmask_b32 v29, 1, v239, vcc\ns_lshl_b32 s37, s13, 2\ns_add_u32 s36, s37, 0x27fe0\n"
    "v_mov_b32 v238, s36\nv_and_b32 v237, 63, v0\ns_mov_b32 s35, 0x27e00\nv_lshl_add_u32 v236, v237, 2, s35\nv_cmp_gt_u32 vcc, 1, v237\ns_nop 1\n"
    "v_cndmask_b32 v30, v236, v238, vcc\ns_and_b32 s34, s23, 7\ns_mul_i32 s33, s34, 0x3000\nv_lshl_add_u32 v176, v251, 9, s33\nv_lshl_add_u32 v176, v250, 6, v176\n"
    "v_lshl_add_u32 v176, v248, 5, v176\nv_lshl_add_u32 v176, v247, 3, v176\ns_add_u32 s32, s23, 1\ns_and_b32 s31, s32, 7\ns_mul_i32 s30, s31, 0x3000\n"
    "v_lshl_add_u32 v177, v251, 9, s30\nv_lshl_add_u32 v177, v250, 6, v177\nv_lshl_add_u32 v177, v248, 5, v177\nv_lshl_add_u32 v177, v247, 3, v177\nv_bfe_u32 v251, v0, 5, 1\n"
    "v_bfe_u32 v250, v0, 2, 2\nv_bfe_u32 v249, v0, 4, 1\nv_xor_b32 v248, v249, v251\nv_and_b32 v247, 3, v0\ns_add_u32 s43, s23, 2\ns_and_b32 s42, s43, 7\n"
    "s_mul_i32 s41, s42, 0x3000\nv_lshl_add_u32 v178, v251, 9, s41\nv_lshl_add_u32 v178, v250, 6, v178\nv_lshl_add_u32 v178, v248, 5, v178\n"
    "v_lshl_add_u32 v178, v247, 3, v178\ns_add_u32 s40, s23, 3\ns_and_b32 s39, s40, 7\ns_mul_i32 s38, s39, 0x3000\nv_lshl_add_u32 v179, v251, 9, s38\n"
    "v_lshl_add_u32 v179, v250, 6, v179\nv_lshl_add_u32 v179, v248, 5, v179\nv_lshl_add_u32 v179, v247, 3, v179\ns_add_u32 s37, s23, 4\ns_and_b32 s36, s37, 7\n"
    "s_mul_i32 s35, s36, 0x3000\nv_lshl_add_u32 v180, v251, 9, s35\nv_lshl_add_u32 v180, v250, 6, v180\nv_lshl_add_u32 v180, v248, 5, v180\n"
    "v_lshl_add_u32 v180, v247, 3, v180\ns_add_u32 s34, s23, 5\ns_and_b32 s33, s34, 7\ns_mul_i32 s32, s33, 0x3000\nv_lshl_add_u32 v181, v251, 9, s32\n"
    "v_lshl_add_u32 v181, v250, 6, v181\nv_lshl_add_u32 v181, v248, 5, v181\nv_lshl_add_u32 v181, v247, 3, v181\nv_bfe_u32 v251, v0, 5, 1\nv_bfe_u32 v250, v0, 2, 2\n"
    "v_bfe_u32 v249, v0, 4, 1\nv_xor_b32 v248, v249, v251\nv_and_b32 v247, 3, v0\ns_add_u32 s43, s23, 6\ns_and_b32 s42, s43, 7\ns_mul_i32 s41, s42, 0x3000\n"
    "v_lshl_add_u32 v182, v251, 9, s41\nv_lshl_add_u32 v182, v250, 6, v182\nv_lshl_add_u32 v182, v248, 5, v182\nv_lshl_add_u32 v182, v247, 3, v182\ns_add_u32 s40, s23, 7\n"
    "s_and_b32 s39, s40, 7\ns_mul_i32 s38, s39, 0x3000\nv_lshl_add_u32 v183, v251, 9, s38\nv_lshl_add_u32 v183, v250, 6, v183\nv_lshl_add_u32 v183, v248, 5, v183\ns_nop 0\n"
    "s_cbranch_execz .Lhop\nv_lshl_add_u32 v183, v247, 3, v183\ns_mul_i32 s37, s13, 0xc00\ns_add_u32 s36, s37, 0x1f600\nv_lshl_add_u32 v33, v0, 4, s36\n"
    "s_lshl_b32 s35, s13, 2\ns_add_u32 s34, s35, 0x27d80\nv_mov_b32 v34, s34\nv_bfe_u32 v246, v0, 2, 4\ns_mov_b32 s33, 0x1006000\nv_lshl_add_u32 v35, v246, 7, s33\n"
    "v_lshl_add_u32 v35, v247, 4, v35\ns_lshr_b32 s32, s21, 2\ns_lshl_b32 s31, s32, 7\ns_add_u32 s30, s31, 0x1004000\nv_lshl_add_u32 v39, v247, 4, s30\n"
    "v_and_b32 v251, 15, v0\nv_mov_b32 v250, 0x102\nv_min_u32 v249, 12, v251\ns_mov_b32 s43, 0xfffffc00\nv_lshl_add_u32 v248, v249, 8, s43\nv_cmp_gt_u32 vcc, 5, v251\n"
    "s_nop 1\nv_cndmask_b32 v247, v248, v250, vcc\nv_cmp_gt_u32 vcc, 4, v251\ns_nop 1\nv_cndmask_b32 v25, v247, v251, vcc\nv_lshlrev_b32 v26, 3, v251\nv_cvt_f32_u32 v25, v25\n"
    "v_cvt_f32_u32 v26, v26\ns_mov_b32 s10, 4\ns_mov_b32 s27, 4\ns_add_u32 s94, s92, .Lgm14-.Lk_all_beg+4\ns_addc_u32 s95, s93, 0\nv_mov_b64 v[40:41], 0\n"
    "v_mov_b64 v[42:43], 0\nv_mov_b64 v[44:45], 0\nv_mov_b64 v[46:47], 0\nv_mov_b64 v[48:49], 0\nv_mov_b64 v[50:51], 0\nv_mov_b64 v[52:53], 0\nv_mov_b64 v[54:55], 0\n"
    "v_mov_b64 v[64:65], 0\nv_mov_b64 v[66:67], 0\nv_mov_b64 v[68:69], 0\nv_mov_b64 v[70:71], 0\nv_mov_b64 v[72:73], 0\nv_mov_b64 v[74:75], 0\nv_mov_b64 v[76:77], 0\n"
    "v_mov_b64 v[78:79], 0\nv_mov_b64 v[80:81], 0\nv_mov_b64 v[82:83], 0\nv_mov_b64 v[84:85], 0\nv_mov_b64 v[86:87], 0\nv_mov_b64 v[88:89], 0\nv_mov_b64 v[90:91], 0\n"
    "v_mov_b64 v[92:93], 0\nv_mov_b64 v[94:95], 0\nv_mov_b64 v[96:97], 0\nv_mov_b64 v[98:99], 0\nv_mov_b64 v[100:101], 0\nv_mov_b64 v[102:103], 0\nv_mov_b64 v[104:105], 0\n"
    "v_mov_b64 v[106:107], 0\nv_mov_b64 v[108:109], 0\nv_mov_b64 v[110:111], 0\nv_mov_b64 v[112:113], 0\nv_mov_b64 v[114:115], 0\nv_mov_b64 v[116:117], 0\n"
    "v_mov_b64 v[118:119], 0\nv_mov_b64 v[120:121], 0\nv_mov_b64 v[122:123], 0\nv_mov_b64 v[124:125], 0\nv_mov_b64 v[126:127], 0\nv_mov_b64 v[128:129], 0\n"
    "v_mov_b64 v[130:131], 0\nv_mov_b64 v[132:133], 0\nv_mov_b64 v[134:135], 0\ns_waitcnt vmcnt(1)\n"
    ".Lkgp9:\ns_bitcmp1_b32 s3, 1\ns_cselect_b32 s44, 0, 16\nv_lshlrev_b32 v240, s44, v167\nv_and_b32 v224, 1, v0\nv_and_b32 v240, 0xffff0000, v240\n"
    "v_lshlrev_b32 v224, 4, v224\ns_mov_b32 s45, 0x3fb8aa3b\nv_mul_f32 v241, s45, v240\nv_xor_b32 v224, 16, v224\nv_fma_f32 v242, v240, s45, -v241\nv_rndne_f32 v243, v241\n"
    "v_fmamk_f32 v242, v240, 0x32a5705f, v242\nv_sub_f32 v241, v241, v243\nv_add_f32 v241, v241, v242\nv_exp_f32 v241, v241\nv_cvt_i32_f32 v243, v243\n"
    "v_lshlrev_b32 v160, v224, v160\nv_lshlrev_b32 v161, v224, v161\nv_lshlrev_b32 v162, v224, v162\nv_lshlrev_b32 v163, v224, v163\nv_lshlrev_b32 v164, v224, v164\n"
    "v_lshlrev_b32 v165, v224, v165\nv_ldexp_f32 v241, v241, v243\nv_and_b32 v160, 0xffff0000, v160\nv_and_b32 v161, 0xffff0000, v161\nv_and_b32 v162, 0xffff0000, v162\n"
    "v_and_b32 v163, 0xffff0000, v163\nv_and_b32 v164, 0xffff0000, v164\nv_and_b32 v165, 0xffff0000, v165\nv_cvt_pk_bf16_f32 v241, 0, v241\nv_mul_f32 v164, v241, v164\n"
    "v_mul_f32 v165, v241, v165\nv_mul_f32 v16, v241, v160\nv_mul_f32 v17, v241, v161\nv_mul_f32 v242, v162, v164\nv_mul_f32 v243, v163, v165\nv_mul_f32 v244, v162, v165\n"
    "v_mul_f32 v245, v163, v164\nv_sub_f32 v242, v242, v243\nv_add_f32 v243, v244, v245\nv_mul_f32 v16, s45, v16\nv_mul_f32 v17, 0.15915494, v17\nv_mul_f32 v244, v242, v242\n"
    "v_mul_f32 v245, v243, v243\nv_add_f32 v244, v244, v245\nv_log_f32 v244, v244\nv_max_f32 v246, abs(v242), abs(v243)\nv_min_f32 v247, abs(v242), abs(v243)\n"
    "v_mul_f32 v18, 0.5, v244\nv_frexp_mant_f32 v248, v246\nv_max_f32 v244, 0xc2700000, v18\nv_rcp_f32 v248, v248\nv_frexp_exp_i32_f32 v246, v246\n"
    "v_max_f32_dpp v244, v244, v244 row_ror:1 row_mask:0xf bank_mask:0xf\nv_frexp_exp_i32_f32 v249, v247\nv_frexp_mant_f32 v247, v247\n"
    "v_max_f32_dpp v244, v244, v244 row_ror:2 row_mask:0xf bank_mask:0xf\nv_mul_f32 v248, v247, v248\nv_sub_u32 v246, v249, v246\n"
    "v_max_f32_dpp v244, v244, v244 row_ror:4 row_mask:0xf bank_mask:0xf\nv_ldexp_f32 v246, v248, v246\nv_mov_b32 v248, 0xbc7a590c\n"
    "v_max_f32_dpp v244, v244, v244 row_ror:8 row_mask:0xf bank_mask:0xf\nv_mul_f32 v247, v246, v246\nv_mov_b32 v245, v244\nv_fmac_f32 v248, 0x3b2d2a58, v247\n"
    "v_fmaak_f32 v248, v247, v248, 0x3d29fb3f\nv_permlane16_swap_b32 v244, v245\nv_fmaak_f32 v248, v247, v248, 0xbd97d4d7\nv_max_f32 v244, v244, v245\n"
    "v_fmaak_f32 v248, v247, v248, 0x3dd931b2\nv_mov_b32 v245, v244\nv_fmaak_f32 v248, v247, v248, 0xbe1160e6\nv_fmaak_f32 v248, v247, v248, 0x3e4cb8bf\n"
    "v_permlane32_swap_b32 v244, v245\nv_fmaak_f32 v248, v247, v248, 0xbeaaaa62\nv_max_f32 v244, v244, v245\nv_mul_f32 v247, v247, v248\nv_ceil_f32 v244, v244\n"
    "v_fmac_f32 v246, v246, v247\nv_add_f32 v244, 0xc1600000, v244\nv_cmp_gt_f32 vcc, abs(v243), abs(v242)\nv_sub_f32 v247, 0x3fc90fdb, v246\nv_sub_f32 v18, v18, v244\n"
    "v_cndmask_b32 v246, v246, v247, vcc\nv_cmp_gt_f32 vcc, 0, v242\nv_sub_f32 v247, 0x40490fdb, v246\nv_exp_f32 v24, v244\nv_cndmask_b32 v246, v246, v247, vcc\n"
    "s_brev_b32 s44, -2\nv_max_f32 v246, 0, v246\nv_mul_f32 v244, 0x43000000, v16\nv_bfi_b32 v246, s44, v246, v243\nv_mul_f32 v245, 0x43000000, v17\n"
    "v_mul_f32 v19, 0xbe22f983, v246\nv_mul_f32 v247, 4.0, v16\nv_mul_f32 v248, 4.0, v17\nv_fract_f32 v245, v245\nv_fract_f32 v248, v248\nv_exp_f32 v244, v244\n"
    "v_exp_f32 v247, v247\nv_cos_f32 v20, v245\nv_sin_f32 v21, v245\nv_cos_f32 v22, v248\nv_sin_f32 v23, v248\nv_mul_f32 v20, v244, v20\nv_mul_f32 v21, v244, v21\n"
    "v_mul_f32 v22, v247, v22\nv_mul_f32 v23, v247, v23\n"
    ".Lkgl13:\nv_mov_b32_dpp v240, v18 row_newbcast:0 row_mask:0xf bank_mask:0xf\nv_mov_b32_dpp v241, v19 row_newbcast:0 row_mask:0xf bank_mask:0xf\n"
    "v_mul_f32_dpp v242, v16, v26 row_newbcast:0 row_mask:0xf bank_mask:0xf\nv_mul_f32_dpp v243, v17, v26 row_newbcast:0 row_mask:0xf bank_mask:0xf\n"
    "v_fmac_f32_dpp v240, v16, v25 row_newbcast:0 row_mask:0xf bank_mask:0xf\nv_fmac_f32_dpp v241, -v17, v25 row_newbcast:0 row_mask:0xf bank_mask:0xf\n"
    "v_fract_f32 v243, v243\nv_exp_f32 v242, v242\nv_fract_f32 v241, v241\nv_exp_f32 v240, v240\nv_cos_f32 v244, v243\ns_cbranch_execz .Lhop\nv_sin_f32 v245, v243\n"
    "v_cos_f32 v246, v241\nv_sin_f32 v247, v241\nv_mul_f32 v244, v242, v244\nv_mul_f32 v245, v242, v245\nv_mul_f32 v246, v240, v246\nv_mul_f32 v247, v240, v247\n"
    "v_mul_f32_dpp v248, v20, v244 row_newbcast:0 row_mask:0xf bank_mask:0xf\nv_mul_f32_dpp v249, v20, v245 row_newbcast:0 row_mask:0xf bank_mask:0xf\n"
    "v_mul_f32_dpp v250, v22, v246 row_newbcast:0 row_mask:0xf bank_mask:0xf\nv_mul_f32_dpp v251, v22, v247 row_newbcast:0 row_mask:0xf bank_mask:0xf\n"
    "v_fmac_f32_dpp v248, -v21, v245 row_newbcast:0 row_mask:0xf bank_mask:0xf\nv_fmac_f32_dpp v249, v21, v244 row_newbcast:0 row_mask:0xf bank_mask:0xf\n"
    "v_fmac_f32_dpp v250, v23, v247 row_newbcast:0 row_mask:0xf bank_mask:0xf\nv_fmac_f32_dpp v251, -v23, v246 row_newbcast:0 row_mask:0xf bank_mask:0xf\n"
    "v_cvt_pk_f16_f32 v232, v244, v245\nv_cvt_pk_f16_f32 v224, v246, v247\nv_cvt_pk_f16_f32 v236, v248, v249\nv_cvt_pk_f16_f32 v228, v250, v251\n"
    "v_mov_b32_dpp v240, v18 row_newbcast:1 row_mask:0xf bank_mask:0xf\nv_mov_b32_dpp v241, v19 row_newbcast:1 row_mask:0xf bank_mask:0xf\n"
    "v_mul_f32_dpp v242, v16, v26 row_newbcast:1 row_mask:0xf bank_mask:0xf\nv_mul_f32_dpp v243, v17, v26 row_newbcast:1 row_mask:0xf bank_mask:0xf\n"
    "v_fmac_f32_dpp v240, v16, v25 row_newbcast:1 row_mask:0xf bank_mask:0xf\nv_fmac_f32_dpp v241, -v17, v25 row_newbcast:1 row_mask:0xf bank_mask:0xf\n"
    "v_fract_f32 v243, v243\nv_exp_f32 v242, v242\nv_fract_f32 v241, v241\nv_exp_f32 v240, v240\nv_cos_f32 v244, v243\nv_sin_f32 v245, v243\nv_cos_f32 v246, v241\n"
    "v_sin_f32 v247, v241\nv_mul_f32 v244, v242, v244\nv_mul_f32 v245, v242, v245\nv_mul_f32 v246, v240, v246\nv_mul_f32 v247, v240, v247\n"
    "v_mul_f32_dpp v248, v20, v244 row_newbcast:1 row_mask:0xf bank_mask:0xf\nv_mul_f32_dpp v249, v20, v245 row_newbcast:1 row_mask:0xf bank_mask:0xf\n"
    "v_mul_f32_dpp v250, v22, v246 row_newbcast:1 row_mask:0xf bank_mask:0xf\nv_mul_f32_dpp v251, v22, v247 row_newbcast:1 row_mask:0xf bank_mask:0xf\n"
    "v_fmac_f32_dpp v248, -v21, v245 row_newbcast:1 row_mask:0xf bank_mask:0xf\nv_fmac_f32_dpp v249, v21, v244 row_newbcast:1 row_mask:0xf bank_mask:0xf\n"
    "v_fmac_f32_dpp v250, v23, v247 row_newbcast:1 row_mask:0xf bank_mask:0xf\nv_fmac_f32_dpp v251, -v23, v246 row_newbcast:1 row_mask:0xf bank_mask:0xf\n"
    "v_cvt_pk_f16_f32 v233, v244, v245\nv_cvt_pk_f16_f32 v225, v246, v247\nv_cvt_pk_f16_f32 v237, v248, v249\nv_cvt_pk_f16_f32 v229, v250, v251\n"
    "v_mov_b32_dpp v240, v18 row_newbcast:2 row_mask:0xf bank_mask:0xf\nv_mov_b32_dpp v241, v19 row_newbcast:2 row_mask:0xf bank_mask:0xf\n"
    "v_mul_f32_dpp v242, v16, v26 row_newbcast:2 row_mask:0xf bank_mask:0xf\nv_mul_f32_dpp v243, v17, v26 row_newbcast:2 row_mask:0xf bank_mask:0xf\n"
    "v_fmac_f32_dpp v240, v16, v25 row_newbcast:2 row_mask:0xf bank_mask:0xf\nv_fmac_f32_dpp v241, -v17, v25 row_newbcast:2 row_mask:0xf bank_mask:0xf\n"
    "v_fract_f32 v243, v243\nv_exp_f32 v242, v242\nv_fract_f32 v241, v241\nv_exp_f32 v240, v240\nv_cos_f32 v244, v243\nv_sin_f32 v245, v243\nv_cos_f32 v246, v241\n"
    "v_sin_f32 v247, v241\nv_mul_f32 v244, v242, v244\nv_mul_f32 v245, v242, v245\nv_mul_f32 v246, v240, v246\nv_mul_f32 v247, v240, v247\n"
    "v_mul_f32_dpp v248, v20, v244 row_newbcast:2 row_mask:0xf bank_mask:0xf\nv_mul_f32_dpp v249, v20, v245 row_newbcast:2 row_mask:0xf bank_mask:0xf\n"
    "v_mul_f32_dpp v250, v22, v246 row_newbcast:2 row_mask:0xf bank_mask:0xf\nv_mul_f32_dpp v251, v22, v247 row_newbcast:2 row_mask:0xf bank_mask:0xf\n"
    "v_fmac_f32_dpp v248, -v21, v245 row_newbcast:2 row_mask:0xf bank_mask:0xf\nv_fmac_f32_dpp v249, v21, v244 row_newbcast:2 row_mask:0xf bank_mask:0xf\n"
    "v_fmac_f32_dpp v250, v23, v247 row_newbcast:2 row_mask:0xf bank_mask:0xf\nv_fmac_f32_dpp v251, -v23, v246 row_newbcast:2 row_mask:0xf bank_mask:0xf\n"
    "v_cvt_pk_f16_f32 v234, v244, v245\nv_cvt_pk_f16_f32 v226, v246, v247\nv_cvt_pk_f16_f32 v238, v248, v249\nv_cvt_pk_f16_f32 v230, v250, v251\nds_read_b32 v57, v56\n"
    "v_mov_b32_dpp v240, v18 row_newbcast:3 row_mask:0xf bank_mask:0xf\nv_mov_b32_dpp v241, v19 row_newbcast:3 row_mask:0xf bank_mask:0xf\n"
    "v_mul_f32_dpp v242, v16, v26 row_newbcast:3 row_mask:0xf bank_mask:0xf\nv_mul_f32_dpp v243, v17, v26 row_newbcast:3 row_mask:0xf bank_mask:0xf\n"
    "v_fmac_f32_dpp v240, v16, v25 row_newbcast:3 row_mask:0xf bank_mask:0xf\nv_fmac_f32_dpp v241, -v17, v25 row_newbcast:3 row_mask:0xf bank_mask:0xf\n"
    "v_fract_f32 v243, v243\nv_exp_f32 v242, v242\nv_fract_f32 v241, v241\nv_exp_f32 v240, v240\nv_cos_f32 v244, v243\nv_sin_f32 v245, v243\nv_cos_f32 v246, v241\n"
    "v_sin_f32 v247, v241\nv_mul_f32 v244, v242, v244\nv_mul_f32 v245, v242, v245\nv_mul_f32 v246, v240, v246\nv_mul_f32 v247, v240, v247\n"
    "v_mul_f32_dpp v248, v20, v244 row_newbcast:3 row_mask:0xf bank_mask:0xf\nv_mul_f32_dpp v249, v20, v245 row_newbcast:3 row_mask:0xf bank_mask:0xf\n"
    "v_mul_f32_dpp v250, v22, v246 row_newbcast:3 row_mask:0xf bank_mask:0xf\nv_mul_f32_dpp v251, v22, v247 row_newbcast:3 row_mask:0xf bank_mask:0xf\n"
    "v_fmac_f32_dpp v248, -v21, v245 row_newbcast:3 row_mask:0xf bank_mask:0xf\nv_fmac_f32_dpp v249, v21, v244 row_newbcast:3 row_mask:0xf bank_mask:0xf\n"
    "v_fmac_f32_dpp v250, v23, v247 row_newbcast:3 row_mask:0xf bank_mask:0xf\nv_fmac_f32_dpp v251, -v23, v246 row_newbcast:3 row_mask:0xf bank_mask:0xf\n"
    "v_cvt_pk_f16_f32 v235, v244, v245\nv_cvt_pk_f16_f32 v227, v246, v247\nv_cvt_pk_f16_f32 v239, v248, v249\nv_cvt_pk_f16_f32 v231, v250, v251\ns_waitcnt lgkmcnt(0)\n"
    "v_readfirstlane_b32 s44, v57\nv_mov_b32_dpp v16, v16 row_ror:12 row_mask:0xf bank_mask:0xf\nv_mov_b32_dpp v17, v17 row_ror:12 row_mask:0xf bank_mask:0xf\n"
    "v_mfma_f32_16x16x32_f16 v[40:43], v[224:227], v[232:235], v[40:43]\nv_mov_b32_dpp v18, v18 row_ror:12 row_mask:0xf bank_mask:0xf\n"
    "v_mov_b32_dpp v19, v19 row_ror:12 row_mask:0xf bank_mask:0xf\nv_mfma_f32_16x16x32_f16 v[44:47], v[224:227], v[236:239], v[44:47]\n"
    "v_mov_b32_dpp v20, v20 row_ror:12 row_mask:0xf bank_mask:0xf\nv_mov_b32_dpp v21, v21 row_ror:12 row_mask:0xf bank_mask:0xf\n"
    "v_mfma_f32_16x16x32_f16 v[48:51], v[228:231], v[232:235], v[48:51]\nv_mov_b32_dpp v22, v22 row_ror:12 row_mask:0xf bank_mask:0xf\n"
    "v_mov_b32_dpp v23, v23 row_ror:12 row_mask:0xf bank_mask:0xf\nv_mfma_f32_16x16x32_f16 v[52:55], v[228:231], v[236:239], v[52:55]\ns_sub_u32 s27, s27, 1\n"
    "s_cmp_eq_u32 s27, 0\ns_cbranch_scc1 .Lkx15\ns_cmp_lt_i32 s44, s10\ns_cbranch_scc1 .Lkgl13\ns_setpc_b64 s[94:95]\n.Lkx15:\ns_nop 4\nv_mul_f32 v40, v40, v24\n"
    "v_mul_f32 v41, v41, v24\nv_mul_f32 v42, v42, v24\nv_mul_f32 v43, v43, v24\nv_mul_f32 v44, v44, v24\nv_mul_f32 v45, v45, v24\nv_mul_f32 v46, v46, v24\n"
    "v_mul_f32 v47, v47, v24\nv_mul_f32 v48, v48, v24\nv_mul_f32 v49, v49, v24\nv_mul_f32 v50, v50, v24\nv_mul_f32 v51, v51, v24\nv_mul_f32 v52, v52, v24\n"
    "v_mul_f32 v53, v53, v24\nv_mul_f32 v54, v54, v24\nv_mul_f32 v55, v55, v24\nv_max3_f32 v240, abs(v40), abs(v41), abs(v42)\nv_max3_f32 v241, abs(v43), abs(v44), abs(v45)\n"
    "v_max3_f32 v240, v240, abs(v46), abs(v47)\nv_max3_f32 v241, v241, abs(v48), abs(v49)\nv_max3_f32 v240, v240, abs(v50), abs(v51)\n"
    "v_max3_f32 v241, v241, abs(v52), abs(v53)\nv_max3_f32 v240, v240, abs(v54), abs(v55)\nv_max_f32 v240, v240, v241\ns_nop 1\n"
    "v_max_f32_dpp v240, v240, v240 row_ror:1 row_mask:0xf bank_mask:0xf\ns_nop 1\nv_max_f32_dpp v240, v240, v240 row_ror:2 row_mask:0xf bank_mask:0xf\ns_nop 1\n"
    "v_max_f32_dpp v240, v240, v240 row_ror:4 row_mask:0xf bank_mask:0xf\ns_nop 1\nv_max_f32_dpp v240, v240, v240 row_ror:8 row_mask:0xf bank_mask:0xf\nv_mov_b32 v241, v240\n"
    "s_nop 1\ns_cbranch_execz .Lhop\nv_permlane16_swap_b32 v240, v241\nv_max_f32 v240, v240, v241\nv_mov_b32 v241, v240\ns_nop 1\nv_permlane32_swap_b32 v240, v241\n"
    "v_max_f32 v240, v240, v241\ns_mov_b64 exec, 1\nds_write_b32 v34, v240\ns_mov_b64 exec, -1\ns_setpc_b64 s[94:95]\n"
    ".Lgm14:\ns_waitcnt vmcnt(0)\nv_readfirstlane_b32 s11, v166\ns_mov_b32 s81, 0x3b000000\ns_mov_b32 s84, -1\ns_mov_b32 s85, 0\nv_mov_b32 v241, 0x3f803f80\n"
    "s_mul_i32 s11, s11, 0x9E3779B1\ns_add_u32 s11, s11, 0x7F4A7C15\nv_mov_b32 v160, v27\nv_xor_b32 v161, 32, v160\nv_xor_b32 v162, 64, v160\nv_xor_b32 v163, 0x60, v160\n"
    "v_mov_b32 v240, 0\nv_writelane_b32 v240, 2, 0\ns_mov_b32 s86, 0x100000\n.Ls18:\nds_read_b32 v243, v56 offset:0\ns_waitcnt lgkmcnt(0)\nv_cmp_gt_i32 vcc, 2, v243\n"
    "s_sub_u32 s86, s86, 1\ns_cbranch_scc1 .Ls19\ns_cbranch_vccz .Ls19\ns_sleep 1\ns_branch .Ls18\n.Ls19:\nds_read_b64_tr_b16 v[208:209], v176 offset:0\n"
    "ds_read_b64_tr_b16 v[210:211], v176 offset:256\nds_read_b64_tr_b16 v[212:213], v176 offset:3072\nds_read_b64_tr_b16 v[214:215], v176 offset:3328\n"
    "ds_read_b128 v[144:147], v160 offset:0\nds_read_b64_tr_b16 v[216:217], v176 offset:6144\nds_read_b64_tr_b16 v[218:219], v176 offset:6400\n"
    "ds_read_b128 v[148:151], v161 offset:0\nds_read_b64_tr_b16 v[220:221], v176 offset:9216\nds_read_b64_tr_b16 v[222:223], v176 offset:9472\n"
    "ds_read_b128 v[152:155], v162 offset:0\nds_read_b64_tr_b16 v[224:225], v176 offset:1024\nds_read_b64_tr_b16 v[226:227], v176 offset:1280\n"
    "ds_read_b128 v[156:159], v163 offset:0\nds_read_b64_tr_b16 v[228:229], v176 offset:4096\nds_read_b64_tr_b16 v[230:231], v176 offset:4352\ns_waitcnt lgkmcnt(11)\n"
    "v_dot2c_f32_bf16 v112, v241, v144\nv_dot2c_f32_bf16 v113, v144, v144\nv_dot2c_f32_bf16 v114, v241, v145\nv_dot2c_f32_bf16 v115, v145, v145\n"
    "v_dot2c_f32_bf16 v116, v241, v146\nv_dot2c_f32_bf16 v117, v146, v146\nv_dot2c_f32_bf16 v118, v241, v147\nv_dot2c_f32_bf16 v119, v147, v147\ns_waitcnt lgkmcnt(8)\n"
    "v_dot2c_f32_bf16 v112, v241, v148\nv_dot2c_f32_bf16 v113, v148, v148\nv_dot2c_f32_bf16 v114, v241, v149\nv_dot2c_f32_bf16 v115, v149, v149\n"
    "v_dot2c_f32_bf16 v116, v241, v150\nv_dot2c_f32_bf16 v117, v150, v150\nv_dot2c_f32_bf16 v118, v241, v151\nv_dot2c_f32_bf16 v119, v151, v151\ns_waitcnt lgkmcnt(5)\n"
    "v_dot2c_f32_bf16 v112, v241, v152\nv_dot2c_f32_bf16 v113, v152, v152\nv_dot2c_f32_bf16 v114, v241, v153\nv_dot2c_f32_bf16 v115, v153, v153\n"
    "v_dot2c_f32_bf16 v116, v241, v154\nv_dot2c_f32_bf16 v117, v154, v154\nv_dot2c_f32_bf16 v118, v241, v155\nv_dot2c_f32_bf16 v119, v155, v155\ns_waitcnt lgkmcnt(2)\n"
    "v_dot2c_f32_bf16 v112, v241, v156\nv_dot2c_f32_bf16 v113, v156, v156\nv_dot2c_f32_bf16 v114, v241, v157\nv_dot2c_f32_bf16 v115, v157, v157\n"
    "v_dot2c_f32_bf16 v116, v241, v158\nv_dot2c_f32_bf16 v117, v158, v158\nv_dot2c_f32_bf16 v118, v241, v159\nv_dot2c_f32_bf16 v119, v159, v159\n"
    "v_mfma_f32_32x32x16_bf16 v[64:79], v[208:211], v[144:147], v[64:79]\nds_read_b64_tr_b16 v[208:209], v176 offset:7168\nds_read_b64_tr_b16 v[210:211], v176 offset:7424\n"
    "ds_add_u32 v30, v240\nv_mfma_f32_32x32x16_bf16 v[64:79], v[212:215], v[148:151], v[64:79]\nds_read_b64_tr_b16 v[212:213], v176 offset:10240\n"
    "ds_read_b64_tr_b16 v[214:215], v176 offset:10496\nds_read_b32 v243, v56\nv_mfma_f32_32x32x16_bf16 v[64:79], v[216:219], v[152:155], v[64:79]\n"
    "ds_read_b64_tr_b16 v[216:217], v176 offset:2048\nds_read_b64_tr_b16 v[218:219], v176 offset:2304\nv_mfma_f32_32x32x16_bf16 v[64:79], v[220:223], v[156:159], v[64:79]\n"
    "ds_read_b64_tr_b16 v[220:221], v176 offset:5120\nds_read_b64_tr_b16 v[222:223], v176 offset:5376\nv_mfma_f32_32x32x16_bf16 v[80:95], v[224:227], v[144:147], v[80:95]\n"
    "ds_read_b64_tr_b16 v[224:225], v176 offset:8192\nds_read_b64_tr_b16 v[226:227], v176 offset:8448\ns_waitcnt lgkmcnt(6)\nv_readfirstlane_b32 s49, v243\n"
    "v_mfma_f32_32x32x16_bf16 v[80:95], v[228:231], v[148:151], v[80:95]\nds_read_b64_tr_b16 v[228:229], v176 offset:11264\nds_read_b64_tr_b16 v[230:231], v176 offset:11520\n"
    "s_cmp_lt_i32 s49, 3\ns_cbranch_scc1 .Lc20\n.Lc21:\nv_mfma_f32_32x32x16_bf16 v[80:95], v[208:211], v[152:155], v[80:95]\nds_read_b64_tr_b16 v[208:209], v177 offset:0\n"
    "ds_read_b64_tr_b16 v[210:211], v177 offset:256\nv_mfma_f32_32x32x16_bf16 v[80:95], v[212:215], v[156:159], v[80:95]\nds_read_b64_tr_b16 v[212:213], v177 offset:3072\n"
    "ds_read_b64_tr_b16 v[214:215], v177 offset:3328\ns_waitcnt lgkmcnt(6)\nv_mfma_f32_32x32x16_bf16 v[96:111], v[216:219], v[144:147], v[96:111]\ns_cmp_lt_i32 s49, 4\n"
    "s_cbranch_scc1 .Lc23\n.Lc24:\nds_read_b128 v[144:147], v160 offset:4096\nds_read_b64_tr_b16 v[216:217], v177 offset:6144\n"
    "ds_read_b64_tr_b16 v[218:219], v177 offset:6400\nv_mfma_f32_32x32x16_bf16 v[96:111], v[220:223], v[148:151], v[96:111]\nds_read_b128 v[148:151], v161 offset:4096\n"
    "ds_read_b64_tr_b16 v[220:221], v177 offset:9216\nds_read_b64_tr_b16 v[222:223], v177 offset:9472\nv_mfma_f32_32x32x16_bf16 v[96:111], v[224:227], v[152:155], v[96:111]\n"
    "ds_read_b128 v[152:155], v162 offset:4096\nds_read_b64_tr_b16 v[224:225], v177 offset:1024\nds_read_b64_tr_b16 v[226:227], v177 offset:1280\ns_waitcnt lgkmcnt(13)\n"
    "v_mfma_f32_32x32x16_bf16 v[96:111], v[228:231], v[156:159], v[96:111]\nds_read_b128 v[156:159], v163 offset:4096\nds_read_b64_tr_b16 v[228:229], v177 offset:4096\n"
    "ds_read_b64_tr_b16 v[230:231], v177 offset:4352\ns_waitcnt lgkmcnt(11)\nv_dot2c_f32_bf16 v112, v241, v144\nv_dot2c_f32_bf16 v113, v144, v144\n"
    "v_dot2c_f32_bf16 v114, v241, v145\nv_dot2c_f32_bf16 v115, v145, v145\nv_dot2c_f32_bf16 v116, v241, v146\nv_dot2c_f32_bf16 v117, v146, v146\n"
    "v_dot2c_f32_bf16 v118, v241, v147\nv_dot2c_f32_bf16 v119, v147, v147\ns_waitcnt lgkmcnt(8)\nv_dot2c_f32_bf16 v112, v241, v148\nv_dot2c_f32_bf16 v113, v148, v148\n"
    "v_dot2c_f32_bf16 v114, v241, v149\nv_dot2c_f32_bf16 v115, v149, v149\nv_dot2c_f32_bf16 v116, v241, v150\nv_dot2c_f32_bf16 v117, v150, v150\n"
    "v_dot2c_f32_bf16 v118, v241, v151\nv_dot2c_f32_bf16 v119, v151, v151\ns_waitcnt lgkmcnt(5)\nv_dot2c_f32_bf16 v112, v241, v152\nv_dot2c_f32_bf16 v113, v152, v152\n"
    "v_dot2c_f32_bf16 v114, v241, v153\nv_dot2c_f32_bf16 v115, v153, v153\nv_dot2c_f32_bf16 v116, v241, v154\nv_dot2c_f32_bf16 v117, v154, v154\n"
    "v_dot2c_f32_bf16 v118, v241, v155\nv_dot2c_f32_bf16 v119, v155, v155\ns_waitcnt lgkmcnt(2)\nv_dot2c_f32_bf16 v112, v241, v156\nv_dot2c_f32_bf16 v113, v156, v156\n"
    "v_dot2c_f32_bf16 v114, v241, v157\nv_dot2c_f32_bf16 v115, v157, v157\nv_dot2c_f32_bf16 v116, v241, v158\nv_dot2c_f32_bf16 v117, v158, v158\n"
    "v_dot2c_f32_bf16 v118, v241, v159\nv_dot2c_f32_bf16 v119, v159, v159\nv_mfma_f32_32x32x16_bf16 v[64:79], v[208:211], v[144:147], v[64:79]\n"
    "ds_read_b64_tr_b16 v[208:209], v177 offset:7168\nds_read_b64_tr_b16 v[210:211], v177 offset:7424\nds_add_u32 v30, v240\n"
    "v_mfma_f32_32x32x16_bf16 v[64:79], v[212:215], v[148:151], v[64:79]\nds_read_b64_tr_b16 v[212:213], v177 offset:10240\nds_read_b64_tr_b16 v[214:215], v177 offset:10496\n"
    "ds_read_b32 v243, v56\ns_nop 0\ns_cbranch_execz .Lhop\nv_mfma_f32_32x32x16_bf16 v[64:79], v[216:219], v[152:155], v[64:79]\n"
    "ds_read_b64_tr_b16 v[216:217], v177 offset:2048\nds_read_b64_tr_b16 v[218:219], v177 offset:2304\nv_mfma_f32_32x32x16_bf16 v[64:79], v[220:223], v[156:159], v[64:79]\n"
    "ds_read_b64_tr_b16 v[220:221], v177 offset:5120\nds_read_b64_tr_b16 v[222:223], v177 offset:5376\nv_mfma_f32_32x32x16_bf16 v[80:95], v[224:227], v[144:147], v[80:95]\n"
    "ds_read_b64_tr_b16 v[224:225], v177 offset:8192\nds_read_b64_tr_b16 v[226:227], v177 offset:8448\ns_waitcnt lgkmcnt(6)\nv_readfirstlane_b32 s49, v243\n"
    "v_mfma_f32_32x32x16_bf16 v[80:95], v[228:231], v[148:151], v[80:95]\nds_read_b64_tr_b16 v[228:229], v177 offset:11264\nds_read_b64_tr_b16 v[230:231], v177 offset:11520\n"
    "s_cmp_lt_i32 s49, 5\ns_cbranch_scc1 .Lc26\n.Lc27:\nv_mfma_f32_32x32x16_bf16 v[80:95], v[208:211], v[152:155], v[80:95]\nds_read_b64_tr_b16 v[208:209], v178 offset:0\n"
    "ds_read_b64_tr_b16 v[210:211], v178 offset:256\nv_mfma_f32_32x32x16_bf16 v[80:95], v[212:215], v[156:159], v[80:95]\nds_read_b64_tr_b16 v[212:213], v178 offset:3072\n"
    "ds_read_b64_tr_b16 v[214:215], v178 offset:3328\ns_waitcnt lgkmcnt(6)\nv_mfma_f32_32x32x16_bf16 v[96:111], v[216:219], v[144:147], v[96:111]\ns_cmp_lt_i32 s49, 6\n"
    "s_cbranch_scc1 .Lc29\n.Lc30:\nds_read_b128 v[144:147], v160 offset:8192\nds_read_b64_tr_b16 v[216:217], v178 offset:6144\n"
    "ds_read_b64_tr_b16 v[218:219], v178 offset:6400\nv_mfma_f32_32x32x16_bf16 v[96:111], v[220:223], v[148:151], v[96:111]\nds_read_b128 v[148:151], v161 offset:8192\n"
    "ds_read_b64_tr_b16 v[220:221], v178 offset:9216\nds_read_b64_tr_b16 v[222:223], v178 offset:9472\nv_mfma_f32_32x32x16_bf16 v[96:111], v[224:227], v[152:155], v[96:111]\n"
    "ds_read_b128 v[152:155], v162 offset:8192\nds_read_b64_tr_b16 v[224:225], v178 offset:1024\nds_read_b64_tr_b16 v[226:227], v178 offset:1280\ns_waitcnt lgkmcnt(13)\n"
    "v_mfma_f32_32x32x16_bf16 v[96:111], v[228:231], v[156:159], v[96:111]\nds_read_b128 v[156:159], v163 offset:8192\nds_read_b64_tr_b16 v[228:229], v178 offset:4096\n"
    "ds_read_b64_tr_b16 v[230:231], v178 offset:4352\ns_waitcnt lgkmcnt(11)\nv_dot2c_f32_bf16 v112, v241, v144\nv_dot2c_f32_bf16 v113, v144, v144\n"
    "v_dot2c_f32_bf16 v114, v241, v145\nv_dot2c_f32_bf16 v115, v145, v145\nv_dot2c_f32_bf16 v116, v241, v146\nv_dot2c_f32_bf16 v117, v146, v146\n"
    "v_dot2c_f32_bf16 v118, v241, v147\nv_dot2c_f32_bf16 v119, v147, v147\ns_waitcnt lgkmcnt(8)\nv_dot2c_f32_bf16 v112, v241, v148\nv_dot2c_f32_bf16 v113, v148, v148\n"
    "v_dot2c_f32_bf16 v114, v241, v149\nv_dot2c_f32_bf16 v115, v149, v149\nv_dot2c_f32_bf16 v116, v241, v150\nv_dot2c_f32_bf16 v117, v150, v150\n"
    "v_dot2c_f32_bf16 v118, v241, v151\nv_dot2c_f32_bf16 v119, v151, v151\ns_waitcnt lgkmcnt(5)\nv_dot2c_f32_bf16 v112, v241, v152\nv_dot2c_f32_bf16 v113, v152, v152\n"
    "v_dot2c_f32_bf16 v114, v241, v153\nv_dot2c_f32_bf16 v115, v153, v153\nv_dot2c_f32_bf16 v116, v241, v154\nv_dot2c_f32_bf16 v117, v154, v154\n"
    "v_dot2c_f32_bf16 v118, v241, v155\nv_dot2c_f32_bf16 v119, v155, v155\ns_waitcnt lgkmcnt(2)\nv_dot2c_f32_bf16 v112, v241, v156\nv_dot2c_f32_bf16 v113, v156, v156\n"
    "v_dot2c_f32_bf16 v114, v241, v157\nv_dot2c_f32_bf16 v115, v157, v157\nv_dot2c_f32_bf16 v116, v241, v158\nv_dot2c_f32_bf16 v117, v158, v158\n"
    "v_dot2c_f32_bf16 v118, v241, v159\nv_dot2c_f32_bf16 v119, v159, v159\nv_mfma_f32_32x32x16_bf16 v[64:79], v[208:211], v[144:147], v[64:79]\n"
    "ds_read_b64_tr_b16 v[208:209], v178 offset:7168\nds_read_b64_tr_b16 v[210:211], v178 offset:7424\nds_add_u32 v30, v240\n"
    "v_mfma_f32_32x32x16_bf16 v[64:79], v[212:215], v[148:151], v[64:79]\nds_read_b64_tr_b16 v[212:213], v178 offset:10240\nds_read_b64_tr_b16 v[214:215], v178 offset:10496\n"
    "ds_read_b32 v243, v56\nv_mfma_f32_32x32x16_bf16 v[64:79], v[216:219], v[152:155], v[64:79]\nds_read_b64_tr_b16 v[216:217], v178 offset:2048\n"
    "ds_read_b64_tr_b16 v[218:219], v178 offset:2304\nv_mfma_f32_32x32x16_bf16 v[64:79], v[220:223], v[156:159], v[64:79]\nds_read_b64_tr_b16 v[220:221], v178 offset:5120\n"
    "ds_read_b64_tr_b16 v[222:223], v178 offset:5376\nv_mfma_f32_32x32x16_bf16 v[80:95], v[224:227], v[144:147], v[80:95]\nds_read_b64_tr_b16 v[224:225], v178 offset:8192\n"
    "ds_read_b64_tr_b16 v[226:227], v178 offset:8448\ns_waitcnt lgkmcnt(6)\nv_readfirstlane_b32 s49, v243\n"
    "v_mfma_f32_32x32x16_bf16 v[80:95], v[228:231], v[148:151], v[80:95]\nds_read_b64_tr_b16 v[228:229], v178 offset:11264\nds_read_b64_tr_b16 v[230:231], v178 offset:11520\n"
    "s_cmp_lt_i32 s49, 7\ns_cbranch_scc1 .Lc32\n.Lc33:\nv_mfma_f32_32x32x16_bf16 v[80:95], v[208:211], v[152:155], v[80:95]\nds_read_b64_tr_b16 v[208:209], v179 offset:0\n"
    "ds_read_b64_tr_b16 v[210:211], v179 offset:256\nv_mfma_f32_32x32x16_bf16 v[80:95], v[212:215], v[156:159], v[80:95]\nds_read_b64_tr_b16 v[212:213], v179 offset:3072\n"
    "ds_read_b64_tr_b16 v[214:215], v179 offset:3328\ns_waitcnt lgkmcnt(6)\nv_mfma_f32_32x32x16_bf16 v[96:111], v[216:219], v[144:147], v[96:111]\ns_cmp_lt_i32 s49, 8\n"
    "s_cbranch_scc1 .Lc35\n.Lc36:\nds_read_b128 v[144:147], v160 offset:0\nds_read_b64_tr_b16 v[216:217], v179 offset:6144\nds_read_b64_tr_b16 v[218:219], v179 offset:6400\n"
    "v_mfma_f32_32x32x16_bf16 v[96:111], v[220:223], v[148:151], v[96:111]\nds_read_b128 v[148:151], v161 offset:0\nds_read_b64_tr_b16 v[220:221], v179 offset:9216\n"
    "ds_read_b64_tr_b16 v[222:223], v179 offset:9472\nv_mfma_f32_32x32x16_bf16 v[96:111], v[224:227], v[152:155], v[96:111]\nds_read_b128 v[152:155], v162 offset:0\n"
    "ds_read_b64_tr_b16 v[224:225], v179 offset:1024\nds_read_b64_tr_b16 v[226:227], v179 offset:1280\ns_waitcnt lgkmcnt(13)\n"
    "v_mfma_f32_32x32x16_bf16 v[96:111], v[228:231], v[156:159], v[96:111]\nds_read_b128 v[156:159], v163 offset:0\nds_read_b64_tr_b16 v[228:229], v179 offset:4096\n"
    "ds_read_b64_tr_b16 v[230:231], v179 offset:4352\ns_waitcnt lgkmcnt(11)\nv_dot2c_f32_bf16 v112, v241, v144\nv_dot2c_f32_bf16 v113, v144, v144\n"
    "v_dot2c_f32_bf16 v114, v241, v145\nv_dot2c_f32_bf16 v115, v145, v145\nv_dot2c_f32_bf16 v116, v241, v146\nv_dot2c_f32_bf16 v117, v146, v146\n"
    "v_dot2c_f32_bf16 v118, v241, v147\nv_dot2c_f32_bf16 v119, v147, v147\ns_waitcnt lgkmcnt(8)\nv_dot2c_f32_bf16 v112, v241, v148\nv_dot2c_f32_bf16 v113, v148, v148\n"
    "v_dot2c_f32_bf16 v114, v241, v149\nv_dot2c_f32_bf16 v115, v149, v149\nv_dot2c_f32_bf16 v116, v241, v150\nv_dot2c_f32_bf16 v117, v150, v150\n"
    "v_dot2c_f32_bf16 v118, v241, v151\nv_dot2c_f32_bf16 v119, v151, v151\ns_waitcnt lgkmcnt(5)\nv_dot2c_f32_bf16 v112, v241, v152\nv_dot2c_f32_bf16 v113, v152, v152\n"
    "v_dot2c_f32_bf16 v114, v241, v153\nv_dot2c_f32_bf16 v115, v153, v153\nv_dot2c_f32_bf16 v116, v241, v154\nv_dot2c_f32_bf16 v117, v154, v154\n"
    "v_dot2c_f32_bf16 v118, v241, v155\nv_dot2c_f32_bf16 v119, v155, v155\ns_waitcnt lgkmcnt(2)\nv_dot2c_f32_bf16 v112, v241, v156\nv_dot2c_f32_bf16 v113, v156, v156\n"
    "v_dot2c_f32_bf16 v114, v241, v157\nv_dot2c_f32_bf16 v115, v157, v157\nv_dot2c_f32_bf16 v116, v241, v158\nv_dot2c_f32_bf16 v117, v158, v158\n"
    "v_dot2c_f32_bf16 v118, v241, v159\nv_dot2c_f32_bf16 v119, v159, v159\nv_mfma_f32_32x32x16_bf16 v[64:79], v[208:211], v[144:147], v[64:79]\n"
    "ds_read_b64_tr_b16 v[208:209], v179 offset:7168\nds_read_b64_tr_b16 v[210:211], v179 offset:7424\nds_add_u32 v30, v240\n"
    "v_mfma_f32_32x32x16_bf16 v[64:79], v[212:215], v[148:151], v[64:79]\nds_read_b64_tr_b16 v[212:213], v179 offset:10240\nds_read_b64_tr_b16 v[214:215], v179 offset:10496\n"
    "s_nop 0\ns_cbranch_execz .Lhop\nds_read_b32 v243, v56\nv_mfma_f32_32x32x16_bf16 v[64:79], v[216:219], v[152:155], v[64:79]\n"
    "ds_read_b64_tr_b16 v[216:217], v179 offset:2048\nds_read_b64_tr_b16 v[218:219], v179 offset:2304\nv_mfma_f32_32x32x16_bf16 v[64:79], v[220:223], v[156:159], v[64:79]\n"
    "ds_read_b64_tr_b16 v[220:221], v179 offset:5120\nds_read_b64_tr_b16 v[222:223], v179 offset:5376\nv_mfma_f32_32x32x16_bf16 v[80:95], v[224:227], v[144:147], v[80:95]\n"
    "ds_read_b64_tr_b16 v[224:225], v179 offset:8192\nds_read_b64_tr_b16 v[226:227], v179 offset:8448\ns_waitcnt lgkmcnt(6)\nv_readfirstlane_b32 s49, v243\n"
    "v_mfma_f32_32x32x16_bf16 v[80:95], v[228:231], v[148:151], v[80:95]\nds_read_b64_tr_b16 v[228:229], v179 offset:11264\nds_read_b64_tr_b16 v[230:231], v179 offset:11520\n"
    "s_cmp_lt_i32 s49, 9\ns_cbranch_scc1 .Lc38\n.Lc39:\nv_mfma_f32_32x32x16_bf16 v[80:95], v[208:211], v[152:155], v[80:95]\nds_read_b64_tr_b16 v[208:209], v180 offset:0\n"
    "ds_read_b64_tr_b16 v[210:211], v180 offset:256\nv_mfma_f32_32x32x16_bf16 v[80:95], v[212:215], v[156:159], v[80:95]\nds_read_b64_tr_b16 v[212:213], v180 offset:3072\n"
    "ds_read_b64_tr_b16 v[214:215], v180 offset:3328\ns_waitcnt lgkmcnt(6)\nv_mfma_f32_32x32x16_bf16 v[96:111], v[216:219], v[144:147], v[96:111]\ns_cmp_lt_i32 s49, 10\n"
    "s_cbranch_scc1 .Lc41\n.Lc42:\nds_read_b128 v[144:147], v160 offset:4096\nds_read_b64_tr_b16 v[216:217], v180 offset:6144\n"
    "ds_read_b64_tr_b16 v[218:219], v180 offset:6400\nv_mfma_f32_32x32x16_bf16 v[96:111], v[220:223], v[148:151], v[96:111]\nds_read_b128 v[148:151], v161 offset:4096\n"
    "ds_read_b64_tr_b16 v[220:221], v180 offset:9216\nds_read_b64_tr_b16 v[222:223], v180 offset:9472\nv_mfma_f32_32x32x16_bf16 v[96:111], v[224:227], v[152:155], v[96:111]\n"
    "ds_read_b128 v[152:155], v162 offset:4096\nds_read_b64_tr_b16 v[224:225], v180 offset:1024\nds_read_b64_tr_b16 v[226:227], v180 offset:1280\ns_waitcnt lgkmcnt(13)\n"
    "v_mfma_f32_32x32x16_bf16 v[96:111], v[228:231], v[156:159], v[96:111]\nds_read_b128 v[156:159], v163 offset:4096\nds_read_b64_tr_b16 v[228:229], v180 offset:4096\n"
    "ds_read_b64_tr_b16 v[230:231], v180 offset:4352\ns_waitcnt lgkmcnt(11)\nv_dot2c_f32_bf16 v112, v241, v144\nv_dot2c_f32_bf16 v113, v144, v144\n"
    "v_dot2c_f32_bf16 v114, v241, v145\nv_dot2c_f32_bf16 v115, v145, v145\nv_dot2c_f32_bf16 v116, v241, v146\nv_dot2c_f32_bf16 v117, v146, v146\n"
    "v_dot2c_f32_bf16 v118, v241, v147\nv_dot2c_f32_bf16 v119, v147, v147\ns_waitcnt lgkmcnt(8)\nv_dot2c_f32_bf16 v112, v241, v148\nv_dot2c_f32_bf16 v113, v148, v148\n"
    "v_dot2c_f32_bf16 v114, v241, v149\nv_dot2c_f32_bf16 v115, v149, v149\nv_dot2c_f32_bf16 v116, v241, v150\nv_dot2c_f32_bf16 v117, v150, v150\n"
    "v_dot2c_f32_bf16 v118, v241, v151\nv_dot2c_f32_bf16 v119, v151, v151\ns_waitcnt lgkmcnt(5)\nv_dot2c_f32_bf16 v112, v241, v152\nv_dot2c_f32_bf16 v113, v152, v152\n"
    "v_dot2c_f32_bf16 v114, v241, v153\nv_dot2c_f32_bf16 v115, v153, v153\nv_dot2c_f32_bf16 v116, v241, v154\nv_dot2c_f32_bf16 v117, v154, v154\n"
    "v_dot2c_f32_bf16 v118, v241, v155\nv_dot2c_f32_bf16 v119, v155, v155\ns_waitcnt lgkmcnt(2)\nv_dot2c_f32_bf16 v112, v241, v156\nv_dot2c_f32_bf16 v113, v156, v156\n"
    "v_dot2c_f32_bf16 v114, v241, v157\nv_dot2c_f32_bf16 v115, v157, v157\nv_dot2c_f32_bf16 v116, v241, v158\nv_dot2c_f32_bf16 v117, v158, v158\n"
    "v_dot2c_f32_bf16 v118, v241, v159\nv_dot2c_f32_bf16 v119, v159, v159\nv_mfma_f32_32x32x16_bf16 v[64:79], v[208:211], v[144:147], v[64:79]\n"
    "ds_read_b64_tr_b16 v[208:209], v180 offset:7168\nds_read_b64_tr_b16 v[210:211], v180 offset:7424\nds_add_u32 v30, v240\n"
    "v_mfma_f32_32x32x16_bf16 v[64:79], v[212:215], v[148:151], v[64:79]\nds_read_b64_tr_b16 v[212:213], v180 offset:10240\nds_read_b64_tr_b16 v[214:215], v180 offset:10496\n"
    "ds_read_b32 v243, v56\nv_mfma_f32_32x32x16_bf16 v[64:79], v[216:219], v[152:155], v[64:79]\nds_read_b64_tr_b16 v[216:217], v180 offset:2048\n"
    "ds_read_b64_tr_b16 v[218:219], v180 offset:2304\nv_mfma_f32_32x32x16_bf16 v[64:79], v[220:223], v[156:159], v[64:79]\nds_read_b64_tr_b16 v[220:221], v180 offset:5120\n"
    "ds_read_b64_tr_b16 v[222:223], v180 offset:5376\nv_mfma_f32_32x32x16_bf16 v[80:95], v[224:227], v[144:147], v[80:95]\nds_read_b64_tr_b16 v[224:225], v180 offset:8192\n"
    "ds_read_b64_tr_b16 v[226:227], v180 offset:8448\ns_waitcnt lgkmcnt(6)\nv_readfirstlane_b32 s49, v243\n"
    "v_mfma_f32_32x32x16_bf16 v[80:95], v[228:231], v[148:151], v[80:95]\nds_read_b64_tr_b16 v[228:229], v180 offset:11264\nds_read_b64_tr_b16 v[230:231], v180 offset:11520\n"
    "s_cmp_lt_i32 s49, 11\ns_cbranch_scc1 .Lc44\n.Lc45:\nv_mfma_f32_32x32x16_bf16 v[80:95], v[208:211], v[152:155], v[80:95]\nds_read_b64_tr_b16 v[208:209], v181 offset:0\n"
    "ds_read_b64_tr_b16 v[210:211], v181 offset:256\nv_mfma_f32_32x32x16_bf16 v[80:95], v[212:215], v[156:159], v[80:95]\nds_read_b64_tr_b16 v[212:213], v181 offset:3072\n"
    "ds_read_b64_tr_b16 v[214:215], v181 offset:3328\ns_waitcnt lgkmcnt(6)\nv_mfma_f32_32x32x16_bf16 v[96:111], v[216:219], v[144:147], v[96:111]\ns_cmp_lt_i32 s49, 12\n"
    "s_cbranch_scc1 .Lc47\n.Lc48:\nds_read_b128 v[144:147], v160 offset:8192\nds_read_b64_tr_b16 v[216:217], v181 offset:6144\n"
    "ds_read_b64_tr_b16 v[218:219], v181 offset:6400\nv_mfma_f32_32x32x16_bf16 v[96:111], v[220:223], v[148:151], v[96:111]\nds_read_b128 v[148:151], v161 offset:8192\n"
    "ds_read_b64_tr_b16 v[220:221], v181 offset:9216\nds_read_b64_tr_b16 v[222:223], v181 offset:9472\nv_mfma_f32_32x32x16_bf16 v[96:111], v[224:227], v[152:155], v[96:111]\n"
    "ds_read_b128 v[152:155], v162 offset:8192\nds_read_b64_tr_b16 v[224:225], v181 offset:1024\nds_read_b64_tr_b16 v[226:227], v181 offset:1280\ns_waitcnt lgkmcnt(13)\n"
    "v_mfma_f32_32x32x16_bf16 v[96:111], v[228:231], v[156:159], v[96:111]\nds_read_b128 v[156:159], v163 offset:8192\nds_read_b64_tr_b16 v[228:229], v181 offset:4096\n"
    "ds_read_b64_tr_b16 v[230:231], v181 offset:4352\ns_waitcnt lgkmcnt(11)\nv_dot2c_f32_bf16 v112, v241, v144\nv_dot2c_f32_bf16 v113, v144, v144\n"
    "v_dot2c_f32_bf16 v114, v241, v145\nv_dot2c_f32_bf16 v115, v145, v145\nv_dot2c_f32_bf16 v116, v241, v146\nv_dot2c_f32_bf16 v117, v146, v146\n"
    "v_dot2c_f32_bf16 v118, v241, v147\nv_dot2c_f32_bf16 v119, v147, v147\ns_waitcnt lgkmcnt(8)\nv_dot2c_f32_bf16 v112, v241, v148\nv_dot2c_f32_bf16 v113, v148, v148\n"
    "v_dot2c_f32_bf16 v114, v241, v149\nv_dot2c_f32_bf16 v115, v149, v149\nv_dot2c_f32_bf16 v116, v241, v150\nv_dot2c_f32_bf16 v117, v150, v150\n"
    "v_dot2c_f32_bf16 v118, v241, v151\nv_dot2c_f32_bf16 v119, v151, v151\ns_waitcnt lgkmcnt(5)\nv_dot2c_f32_bf16 v112, v241, v152\nv_dot2c_f32_bf16 v113, v152, v152\n"
    "v_dot2c_f32_bf16 v114, v241, v153\nv_dot2c_f32_bf16 v115, v153, v153\nv_dot2c_f32_bf16 v116, v241, v154\nv_dot2c_f32_bf16 v117, v154, v154\n"
    "v_dot2c_f32_bf16 v118, v241, v155\nv_dot2c_f32_bf16 v119, v155, v155\ns_waitcnt lgkmcnt(2)\nv_dot2c_f32_bf16 v112, v241, v156\nv_dot2c_f32_bf16 v113, v156, v156\n"
    "v_dot2c_f32_bf16 v114, v241, v157\nv_dot2c_f32_bf16 v115, v157, v157\nv_dot2c_f32_bf16 v116, v241, v158\nv_dot2c_f32_bf16 v117, v158, v158\n"
    "v_dot2c_f32_bf16 v118, v241, v159\nv_dot2c_f32_bf16 v119, v159, v159\nv_mfma_f32_32x32x16_bf16 v[64:79], v[208:211], v[144:147], v[64:79]\n"
    "ds_read_b64_tr_b16 v[208:209], v181 offset:7168\nds_read_b64_tr_b16 v[210:211], v181 offset:7424\nds_add_u32 v30, v240\n"
    "v_mfma_f32_32x32x16_bf16 v[64:79], v[212:215], v[148:151], v[64:79]\nds_read_b64_tr_b16 v[212:213], v181 offset:10240\ns_nop 0\ns_cbranch_execz .Lhop\n"
    "ds_read_b64_tr_b16 v[214:215], v181 offset:10496\nds_read_b32 v243, v56\nv_mfma_f32_32x32x16_bf16 v[64:79], v[216:219], v[152:155], v[64:79]\n"
    "ds_read_b64_tr_b16 v[216:217], v181 offset:2048\nds_read_b64_tr_b16 v[218:219], v181 offset:2304\nv_mfma_f32_32x32x16_bf16 v[64:79], v[220:223], v[156:159], v[64:79]\n"
    "ds_read_b64_tr_b16 v[220:221], v181 offset:5120\nds_read_b64_tr_b16 v[222:223], v181 offset:5376\nv_mfma_f32_32x32x16_bf16 v[80:95], v[224:227], v[144:147], v[80:95]\n"
    "ds_read_b64_tr_b16 v[224:225], v181 offset:8192\nds_read_b64_tr_b16 v[226:227], v181 offset:8448\ns_waitcnt lgkmcnt(6)\nv_readfirstlane_b32 s49, v243\n"
    "v_mfma_f32_32x32x16_bf16 v[80:95], v[228:231], v[148:151], v[80:95]\nds_read_b64_tr_b16 v[228:229], v181 offset:11264\nds_read_b64_tr_b16 v[230:231], v181 offset:11520\n"
    "s_cmp_lt_i32 s49, 13\ns_cbranch_scc1 .Lc50\n.Lc51:\nv_mfma_f32_32x32x16_bf16 v[80:95], v[208:211], v[152:155], v[80:95]\nds_read_b64_tr_b16 v[208:209], v182 offset:0\n"
    "ds_read_b64_tr_b16 v[210:211], v182 offset:256\nv_mfma_f32_32x32x16_bf16 v[80:95], v[212:215], v[156:159], v[80:95]\nds_read_b64_tr_b16 v[212:213], v182 offset:3072\n"
    "ds_read_b64_tr_b16 v[214:215], v182 offset:3328\ns_waitcnt lgkmcnt(6)\nv_mfma_f32_32x32x16_bf16 v[96:111], v[216:219], v[144:147], v[96:111]\ns_cmp_lt_i32 s49, 14\n"
    "s_cbranch_scc1 .Lc53\n.Lc54:\nds_read_b128 v[144:147], v160 offset:0\nds_read_b64_tr_b16 v[216:217], v182 offset:6144\nds_read_b64_tr_b16 v[218:219], v182 offset:6400\n"
    "v_mfma_f32_32x32x16_bf16 v[96:111], v[220:223], v[148:151], v[96:111]\nds_read_b128 v[148:151], v161 offset:0\nds_read_b64_tr_b16 v[220:221], v182 offset:9216\n"
    "ds_read_b64_tr_b16 v[222:223], v182 offset:9472\nv_mfma_f32_32x32x16_bf16 v[96:111], v[224:227], v[152:155], v[96:111]\nds_read_b128 v[152:155], v162 offset:0\n"
    "ds_read_b64_tr_b16 v[224:225], v182 offset:1024\nds_read_b64_tr_b16 v[226:227], v182 offset:1280\ns_waitcnt lgkmcnt(13)\n"
    "v_mfma_f32_32x32x16_bf16 v[96:111], v[228:231], v[156:159], v[96:111]\nds_read_b128 v[156:159], v163 offset:0\nds_read_b64_tr_b16 v[228:229], v182 offset:4096\n"
    "ds_read_b64_tr_b16 v[230:231], v182 offset:4352\ns_waitcnt lgkmcnt(11)\nv_dot2c_f32_bf16 v112, v241, v144\nv_dot2c_f32_bf16 v113, v144, v144\n"
    "v_dot2c_f32_bf16 v114, v241, v145\nv_dot2c_f32_bf16 v115, v145, v145\nv_dot2c_f32_bf16 v116, v241, v146\nv_dot2c_f32_bf16 v117, v146, v146\n"
    "v_dot2c_f32_bf16 v118, v241, v147\nv_dot2c_f32_bf16 v119, v147, v147\ns_waitcnt lgkmcnt(8)\nv_dot2c_f32_bf16 v112, v241, v148\nv_dot2c_f32_bf16 v113, v148, v148\n"
    "v_dot2c_f32_bf16 v114, v241, v149\nv_dot2c_f32_bf16 v115, v149, v149\nv_dot2c_f32_bf16 v116, v241, v150\nv_dot2c_f32_bf16 v117, v150, v150\n"
    "v_dot2c_f32_bf16 v118, v241, v151\nv_dot2c_f32_bf16 v119, v151, v151\ns_waitcnt lgkmcnt(5)\nv_dot2c_f32_bf16 v112, v241, v152\nv_dot2c_f32_bf16 v113, v152, v152\n"
    "v_dot2c_f32_bf16 v114, v241, v153\nv_dot2c_f32_bf16 v115, v153, v153\nv_dot2c_f32_bf16 v116, v241, v154\nv_dot2c_f32_bf16 v117, v154, v154\n"
    "v_dot2c_f32_bf16 v118, v241, v155\nv_dot2c_f32_bf16 v119, v155, v155\ns_waitcnt lgkmcnt(2)\nv_dot2c_f32_bf16 v112, v241, v156\nv_dot2c_f32_bf16 v113, v156, v156\n"
    "v_dot2c_f32_bf16 v114, v241, v157\nv_dot2c_f32_bf16 v115, v157, v157\nv_dot2c_f32_bf16 v116, v241, v158\nv_dot2c_f32_bf16 v117, v158, v158\n"
    "v_dot2c_f32_bf16 v118, v241, v159\nv_dot2c_f32_bf16 v119, v159, v159\nv_mfma_f32_32x32x16_bf16 v[64:79], v[208:211], v[144:147], v[64:79]\n"
    "ds_read_b64_tr_b16 v[208:209], v182 offset:7168\nds_read_b64_tr_b16 v[210:211], v182 offset:7424\nds_add_u32 v30, v240\n"
    "v_mfma_f32_32x32x16_bf16 v[64:79], v[212:215], v[148:151], v[64:79]\nds_read_b64_tr_b16 v[212:213], v182 offset:10240\nds_read_b64_tr_b16 v[214:215], v182 offset:10496\n"
    "ds_read_b32 v243, v56\nv_mfma_f32_32x32x16_bf16 v[64:79], v[216:219], v[152:155], v[64:79]\nds_read_b64_tr_b16 v[216:217], v182 offset:2048\n"
    "ds_read_b64_tr_b16 v[218:219], v182 offset:2304\nv_mfma_f32_32x32x16_bf16 v[64:79], v[220:223], v[156:159], v[64:79]\nds_read_b64_tr_b16 v[220:221], v182 offset:5120\n"
    "ds_read_b64_tr_b16 v[222:223], v182 offset:5376\nv_mfma_f32_32x32x16_bf16 v[80:95], v[224:227], v[144:147], v[80:95]\nds_read_b64_tr_b16 v[224:225], v182 offset:8192\n"
    "ds_read_b64_tr_b16 v[226:227], v182 offset:8448\ns_waitcnt lgkmcnt(6)\nv_readfirstlane_b32 s49, v243\n"
    "v_mfma_f32_32x32x16_bf16 v[80:95], v[228:231], v[148:151], v[80:95]\nds_read_b64_tr_b16 v[228:229], v182 offset:11264\nds_read_b64_tr_b16 v[230:231], v182 offset:11520\n"
    "s_cmp_lt_i32 s49, 15\ns_cbranch_scc1 .Lc56\n.Lc57:\nv_mfma_f32_32x32x16_bf16 v[80:95], v[208:211], v[152:155], v[80:95]\nds_read_b64_tr_b16 v[208:209], v183 offset:0\n"
    "ds_read_b64_tr_b16 v[210:211], v183 offset:256\nv_mfma_f32_32x32x16_bf16 v[80:95], v[212:215], v[156:159], v[80:95]\nds_read_b64_tr_b16 v[212:213], v183 offset:3072\n"
    "ds_read_b64_tr_b16 v[214:215], v183 offset:3328\ns_waitcnt lgkmcnt(6)\nv_mfma_f32_32x32x16_bf16 v[96:111], v[216:219], v[144:147], v[96:111]\ns_cmp_lt_i32 s49, 16\n"
    "s_cbranch_scc1 .Lc59\n.Lc60:\nds_read_b128 v[144:147], v160 offset:4096\nds_read_b64_tr_b16 v[216:217], v183 offset:6144\n"
    "ds_read_b64_tr_b16 v[218:219], v183 offset:6400\nv_mfma_f32_32x32x16_bf16 v[96:111], v[220:223], v[148:151], v[96:111]\nds_read_b128 v[148:151], v161 offset:4096\n"
    "ds_read_b64_tr_b16 v[220:221], v183 offset:9216\nds_read_b64_tr_b16 v[222:223], v183 offset:9472\nv_mfma_f32_32x32x16_bf16 v[96:111], v[224:227], v[152:155], v[96:111]\n"
    "ds_read_b128 v[152:155], v162 offset:4096\nds_read_b64_tr_b16 v[224:225], v183 offset:1024\nds_read_b64_tr_b16 v[226:227], v183 offset:1280\ns_waitcnt lgkmcnt(13)\n"
    "v_mfma_f32_32x32x16_bf16 v[96:111], v[228:231], v[156:159], v[96:111]\nds_read_b128 v[156:159], v163 offset:4096\nds_read_b64_tr_b16 v[228:229], v183 offset:4096\n"
    "ds_read_b64_tr_b16 v[230:231], v183 offset:4352\ns_waitcnt lgkmcnt(11)\nv_dot2c_f32_bf16 v112, v241, v144\nv_dot2c_f32_bf16 v113, v144, v144\n"
    "v_dot2c_f32_bf16 v114, v241, v145\nv_dot2c_f32_bf16 v115, v145, v145\nv_dot2c_f32_bf16 v116, v241, v146\nv_dot2c_f32_bf16 v117, v146, v146\n"
    "v_dot2c_f32_bf16 v118, v241, v147\nv_dot2c_f32_bf16 v119, v147, v147\ns_waitcnt lgkmcnt(8)\nv_dot2c_f32_bf16 v112, v241, v148\nv_dot2c_f32_bf16 v113, v148, v148\n"
    "v_dot2c_f32_bf16 v114, v241, v149\nv_dot2c_f32_bf16 v115, v149, v149\nv_dot2c_f32_bf16 v116, v241, v150\nv_dot2c_f32_bf16 v117, v150, v150\n"
    "v_dot2c_f32_bf16 v118, v241, v151\nv_dot2c_f32_bf16 v119, v151, v151\ns_waitcnt lgkmcnt(5)\nv_dot2c_f32_bf16 v112, v241, v152\nv_dot2c_f32_bf16 v113, v152, v152\n"
    "v_dot2c_f32_bf16 v114, v241, v153\nv_dot2c_f32_bf16 v115, v153, v153\nv_dot2c_f32_bf16 v116, v241, v154\nv_dot2c_f32_bf16 v117, v154, v154\n"
    "v_dot2c_f32_bf16 v118, v241, v155\nv_dot2c_f32_bf16 v119, v155, v155\ns_waitcnt lgkmcnt(2)\nv_dot2c_f32_bf16 v112, v241, v156\nv_dot2c_f32_bf16 v113, v156, v156\n"
    "v_dot2c_f32_bf16 v114, v241, v157\nv_dot2c_f32_bf16 v115, v157, v157\nv_dot2c_f32_bf16 v116, v241, v158\nv_dot2c_f32_bf16 v117, v158, v158\n"
    "v_dot2c_f32_bf16 v118, v241, v159\nv_dot2c_f32_bf16 v119, v159, v159\nv_mfma_f32_32x32x16_bf16 v[64:79], v[208:211], v[144:147], v[64:79]\n"
    "ds_read_b64_tr_b16 v[208:209], v183 offset:7168\nds_read_b64_tr_b16 v[210:211], v183 offset:7424\nds_add_u32 v30, v240\n"
    "v_mfma_f32_32x32x16_bf16 v[64:79], v[212:215], v[148:151], v[64:79]\ns_nop 0\ns_cbranch_execz .Lhop\nds_read_b64_tr_b16 v[212:213], v183 offset:10240\n"
    "ds_read_b64_tr_b16 v[214:215], v183 offset:10496\nv_add_f32 v112, v112, v114\nv_add_f32 v113, v113, v115\nv_add_f32 v116, v116, v118\nv_add_f32 v117, v117, v119\n"
    "v_mfma_f32_32x32x16_bf16 v[64:79], v[216:219], v[152:155], v[64:79]\nds_read_b64_tr_b16 v[216:217], v183 offset:2048\nds_read_b64_tr_b16 v[218:219], v183 offset:2304\n"
    "v_add_f32 v112, v112, v116\nv_add_f32 v113, v113, v117\nv_mfma_f32_32x32x16_bf16 v[64:79], v[220:223], v[156:159], v[64:79]\nds_read_b32 v243, v28\n"
    "ds_read_b32 v124, v38 offset:0\nds_read_b32 v128, v38 offset:128\nds_read_b32 v132, v38 offset:256\nds_read_b64_tr_b16 v[220:221], v183 offset:5120\n"
    "ds_read_b64_tr_b16 v[222:223], v183 offset:5376\nv_permlane32_swap_b32 v112, v113\nv_add_f32 v138, v112, v113\nv_add_f32 v139, v112, v113\n"
    "v_mfma_f32_32x32x16_bf16 v[80:95], v[224:227], v[144:147], v[80:95]\nds_read_b64_tr_b16 v[224:225], v183 offset:8192\nds_read_b64_tr_b16 v[226:227], v183 offset:8448\n"
    "v_permlane32_swap_b32 v138, v139\nv_mul_f32 v138, s81, v138\nv_mul_f32 v112, v138, v138\nv_cvt_pk_bf16_f32 v138, -v138, -v138\ns_waitcnt lgkmcnt(12)\n"
    "v_mfma_f32_32x32x16_bf16 v[80:95], v[228:231], v[148:151], v[80:95]\nds_read_b64_tr_b16 v[228:229], v183 offset:11264\nds_read_b64_tr_b16 v[230:231], v183 offset:11520\n"
    "v_cndmask_b32 v120, 0, v138, s[84:85]\nv_fma_f32 v139, v139, s81, -v112\nv_max_f32 v139, 0, v139\nv_cvt_pk_bf16_f32 v139, 0, v139\n"
    "v_mfma_f32_32x32x16_bf16 v[80:95], v[208:211], v[152:155], v[80:95]\ns_waitcnt lgkmcnt(6)\nv_cmp_gt_i32 vcc, v29, v243\nv_add_f32 v139, 0x3727c5ac, v139\n"
    "v_cvt_pk_bf16_f32 v139, 0, v139\nv_sqrt_f32 v139, v139\nv_mfma_f32_32x32x16_bf16 v[80:95], v[212:215], v[156:159], v[80:95]\nv_cvt_pk_bf16_f32 v139, 0, v139\n"
    "v_rcp_f32 v136, v139\nv_mov_b64 v[232:233], 0\nv_mov_b64 v[234:235], 0\ns_cbranch_vccnz .Lt62\n.Lt63:\n"
    "v_mfma_f32_32x32x16_bf16 v[64:79], v[124:127], v[120:123], v[64:79]\nv_mov_b32 v137, v136\nv_mov_b64 v[236:237], 0\nv_mov_b64 v[238:239], 0\nv_mov_b64 v[244:245], 0\n"
    "v_mov_b64 v[246:247], 0\nv_mfma_f32_32x32x16_bf16 v[80:95], v[128:131], v[120:123], v[80:95]\nv_mov_b64 v[248:249], 0\nv_mov_b64 v[250:251], 0\n"
    "s_mov_b32 s44, 0x00ff00ff\ns_mov_b32 s45, 0xff00ff00\ns_mov_b32 s46, s44\ns_mov_b32 s47, s44\ns_waitcnt lgkmcnt(0)\n"
    "v_mfma_f32_32x32x16_bf16 v[96:111], v[216:219], v[144:147], v[96:111]\nv_mul_f32 v64, v64, v136\nv_mul_f32 v65, v65, v136\nv_mul_f32 v66, v66, v136\n"
    "v_mul_f32 v67, v67, v136\nv_cvt_pk_bf16_f32 v64, v64, v65\nv_cvt_pk_bf16_f32 v65, v66, v67\nds_write_b64 v37, v[64:65] offset:0\n"
    "v_mfma_f32_32x32x16_bf16 v[96:111], v[220:223], v[148:151], v[96:111]\nv_mul_f32 v68, v68, v136\nv_mul_f32 v69, v69, v136\nv_mul_f32 v70, v70, v136\n"
    "v_mul_f32 v71, v71, v136\nv_cvt_pk_bf16_f32 v68, v68, v69\nv_cvt_pk_bf16_f32 v69, v70, v71\nds_write_b64 v37, v[68:69] offset:16\n"
    "v_mfma_f32_32x32x16_bf16 v[96:111], v[224:227], v[152:155], v[96:111]\nv_mul_f32 v72, v72, v136\nv_mul_f32 v73, v73, v136\nv_mul_f32 v74, v74, v136\n"
    "v_mul_f32 v75, v75, v136\nv_cvt_pk_bf16_f32 v72, v72, v73\nv_cvt_pk_bf16_f32 v73, v74, v75\nds_write_b64 v37, v[72:73] offset:32\n"
    "v_mfma_f32_32x32x16_bf16 v[96:111], v[228:231], v[156:159], v[96:111]\nv_mul_f32 v76, v76, v136\nv_mul_f32 v77, v77, v136\nv_mul_f32 v78, v78, v136\n"
    "v_mul_f32 v79, v79, v136\nv_cvt_pk_bf16_f32 v76, v76, v77\nv_cvt_pk_bf16_f32 v77, v78, v79\nds_write_b64 v37, v[76:77] offset:48\n"
    "v_mfma_f32_32x32x16_bf16 v[96:111], v[132:135], v[120:123], v[96:111]\nv_pk_mul_f32 v[80:81], v[80:81], v[136:137]\nv_pk_mul_f32 v[82:83], v[82:83], v[136:137]\n"
    "v_cvt_pk_bf16_f32 v80, v80, v81\nv_cvt_pk_bf16_f32 v81, v82, v83\nds_write_b64 v37, v[80:81] offset:64\nv_pk_mul_f32 v[84:85], v[84:85], v[136:137]\n"
    "v_pk_mul_f32 v[86:87], v[86:87], v[136:137]\nv_cvt_pk_bf16_f32 v84, v84, v85\nv_cvt_pk_bf16_f32 v85, v86, v87\nds_write_b64 v37, v[84:85] offset:80\n"
    "v_pk_mul_f32 v[88:89], v[88:89], v[136:137]\nv_pk_mul_f32 v[90:91], v[90:91], v[136:137]\nv_cvt_pk_bf16_f32 v88, v88, v89\nv_cvt_pk_bf16_f32 v89, v90, v91\n"
    "ds_write_b64 v37, v[88:89] offset:96\nv_pk_mul_f32 v[92:93], v[92:93], v[136:137]\nv_pk_mul_f32 v[94:95], v[94:95], v[136:137]\nv_cvt_pk_bf16_f32 v92, v92, v93\n"
    "v_cvt_pk_bf16_f32 v93, v94, v95\nds_write_b64 v37, v[92:93] offset:112\nv_pk_mul_f32 v[96:97], v[96:97], v[136:137]\nv_pk_mul_f32 v[98:99], v[98:99], v[136:137]\n"
    "v_cvt_pk_bf16_f32 v96, v96, v97\nv_cvt_pk_bf16_f32 v97, v98, v99\nds_write_b64 v37, v[96:97] offset:128\nv_pk_mul_f32 v[100:101], v[100:101], v[136:137]\n"
    "v_pk_mul_f32 v[102:103], v[102:103], v[136:137]\nv_cvt_pk_bf16_f32 v100, v100, v101\nv_cvt_pk_bf16_f32 v101, v102, v103\nds_write_b64 v37, v[100:101] offset:144\n"
    "v_pk_mul_f32 v[104:105], v[104:105], v[136:137]\nv_pk_mul_f32 v[106:107], v[106:107], v[136:137]\nv_cvt_pk_bf16_f32 v104, v104, v105\nv_cvt_pk_bf16_f32 v105, v106, v107\n"
    "ds_write_b64 v37, v[104:105] offset:160\nv_pk_mul_f32 v[108:109], v[108:109], v[136:137]\nv_pk_mul_f32 v[110:111], v[110:111], v[136:137]\n"
    "v_cvt_pk_bf16_f32 v108, v108, v109\nv_cvt_pk_bf16_f32 v109, v110, v111\nds_write_b64 v37, v[108:109] offset:176\ns_branch .Latt12\n.Lc20:\ns_mov_b32 s86, 0x100000\n"
    ".Lc22:\nds_read_b32 v243, v56\ns_waitcnt lgkmcnt(0)\nv_readfirstlane_b32 s49, v243\ns_sub_u32 s86, s86, 1\ns_cbranch_scc1 .Lc21\ns_cmp_ge_i32 s49, 3\n"
    "s_cbranch_scc1 .Lc21\ns_branch .Lc22\n.Lc23:\ns_mov_b32 s86, 0x100000\n.Lc25:\nds_read_b32 v243, v56\ns_waitcnt lgkmcnt(0)\nv_readfirstlane_b32 s49, v243\n"
    "s_sub_u32 s86, s86, 1\ns_cbranch_scc1 .Lc24\ns_cmp_ge_i32 s49, 4\ns_cbranch_scc1 .Lc24\ns_branch .Lc25\n.Lc26:\ns_mov_b32 s86, 0x100000\n.Lc28:\nds_read_b32 v243, v56\n"
    "s_waitcnt lgkmcnt(0)\nv_readfirstlane_b32 s49, v243\ns_sub_u32 s86, s86, 1\ns_cbranch_scc1 .Lc27\ns_cmp_ge_i32 s49, 5\ns_cbranch_execz .Lhop\ns_cbranch_scc1 .Lc27\n"
    "s_branch .Lc28\n.Lc29:\ns_mov_b32 s86, 0x100000\n.Lc31:\nds_read_b32 v243, v56\ns_waitcnt lgkmcnt(0)\nv_readfirstlane_b32 s49, v243\ns_sub_u32 s86, s86, 1\n"
    "s_cbranch_scc1 .Lc30\ns_cmp_ge_i32 s49, 6\ns_cbranch_scc1 .Lc30\ns_branch .Lc31\n.Lc32:\ns_mov_b32 s86, 0x100000\n.Lc34:\nds_read_b32 v243, v56\ns_waitcnt lgkmcnt(0)\n"
    "v_readfirstlane_b32 s49, v243\ns_sub_u32 s86, s86, 1\ns_cbranch_scc1 .Lc33\ns_cmp_ge_i32 s49, 7\ns_cbranch_scc1 .Lc33\ns_branch .Lc34\n.Lc35:\ns_mov_b32 s86, 0x100000\n"
    ".Lc37:\nds_read_b32 v243, v56\ns_waitcnt lgkmcnt(0)\nv_readfirstlane_b32 s49, v243\ns_sub_u32 s86, s86, 1\ns_cbranch_scc1 .Lc36\ns_cmp_ge_i32 s49, 8\n"
    "s_cbranch_scc1 .Lc36\ns_branch .Lc37\n.Lc38:\ns_mov_b32 s86, 0x100000\n.Lc40:\nds_read_b32 v243, v56\ns_waitcnt lgkmcnt(0)\nv_readfirstlane_b32 s49, v243\n"
    "s_sub_u32 s86, s86, 1\ns_cbranch_scc1 .Lc39\ns_cmp_ge_i32 s49, 9\ns_cbranch_scc1 .Lc39\ns_branch .Lc40\n.Lc41:\ns_mov_b32 s86, 0x100000\n.Lc43:\nds_read_b32 v243, v56\n"
    "s_waitcnt lgkmcnt(0)\nv_readfirstlane_b32 s49, v243\ns_sub_u32 s86, s86, 1\ns_cbranch_scc1 .Lc42\ns_cmp_ge_i32 s49, 10\ns_cbranch_scc1 .Lc42\ns_branch .Lc43\n.Lc44:\n"
    "s_mov_b32 s86, 0x100000\n.Lc46:\nds_read_b32 v243, v56\ns_waitcnt lgkmcnt(0)\nv_readfirstlane_b32 s49, v243\ns_sub_u32 s86, s86, 1\ns_cbranch_scc1 .Lc45\n"
    "s_cmp_ge_i32 s49, 11\ns_cbranch_scc1 .Lc45\ns_branch .Lc46\n.Lc47:\ns_mov_b32 s86, 0x100000\n.Lc49:\nds_read_b32 v243, v56\ns_waitcnt lgkmcnt(0)\n"
    "v_readfirstlane_b32 s49, v243\ns_sub_u32 s86, s86, 1\ns_cbranch_scc1 .Lc48\ns_cmp_ge_i32 s49, 12\ns_cbranch_scc1 .Lc48\ns_branch .Lc49\n.Lc50:\ns_mov_b32 s86, 0x100000\n"
    ".Lc52:\nds_read_b32 v243, v56\ns_waitcnt lgkmcnt(0)\nv_readfirstlane_b32 s49, v243\ns_sub_u32 s86, s86, 1\ns_cbranch_scc1 .Lc51\ns_cmp_ge_i32 s49, 13\n"
    "s_cbranch_scc1 .Lc51\ns_branch .Lc52\n.Lc53:\ns_mov_b32 s86, 0x100000\n.Lc55:\nds_read_b32 v243, v56\ns_waitcnt lgkmcnt(0)\nv_readfirstlane_b32 s49, v243\n"
    "s_sub_u32 s86, s86, 1\ns_cbranch_scc1 .Lc54\ns_cmp_ge_i32 s49, 14\ns_cbranch_scc1 .Lc54\ns_branch .Lc55\n.Lc56:\ns_mov_b32 s86, 0x100000\n.Lc58:\nds_read_b32 v243, v56\n"
    "s_waitcnt lgkmcnt(0)\nv_readfirstlane_b32 s49, v243\ns_sub_u32 s86, s86, 1\ns_cbranch_scc1 .Lc57\ns_cmp_ge_i32 s49, 15\ns_cbranch_scc1 .Lc57\ns_branch .Lc58\n.Lc59:\n"
    "s_mov_b32 s86, 0x100000\n.Lc61:\nds_read_b32 v243, v56\ns_waitcnt lgkmcnt(0)\nv_readfirstlane_b32 s49, v243\ns_sub_u32 s86, s86, 1\ns_cbranch_scc1 .Lc60\n"
    "s_cmp_ge_i32 s49, 16\ns_cbranch_scc1 .Lc60\ns_branch .Lc61\n.Lt62:\ns_mov_b32 s86, 0x100000\n.Ls64:\nds_read_b32 v243, v28 offset:0\ns_waitcnt lgkmcnt(0)\n"
    "v_cmp_gt_i32 vcc, v29, v243\ns_sub_u32 s86, s86, 1\ns_cbranch_scc1 .Ls65\ns_cbranch_vccz .Ls65\ns_branch .Ls64\n.Ls65:\nds_read_b32 v124, v38 offset:0\n"
    "ds_read_b32 v128, v38 offset:128\nds_read_b32 v132, v38 offset:256\ns_waitcnt lgkmcnt(0)\ns_branch .Lt63\n"
    ".Lhpre66:\nv_and_b32 v224, 127, v0\ns_getpc_b64 s[44:45]\ns_add_u32 s44, s44, g_tabs@rel32@lo+4\ns_addc_u32 s45, s45, g_tabs@rel32@hi+12\nv_lshlrev_b32 v224, 4, v224\n"
    "global_load_dwordx4 v[48:51], v224, s[44:45] offset:2064\nglobal_load_dwordx4 v[52:55], v224, s[44:45]\nglobal_load_dwordx2 v[62:63], v224, s[44:45] offset:16\n"
    "s_lshl_b32 s43, s22, 17\ns_lshl_b32 s42, s13, 15\ns_add_u32 s44, s43, s42\ns_add_u32 s44, s44, 0xe01000\nv_and_b32 v231, 63, v0\nv_lshlrev_b32 v59, 4, v231\n"
    "s_add_u32 s68, s4, s44\ns_addc_u32 s69, s5, 0\ns_add_u32 s70, s68, 8192\ns_addc_u32 s71, s69, 0\ns_add_u32 s72, s68, 16384\ns_addc_u32 s73, s69, 0\n"
    "s_add_u32 s74, s68, 24576\ns_addc_u32 s75, s69, 0\nv_bfe_u32 v251, v0, 5, 1\nv_and_b32 v250, 31, v0\ns_lshl_b32 s43, s13, 11\ns_add_u32 s42, s43, 0x1d000\n"
    "v_lshl_add_u32 v40, v251, 8, s42\nv_lshl_add_u32 v40, v250, 2, v40\nv_and_b32 v249, 63, v0\nv_lshl_add_u32 v41, v249, 4, s42\ns_mov_b32 s41, 0x1f600\n"
    "v_lshl_add_u32 v42, v249, 4, s41\nv_bfe_u32 v248, v0, 3, 3\nv_and_b32 v247, 7, v0\ns_lshl_b32 s40, s22, 19\ns_lshl_b32 s39, s13, 17\ns_lshl_b32 s38, s21, 7\n"
    "s_add_u32 s37, s12, s40\ns_add_u32 s37, s37, s39\ns_add_u32 s37, s37, s38\ns_add_u32 s37, s37, 0x200000\nv_lshl_add_u32 v43, v248, 13, s37\n"
    "v_lshl_add_u32 v43, v247, 4, v43\ns_add_u32 s36, s12, s40\ns_add_u32 s36, s36, s39\ns_add_u32 s36, s36, s38\ns_add_u32 s36, s36, 0x210000\n"
    "v_lshl_add_u32 v44, v248, 13, s36\nv_lshl_add_u32 v44, v247, 4, v44\ns_lshl_b32 s35, s13, 8\ns_add_u32 s34, s35, 0x27600\nv_lshl_add_u32 v45, v249, 2, s34\n"
    "s_add_u32 s33, s43, 0x1d400\nv_lshl_add_u32 v46, v251, 8, s33\nv_lshl_add_u32 v46, v250, 2, v46\nv_and_b32 v251, 31, v0\nv_bfe_u32 v250, v0, 5, 1\n"
    "s_lshl_b32 s43, s21, 15\ns_lshl_b32 s42, s22, 8\ns_lshl_b32 s41, s13, 6\ns_add_u32 s40, s43, s42\ns_add_u32 s40, s40, s41\nv_lshl_add_u32 v47, v251, 10, s40\n"
    "v_lshl_add_u32 v47, v250, 4, v47\nv_and_b32 v249, 63, v0\ns_lshr_b32 s39, s13, 1\ns_lshl_b32 s38, s39, 13\ns_and_b32 s37, s13, 1\ns_lshl_b32 s36, s37, 11\n"
    "s_add_u32 s35, s38, s36\ns_add_u32 s35, s35, 0x1f600\nv_lshl_add_u32 v60, v249, 4, s35\nv_and_b32 v248, 1, v0\ns_lshl_b32 s34, s39, 3\ns_add_u32 s33, s34, 0x27fc0\n"
    "v_lshl_add_u32 v61, v248, 2, s33\ns_add_u32 s32, s34, 0x27d80\ns_cbranch_execz .Lhop\nv_mov_b32 v252, s32\nv_and_b32 v247, 0x7f, v0\ns_mul_i32 s31, s39, 0x2100\n"
    "s_add_u32 s30, s31, 0x18000\nv_lshl_add_u32 v253, v247, 4, s30\ns_lshr_b32 s43, s13, 1\ns_mul_i32 s42, s43, 0x600\ns_add_u32 s41, s42, 0x1c200\nv_mov_b32 v254, s41\n"
    "s_lshl_b32 s40, s13, 2\ns_add_u32 s39, s40, 0x27f80\nv_mov_b32 v25, s39\nv_mov_b64 v[232:233], 0\nv_mov_b64 v[234:235], 0\nv_mov_b64 v[236:237], 0\n"
    "v_mov_b64 v[238:239], 0\nv_mov_b64 v[244:245], 0\nv_mov_b64 v[246:247], 0\nv_mov_b64 v[248:249], 0\nv_mov_b64 v[250:251], 0\ns_setpc_b64 s[94:95]\n.Lhcon67_0:\n"
    "s_add_u32 s43, s12, 0x400000\nv_lshl_add_u32 v16, v0, 4, s43\ns_lshl_b32 s42, s18, 13\ns_add_u32 s41, s12, s42\ns_add_u32 s41, s41, 0x200000\n"
    "v_lshl_add_u32 v17, v0, 4, s41\nv_lshrrev_b32 v231, 3, v0\nv_mul_u32_u24 v230, 0x50, v231\nv_and_b32 v229, 7, v0\nv_add_u32 v18, 0x1d550, v230\n"
    "v_lshl_add_u32 v18, v229, 3, v18\nv_bfe_u32 v228, v0, 4, 3\nv_xor_b32 v227, v0, v228\ns_mov_b32 s40, 0x19400\nv_lshl_add_u32 v19, v227, 3, s40\nv_bfe_u32 v226, v0, 4, 1\n"
    "v_xor_b32 v225, v0, v226\ns_mov_b32 s39, 0x17c00\nv_lshl_add_u32 v20, v225, 3, s39\ns_lshr_b32 s43, s12, 23\ns_add_u32 s42, s43, 3\nv_lshrrev_b32 v231, s42, v0\n"
    "s_lshr_b32 s41, s12, 22\ns_sub_u32 s40, 15, s41\nv_lshlrev_b32 v230, s40, v231\ns_lshr_b32 s39, s12, 20\ns_add_u32 s38, s39, 7\nv_and_b32 v229, s38, v0\n"
    "s_lshl_b32 s37, s18, 7\ns_add_u32 s36, s37, 0x600000\ns_lshl_b32 s35, s24, 18\ns_lshl_b32 s34, s25, 8\ns_add_u32 s33, s35, s34\ns_add_u32 s33, s33, 0x1200000\n"
    "s_cmp_lt_u32 s12, 1\ns_cselect_b32 s32, s36, s33\nv_add_u32 v21, s32, v230\nv_lshl_add_u32 v21, v229, 4, v21\nv_and_b32 v231, 63, v0\nv_lshlrev_b32 v230, 4, v231\n"
    "v_mov_b32 v229, 0xfc0\ns_lshr_b32 s43, s13, 1\ns_mul_i32 s42, s43, 26\nv_add_u32 v228, s42, v231\nv_cmp_gt_u32 vcc, 64, v228\ns_nop 1\n"
    "v_cndmask_b32 v227, v229, v230, vcc\ns_lshr_b32 s41, s3, 2\ns_mul_i32 s40, s41, 0x1950\ns_mul_i32 s39, s13, 0x150\ns_add_u32 s38, s40, s39\ns_add_u32 s38, s38, 0x1d000\n"
    "s_mul_i32 s37, s41, 0x3180\ns_and_b32 s36, s13, 1\ns_mul_i32 s35, s36, 0xfc0\ns_add_u32 s34, s37, s35\ns_add_u32 s34, s34, 0x20300\ns_cmp_lt_u32 s13, 2\n"
    "s_cselect_b32 s33, s38, s34\nv_add_u32 v22, s33, v227\nv_and_b32 v226, 31, v0\nv_sub_u32 v225, 0x3ff, v226\nv_bfe_u32 v224, v0, 5, 1\nv_lshl_add_u32 v175, v224, 3, v225\n"
    "v_and_b32 v174, 3, v175\nv_mul_u32_u24 v173, 0x840, v174\nv_lshrrev_b32 v172, 2, v175\ns_mul_i32 s32, s43, 0x2100\ns_add_u32 s31, s32, 0x17800\nv_add_u32 v23, s31, v173\n"
    "v_lshl_add_u32 v23, v172, 3, v23\ns_lshr_b32 s43, s12, 23\ns_add_u32 s42, s43, 4\nv_mov_b32 v26, s42\ns_setpc_b64 s[94:95]\n.Lhcon67_1:\nv_and_b32 v231, 63, v0\n"
    "v_add_u32 v230, 3, v231\nv_lshrrev_b32 v229, 2, v230\nv_mul_u32_u24 v228, 0x50, v229\nv_and_b32 v227, 3, v230\ns_lshr_b32 s43, s13, 1\ns_mul_i32 s42, s43, 0x1950\n"
    "s_add_u32 s41, s42, 0x1d500\nv_add_u32 v10, s41, v228\nv_lshl_add_u32 v10, v227, 4, v10\nv_add_u32 v226, 2, v231\nv_lshrrev_b32 v225, 2, v226\n"
    "v_mul_u32_u24 v224, 0x50, v225\nv_and_b32 v175, 3, v226\ns_add_u32 s40, s42, 0x1d508\nv_add_u32 v11, s40, v224\nv_lshl_add_u32 v11, v175, 4, v11\n"
    "v_bfe_u32 v174, v0, 2, 4\nv_mul_u32_u24 v173, 0x50, v174\nv_and_b32 v172, 3, v0\ns_add_u32 s39, s42, 0x1d550\nv_add_u32 v12, s39, v173\nv_lshl_add_u32 v12, v172, 4, v12\n"
    "s_and_b32 s38, s13, 1\nv_add_u32 v171, 1, v231\nv_lshrrev_b32 v170, 5, v171\nv_mul_u32_u24 v169, 0x240, v170\nv_and_b32 v168, 31, v171\ns_mul_i32 s37, s43, 0x3180\n"
    "s_add_u32 s36, s37, 0x20540\nv_add_u32 v167, s36, v169\nv_lshl_add_u32 v167, v168, 4, v167\nv_lshrrev_b32 v166, 6, v171\nv_mul_u32_u24 v165, 0x480, v166\n"
    "v_and_b32 v164, 63, v171\ns_add_u32 s35, s37, 0x22280\nv_add_u32 v163, s35, v165\nv_lshl_add_u32 v163, v164, 4, v163\ns_cmp_lt_u32 s38, 1\ns_cselect_b64 vcc, -1, 0\n"
    "v_cndmask_b32 v13, v163, v167, vcc\ns_and_b32 s43, s13, 1\nv_and_b32 v231, 63, v0\nv_add_u32 v230, 2, v231\nv_lshrrev_b32 v229, 5, v230\nv_mul_u32_u24 v228, 0x240, v229\n"
    "v_and_b32 v227, 31, v230\ns_lshr_b32 s42, s13, 1\ns_mul_i32 s41, s42, 0x3180\ns_add_u32 s40, s41, 0x20540\nv_add_u32 v226, s40, v228\nv_lshl_add_u32 v226, v227, 4, v226\n"
    "s_add_u32 s39, s41, 0x22b80\nv_lshl_add_u32 v225, v231, 4, s39\ns_cmp_lt_u32 s43, 1\ns_cselect_b64 vcc, -1, 0\nv_cndmask_b32 v14, v225, v226, vcc\n"
    "v_add_u32 v224, 1, v231\nv_lshrrev_b32 v175, 5, v224\nv_mul_u32_u24 v174, 0x240, v175\nv_and_b32 v173, 31, v224\nv_add_u32 v172, s40, v174\n"
    "v_lshl_add_u32 v172, v173, 4, v172\nv_lshrrev_b32 v171, 6, v224\nv_mul_u32_u24 v170, 0x480, v171\nv_and_b32 v169, 63, v224\ns_add_u32 s38, s41, 0x22280\n"
    "v_add_u32 v168, s38, v170\nv_lshl_add_u32 v168, v169, 4, v168\ns_cmp_lt_u32 s43, 1\ns_cselect_b64 vcc, -1, 0\nv_cndmask_b32 v167, v168, v172, vcc\n"
    "s_mov_b32 s37, 0xfffffb40\ns_cmp_lt_u32 s43, 1\ns_cselect_b32 s36, s37, 0x1f00\ns_add_u32 s35, s41, s36\ns_add_u32 s35, s35, 0x20300\nv_mov_b32 v166, s35\n"
    "v_cmp_gt_u32 vcc, 64, v224\ns_nop 1\nv_cndmask_b32 v15, v166, v167, vcc\ns_add_u32 s34, s41, 0x1fe40\nv_mov_b32 v165, s34\nv_cmp_gt_u32 vcc, 64, v230\ns_nop 1\n"
    "v_cndmask_b32 v35, v165, v226, vcc\ns_lshl_b32 s33, s18, 13\ns_lshl_b32 s32, s13, 11\ns_add_u32 s31, s33, s32\ns_cbranch_execz .Lhop\ns_add_u32 s31, s31, 0x800000\n"
    "v_lshl_add_u32 v37, v231, 4, s31\nv_lshrrev_b32 v231, 5, v0\nv_and_b32 v230, 3, v0\ns_lshl_b32 s43, s18, 6\ns_add_u32 s42, s43, 0xd80000\n"
    "v_lshl_add_u32 v38, v231, 14, s42\nv_lshl_add_u32 v38, v230, 4, v38\ns_setpc_b64 s[94:95]\n.Lhcon67_2:\ns_lshr_b32 s43, s13, 1\ns_lshl_b32 s42, s43, 3\n"
    "s_add_u32 s41, s42, 0x27fb0\nv_mov_b32 v24, s41\nv_add_u32 v231, 0xffffff86, v0\nv_lshrrev_b32 v230, 3, v231\nv_mul_u32_u24 v229, 0x50, v230\nv_and_b32 v228, 7, v231\n"
    "v_add_u32 v30, 0x1d004, v229\nv_lshl_add_u32 v30, v228, 3, v30\nv_add_u32 v227, 0xffffff87, v0\nv_lshrrev_b32 v226, 3, v227\nv_mul_u32_u24 v225, 0x50, v226\n"
    "v_and_b32 v224, 7, v227\nv_add_u32 v31, 0x1d000, v225\nv_lshl_add_u32 v31, v224, 3, v31\ns_and_b32 s40, s13, 1\nv_bfe_u32 v175, v0, 4, 1\n"
    "v_mul_u32_u24 v174, 0x480, v175\nv_mov_b32 v173, 0\ns_cmp_lt_u32 s40, 1\ns_cselect_b64 vcc, -1, 0\nv_cndmask_b32 v172, v173, v174, vcc\nv_bfe_u32 v171, v0, 1, 1\n"
    "v_mul_u32_u24 v170, 0x240, v171\nv_bfe_u32 v169, v0, 5, 1\nv_bfe_u32 v168, v0, 2, 2\nv_and_b32 v167, 1, v0\ns_mul_i32 s39, s43, 0x3180\ns_add_u32 s38, s39, 0x20300\n"
    "v_add_u32 v27, s38, v172\nv_add_u32 v27, v27, v170\nv_lshl_add_u32 v27, v169, 7, v27\nv_lshl_add_u32 v27, v168, 4, v27\nv_lshl_add_u32 v27, v167, 3, v27\n"
    "v_and_b32 v166, 3, v0\ns_add_u32 s37, s39, 0x22280\nv_lshl_add_u32 v29, v169, 8, s37\nv_lshl_add_u32 v29, v168, 5, v29\nv_lshl_add_u32 v29, v166, 3, v29\n"
    "v_add_u32 v28, s37, v174\nv_lshl_add_u32 v28, v169, 8, v28\nv_lshl_add_u32 v28, v168, 5, v28\nv_lshl_add_u32 v28, v166, 3, v28\nv_and_b32 v165, 31, v0\n"
    "v_bfe_u32 v164, v0, 3, 1\nv_xor_b32 v163, v169, v164\ns_mul_i32 s36, s43, 0x2800\ns_add_u32 s35, s36, 0x18000\nv_lshl_add_u32 v32, v165, 4, s35\n"
    "v_lshl_add_u32 v32, v163, 3, v32\nv_xor_b32 v162, v169, v168\ns_add_u32 s34, s36, 0x18c00\nv_lshl_add_u32 v33, v165, 5, s34\nv_lshl_add_u32 v33, v162, 3, v33\n"
    "v_bfe_u32 v161, v0, 4, 2\nv_xor_b32 v160, v0, v161\ns_mov_b32 s33, 0x18400\nv_lshl_add_u32 v34, v160, 3, s33\nv_xor_b32 v36, 16, v33\ns_bitcmp0_b32 s13, 0\n"
    "s_cselect_b32 s30, 256, 0\nv_add_u32 v39, s30, v23\ns_setpc_b64 s[94:95]\n"
    ".Llstb7:\nv_mov_b32 v72, 1\nds_write_b32 v56, v72 offset:32\ns_mov_b32 s58, 2\ns_mov_b32 s57, 98304\ns_add_u32 s50, s50, 1\ns_and_b32 s50, s50, 7\n"
    "s_sub_u32 s62, s61, 16384\ns_max_i32 s62, s62, 0\ns_add_u32 s62, s8, s62\ns_addc_u32 s63, s9, 0\nv_and_b32 v251, 63, v0\nv_lshlrev_b32 v71, 4, v251\n"
    "v_bfe_u32 v250, v0, 2, 4\nv_and_b32 v249, 3, v0\ns_mov_b32 s43, 0x1002000\nv_lshl_add_u32 v57, v250, 7, s43\nv_lshl_add_u32 v57, v249, 4, v57\ns_lshl_b32 s42, s19, 7\n"
    "s_lshl_b32 s41, s20, 2\ns_add_u32 s40, s42, s41\ns_add_u32 s40, s40, 0x1004000\nv_mov_b32 v58, s40\ns_lshl_b32 s39, s19, 17\ns_lshl_b32 s38, s20, 11\n"
    "s_add_u32 s37, s39, s38\nv_lshl_add_u32 v79, v251, 4, s37\nv_mov_b32 v80, 0x1000000\ns_lshl_b32 s36, s25, 7\ns_add_u32 s35, s36, 0xe04000\n"
    "v_lshl_add_u32 v188, v250, 15, s35\nv_lshl_add_u32 v188, v249, 12, v188\ns_lshl_b32 s34, s18, 13\ns_add_u32 s33, s34, 0x201000\nv_lshl_add_u32 v186, v251, 4, s33\n"
    "v_bfe_u32 v248, v0, 3, 1\nv_and_b32 v247, 7, v0\ns_lshl_b32 s32, s2, 7\ns_add_u32 s31, s32, 0x400000\nv_lshl_add_u32 v187, v248, 23, s31\n"
    "v_lshl_add_u32 v187, v247, 4, v187\nv_mov_b64 v[180:181], -1\nv_mov_b64 v[182:183], -1\nglobal_load_dword v78, v80, s[4:5] sc1\ns_mov_b32 m0, 146432\n"
    "v_xor_b32 v72, 32, v71\nglobal_load_lds_dwordx4 v72, s[62:63] offset:2048\nv_xor_b32 v73, 48, v71\nglobal_load_lds_dwordx4 v73, s[62:63] offset:3072\n"
    "s_mov_b32 m0, 150528\nv_xor_b32 v72, 4160, v71\nglobal_load_lds_dwordx4 v72, s[62:63] offset:0\nv_xor_b32 v73, 4176, v71\n"
    "global_load_lds_dwordx4 v73, s[62:63] offset:1024\nv_xor_b32 v72, 4192, v71\nglobal_load_lds_dwordx4 v72, s[62:63] offset:2048\nv_xor_b32 v73, 4208, v71\n"
    "global_load_lds_dwordx4 v73, s[62:63] offset:3072\ns_mov_b32 m0, 154624\nv_xor_b32 v72, 8320, v71\nglobal_load_lds_dwordx4 v72, s[62:63] offset:0\n"
    "v_xor_b32 v73, 8336, v71\nglobal_load_lds_dwordx4 v73, s[62:63] offset:1024\nv_xor_b32 v72, 8352, v71\nglobal_load_lds_dwordx4 v72, s[62:63] offset:2048\n"
    "v_xor_b32 v73, 8368, v71\nglobal_load_lds_dwordx4 v73, s[62:63] offset:3072\ns_mov_b32 m0, 158720\nv_xor_b32 v72, 12480, v71\n"
    "global_load_lds_dwordx4 v72, s[62:63] offset:0\nv_xor_b32 v73, 12496, v71\nglobal_load_lds_dwordx4 v73, s[62:63] offset:1024\nv_xor_b32 v72, 12512, v71\n"
    "global_load_lds_dwordx4 v72, s[62:63] offset:2048\nv_xor_b32 v73, 12528, v71\nglobal_load_lds_dwordx4 v73, s[62:63] offset:3072\ns_call_b64 s[94:95], .Lhpre66\n"
    "s_setprio 2\n.Ll68:\nds_read_b128 v[74:77], v56 offset:96\ns_mul_i32 s59, s50, 196608\ns_mul_i32 s56, s50, 12288\ns_add_u32 s52, s66, s59\ns_addc_u32 s53, s67, 0\n"
    "s_mov_b32 m0, s56\ns_nop 0\ns_nop 0\ns_cbranch_execz .Lhop\nglobal_load_lds_dwordx4 v64, s[52:53] offset:0\nglobal_load_lds_dwordx4 v64, s[52:53] offset:1024\n"
    "global_load_lds_dwordx4 v64, s[52:53] offset:2048\ns_add_u32 s56, s56, 3072\ns_mov_b32 m0, s56\ns_add_u32 s52, s52, 49152\ns_addc_u32 s53, s53, 0\n"
    "global_load_lds_dwordx4 v64, s[52:53] offset:0\nglobal_load_lds_dwordx4 v64, s[52:53] offset:1024\nglobal_load_lds_dwordx4 v64, s[52:53] offset:2048\n"
    "s_add_u32 s56, s56, 3072\ns_mov_b32 m0, s56\ns_add_u32 s52, s52, 49152\ns_addc_u32 s53, s53, 0\nglobal_load_lds_dwordx4 v64, s[52:53] offset:0\n"
    "global_load_lds_dwordx4 v64, s[52:53] offset:1024\nglobal_load_lds_dwordx4 v64, s[52:53] offset:2048\ns_add_u32 s56, s56, 3072\ns_lshl_b32 s59, s50, 7\n"
    "s_add_u32 s54, s48, s59\ns_addc_u32 s55, s49, 0\ns_add_u32 s57, s57, 4096\ns_cmp_eq_u32 s57, 110592\ns_cselect_b32 s57, 98304, s57\ns_sub_u32 s61, s58, 4\n"
    "s_waitcnt lgkmcnt(0)\nv_min3_i32 v74, v74, v75, v76\nv_min_i32 v74, v74, v77\nv_cmp_gt_i32 vcc, s61, v74\ns_add_u32 s59, s58, -1\ns_cmp_lg_u32 s58, 2\n"
    "s_cbranch_scc1 .Ll72\ns_mov_b32 s59, s58\ns_waitcnt vmcnt(23)\n.Ll72:\nv_mov_b32 v70, s59\ns_mov_b64 exec, 1\ns_waitcnt vmcnt(25)\nds_max_i32 v56, v70\n"
    "s_mov_b64 exec, -1\ns_mov_b32 m0, s56\ns_add_u32 s52, s52, 49152\ns_addc_u32 s53, s53, 0\nglobal_load_lds_dwordx4 v64, s[52:53] offset:0\n"
    "global_load_lds_dwordx4 v64, s[52:53] offset:1024\nglobal_load_lds_dwordx4 v64, s[52:53] offset:2048\ns_add_u32 s56, s56, 3072\ns_cbranch_vccnz .Ll69\n.Ll70:\n"
    "s_add_u32 m0, s57, 0\ns_nop 0\nglobal_load_lds_dwordx4 v65, s[54:55] offset:0\nglobal_load_lds_dwordx4 v66, s[54:55] offset:1024\n"
    "global_load_lds_dwordx4 v67, s[54:55] offset:2048\nglobal_load_lds_dwordx4 v68, s[54:55] offset:3072\ns_add_u32 m0, s57, 12288\ns_add_u32 s54, s54, 32768\n"
    "s_addc_u32 s55, s55, 0\nglobal_load_lds_dwordx4 v65, s[54:55] offset:0\nglobal_load_lds_dwordx4 v66, s[54:55] offset:1024\n"
    "global_load_lds_dwordx4 v67, s[54:55] offset:2048\nglobal_load_lds_dwordx4 v68, s[54:55] offset:3072\ns_add_u32 m0, s57, 24576\ns_add_u32 s54, s54, 32768\n"
    "s_addc_u32 s55, s55, 0\nglobal_load_lds_dwordx4 v65, s[54:55] offset:0\nglobal_load_lds_dwordx4 v66, s[54:55] offset:1024\n"
    "global_load_lds_dwordx4 v67, s[54:55] offset:2048\nglobal_load_lds_dwordx4 v68, s[54:55] offset:3072\ns_add_u32 s59, s58, 0\nv_mov_b32 v70, s59\ns_mov_b64 exec, 1\n"
    "s_waitcnt vmcnt(24)\nds_max_i32 v56, v70\ns_mov_b64 exec, -1\ns_add_u32 m0, s57, 36864\ns_add_u32 s54, s54, 32768\ns_addc_u32 s55, s55, 0\n"
    "global_load_lds_dwordx4 v65, s[54:55] offset:0\nglobal_load_lds_dwordx4 v66, s[54:55] offset:1024\nglobal_load_lds_dwordx4 v67, s[54:55] offset:2048\n"
    "global_load_lds_dwordx4 v68, s[54:55] offset:3072\ns_cmp_lg_u32 s58, 12\ns_cbranch_scc1 .Ll73\nglobal_load_dwordx4 v[84:87], v57, s[4:5] sc1\n.Ll73:\n"
    "s_add_u32 s50, s50, 1\ns_and_b32 s50, s50, 7\ns_add_u32 s58, s58, 2\ns_cmp_lt_u32 s58, 16\ns_cbranch_scc1 .Ll68\nv_readfirstlane_b32 s11, v78\n"
    "s_mul_i32 s11, s11, 0x9E3779B1\ns_add_u32 s11, s11, 0x7F4A7C15\nv_xor_b32 v84, s11, v84\nv_xor_b32 v85, s11, v85\nv_xor_b32 v86, s11, v86\nv_xor_b32 v87, s11, v87\n"
    "v_or3_b32 v78, v84, v85, v86\nv_or_b32 v78, v78, v87\nv_cmp_ne_u32 vcc, 0, v78\ns_cmp_eq_u64 vcc, 0\ns_cselect_b32 s48, 18, 17\nv_mov_b32 v73, s48\ns_mov_b64 exec, 1\n"
    "ds_write_b32 v56, v73 offset:4\ns_mov_b64 exec, -1\ns_add_u32 s59, s58, -1\nv_mov_b32 v70, s59\ns_mov_b64 exec, 1\ns_waitcnt vmcnt(16)\nds_max_i32 v56, v70\n"
    "s_mov_b64 exec, -1\nglobal_store_dwordx4 v79, v[180:183], s[4:5] offset:0 sc1\nglobal_store_dwordx4 v79, v[180:183], s[4:5] offset:1024 sc1\nv_add_u32 v79, 0x8000, v79\n"
    "global_store_dwordx4 v79, v[180:183], s[4:5] offset:0 sc1\nglobal_store_dwordx4 v79, v[180:183], s[4:5] offset:1024 sc1\nv_add_u32 v79, 0x8000, v79\n"
    "global_store_dwordx4 v79, v[180:183], s[4:5] offset:0 sc1\nglobal_store_dwordx4 v79, v[180:183], s[4:5] offset:1024 sc1\nv_add_u32 v79, 0x8000, v79\n"
    "global_store_dwordx4 v79, v[180:183], s[4:5] offset:0 sc1\nglobal_store_dwordx4 v79, v[180:183], s[4:5] offset:1024 sc1\nv_add_u32 v79, 0xffffc000, v188\n"
    "v_mov_b32 v240, s11\ns_cbranch_vccnz .Ll73x\nglobal_load_dword v255, v79, s[4:5] sc1\ns_add_u32 s59, s58, 0\nv_mov_b32 v70, s59\ns_mov_b64 exec, 1\ns_waitcnt vmcnt(9)\n"
    "ds_max_i32 v56, v70\ns_mov_b64 exec, -1\ns_setprio 0\ns_waitcnt vmcnt(1)\n.Ll73y:\ns_mov_b64 exec, 1\nglobal_store_dword v58, v240, s[4:5] sc1\ns_mov_b64 exec, -1\n"
    "global_store_dwordx4 v186, v[180:183], s[4:5] offset:-4096 sc1\nglobal_store_dwordx4 v186, v[180:183], s[4:5] offset:-3072 sc1\n"
    "global_store_dwordx4 v186, v[180:183], s[4:5] offset:-2048 sc1\nglobal_store_dwordx4 v186, v[180:183], s[4:5] offset:-1024 sc1\n"
    "global_store_dwordx4 v186, v[180:183], s[4:5] offset:0 sc1\nglobal_store_dwordx4 v186, v[180:183], s[4:5] offset:1024 sc1\n"
    "global_store_dwordx4 v186, v[180:183], s[4:5] offset:2048 sc1\nglobal_store_dwordx4 v186, v[180:183], s[4:5] offset:3072 sc1\n"
    "global_store_dwordx4 v187, v[180:183], s[4:5] sc1\ns_branch .Lafl11\n.Ll69:\ns_mov_b32 s60, 0x100000\n.Ll69x:\nds_read_b128 v[74:77], v56 offset:96\n"
    "s_waitcnt lgkmcnt(0)\nv_min3_i32 v74, v74, v75, v76\nv_min_i32 v74, v74, v77\nv_cmp_gt_i32 vcc, s61, v74\ns_cbranch_vccz .Ll70\ns_sub_u32 s60, s60, 1\n"
    "s_cbranch_scc1 .Ll70\nv_mov_b32 v70, s58\ns_mov_b64 exec, 1\ns_waitcnt vmcnt(12)\nds_max_i32 v56, v70\ns_mov_b64 exec, -1\ns_sleep 1\ns_branch .Ll69x\n"
    ".Lhalo10:\nv_and_b32 v251, 0x7f, v0\ns_lshl_b32 s43, s18, 11\ns_add_u32 s42, s43, 0xe00000\nv_lshl_add_u32 v57, v251, 4, s42\ns_lshr_b32 s41, s2, 4\n"
    "s_lshl_b32 s40, s41, 7\ns_and_b32 s39, s2, 15\ns_lshl_b32 s38, s39, 2\ns_add_u32 s37, s40, s38\ns_cbranch_execz .Lhop\ns_add_u32 s37, s37, 0x1002000\n"
    "v_mov_b32 v177, s37\nv_mov_b32 v178, 0x27f98\nv_bfe_u32 v251, v0, 5, 1\nv_mul_u32_u24 v250, 0xc00, v251\nv_bfe_u32 v249, v0, 4, 1\nv_bfe_u32 v248, v0, 2, 2\n"
    "v_and_b32 v247, 3, v0\ns_lshl_b32 s43, s13, 10\ns_add_u32 s42, s43, 0xfffffc00\nv_add_u32 v180, s42, v250\nv_lshl_add_u32 v180, v249, 9, v180\n"
    "v_lshl_add_u32 v180, v248, 6, v180\nv_lshl_add_u32 v180, v247, 3, v180\nv_lshl_add_u32 v180, v249, 5, v180\nv_and_b32 v246, 15, v0\nv_bfe_u32 v245, v0, 4, 2\n"
    "v_xor_b32 v244, v245, v246\ns_mov_b32 s41, 0x23c00\nv_lshl_add_u32 v181, v246, 10, s41\nv_lshl_add_u32 v181, v244, 4, v181\ns_lshl_b32 s40, s13, 7\n"
    "s_add_u32 s39, s40, 0x27b80\nv_lshl_add_u32 v58, v245, 4, s39\nv_mul_u32_u24 v243, 0xd0, v246\ns_lshl_b32 s38, s13, 6\ns_add_u32 s37, s38, 0x17fc0\n"
    "v_add_u32 v184, s37, v243\nv_lshl_add_u32 v184, v245, 3, v184\ns_mov_b32 s36, 0x27f80\nv_lshl_add_u32 v185, v247, 2, s36\ns_add_u32 s44, s23, 0\ns_and_b32 s44, s44, 7\n"
    "s_mul_i32 s45, s44, 12288\ns_lshl_b32 s44, s44, 7\nv_add_u32 v192, s45, v180\nv_xor_b32 v208, s44, v181\nv_xor_b32 v193, 32, v192\nv_xor_b32 v209, 64, v208\n"
    "s_add_u32 s44, s23, 1\ns_and_b32 s44, s44, 7\ns_mul_i32 s45, s44, 12288\ns_lshl_b32 s44, s44, 7\nv_add_u32 v194, s45, v180\nv_xor_b32 v210, s44, v181\n"
    "v_xor_b32 v195, 32, v194\nv_xor_b32 v211, 64, v210\ns_add_u32 s44, s23, 2\ns_and_b32 s44, s44, 7\ns_mul_i32 s45, s44, 12288\ns_lshl_b32 s44, s44, 7\n"
    "v_add_u32 v196, s45, v180\nv_xor_b32 v212, s44, v181\nv_xor_b32 v197, 32, v196\nv_xor_b32 v213, 64, v212\ns_add_u32 s44, s23, 3\ns_and_b32 s44, s44, 7\n"
    "s_mul_i32 s45, s44, 12288\ns_lshl_b32 s44, s44, 7\nv_add_u32 v198, s45, v180\nv_xor_b32 v214, s44, v181\nv_xor_b32 v199, 32, v198\nv_xor_b32 v215, 64, v214\n"
    "s_add_u32 s44, s23, 4\ns_and_b32 s44, s44, 7\ns_mul_i32 s45, s44, 12288\ns_lshl_b32 s44, s44, 7\nv_add_u32 v200, s45, v180\nv_xor_b32 v216, s44, v181\n"
    "v_xor_b32 v201, 32, v200\nv_xor_b32 v217, 64, v216\ns_add_u32 s44, s23, 5\ns_and_b32 s44, s44, 7\ns_mul_i32 s45, s44, 12288\ns_lshl_b32 s44, s44, 7\n"
    "v_add_u32 v202, s45, v180\nv_xor_b32 v218, s44, v181\nv_xor_b32 v203, 32, v202\nv_xor_b32 v219, 64, v218\ns_add_u32 s44, s23, 6\ns_and_b32 s44, s44, 7\n"
    "s_mul_i32 s45, s44, 12288\ns_lshl_b32 s44, s44, 7\nv_add_u32 v204, s45, v180\nv_xor_b32 v220, s44, v181\nv_xor_b32 v205, 32, v204\nv_xor_b32 v221, 64, v220\n"
    "s_add_u32 s44, s23, 7\ns_and_b32 s44, s44, 7\ns_mul_i32 s45, s44, 12288\ns_lshl_b32 s44, s44, 7\nv_add_u32 v206, s45, v180\nv_xor_b32 v222, s44, v181\n"
    "v_xor_b32 v207, 32, v206\nv_xor_b32 v223, 64, v222\ns_cmp_eq_u32 s13, 1\ns_cbranch_scc1 .Lh74\ns_waitcnt vmcnt(0)\nv_lshl_or_b32 v168, v169, 16, v168\n"
    "v_lshl_or_b32 v169, v171, 16, v170\nv_lshl_or_b32 v170, v173, 16, v172\nv_lshl_or_b32 v171, v175, 16, v174\nv_readfirstlane_b32 s11, v166\n"
    "global_store_dwordx4 v57, v[168:171], s[4:5] sc1\ns_mul_i32 s11, s11, 0x9E3779B1\ns_add_u32 s11, s11, 0x7F4A7C15\n.Lh74:\ns_call_b64 s[94:95], .Lhpre66\n"
    "s_mul_i32 s88, s13, 12\ns_add_u32 s88, s88, -8\ns_lshl_b32 s90, s88, 10\ns_add_u32 s90, s92, s90\ns_addc_u32 s91, s93, 0\ns_sub_u32 s89, 12, 1\ns_mov_b64 exec, 0\n"
    "s_swappc_b64 s[94:95], s[90:91]\n.Lz208:\nv_mov_b32 v176, 1\nv_mov_b32 v179, s11\ns_mov_b64 exec, 1\ns_waitcnt vmcnt(0)\nds_write_b32 v25, v176 offset:16\n"
    "s_mov_b64 exec, -1\ns_cmp_lg_u32 s13, 3\ns_cbranch_scc1 .Lh75\ns_mov_b32 s86, 0x100000\n.Ls76:\nds_read_b32 v176, v178 offset:0\ns_waitcnt lgkmcnt(0)\n"
    "v_cmp_gt_i32 vcc, 1, v176\ns_sub_u32 s86, s86, 1\ns_cbranch_scc1 .Ls77\ns_cbranch_vccz .Ls77\ns_sleep 1\ns_branch .Ls76\n.Ls77:\ns_mov_b64 exec, 1\n"
    "global_store_dword v177, v179, s[4:5] sc1\ns_mov_b64 exec, -1\n.Lh75:\ns_mov_b32 s48, 0\ns_cmp_lt_i32 s48, 3\ns_cbranch_scc1 .Lc78\n.Lc79:\nds_read_b128 v[64:67], v208\n"
    "ds_read_b128 v[68:71], v209\nds_read_b128 v[72:75], v210\nds_read_b128 v[76:79], v211\nds_read_b64_tr_b16 v[128:129], v192 offset:0\n"
    "ds_read_b64_tr_b16 v[130:131], v192 offset:256\nds_read_b64_tr_b16 v[132:133], v193 offset:0\nds_read_b64_tr_b16 v[134:135], v193 offset:256\n"
    "ds_read_b64_tr_b16 v[136:137], v192 offset:6144\nds_read_b64_tr_b16 v[138:139], v192 offset:6400\nds_read_b64_tr_b16 v[140:141], v193 offset:6144\n"
    "ds_read_b64_tr_b16 v[142:143], v193 offset:6400\ns_waitcnt lgkmcnt(0)\ns_setprio 3\nv_mfma_f32_16x16x32_bf16 v[152:155], v[64:67], v[64:67], 0\n"
    "v_mfma_f32_16x16x32_bf16 v[152:155], v[68:71], v[68:71], v[152:155]\nv_mfma_f32_16x16x32_bf16 v[152:155], v[72:75], v[72:75], v[152:155]\n"
    "v_mfma_f32_16x16x32_bf16 v[152:155], v[76:79], v[76:79], v[152:155]\nv_mfma_f32_16x16x32_bf16 v[144:147], v[128:131], v[64:67], 0\n"
    "v_mfma_f32_16x16x32_bf16 v[148:151], v[132:135], v[64:67], 0\nv_mfma_f32_16x16x32_bf16 v[144:147], v[136:139], v[68:71], v[144:147]\n"
    "v_mfma_f32_16x16x32_bf16 v[148:151], v[140:143], v[68:71], v[148:151]\ns_setprio 0\ns_cmp_lt_i32 s48, 3\ns_cbranch_scc1 .Lc81\n.Lc82:\nds_read_b128 v[80:83], v212\n"
    "ds_read_b128 v[84:87], v213\nds_read_b128 v[88:91], v214\nds_read_b128 v[92:95], v215\nds_read_b64_tr_b16 v[128:129], v194 offset:0\n"
    "ds_read_b64_tr_b16 v[130:131], v194 offset:256\nds_read_b64_tr_b16 v[132:133], v195 offset:0\nds_read_b64_tr_b16 v[134:135], v195 offset:256\n"
    "ds_read_b64_tr_b16 v[136:137], v194 offset:6144\nds_read_b64_tr_b16 v[138:139], v194 offset:6400\nds_read_b64_tr_b16 v[140:141], v195 offset:6144\n"
    "ds_read_b64_tr_b16 v[142:143], v195 offset:6400\ns_waitcnt lgkmcnt(0)\ns_setprio 3\nv_mfma_f32_16x16x32_bf16 v[152:155], v[80:83], v[80:83], v[152:155]\n"
    "v_mfma_f32_16x16x32_bf16 v[152:155], v[84:87], v[84:87], v[152:155]\ns_nop 0\ns_cbranch_execz .Lhop\nv_mfma_f32_16x16x32_bf16 v[152:155], v[88:91], v[88:91], v[152:155]\n"
    "v_mfma_f32_16x16x32_bf16 v[152:155], v[92:95], v[92:95], v[152:155]\nv_mfma_f32_16x16x32_bf16 v[144:147], v[128:131], v[72:75], v[144:147]\n"
    "v_mfma_f32_16x16x32_bf16 v[148:151], v[132:135], v[72:75], v[148:151]\nv_mfma_f32_16x16x32_bf16 v[144:147], v[136:139], v[76:79], v[144:147]\n"
    "v_mfma_f32_16x16x32_bf16 v[148:151], v[140:143], v[76:79], v[148:151]\ns_setprio 0\ns_cmp_lt_i32 s48, 5\ns_cbranch_scc1 .Lc84\n.Lc85:\nds_read_b128 v[96:99], v216\n"
    "ds_read_b128 v[100:103], v217\nds_read_b128 v[104:107], v218\nds_read_b128 v[108:111], v219\nds_read_b64_tr_b16 v[128:129], v196 offset:0\n"
    "ds_read_b64_tr_b16 v[130:131], v196 offset:256\nds_read_b64_tr_b16 v[132:133], v197 offset:0\nds_read_b64_tr_b16 v[134:135], v197 offset:256\n"
    "ds_read_b64_tr_b16 v[136:137], v196 offset:6144\nds_read_b64_tr_b16 v[138:139], v196 offset:6400\nds_read_b64_tr_b16 v[140:141], v197 offset:6144\n"
    "ds_read_b64_tr_b16 v[142:143], v197 offset:6400\ns_waitcnt lgkmcnt(0)\ns_setprio 3\nv_mfma_f32_16x16x32_bf16 v[152:155], v[96:99], v[96:99], v[152:155]\n"
    "v_mfma_f32_16x16x32_bf16 v[152:155], v[100:103], v[100:103], v[152:155]\nv_mfma_f32_16x16x32_bf16 v[152:155], v[104:107], v[104:107], v[152:155]\n"
    "v_mfma_f32_16x16x32_bf16 v[152:155], v[108:111], v[108:111], v[152:155]\nv_mfma_f32_16x16x32_bf16 v[144:147], v[128:131], v[80:83], v[144:147]\n"
    "v_mfma_f32_16x16x32_bf16 v[148:151], v[132:135], v[80:83], v[148:151]\nv_mfma_f32_16x16x32_bf16 v[144:147], v[136:139], v[84:87], v[144:147]\n"
    "v_mfma_f32_16x16x32_bf16 v[148:151], v[140:143], v[84:87], v[148:151]\ns_setprio 0\ns_cmp_lt_i32 s48, 7\ns_cbranch_scc1 .Lc87\n.Lc88:\nds_read_b128 v[112:115], v220\n"
    "ds_read_b128 v[116:119], v221\nds_read_b128 v[120:123], v222\nds_read_b128 v[124:127], v223\nds_read_b64_tr_b16 v[128:129], v198 offset:0\n"
    "ds_read_b64_tr_b16 v[130:131], v198 offset:256\nds_read_b64_tr_b16 v[132:133], v199 offset:0\nds_read_b64_tr_b16 v[134:135], v199 offset:256\n"
    "ds_read_b64_tr_b16 v[136:137], v198 offset:6144\nds_read_b64_tr_b16 v[138:139], v198 offset:6400\nds_read_b64_tr_b16 v[140:141], v199 offset:6144\n"
    "ds_read_b64_tr_b16 v[142:143], v199 offset:6400\ns_waitcnt lgkmcnt(0)\ns_setprio 3\nv_mfma_f32_16x16x32_bf16 v[152:155], v[112:115], v[112:115], v[152:155]\n"
    "v_mfma_f32_16x16x32_bf16 v[152:155], v[116:119], v[116:119], v[152:155]\nv_mfma_f32_16x16x32_bf16 v[152:155], v[120:123], v[120:123], v[152:155]\n"
    "v_mfma_f32_16x16x32_bf16 v[152:155], v[124:127], v[124:127], v[152:155]\nv_mfma_f32_16x16x32_bf16 v[144:147], v[128:131], v[88:91], v[144:147]\n"
    "v_mfma_f32_16x16x32_bf16 v[148:151], v[132:135], v[88:91], v[148:151]\nv_mfma_f32_16x16x32_bf16 v[144:147], v[136:139], v[92:95], v[144:147]\n"
    "v_mfma_f32_16x16x32_bf16 v[148:151], v[140:143], v[92:95], v[148:151]\ns_setprio 0\ns_cmp_lt_i32 s48, 9\ns_cbranch_scc1 .Lc90\n.Lc91:\n"
    "ds_read_b64_tr_b16 v[128:129], v200 offset:0\nds_read_b64_tr_b16 v[130:131], v200 offset:256\nds_read_b64_tr_b16 v[132:133], v201 offset:0\n"
    "ds_read_b64_tr_b16 v[134:135], v201 offset:256\nds_read_b64_tr_b16 v[136:137], v200 offset:6144\nds_read_b64_tr_b16 v[138:139], v200 offset:6400\n"
    "ds_read_b64_tr_b16 v[140:141], v201 offset:6144\nds_read_b64_tr_b16 v[142:143], v201 offset:6400\ns_waitcnt lgkmcnt(0)\ns_setprio 3\n"
    "v_mfma_f32_16x16x32_bf16 v[144:147], v[128:131], v[96:99], v[144:147]\nv_mfma_f32_16x16x32_bf16 v[148:151], v[132:135], v[96:99], v[148:151]\n"
    "v_mfma_f32_16x16x32_bf16 v[144:147], v[136:139], v[100:103], v[144:147]\nv_mfma_f32_16x16x32_bf16 v[148:151], v[140:143], v[100:103], v[148:151]\ns_setprio 0\n"
    "s_mov_b32 s44, 0xaaaaaaaa\ns_mov_b32 s45, s44\ns_mov_b32 s46, 0xcccccccc\ns_mov_b32 s47, s46\nv_cndmask_b32 v156, v152, v153, s[44:45]\n"
    "v_cndmask_b32 v157, v154, v155, s[44:45]\ns_mov_b32 s44, 0x00f0000f\ns_mov_b32 s45, 0xf0000f00\nv_cndmask_b32 v156, v156, v157, s[46:47]\ns_mov_b64 s[46:47], 0xffff\n"
    "v_cndmask_b32 v156, 0, v156, s[44:45]\nv_cndmask_b32 v157, 0, v153, s[46:47]\ns_mov_b32 s44, 0x3b000000\ns_nop 0\nv_permlane32_swap_b32 v156, v157\n"
    "v_add_f32 v158, v156, v157\nv_add_f32 v159, v156, v157\ns_nop 1\nv_permlane16_swap_b32 v158, v159\nv_add_f32 v156, v158, v159\nv_add_f32 v157, v158, v159\ns_nop 1\n"
    "v_permlane32_swap_b32 v156, v157\nv_mul_f32 v157, s44, v157\nv_mul_f32 v158, v157, v157\nv_fma_f32 v158, v156, s44, -v158\nv_max_f32 v158, 0, v158\n"
    "v_cvt_pk_bf16_f32 v158, 0, v158\nv_add_f32 v158, 0x3727c5ac, v158\nv_cvt_pk_bf16_f32 v158, 0, v158\nv_sqrt_f32 v158, v158\nv_cvt_pk_bf16_f32 v157, 0, -v157\n"
    "v_cvt_pk_bf16_f32 v158, 0, v158\nv_rcp_f32 v158, v158\nv_mov_b32 v159, 1\ns_mov_b32 s46, 0xffff0000\ns_mov_b32 s44, 0x00020002\ns_mov_b32 s45, s44\ns_cmp_lt_i32 s48, 11\n"
    "s_cbranch_scc1 .Lc93\n.Lc94:\nds_read_b64_tr_b16 v[128:129], v202 offset:0\nds_read_b64_tr_b16 v[130:131], v202 offset:256\nds_read_b64_tr_b16 v[132:133], v203 offset:0\n"
    "ds_read_b64_tr_b16 v[134:135], v203 offset:256\nds_read_b64_tr_b16 v[136:137], v202 offset:6144\nds_read_b64_tr_b16 v[138:139], v202 offset:6400\n"
    "ds_read_b64_tr_b16 v[140:141], v203 offset:6144\nds_read_b64_tr_b16 v[142:143], v203 offset:6400\ns_waitcnt lgkmcnt(0)\ns_setprio 3\n"
    "v_mfma_f32_16x16x32_bf16 v[144:147], v[128:131], v[104:107], v[144:147]\nv_mfma_f32_16x16x32_bf16 v[148:151], v[132:135], v[104:107], v[148:151]\n"
    "v_mfma_f32_16x16x32_bf16 v[144:147], v[136:139], v[108:111], v[144:147]\nv_mfma_f32_16x16x32_bf16 v[148:151], v[140:143], v[108:111], v[148:151]\ns_setprio 0\n"
    "s_cmp_lt_i32 s48, 13\ns_cbranch_scc1 .Lc96\n.Lc97:\nds_read_b64_tr_b16 v[128:129], v204 offset:0\nds_read_b64_tr_b16 v[130:131], v204 offset:256\n"
    "ds_read_b64_tr_b16 v[132:133], v205 offset:0\nds_read_b64_tr_b16 v[134:135], v205 offset:256\nds_read_b64_tr_b16 v[136:137], v204 offset:6144\n"
    "ds_read_b64_tr_b16 v[138:139], v204 offset:6400\nds_read_b64_tr_b16 v[140:141], v205 offset:6144\nds_read_b64_tr_b16 v[142:143], v205 offset:6400\ns_waitcnt lgkmcnt(0)\n"
    "s_setprio 3\nv_mfma_f32_16x16x32_bf16 v[144:147], v[128:131], v[112:115], v[144:147]\nv_mfma_f32_16x16x32_bf16 v[148:151], v[132:135], v[112:115], v[148:151]\n"
    "v_mfma_f32_16x16x32_bf16 v[144:147], v[136:139], v[116:119], v[144:147]\nv_mfma_f32_16x16x32_bf16 v[148:151], v[140:143], v[116:119], v[148:151]\ns_setprio 0\n"
    "s_cmp_lt_i32 s48, 15\ns_cbranch_scc1 .Lc99\n.Lc100:\nds_read_b32 v156, v56 offset:4\nds_read_b64_tr_b16 v[128:129], v206 offset:0\n"
    "ds_read_b64_tr_b16 v[130:131], v206 offset:256\ns_cbranch_execz .Lhop\nds_read_b64_tr_b16 v[132:133], v207 offset:0\nds_read_b64_tr_b16 v[134:135], v207 offset:256\n"
    "ds_read_b64_tr_b16 v[136:137], v206 offset:6144\nds_read_b64_tr_b16 v[138:139], v206 offset:6400\nds_read_b64_tr_b16 v[140:141], v207 offset:6144\n"
    "ds_read_b64_tr_b16 v[142:143], v207 offset:6400\ns_waitcnt lgkmcnt(0)\ns_setprio 3\nv_mfma_f32_16x16x32_bf16 v[144:147], v[128:131], v[120:123], v[144:147]\n"
    "v_mfma_f32_16x16x32_bf16 v[148:151], v[132:135], v[120:123], v[148:151]\nv_mfma_f32_16x16x32_bf16 v[144:147], v[136:139], v[124:127], v[144:147]\n"
    "v_mfma_f32_16x16x32_bf16 v[148:151], v[140:143], v[124:127], v[148:151]\ns_setprio 3\nv_readfirstlane_b32 s48, v156\ns_nop 4\nv_and_b32 v160, s46, v144\n"
    "v_and_b32 v161, s46, v145\nv_and_b32 v162, s46, v146\nv_and_b32 v163, s46, v147\nv_and_b32 v164, s46, v148\nv_and_b32 v165, s46, v149\nv_and_b32 v166, s46, v150\n"
    "v_and_b32 v167, s46, v151\nv_sub_f32 v168, v144, v160\nv_sub_f32 v169, v145, v161\nv_sub_f32 v170, v146, v162\nv_sub_f32 v171, v147, v163\nv_sub_f32 v172, v148, v164\n"
    "v_sub_f32 v173, v149, v165\nv_sub_f32 v174, v150, v166\nv_sub_f32 v175, v151, v167\nv_cvt_pk_bf16_f32 v160, v160, v168\nv_cvt_pk_bf16_f32 v161, v161, v169\n"
    "v_cvt_pk_bf16_f32 v162, v162, v170\nv_cvt_pk_bf16_f32 v163, v163, v171\nv_cvt_pk_bf16_f32 v164, v164, v172\nv_cvt_pk_bf16_f32 v165, v165, v173\n"
    "v_cvt_pk_bf16_f32 v166, v166, v174\nv_cvt_pk_bf16_f32 v167, v167, v175\ns_mov_b64 exec, s[44:45]\nds_write_b128 v58, v[160:163]\nds_write_b128 v58, v[164:167] offset:64\n"
    "ds_write_b32 v25, v159 offset:32\ns_mov_b64 exec, -1\ns_setprio 0\n"
    ".Lafl11:\nglobal_load_dwordx4 v[224:227], v47, s[8:9] offset:0\nglobal_load_dwordx4 v[228:231], v47, s[8:9] offset:32\ns_mov_b32 s44, 0x00ff00ff\n"
    "s_mov_b32 s45, 0xff00ff00\ns_mov_b32 s46, s44\ns_mov_b32 s47, s44\ns_sub_u32 s26, s48, 17\ns_cmp_eq_u32 s26, 0\ns_cbranch_scc1 .La102\n"
    "global_load_dwordx4 v[64:67], v59, s[68:69] offset:-4096 sc1\nglobal_load_dwordx4 v[68:71], v59, s[68:69] offset:-3072 sc1\n"
    "global_load_dwordx4 v[72:75], v59, s[68:69] offset:-2048 sc1\nglobal_load_dwordx4 v[76:79], v59, s[68:69] offset:-1024 sc1\n"
    "global_load_dwordx4 v[80:83], v59, s[68:69] offset:0 sc1\nglobal_load_dwordx4 v[84:87], v59, s[68:69] offset:1024 sc1\n"
    "global_load_dwordx4 v[88:91], v59, s[68:69] offset:2048 sc1\nglobal_load_dwordx4 v[92:95], v59, s[68:69] offset:3072 sc1\n"
    "global_load_dwordx4 v[96:99], v59, s[70:71] offset:-4096 sc1\nglobal_load_dwordx4 v[100:103], v59, s[70:71] offset:-3072 sc1\n"
    "global_load_dwordx4 v[104:107], v59, s[70:71] offset:-2048 sc1\nglobal_load_dwordx4 v[108:111], v59, s[70:71] offset:-1024 sc1\n"
    "global_load_dwordx4 v[112:115], v59, s[70:71] offset:0 sc1\nglobal_load_dwordx4 v[116:119], v59, s[70:71] offset:1024 sc1\n"
    "global_load_dwordx4 v[120:123], v59, s[70:71] offset:2048 sc1\nglobal_load_dwordx4 v[124:127], v59, s[70:71] offset:3072 sc1\n.La104:\ns_cmp_lt_u32 s13, 2\n"
    "s_cbranch_scc1 .La105\nv_fmac_f32_dpp v144, v144, v157 row_newbcast:1 row_mask:0xf bank_mask:0xf\n"
    "v_fmac_f32_dpp v145, v145, v157 row_newbcast:1 row_mask:0xf bank_mask:0xf\nv_fmac_f32_dpp v146, v146, v157 row_newbcast:1 row_mask:0xf bank_mask:0xf\n"
    "v_fmac_f32_dpp v147, v147, v157 row_newbcast:1 row_mask:0xf bank_mask:0xf\nv_fmac_f32_dpp v148, v148, v157 row_newbcast:1 row_mask:0xf bank_mask:0xf\n"
    "v_fmac_f32_dpp v149, v149, v157 row_newbcast:1 row_mask:0xf bank_mask:0xf\nv_fmac_f32_dpp v150, v150, v157 row_newbcast:1 row_mask:0xf bank_mask:0xf\n"
    "v_fmac_f32_dpp v151, v151, v157 row_newbcast:1 row_mask:0xf bank_mask:0xf\nv_mul_f32 v144, v144, v158\nv_mul_f32 v145, v145, v158\nv_mul_f32 v146, v146, v158\n"
    "v_mul_f32 v147, v147, v158\nv_mul_f32 v148, v148, v158\nv_mul_f32 v149, v149, v158\nv_mul_f32 v150, v150, v158\nv_mul_f32 v151, v151, v158\n"
    "v_cvt_pk_bf16_f32 v144, v144, v145\nv_cvt_pk_bf16_f32 v145, v146, v147\nv_cvt_pk_bf16_f32 v146, v148, v149\nv_cvt_pk_bf16_f32 v147, v150, v151\ns_mov_b32 s86, 0x100000\n"
    ".Ls107:\nds_read_b32 v240, v185 offset:96\ns_waitcnt lgkmcnt(0)\nv_cmp_gt_i32 vcc, 16, v240\ns_sub_u32 s86, s86, 1\ns_cbranch_scc1 .Ls108\ns_cbranch_vccz .Ls108\n"
    "s_sleep 1\ns_branch .Ls107\n.Ls108:\nds_write2_b64 v184, v[144:145], v[146:147] offset1:4\ns_branch .La106\n.Lc78:\ns_mov_b32 s86, 0x100000\n.Lc80:\n"
    "ds_read_b32 v156, v56\ns_waitcnt lgkmcnt(0)\nv_readfirstlane_b32 s48, v156\ns_sub_u32 s86, s86, 1\ns_cbranch_scc1 .Lc79\ns_cmp_ge_i32 s48, 3\ns_cbranch_scc1 .Lc79\n"
    "s_sleep 1\ns_branch .Lc80\n.Lc81:\ns_mov_b32 s86, 0x100000\n.Lc83:\nds_read_b32 v176, v56\ns_waitcnt lgkmcnt(0)\nv_readfirstlane_b32 s48, v176\ns_sub_u32 s86, s86, 1\n"
    "s_cbranch_scc1 .Lc82\ns_cmp_ge_i32 s48, 3\ns_cbranch_scc1 .Lc82\ns_sleep 1\ns_branch .Lc83\n.Lc84:\ns_mov_b32 s86, 0x100000\n.Lc86:\nds_read_b32 v176, v56\n"
    "s_waitcnt lgkmcnt(0)\nv_readfirstlane_b32 s48, v176\ns_sub_u32 s86, s86, 1\ns_cbranch_scc1 .Lc85\ns_cmp_ge_i32 s48, 5\ns_cbranch_scc1 .Lc85\ns_sleep 1\ns_branch .Lc86\n"
    ".Lc87:\ns_mov_b32 s86, 0x100000\n.Lc89:\nds_read_b32 v176, v56\ns_waitcnt lgkmcnt(0)\nv_readfirstlane_b32 s48, v176\ns_sub_u32 s86, s86, 1\ns_cbranch_scc1 .Lc88\n"
    "s_cmp_ge_i32 s48, 7\ns_cbranch_scc1 .Lc88\ns_sleep 1\ns_branch .Lc89\n.Lc90:\ns_mov_b32 s86, 0x100000\n.Lc92:\nds_read_b32 v176, v56\ns_waitcnt lgkmcnt(0)\n"
    "v_readfirstlane_b32 s48, v176\ns_sub_u32 s86, s86, 1\ns_cbranch_scc1 .Lc91\ns_cmp_ge_i32 s48, 9\ns_cbranch_scc1 .Lc91\ns_sleep 1\ns_branch .Lc92\n.Lc93:\n"
    "s_mov_b32 s86, 0x100000\n.Lc95:\nds_read_b32 v176, v56\ns_waitcnt lgkmcnt(0)\nv_readfirstlane_b32 s48, v176\ns_sub_u32 s86, s86, 1\ns_cbranch_scc1 .Lc94\n"
    "s_cmp_ge_i32 s48, 11\ns_cbranch_scc1 .Lc94\ns_sleep 1\ns_branch .Lc95\n.Lc96:\ns_mov_b32 s86, 0x100000\n.Lc98:\nds_read_b32 v176, v56\ns_waitcnt lgkmcnt(0)\n"
    "v_readfirstlane_b32 s48, v176\ns_sub_u32 s86, s86, 1\ns_cbranch_scc1 .Lc97\ns_cmp_ge_i32 s48, 13\ns_cbranch_scc1 .Lc97\ns_sleep 1\ns_branch .Lc98\n.Lc99:\n"
    "s_mov_b32 s86, 0x100000\n.Lc101:\nds_read_b32 v176, v56\ns_waitcnt lgkmcnt(0)\nv_readfirstlane_b32 s48, v176\ns_sub_u32 s86, s86, 1\ns_cbranch_scc1 .Lc100\n"
    "s_cmp_ge_i32 s48, 15\ns_cbranch_scc1 .Lc100\ns_sleep 1\ns_branch .Lc101\ns_cbranch_execz .Lhop\n.La105:\ns_xor_b32 s48, s26, 1\ns_or_b32 s48, s48, s13\n"
    "s_cbranch_scc1 .La106\nglobal_load_dword v255, v188, s[4:5] sc1\n.La106:\n"
    ".Latt12:\ns_setprio 1\ns_waitcnt lgkmcnt(0)\ns_barrier\nds_read_b128 v[192:195], v6\nds_read_b128 v[196:199], v6 offset:208\ns_mov_b64 exec, s[44:45]\n"
    "ds_read_b128 v[232:235], v7\nds_read_b128 v[236:239], v7 offset:208\ns_mov_b64 exec, -1\nds_read_b64_tr_b16 v[200:201], v8 offset:0\n"
    "ds_read_b64_tr_b16 v[202:203], v8 offset:32\nds_read_b64_tr_b16 v[204:205], v8 offset:208\nds_read_b64_tr_b16 v[206:207], v8 offset:240\ns_mov_b32 s48, 0x3eb8aa3b\n"
    "v_mov_b32 v220, 0x3f803f80\nv_mov_b32 v221, 0x3f803f80\nv_mov_b32 v222, 0x3f803f80\nv_mov_b32 v223, 0x3f803f80\ns_waitcnt lgkmcnt(5)\n"
    "v_mfma_f32_16x16x32_bf16 v[208:211], v[192:195], v[232:235], v[2:5]\ns_waitcnt lgkmcnt(4)\nv_mfma_f32_16x16x32_bf16 v[212:215], v[196:199], v[236:239], v[2:5]\ns_nop 5\n"
    "v_max3_f32 v216, v208, v209, v210\ns_nop 0\nv_max3_f32 v217, v212, v213, v214\nv_max_f32 v216, v216, v211\nv_max_f32 v217, v217, v215\ns_nop 1\n"
    "v_permlane16_swap_b32 v216, v217\nv_max_f32 v218, v216, v217\nv_max_f32 v217, v216, v217\ns_nop 1\nv_permlane32_swap_b32 v218, v217\nv_max_f32 v218, v218, v217\n"
    "v_mul_f32 v216, 0xbeb8aa3b, v218\nv_mul_f32 v217, 0xbeb8aa3b, v218\ns_nop 1\nv_permlane16_swap_b32 v216, v217\n"
    "v_pk_fma_f32 v[208:209], v[208:209], s[48:49], v[216:217] op_sel:[0,0,0] op_sel_hi:[1,0,0]\n"
    "v_pk_fma_f32 v[210:211], v[210:211], s[48:49], v[216:217] op_sel:[0,0,0] op_sel_hi:[1,0,0]\n"
    "v_pk_fma_f32 v[212:213], v[212:213], s[48:49], v[216:217] op_sel:[0,0,1] op_sel_hi:[1,0,1]\n"
    "v_pk_fma_f32 v[214:215], v[214:215], s[48:49], v[216:217] op_sel:[0,0,1] op_sel_hi:[1,0,1]\nv_exp_f32 v208, v208\nv_exp_f32 v209, v209\nv_exp_f32 v210, v210\n"
    "v_exp_f32 v211, v211\nv_exp_f32 v212, v212\nv_exp_f32 v213, v213\nv_exp_f32 v214, v214\nv_exp_f32 v215, v215\ns_mov_b64 exec, s[46:47]\n"
    "v_cvt_pk_bf16_f32 v244, v208, v209\nv_cvt_pk_bf16_f32 v245, v210, v211\nv_cvt_pk_bf16_f32 v248, v212, v213\nv_cvt_pk_bf16_f32 v249, v214, v215\ns_not_b64 exec, exec\n"
    "v_cvt_pk_bf16_f32 v246, v208, v209\nv_cvt_pk_bf16_f32 v247, v210, v211\nv_cvt_pk_bf16_f32 v250, v212, v213\nv_cvt_pk_bf16_f32 v251, v214, v215\ns_mov_b64 exec, -1\n"
    "s_waitcnt lgkmcnt(0)\nv_mfma_f32_16x16x32_bf16 v[208:211], v[220:223], v[244:247], 0\nv_mfma_f32_16x16x32_bf16 v[212:215], v[220:223], v[248:251], 0\n"
    "v_mfma_f32_16x16x32_bf16 v[192:195], v[200:203], v[244:247], 0\nv_mfma_f32_16x16x32_bf16 v[196:199], v[204:207], v[248:251], 0\ns_nop 4\nv_rcp_f32 v216, v208\n"
    "v_rcp_f32 v217, v212\ns_nop 0\nv_pk_mul_f32 v[192:193], v[192:193], v[216:217] op_sel:[0,0] op_sel_hi:[1,0]\n"
    "v_pk_mul_f32 v[194:195], v[194:195], v[216:217] op_sel:[0,0] op_sel_hi:[1,0]\nv_pk_mul_f32 v[196:197], v[196:197], v[216:217] op_sel:[0,1] op_sel_hi:[1,1]\n"
    "v_pk_mul_f32 v[198:199], v[198:199], v[216:217] op_sel:[0,1] op_sel_hi:[1,1]\nv_cvt_pk_bf16_f32 v200, v192, v193\nv_cvt_pk_bf16_f32 v201, v194, v195\n"
    "v_cvt_pk_bf16_f32 v202, v196, v197\nv_cvt_pk_bf16_f32 v203, v198, v199\ns_nop 0\nv_permlane16_swap_b32 v200, v202\nv_permlane16_swap_b32 v201, v203\n"
    "global_store_dwordx4 v9, v[200:203], s[4:5] sc1\ns_setprio 0\ns_cmp_gt_u32 s3, 3\ns_cbranch_scc1 .Lhb110\n"
    "s_cmp_lg_u32 s12, 0\ns_cbranch_scc1 .Lb109\ns_cmp_eq_u32 s27, 0\ns_cbranch_scc1 .Lb118\ns_mov_b32 s10, 64\ns_call_b64 s[94:95], .Lkgl13\n.Lb118:\n"
    "ds_write_b128 v33, v[40:43] offset:0\nds_write_b128 v33, v[44:47] offset:1024\nds_write_b128 v33, v[48:51] offset:2048\nds_write_b128 v33, v[52:55] offset:3072\n"
    "v_mov_b32 v240, v35\nv_mov_b32 v241, v39\nglobal_load_dwordx4 v[252:255], v39, s[4:5] sc1\ns_waitcnt lgkmcnt(0)\ns_barrier\ns_call_b64 s[94:95], .Lgsa119\n"
    "s_add_u32 s43, s12, 0x400000\nv_lshl_add_u32 v16, v0, 4, s43\ns_lshl_b32 s42, s18, 13\ns_add_u32 s41, s12, s42\ns_add_u32 s41, s41, 0x200000\n"
    "v_lshl_add_u32 v17, v0, 4, s41\nv_lshrrev_b32 v127, 3, v0\nv_mul_u32_u24 v126, 0x50, v127\nv_and_b32 v125, 7, v0\nv_add_u32 v18, 0x1d550, v126\n"
    "v_lshl_add_u32 v18, v125, 3, v18\nv_bfe_u32 v124, v0, 4, 3\nv_xor_b32 v123, v0, v124\ns_mov_b32 s40, 0x19400\nv_lshl_add_u32 v19, v123, 3, s40\nv_bfe_u32 v122, v0, 4, 1\n"
    "v_xor_b32 v121, v0, v122\ns_mov_b32 s39, 0x17c00\nv_lshl_add_u32 v20, v121, 3, s39\ns_lshr_b32 s43, s12, 23\ns_add_u32 s42, s43, 3\nv_lshrrev_b32 v127, s42, v0\n"
    "s_lshr_b32 s41, s12, 22\ns_sub_u32 s40, 15, s41\nv_lshlrev_b32 v126, s40, v127\ns_lshr_b32 s39, s12, 20\ns_add_u32 s38, s39, 7\nv_and_b32 v125, s38, v0\n"
    "s_lshl_b32 s37, s18, 7\ns_add_u32 s36, s37, 0x600000\ns_lshl_b32 s35, s24, 18\ns_lshl_b32 s34, s25, 8\ns_add_u32 s33, s35, s34\ns_add_u32 s33, s33, 0x1200000\n"
    "s_cmp_lt_u32 s12, 1\ns_cselect_b32 s32, s36, s33\nv_add_u32 v21, s32, v126\nv_lshl_add_u32 v21, v125, 4, v21\nv_and_b32 v127, 63, v0\nv_lshlrev_b32 v126, 4, v127\n"
    "v_mov_b32 v125, 0xfc0\ns_lshr_b32 s43, s13, 1\ns_mul_i32 s42, s43, 26\nv_add_u32 v124, s42, v127\nv_cmp_gt_u32 vcc, 64, v124\ns_nop 1\n"
    "v_cndmask_b32 v123, v125, v126, vcc\ns_lshr_b32 s41, s3, 2\ns_mul_i32 s40, s41, 0x1950\ns_mul_i32 s39, s13, 0x150\ns_add_u32 s38, s40, s39\ns_add_u32 s38, s38, 0x1d000\n"
    "s_mul_i32 s37, s41, 0x3180\ns_and_b32 s36, s13, 1\ns_mul_i32 s35, s36, 0xfc0\ns_add_u32 s34, s37, s35\ns_add_u32 s34, s34, 0x20300\ns_cmp_lt_u32 s13, 2\n"
    "s_cselect_b32 s33, s38, s34\nv_add_u32 v22, s33, v123\nv_and_b32 v122, 31, v0\n.Lg115:\ns_waitcnt vmcnt(0)\nv_xor_b32 v252, s11, v252\nv_xor_b32 v253, s11, v253\n"
    "v_xor_b32 v254, s11, v254\nv_xor_b32 v255, s11, v255\nv_or3_b32 v224, v252, v253, v254\nv_or_b32 v224, v224, v255\nv_cmp_ne_u32 vcc, 0, v224\ns_cbranch_vccz .Lg116\n"
    "s_sub_u32 s80, s80, 1\ns_cbranch_scc1 .Lg116\ns_sleep 2\nglobal_load_dwordx4 v[252:255], v241, s[4:5] sc1\ns_cbranch_execz .Lhop\ns_branch .Lg115\n.Lg116:\n"
    "s_mov_b32 s80, 0x10000\ns_mov_b32 s86, 0x100000\n.Ls120:\nds_read_b32 v244, v58 offset:64\ns_waitcnt lgkmcnt(0)\nv_cmp_gt_i32 vcc, 2, v244\ns_sub_u32 s86, s86, 1\n"
    "s_cbranch_scc1 .Ls121\ns_cbranch_vccz .Ls121\ns_sleep 1\ns_branch .Ls120\n.Ls121:\ns_sleep 6\ns_branch .Lgst112\n.Lgsa119:\ns_lshl_b32 s43, s21, 15\n"
    "s_add_u32 s42, s12, s43\nv_lshl_add_u32 v192, v0, 4, s42\ns_lshl_b32 s41, s13, 10\ns_add_u32 s51, s41, 0x1f600\ns_mov_b32 s40, 0x1f600\nv_lshl_add_u32 v57, v0, 4, s40\n"
    "v_and_b32 v239, 3, v0\ns_mov_b32 s39, 0x27f80\nv_lshl_add_u32 v58, v239, 2, s39\nv_and_b32 v238, 0x7f, v0\ns_lshr_b32 s38, s13, 1\ns_mul_i32 s37, s38, 0x2100\n"
    "s_add_u32 s36, s37, 0x18000\nv_lshl_add_u32 v59, v238, 4, s36\nv_and_b32 v237, 31, v0\ns_mov_b32 s35, 0x27600\nv_lshl_add_u32 v39, v237, 2, s35\ns_lshl_b32 s34, s22, 13\n"
    "s_lshl_b32 s33, s21, 7\ns_add_u32 s32, s12, s34\ns_add_u32 s32, s32, s33\ns_add_u32 s32, s32, 0x400000\nv_lshl_add_u32 v56, v237, 2, s32\nv_add_u32 v193, 4096, v192\n"
    "v_add_u32 v194, 8192, v192\nv_add_u32 v195, 12288, v192\nv_add_u32 v196, 16384, v192\nv_add_u32 v197, 20480, v192\nv_add_u32 v198, 24576, v192\n"
    "v_add_u32 v199, 28672, v192\ns_mov_b32 s80, 0x10000\ns_setpc_b64 s[94:95]\n.Lb109:\ns_call_b64 s[94:95], .Lgsa119\ns_barrier\nv_alignbit_b32 v228, v11, v10, 16\n"
    "v_alignbit_b32 v229, v12, v11, 16\nv_alignbit_b32 v230, v13, v12, 16\nv_alignbit_b32 v231, v14, v13, 16\nds_write_b128 v59, v[10:13]\nv_mov_b32 v232, v11\n"
    "v_mov_b32 v233, v12\nv_mov_b32 v234, v13\nv_mov_b32 v235, v14\nds_write_b128 v59, v[228:231] offset:2112\nv_alignbit_b32 v236, v12, v11, 16\n"
    "v_alignbit_b32 v237, v13, v12, 16\nv_alignbit_b32 v238, v14, v13, 16\nv_alignbit_b32 v239, v15, v14, 16\nds_write_b128 v59, v[232:235] offset:4224\n"
    "ds_write_b128 v59, v[236:239] offset:6336\ns_waitcnt vmcnt(0)\ns_sleep 7\n.Lgst112:\ns_add_u32 m0, s51, 0\ns_nop 2\nglobal_load_lds_dwordx4 v192, s[4:5] sc1\n"
    "s_add_u32 m0, s51, 4096\ns_nop 2\nglobal_load_lds_dwordx4 v193, s[4:5] sc1\ns_add_u32 m0, s51, 8192\ns_nop 2\nglobal_load_lds_dwordx4 v194, s[4:5] sc1\n"
    "s_add_u32 m0, s51, 12288\ns_nop 2\nglobal_load_lds_dwordx4 v195, s[4:5] sc1\ns_add_u32 m0, s51, 16384\ns_nop 2\nglobal_load_lds_dwordx4 v196, s[4:5] sc1\n"
    "s_add_u32 m0, s51, 20480\ns_nop 2\nglobal_load_lds_dwordx4 v197, s[4:5] sc1\ns_add_u32 m0, s51, 24576\ns_nop 2\nglobal_load_lds_dwordx4 v198, s[4:5] sc1\n"
    "s_add_u32 m0, s51, 28672\ns_nop 2\nglobal_load_lds_dwordx4 v199, s[4:5] sc1\ns_cmp_lg_u32 s12, 0\ns_cbranch_scc1 .Lb118y\nv_sub_u32 v121, 0x3ff, v122\n"
    "v_bfe_u32 v120, v0, 5, 1\nv_lshl_add_u32 v119, v120, 3, v121\nv_and_b32 v118, 3, v119\nv_mul_u32_u24 v117, 0x840, v118\nv_lshrrev_b32 v116, 2, v119\n"
    "s_mul_i32 s32, s43, 0x2100\ns_add_u32 s31, s32, 0x17800\nv_add_u32 v23, s31, v117\nv_lshl_add_u32 v23, v116, 3, v23\ns_lshl_b32 s43, s13, 2\ns_add_u32 s42, s43, 0x27f80\n"
    "v_mov_b32 v25, s42\ns_lshr_b32 s41, s12, 23\ns_add_u32 s40, s41, 4\nv_mov_b32 v26, s40\nv_and_b32 v127, 31, v0\nv_mul_u32_u24 v126, 0x50, v127\nv_bfe_u32 v125, v0, 5, 1\n"
    "s_lshr_b32 s39, s13, 1\ns_mul_i32 s38, s39, 0x1950\ns_and_b32 s37, s13, 1\ns_mul_i32 s36, s37, 0xa00\ns_add_u32 s35, s38, s36\ns_add_u32 s35, s35, 0x1d000\n"
    "v_add_u32 v27, s35, v126\nv_lshl_add_u32 v27, v125, 4, v27\nv_bfe_u32 v124, v0, 1, 3\nv_xor_b32 v123, v125, v124\ns_mul_i32 s34, s39, 0x2800\ns_lshl_b32 s33, s37, 11\n"
    "s_add_u32 s32, s34, s33\ns_add_u32 s32, s32, 0x19400\nv_lshl_add_u32 v28, v127, 6, s32\nv_lshl_add_u32 v28, v123, 3, v28\nv_xor_b32 v29, 16, v28\nv_xor_b32 v30, 32, v28\n"
    "v_xor_b32 v31, 48, v28\ns_lshr_b32 s43, s12, 23\ns_lshl_b32 s42, s43, 13\ns_lshr_b32 s41, s18, 4\ns_lshl_b32 s40, s41, 7\ns_and_b32 s39, s18, 15\ns_lshl_b32 s38, s39, 2\n"
    "s_add_u32 s37, s42, s40\ns_add_u32 s37, s37, s38\ns_add_u32 s37, s37, 0x1008000\nv_mov_b32 v32, s37\nv_bfe_u32 v127, v0, 2, 4\nv_and_b32 v126, 3, v0\n"
    "s_mov_b32 s36, 0x1008000\nv_lshl_add_u32 v125, v127, 7, s36\nv_lshl_add_u32 v125, v126, 4, v125\nv_and_b32 v124, 7, v0\ns_lshl_b32 s35, s24, 3\n"
    "v_add_u32 v123, s35, v124\nv_lshrrev_b32 v122, 2, v123\nv_and_b32 v121, 3, v123\ns_mov_b32 s34, 0x100a000\nv_lshl_add_u32 v120, v122, 7, s34\n"
    "v_lshl_add_u32 v120, v121, 4, v120\ns_cmp_lt_u32 s12, 1\ns_cselect_b64 vcc, -1, 0\nv_cndmask_b32 v33, v120, v125, vcc\nv_and_b32 v119, 63, v0\ns_lshl_b32 s33, s18, 13\n"
    "s_lshl_b32 s32, s13, 11\ns_add_u32 s31, s33, s32\ns_add_u32 s31, s31, 0x600000\nv_lshl_add_u32 v40, v119, 4, s31\nv_bfe_u32 v127, v0, 4, 1\nv_and_b32 v126, 15, v0\n"
    "s_lshl_b32 s43, s18, 10\ns_lshl_b32 s42, s13, 8\ns_add_u32 s41, s43, s42\ns_add_u32 s41, s41, 0xd00000\nv_lshl_add_u32 v41, v127, 19, s41\n"
    "v_lshl_add_u32 v41, v126, 4, v41\ns_cbranch_execz .Lhop\nv_lshrrev_b32 v125, 5, v0\nv_and_b32 v124, 3, v0\ns_lshl_b32 s40, s18, 6\ns_add_u32 s39, s40, 0xd80000\n"
    "v_lshl_add_u32 v24, v125, 14, s39\nv_lshl_add_u32 v24, v124, 4, v24\nv_mov_b64 v[52:53], -1\nv_mov_b64 v[54:55], -1\ns_branch .Lb118x\n.Lb118y:\n"
    "s_add_u32 s43, s12, 0x400000\nv_lshl_add_u32 v16, v0, 4, s43\ns_lshl_b32 s42, s18, 13\ns_add_u32 s41, s12, s42\ns_add_u32 s41, s41, 0x200000\n"
    "v_lshl_add_u32 v17, v0, 4, s41\ns_lshr_b32 s43, s12, 23\ns_add_u32 s42, s43, 3\nv_lshrrev_b32 v127, s42, v0\ns_lshr_b32 s41, s12, 22\ns_sub_u32 s40, 15, s41\n"
    "v_lshlrev_b32 v126, s40, v127\ns_lshr_b32 s39, s12, 20\ns_add_u32 s38, s39, 7\nv_and_b32 v125, s38, v0\ns_lshl_b32 s37, s18, 7\ns_add_u32 s36, s37, 0x600000\n"
    "s_lshl_b32 s35, s24, 18\ns_lshl_b32 s34, s25, 8\ns_add_u32 s33, s35, s34\ns_add_u32 s33, s33, 0x1200000\ns_cmp_lt_u32 s12, 1\ns_cselect_b32 s32, s36, s33\n"
    "v_add_u32 v21, s32, v126\nv_lshl_add_u32 v21, v125, 4, v21\ns_add_u32 s31, s43, 4\nv_mov_b32 v26, s31\ns_lshr_b32 s43, s12, 23\ns_lshl_b32 s42, s43, 13\n"
    "s_lshr_b32 s41, s18, 4\ns_lshl_b32 s40, s41, 7\ns_and_b32 s39, s18, 15\ns_lshl_b32 s38, s39, 2\ns_add_u32 s37, s42, s40\ns_add_u32 s37, s37, s38\n"
    "s_add_u32 s37, s37, 0x1008000\nv_mov_b32 v32, s37\nv_bfe_u32 v127, v0, 2, 4\nv_and_b32 v126, 3, v0\ns_mov_b32 s36, 0x1008000\nv_lshl_add_u32 v125, v127, 7, s36\n"
    "v_lshl_add_u32 v125, v126, 4, v125\nv_and_b32 v124, 7, v0\ns_lshl_b32 s35, s24, 3\nv_add_u32 v123, s35, v124\nv_lshrrev_b32 v122, 2, v123\nv_and_b32 v121, 3, v123\n"
    "s_mov_b32 s34, 0x100a000\nv_lshl_add_u32 v120, v122, 7, s34\nv_lshl_add_u32 v120, v121, 4, v120\ns_cmp_lt_u32 s12, 1\ns_cselect_b64 vcc, -1, 0\n"
    "v_cndmask_b32 v33, v120, v125, vcc\ns_mov_b32 s33, 0x100e000\nv_lshl_add_u32 v240, v127, 7, s33\nv_lshl_add_u32 v240, v126, 4, v240\nv_and_b32 v119, 63, v0\n"
    "s_lshl_b32 s32, s18, 13\ns_lshl_b32 s31, s13, 11\ns_add_u32 s30, s32, s31\ns_add_u32 s30, s30, 0x1200000\nv_lshl_add_u32 v40, v119, 4, s30\nv_mov_b64 v[52:53], -1\n"
    "v_mov_b64 v[54:55], -1\n.Lb118x:\ns_setprio 3\ns_waitcnt vmcnt(7)\n.Lr122:\nds_read_b128 v[208:211], v57 offset:0\ns_waitcnt lgkmcnt(0)\nv_max3_u32 v244, v208, v209, 0\n"
    "v_max3_u32 v244, v210, v211, v244\nv_cmp_eq_u32 vcc, -1, v244\ns_cbranch_vccnz .Lw123\n.Lw123x:\ns_barrier\ns_waitcnt vmcnt(6)\n.Lr124:\n"
    "ds_read_b128 v[212:215], v57 offset:4096\ns_waitcnt lgkmcnt(0)\nv_max3_u32 v244, v212, v213, 0\nv_max3_u32 v244, v214, v215, v244\nv_cmp_eq_u32 vcc, -1, v244\n"
    "s_cbranch_vccnz .Lw125\n.Lw125x:\ns_barrier\ns_waitcnt vmcnt(5)\n.Lr126:\nds_read_b128 v[216:219], v57 offset:8192\ns_waitcnt vmcnt(4)\n"
    "ds_read_b128 v[220:223], v57 offset:12288\ns_waitcnt lgkmcnt(1)\nv_max3_u32 v244, v216, v217, 0\nv_max3_u32 v244, v218, v219, v244\ns_waitcnt lgkmcnt(0)\n"
    "v_max3_u32 v244, v220, v221, v244\nv_max3_u32 v244, v222, v223, v244\nv_cmp_eq_u32 vcc, -1, v244\ns_cbranch_vccnz .Lw127\n.Lw127x:\ns_barrier\ns_waitcnt vmcnt(3)\n"
    ".Lr128:\nds_read_b128 v[224:227], v57 offset:16384\ns_waitcnt vmcnt(2)\nds_read_b128 v[228:231], v57 offset:20480\ns_waitcnt lgkmcnt(1)\nv_max3_u32 v244, v224, v225, 0\n"
    "v_max3_u32 v244, v226, v227, v244\ns_waitcnt lgkmcnt(0)\nv_max3_u32 v244, v228, v229, v244\nv_max3_u32 v244, v230, v231, v244\nv_cmp_eq_u32 vcc, -1, v244\n"
    "s_cbranch_vccnz .Lw129\n.Lw129x:\ns_barrier\ns_waitcnt vmcnt(0)\ns_cmp_lg_u32 s13, 1\ns_cbranch_scc1 .Lw129z\nglobal_load_dwordx4 v[252:255], v240, s[4:5] sc1\n.Lw129z:\n"
    "global_store_dwordx4 v40, v[52:55], s[4:5] sc1\nglobal_store_dwordx4 v40, v[52:55], s[4:5] offset:1024 sc1\ns_cmp_lg_u32 s12, 0\ns_cbranch_scc1 .Lw129y\n"
    "global_store_dwordx4 v41, v[52:55], s[4:5] sc1\n.Lw129y:\n.Lr130:\nds_read_b128 v[232:235], v57 offset:24576\nds_read_b128 v[236:239], v57 offset:28672\n"
    "s_waitcnt lgkmcnt(1)\nv_max3_u32 v244, v232, v233, 0\nv_max3_u32 v244, v234, v235, v244\ns_waitcnt lgkmcnt(0)\nv_max3_u32 v244, v236, v237, v244\n"
    "v_max3_u32 v244, v238, v239, v244\nv_cmp_eq_u32 vcc, -1, v244\ns_cbranch_vccnz .Lw131\n.Lw131x:\ns_setprio 0\ns_barrier\n"
    "s_add_u32 s58, s4, 8192\ns_addc_u32 s59, s5, 0\ns_add_u32 s60, s4, 16384\ns_addc_u32 s61, s5, 0\ns_add_u32 s62, s4, 24576\ns_addc_u32 s63, s5, 0\n"
    "s_mov_b32 s45, 0x3c000000\ns_cmp_lg_u32 s13, 0\ns_cbranch_scc1 .Lg136\ns_waitcnt vmcnt(0)\ns_setprio 3\ns_mov_b32 exec_hi, 0\ns_barrier\n"
    "ds_read2_b32 v[44:45], v39 offset0:0 offset1:32\nds_read2_b32 v[46:47], v39 offset0:64 offset1:96\nds_read2_b32 v[48:49], v39 offset0:128 offset1:160\n"
    "ds_read2_b32 v[50:51], v39 offset0:192 offset1:224\ns_waitcnt lgkmcnt(0)\nv_pk_add_f32 v[44:45], v[44:45], v[46:47]\nv_pk_add_f32 v[48:49], v[48:49], v[50:51]\n"
    "v_pk_add_f32 v[40:41], v[44:45], v[48:49]\nv_mul_f32 v40, s45, v40\nv_mul_f32 v42, v40, v40\nv_fma_f32 v41, v41, s45, -v42\nv_max_f32 v41, 0, v41\nv_sqrt_f32 v41, v41\n"
    "s_nop 0\nv_cvt_pk_bf16_f32 v40, v40, v41\nglobal_store_dword v56, v40, s[4:5] sc1\ns_mov_b32 exec_hi, -1\ns_setprio 0\ns_branch .Lln114\n.Lg136:\ns_cmp_eq_u32 s13, 1\n"
    "s_cbranch_scc1 .Lg135\n.Lg134:\ns_waitcnt vmcnt(0)\ns_barrier\ns_branch .Lln114\n.Lg135:\ns_waitcnt vmcnt(2)\ns_cmp_lg_u32 s12, 0\ns_cbranch_scc1 .Lg137\n"
    "s_mov_b32 s80, 0x10000\n.Lg133:\nv_xor_b32 v252, s11, v252\nv_xor_b32 v253, s11, v253\nv_xor_b32 v254, s11, v254\nv_xor_b32 v255, s11, v255\n"
    "v_or3_b32 v224, v252, v253, v254\nv_or_b32 v224, v224, v255\nv_cmp_ne_u32 vcc, 0, v224\ns_cbranch_vccz .Lg134\ns_sub_u32 s80, s80, 1\ns_cbranch_scc1 .Lg134\n"
    "s_cbranch_execz .Lhop\ns_sleep 2\nglobal_load_dwordx4 v[252:255], v240, s[4:5] sc1\ns_waitcnt vmcnt(0)\ns_branch .Lg133\n.Lg137:\ns_sub_u32 s87, s100, 300\n"
    "v_subrev_u32 v252, s87, v252\nv_subrev_u32 v253, s87, v253\nv_subrev_u32 v254, s87, v254\nv_subrev_u32 v255, s87, v255\nv_max3_u32 v252, v252, v253, v254\n"
    "v_max_u32 v252, v252, v255\ns_nop 1\nv_max_u32_dpp v252, v252, v252 row_ror:1 row_mask:0xf bank_mask:0xf\ns_nop 1\n"
    "v_max_u32_dpp v252, v252, v252 row_ror:2 row_mask:0xf bank_mask:0xf\ns_nop 1\nv_max_u32_dpp v252, v252, v252 row_ror:4 row_mask:0xf bank_mask:0xf\ns_nop 1\n"
    "v_max_u32_dpp v252, v252, v252 row_ror:8 row_mask:0xf bank_mask:0xf\nv_mov_b32 v253, v252\ns_nop 1\nv_permlane16_swap_b32 v252, v253\nv_max_u32 v252, v252, v253\n"
    "v_mov_b32 v253, v252\ns_nop 1\nv_permlane32_swap_b32 v252, v253\nv_max_u32 v252, v252, v253\ns_nop 0\nv_readfirstlane_b32 s87, v252\ns_min_u32 s87, s87, 400\n"
    "s_max_u32 s87, s87, 300\ns_sub_u32 s87, s87, 300\ns_add_u32 s78, s78, 5200\ns_waitcnt vmcnt(0)\ns_barrier\ns_memrealtime s[100:101]\ns_memtime s[76:77]\n"
    "s_waitcnt lgkmcnt(0)\ns_sub_u32 s76, s76, s78\ns_max_i32 s76, s76, 0\ns_mul_i32 s76, s76, 49\ns_lshr_b32 s76, s76, 10\ns_sub_u32 s87, s87, s76\ns_max_i32 s87, s87, 0\n"
    "s_waitcnt lgkmcnt(0)\ns_add_u32 s87, s87, s100\ns_add_u32 s87, s87, 30\n.Lj138:\ns_memrealtime s[100:101]\ns_waitcnt lgkmcnt(0)\ns_sub_u32 s101, s87, s100\n"
    "s_cmp_le_i32 s101, 0\ns_cbranch_scc0 .Lj138\ns_branch .Lln114\n"
    ".Lhb110:\ns_cmp_lg_u32 s12, 0\ns_cbranch_scc1 .Lh139\ns_setprio 1\ns_cmp_eq_u32 s13, 0\ns_cbranch_scc1 .Lh139y\n.Lh139x:\ns_cmp_eq_u32 s26, 0\ns_waitcnt lgkmcnt(0)\n"
    "s_barrier\ns_cbranch_scc1 .Lh140\n.Lh141:\nds_read_b64 v[216:217], v252\nds_read_b128 v[192:195], v60 offset:0\nds_read_b128 v[196:199], v60 offset:1024\n"
    "ds_read_b128 v[200:203], v60 offset:4096\nds_read_b128 v[204:207], v60 offset:5120\nv_mov_b32 v220, 0\nv_mov_b32 v221, 2\ns_mov_b64 exec, 1\n"
    "ds_write_b32 v25, v221 offset:64\ns_mov_b64 exec, -1\nds_write_b16 v254, v220 offset:1400\ns_waitcnt vmcnt(17)\n"
    "global_load_dwordx4 v[128:131], v59, s[72:73] offset:-4096 sc1\nv_add_u32_sdwa v240, v254, v48 dst_sel:DWORD dst_unused:UNUSED_PAD src0_sel:DWORD src1_sel:WORD_0\n"
    "v_add_u32_sdwa v241, v254, v48 dst_sel:DWORD dst_unused:UNUSED_PAD src0_sel:DWORD src1_sel:WORD_1\n"
    "v_add_u32_sdwa v242, v254, v49 dst_sel:DWORD dst_unused:UNUSED_PAD src0_sel:DWORD src1_sel:WORD_0\n"
    "v_add_u32_sdwa v243, v254, v49 dst_sel:DWORD dst_unused:UNUSED_PAD src0_sel:DWORD src1_sel:WORD_1\n"
    "v_add_u32_sdwa v244, v254, v50 dst_sel:DWORD dst_unused:UNUSED_PAD src0_sel:DWORD src1_sel:WORD_0\nglobal_load_dwordx4 v[132:135], v59, s[72:73] offset:-3072 sc1\n"
    "v_add_u32_sdwa v245, v254, v50 dst_sel:DWORD dst_unused:UNUSED_PAD src0_sel:DWORD src1_sel:WORD_1\n"
    "v_add_u32_sdwa v246, v254, v51 dst_sel:DWORD dst_unused:UNUSED_PAD src0_sel:DWORD src1_sel:WORD_0\n"
    "v_add_u32_sdwa v247, v254, v51 dst_sel:DWORD dst_unused:UNUSED_PAD src0_sel:DWORD src1_sel:WORD_1\ns_waitcnt lgkmcnt(6)\nv_mov_b32 v218, 3\nv_add_f32 v216, v216, v217\n"
    "global_load_dwordx4 v[136:139], v59, s[72:73] offset:-2048 sc1\nv_lshrrev_b32 v216, 23, v216\nv_sub_u32 v216, 0x10c, v216\nv_min_u32 v216, 0xfc, v216\n"
    "v_sub_u32 v217, 0xfe, v216\nv_lshlrev_b32 v216, 23, v216\nglobal_load_dwordx4 v[140:143], v59, s[72:73] offset:-1024 sc1\nv_lshlrev_b32 v217, 23, v217\n"
    "s_waitcnt lgkmcnt(2)\nv_pk_add_f32 v[208:209], v[192:193], v[200:201]\nv_pk_add_f32 v[210:211], v[194:195], v[202:203]\nv_pk_add_f32 v[212:213], v[196:197], v[204:205]\n"
    "v_pk_add_f32 v[214:215], v[198:199], v[206:207]\nglobal_load_dwordx4 v[144:147], v59, s[72:73] offset:0 sc1\n"
    "v_pk_mul_f32 v[208:209], v[208:209], v[216:217] op_sel_hi:[1,0]\nv_pk_mul_f32 v[210:211], v[210:211], v[216:217] op_sel_hi:[1,0]\n"
    "v_pk_mul_f32 v[212:213], v[212:213], v[216:217] op_sel_hi:[1,0]\nv_pk_mul_f32 v[214:215], v[214:215], v[216:217] op_sel_hi:[1,0]\nv_cvt_pk_f16_f32 v208, v208, v209\n"
    "global_load_dwordx4 v[148:151], v59, s[72:73] offset:1024 sc1\nv_cvt_pk_f16_f32 v209, v210, v211\nv_cvt_pk_f16_f32 v210, v212, v213\nv_cvt_pk_f16_f32 v211, v214, v215\n"
    "ds_write_b16 v240, v208\nds_write_b16_d16_hi v241, v208\nglobal_load_dwordx4 v[152:155], v59, s[72:73] offset:2048 sc1\nds_write_b16 v242, v209\n"
    "ds_write_b16_d16_hi v243, v209\nds_write_b16 v244, v210\nds_write_b16_d16_hi v245, v210\nds_write_b16 v246, v211\n"
    "global_load_dwordx4 v[156:159], v59, s[72:73] offset:3072 sc1\nds_write_b16_d16_hi v247, v211\ns_mov_b64 exec, 1\nds_write_b32 v25, v218 offset:64\n"
    "ds_write_b32 v252, v217 offset:32\ns_mov_b64 exec, -1\nv_add_u32_sdwa v232, v254, v52 dst_sel:DWORD dst_unused:UNUSED_PAD src0_sel:DWORD src1_sel:WORD_0\n"
    "v_add_u32_sdwa v233, v254, v52 dst_sel:DWORD dst_unused:UNUSED_PAD src0_sel:DWORD src1_sel:WORD_1\n"
    "v_add_u32_sdwa v234, v254, v53 dst_sel:DWORD dst_unused:UNUSED_PAD src0_sel:DWORD src1_sel:WORD_0\n"
    "v_add_u32_sdwa v235, v254, v53 dst_sel:DWORD dst_unused:UNUSED_PAD src0_sel:DWORD src1_sel:WORD_1\nglobal_load_dwordx4 v[160:163], v59, s[74:75] offset:-4096 sc1\n"
    "v_add_u32_sdwa v236, v254, v54 dst_sel:DWORD dst_unused:UNUSED_PAD src0_sel:DWORD src1_sel:WORD_0\n"
    "v_add_u32_sdwa v237, v254, v54 dst_sel:DWORD dst_unused:UNUSED_PAD src0_sel:DWORD src1_sel:WORD_1\n"
    "v_add_u32_sdwa v238, v254, v55 dst_sel:DWORD dst_unused:UNUSED_PAD src0_sel:DWORD src1_sel:WORD_0\n"
    "v_add_u32_sdwa v239, v254, v55 dst_sel:DWORD dst_unused:UNUSED_PAD src0_sel:DWORD src1_sel:WORD_1\n"
    "v_add_u32_sdwa v248, v254, v62 dst_sel:DWORD dst_unused:UNUSED_PAD src0_sel:DWORD src1_sel:WORD_0\nglobal_load_dwordx4 v[164:167], v59, s[74:75] offset:-3072 sc1\n"
    "v_add_u32_sdwa v249, v254, v62 dst_sel:DWORD dst_unused:UNUSED_PAD src0_sel:DWORD src1_sel:WORD_1\n"
    "v_add_u32_sdwa v250, v254, v63 dst_sel:DWORD dst_unused:UNUSED_PAD src0_sel:DWORD src1_sel:WORD_0\ns_mov_b32 s86, 0x100000\n.Ls148:\nds_read_b32 v216, v61 offset:0\n"
    "s_waitcnt lgkmcnt(0)\nv_cmp_gt_i32 vcc, 3, v216\ns_sub_u32 s86, s86, 1\ns_cbranch_scc1 .Ls149\ns_cbranch_vccz .Ls149\ns_branch .Ls148\n.Ls149:\nds_read_u16 v200, v232\n"
    "ds_read_u16 v201, v233\nglobal_load_dwordx4 v[168:171], v59, s[74:75] offset:-2048 sc1\nds_read_u16 v202, v234\nds_read_u16 v203, v235\nds_read_u16 v204, v236\n"
    "ds_read_u16 v205, v237\nds_read_u16 v206, v238\nglobal_load_dwordx4 v[172:175], v59, s[74:75] offset:-1024 sc1\nds_read_u16 v207, v239\nds_read_u16 v208, v248\n"
    "ds_read_u16 v209, v249\nds_read_u16 v210, v250\ns_waitcnt lgkmcnt(0)\nv_lshl_or_b32 v192, v201, 16, v200\nglobal_load_dwordx4 v[176:179], v59, s[74:75] offset:0 sc1\n"
    "v_lshl_or_b32 v193, v203, 16, v202\ns_cbranch_execz .Lhop\nv_lshl_or_b32 v194, v205, 16, v204\nv_lshl_or_b32 v195, v207, 16, v206\nv_lshl_or_b32 v196, v209, 16, v208\n"
    "v_mov_b32 v197, v210\nglobal_load_dwordx4 v[180:183], v59, s[74:75] offset:1024 sc1\nv_alignbit_b32 v212, v193, v192, 16\nv_alignbit_b32 v213, v194, v193, 16\n"
    "v_alignbit_b32 v214, v195, v194, 16\nv_alignbit_b32 v215, v196, v195, 16\nds_write_b128 v253, v[192:195]\nglobal_load_dwordx4 v[184:187], v59, s[74:75] offset:2048 sc1\n"
    "v_mov_b32 v216, v193\nv_mov_b32 v217, v194\nv_mov_b32 v218, v195\nv_mov_b32 v219, v196\nds_write_b128 v253, v[212:215] offset:2112\n"
    "global_load_dwordx4 v[188:191], v59, s[74:75] offset:3072 sc1\nv_alignbit_b32 v220, v194, v193, 16\nv_alignbit_b32 v221, v195, v194, 16\n"
    "v_alignbit_b32 v222, v196, v195, 16\nv_alignbit_b32 v223, v197, v196, 16\nds_write_b128 v253, v[216:219] offset:4224\nds_write_b128 v253, v[220:223] offset:6336\n"
    "s_waitcnt vmcnt(16)\nv_permlane32_swap_b32 v224, v226\nv_permlane32_swap_b32 v225, v227\nv_permlane32_swap_b32 v228, v230\nv_permlane32_swap_b32 v229, v231\n"
    "v_mov_b64 v[240:241], v[224:225]\nv_mov_b64 v[242:243], v[226:227]\nv_mov_b64 v[244:245], v[228:229]\nv_mov_b64 v[246:247], v[230:231]\nv_mov_b64 v[252:253], -1\n"
    "v_mov_b64 v[254:255], -1\ns_branch .Lhwo113\n.Lh139y:\nv_mov_b32 v240, s11\nv_add_u32 v58, 0x2000, v58\ns_mov_b64 exec, 1\ns_waitcnt vmcnt(19)\n"
    "global_store_dword v58, v240, s[4:5] sc1\ns_mov_b64 exec, -1\ns_branch .Lh139x\n.Lh139:\ns_barrier\nglobal_load_dword v240, v47, s[4:5] offset:0 sc1\n"
    "global_load_dword v241, v47, s[4:5] offset:128 sc1\nglobal_load_dword v242, v47, s[4:5] offset:512 sc1\nglobal_load_dword v243, v47, s[4:5] offset:640 sc1\n"
    "global_load_dword v244, v47, s[4:5] offset:1024 sc1\nglobal_load_dword v245, v47, s[4:5] offset:1152 sc1\nglobal_load_dword v246, v47, s[4:5] offset:1536 sc1\n"
    "global_load_dword v247, v47, s[4:5] offset:1664 sc1\nglobal_load_dwordx4 v[128:131], v59, s[72:73] offset:-4096 sc1\nv_bfe_u32 v223, v0, 5, 1\nv_and_b32 v222, 31, v0\n"
    "s_lshl_b32 s43, s13, 11\nglobal_load_dwordx4 v[132:135], v59, s[72:73] offset:-3072 sc1\ns_add_u32 s42, s43, 0x1d000\nv_lshl_add_u32 v40, v223, 8, s42\n"
    "v_lshl_add_u32 v40, v222, 2, v40\nv_and_b32 v221, 63, v0\nglobal_load_dwordx4 v[136:139], v59, s[72:73] offset:-2048 sc1\nv_lshl_add_u32 v41, v221, 4, s42\n"
    "s_mov_b32 s41, 0x1f600\nv_lshl_add_u32 v42, v221, 4, s41\nv_bfe_u32 v220, v0, 3, 3\nglobal_load_dwordx4 v[140:143], v59, s[72:73] offset:-1024 sc1\n"
    "v_and_b32 v219, 7, v0\ns_lshl_b32 s40, s22, 19\ns_lshl_b32 s39, s13, 17\ns_lshl_b32 s38, s21, 7\nglobal_load_dwordx4 v[144:147], v59, s[72:73] offset:0 sc1\n"
    "s_add_u32 s37, s12, s40\ns_add_u32 s37, s37, s39\ns_add_u32 s37, s37, s38\nglobal_load_dwordx4 v[148:151], v59, s[72:73] offset:1024 sc1\ns_add_u32 s37, s37, 0x200000\n"
    "v_lshl_add_u32 v43, v220, 13, s37\nv_lshl_add_u32 v43, v219, 4, v43\ns_add_u32 s36, s12, s40\nglobal_load_dwordx4 v[152:155], v59, s[72:73] offset:2048 sc1\n"
    "s_add_u32 s36, s36, s39\ns_add_u32 s36, s36, s38\ns_add_u32 s36, s36, 0x210000\nv_lshl_add_u32 v44, v220, 13, s36\n"
    "global_load_dwordx4 v[156:159], v59, s[72:73] offset:3072 sc1\nv_lshl_add_u32 v44, v219, 4, v44\ns_lshl_b32 s35, s13, 8\ns_add_u32 s34, s35, 0x27600\n"
    "v_lshl_add_u32 v45, v221, 2, s34\nglobal_load_dwordx4 v[160:163], v59, s[74:75] offset:-4096 sc1\ns_add_u32 s33, s43, 0x1d400\nv_lshl_add_u32 v46, v223, 8, s33\n"
    "v_lshl_add_u32 v46, v222, 2, v46\nglobal_load_dwordx4 v[164:167], v59, s[74:75] offset:-3072 sc1\ns_add_u32 s32, s12, 0x400000\nv_lshl_add_u32 v16, v0, 4, s32\n"
    "s_lshl_b32 s31, s18, 13\ns_add_u32 s30, s12, s31\nglobal_load_dwordx4 v[168:171], v59, s[74:75] offset:-2048 sc1\ns_add_u32 s30, s30, 0x200000\n"
    "v_lshl_add_u32 v17, v0, 4, s30\ns_lshr_b32 s43, s12, 23\ns_add_u32 s42, s43, 3\nglobal_load_dwordx4 v[172:175], v59, s[74:75] offset:-1024 sc1\n"
    "v_lshrrev_b32 v223, s42, v0\ns_lshr_b32 s41, s12, 22\ns_sub_u32 s40, 15, s41\nv_lshlrev_b32 v222, s40, v223\nglobal_load_dwordx4 v[176:179], v59, s[74:75] offset:0 sc1\n"
    "s_lshr_b32 s39, s12, 20\ns_add_u32 s38, s39, 7\nv_and_b32 v221, s38, v0\nglobal_load_dwordx4 v[180:183], v59, s[74:75] offset:1024 sc1\ns_lshl_b32 s37, s18, 7\n"
    "s_add_u32 s36, s37, 0x600000\ns_lshl_b32 s35, s24, 18\ns_lshl_b32 s34, s25, 8\nglobal_load_dwordx4 v[184:187], v59, s[74:75] offset:2048 sc1\ns_add_u32 s33, s35, s34\n"
    "s_add_u32 s33, s33, 0x1200000\ns_cmp_lt_u32 s12, 1\ns_cselect_b32 s32, s36, s33\nglobal_load_dwordx4 v[188:191], v59, s[74:75] offset:3072 sc1\nv_add_u32 v21, s32, v222\n"
    "v_lshl_add_u32 v21, v221, 4, v21\ns_add_u32 s31, s43, 4\nv_mov_b32 v26, s31\ns_waitcnt vmcnt(16)\ns_mov_b32 s80, 0x10000\nv_max3_u32 v232, v240, v241, v242\n"
    "v_max3_u32 v233, v243, v244, v245\nv_max3_u32 v232, v232, v246, v247\nv_max_u32 v232, v232, v233\nv_cmp_eq_u32 vcc, -1, v232\ns_cbranch_vccnz .Lw147\n"
    ".Lhwo113:\nv_lshlrev_b32 v192, 16, v240\nv_and_b32 v193, 0xffff0000, v240\nv_lshlrev_b32 v194, 16, v241\nv_and_b32 v195, 0xffff0000, v241\nv_lshlrev_b32 v196, 16, v242\n"
    "v_and_b32 v197, 0xffff0000, v242\nv_lshlrev_b32 v198, 16, v243\nv_and_b32 v199, 0xffff0000, v243\nv_lshlrev_b32 v200, 16, v244\nv_and_b32 v201, 0xffff0000, v244\n"
    "v_lshlrev_b32 v202, 16, v245\nv_and_b32 v203, 0xffff0000, v245\nv_lshlrev_b32 v204, 16, v246\nv_and_b32 v205, 0xffff0000, v246\nv_lshlrev_b32 v206, 16, v247\n"
    "v_and_b32 v207, 0xffff0000, v247\ns_add_u32 s58, s4, 8192\ns_addc_u32 s59, s5, 0\ns_add_u32 s60, s4, 16384\ns_addc_u32 s61, s5, 0\ns_add_u32 s62, s4, 24576\n"
    "s_addc_u32 s63, s5, 0\ns_add_u32 s54, s4, 2097152\ns_addc_u32 s55, s5, 0\ns_mov_b32 s48, 0x3f803f80\ns_waitcnt lgkmcnt(0)\ns_setprio 2\ns_barrier\n"
    "ds_read_b128 v[208:211], v42 offset:0\nds_read_b128 v[212:215], v42 offset:1024\ns_nop 0\ns_cbranch_execz .Lhop\nds_read_b128 v[216:219], v42 offset:2048\n"
    "ds_read_b128 v[220:223], v42 offset:3072\ns_waitcnt lgkmcnt(3)\nv_mfma_f32_32x32x16_bf16 v[192:207], v[64:67], v[208:211], v[192:207]\ns_barrier\n"
    "ds_read_b128 v[224:227], v42 offset:4096\ns_waitcnt lgkmcnt(3)\nv_mfma_f32_32x32x16_bf16 v[192:207], v[68:71], v[212:215], v[192:207]\n"
    "ds_read_b128 v[228:231], v42 offset:5120\ns_waitcnt lgkmcnt(3)\nv_mfma_f32_32x32x16_bf16 v[192:207], v[72:75], v[216:219], v[192:207]\n"
    "ds_read_b128 v[232:235], v42 offset:6144\ns_waitcnt lgkmcnt(3)\nv_mfma_f32_32x32x16_bf16 v[192:207], v[76:79], v[220:223], v[192:207]\n"
    "ds_read_b128 v[208:211], v42 offset:7168\ns_waitcnt lgkmcnt(3)\nv_mfma_f32_32x32x16_bf16 v[192:207], v[80:83], v[224:227], v[192:207]\ns_barrier\n"
    "ds_read_b128 v[212:215], v42 offset:8192\ns_waitcnt lgkmcnt(3)\nv_mfma_f32_32x32x16_bf16 v[192:207], v[84:87], v[228:231], v[192:207]\n"
    "ds_read_b128 v[216:219], v42 offset:9216\ns_waitcnt lgkmcnt(3)\nv_mfma_f32_32x32x16_bf16 v[192:207], v[88:91], v[232:235], v[192:207]\n"
    "ds_read_b128 v[220:223], v42 offset:10240\ns_waitcnt lgkmcnt(3)\nv_mfma_f32_32x32x16_bf16 v[192:207], v[92:95], v[208:211], v[192:207]\n"
    "ds_read_b128 v[224:227], v42 offset:11264\ns_waitcnt lgkmcnt(3)\nv_mfma_f32_32x32x16_bf16 v[192:207], v[96:99], v[212:215], v[192:207]\n"
    "ds_read_b128 v[228:231], v42 offset:12288\ns_waitcnt lgkmcnt(3)\nv_mfma_f32_32x32x16_bf16 v[192:207], v[100:103], v[216:219], v[192:207]\n"
    "ds_read_b128 v[232:235], v42 offset:13312\ns_waitcnt lgkmcnt(3)\nv_mfma_f32_32x32x16_bf16 v[192:207], v[104:107], v[220:223], v[192:207]\n"
    "ds_read_b128 v[208:211], v42 offset:14336\ns_waitcnt lgkmcnt(3)\nv_mfma_f32_32x32x16_bf16 v[192:207], v[108:111], v[224:227], v[192:207]\n"
    "ds_read_b128 v[212:215], v42 offset:15360\ns_waitcnt lgkmcnt(3)\nv_mfma_f32_32x32x16_bf16 v[192:207], v[112:115], v[228:231], v[192:207]\ns_barrier\n"
    "ds_read_b128 v[216:219], v42 offset:16384\nds_read_b128 v[220:223], v42 offset:17408\ns_waitcnt lgkmcnt(4)\n"
    "v_mfma_f32_32x32x16_bf16 v[192:207], v[116:119], v[232:235], v[192:207]\nds_read_b128 v[224:227], v42 offset:18432\ns_waitcnt lgkmcnt(4)\n"
    "v_mfma_f32_32x32x16_bf16 v[192:207], v[120:123], v[208:211], v[192:207]\nds_read_b128 v[228:231], v42 offset:19456\ns_waitcnt lgkmcnt(4)\n"
    "v_mfma_f32_32x32x16_bf16 v[192:207], v[124:127], v[212:215], v[192:207]\nds_read_b128 v[232:235], v42 offset:20480\ns_waitcnt vmcnt(8) lgkmcnt(4)\n"
    "v_mfma_f32_32x32x16_bf16 v[192:207], v[128:131], v[216:219], v[192:207]\nds_read_b128 v[208:211], v42 offset:21504\nds_read_b128 v[212:215], v42 offset:22528\n"
    "s_waitcnt lgkmcnt(5)\nv_mfma_f32_32x32x16_bf16 v[192:207], v[132:135], v[220:223], v[192:207]\nds_read_b128 v[216:219], v42 offset:23552\ns_waitcnt lgkmcnt(5)\n"
    "v_mfma_f32_32x32x16_bf16 v[192:207], v[136:139], v[224:227], v[192:207]\ns_barrier\nds_read_b128 v[220:223], v42 offset:24576\ns_waitcnt lgkmcnt(5)\n"
    "v_mfma_f32_32x32x16_bf16 v[192:207], v[140:143], v[228:231], v[192:207]\nds_read_b128 v[224:227], v42 offset:25600\ns_waitcnt lgkmcnt(5)\n"
    "v_mfma_f32_32x32x16_bf16 v[192:207], v[144:147], v[232:235], v[192:207]\nds_read_b128 v[228:231], v42 offset:26624\nds_read_b128 v[232:235], v42 offset:27648\n"
    "s_waitcnt lgkmcnt(6)\nv_mfma_f32_32x32x16_bf16 v[192:207], v[148:151], v[208:211], v[192:207]\nds_read_b128 v[208:211], v42 offset:28672\ns_waitcnt lgkmcnt(6)\n"
    "v_mfma_f32_32x32x16_bf16 v[192:207], v[152:155], v[212:215], v[192:207]\nds_read_b128 v[212:215], v42 offset:29696\ns_waitcnt lgkmcnt(6)\n"
    "v_mfma_f32_32x32x16_bf16 v[192:207], v[156:159], v[216:219], v[192:207]\nds_read_b128 v[216:219], v42 offset:30720\ns_waitcnt vmcnt(0) lgkmcnt(6)\n"
    "v_mfma_f32_32x32x16_bf16 v[192:207], v[160:163], v[220:223], v[192:207]\nds_read_b128 v[220:223], v42 offset:31744\ns_waitcnt lgkmcnt(6)\n"
    "v_mfma_f32_32x32x16_bf16 v[192:207], v[164:167], v[224:227], v[192:207]\ns_waitcnt lgkmcnt(5)\nv_mfma_f32_32x32x16_bf16 v[192:207], v[168:171], v[228:231], v[192:207]\n"
    "s_waitcnt lgkmcnt(4)\nv_mfma_f32_32x32x16_bf16 v[192:207], v[172:175], v[232:235], v[192:207]\ns_waitcnt lgkmcnt(3)\n"
    "v_mfma_f32_32x32x16_bf16 v[192:207], v[176:179], v[208:211], v[192:207]\ns_waitcnt lgkmcnt(2)\nv_mfma_f32_32x32x16_bf16 v[192:207], v[180:183], v[212:215], v[192:207]\n"
    "s_waitcnt lgkmcnt(1)\nv_mfma_f32_32x32x16_bf16 v[192:207], v[184:187], v[216:219], v[192:207]\ns_waitcnt lgkmcnt(0)\n"
    "v_mfma_f32_32x32x16_bf16 v[192:207], v[188:191], v[220:223], v[192:207]\ns_nop 11\nv_cvt_pk_bf16_f32 v208, v192, v193\nv_cvt_pk_bf16_f32 v209, v194, v195\n"
    "v_cvt_pk_bf16_f32 v210, v196, v197\nv_cvt_pk_bf16_f32 v211, v198, v199\nv_cvt_pk_bf16_f32 v212, v200, v201\nv_cvt_pk_bf16_f32 v213, v202, v203\n"
    "v_cvt_pk_bf16_f32 v214, v204, v205\nv_cvt_pk_bf16_f32 v215, v206, v207\nv_dot2_f32_bf16 v56, v208, s48, 0\nv_dot2_f32_bf16 v57, v208, v208, 0\n"
    "v_dot2_f32_bf16 v58, v209, s48, 0\nv_dot2_f32_bf16 v59, v209, v209, 0\nv_dot2_f32_bf16 v56, v210, s48, v56\nv_dot2_f32_bf16 v57, v210, v210, v57\n"
    "v_dot2_f32_bf16 v58, v211, s48, v58\nv_dot2_f32_bf16 v59, v211, v211, v59\nv_dot2_f32_bf16 v56, v212, s48, v56\nv_dot2_f32_bf16 v57, v212, v212, v57\n"
    "v_dot2_f32_bf16 v58, v213, s48, v58\nv_dot2_f32_bf16 v59, v213, v213, v59\nv_dot2_f32_bf16 v56, v214, s48, v56\nv_dot2_f32_bf16 v57, v214, v214, v57\n"
    "v_dot2_f32_bf16 v58, v215, s48, v58\nv_dot2_f32_bf16 v59, v215, v215, v59\nds_write2_b32 v40, v208, v209 offset1:32\ns_nop 1\nv_pk_add_f32 v[56:57], v[56:57], v[58:59]\n"
    "s_nop 1\nv_permlane32_swap_b32 v56, v57\nv_add_f32 v56, v56, v57\nds_write_b32 v45, v56\nds_write2_b32 v40, v210, v211 offset0:128 offset1:160\n"
    "ds_read_b128 v[216:219], v41\nds_write2_b32 v46, v212, v213 offset1:32\ns_waitcnt lgkmcnt(3)\ns_barrier\nds_write2_b32 v46, v214, v215 offset0:128 offset1:160\n"
    "ds_read_b128 v[220:223], v41 offset:1024\ns_setprio 0\ns_waitcnt lgkmcnt(3)\nglobal_store_dwordx4 v43, v[216:219], s[4:5] sc1\ns_waitcnt lgkmcnt(0)\n"
    "global_store_dwordx4 v44, v[220:223], s[4:5] sc1\ns_cmp_lg_u32 s12, 0\ns_cbranch_scc1 .Lz150\ns_call_b64 s[94:95], .Lhcon67_1\ns_call_b64 s[94:95], .Lhcon67_0\n"
    "s_call_b64 s[94:95], .Lhcon67_2\ns_sleep 2\n.Lz150:\nds_read_b64 v[128:129], v23 offset:896\nds_read_b64 v[130:131], v23 offset:904\n"
    "ds_read_b64 v[132:133], v23 offset:864\nds_read_b64 v[134:135], v23 offset:872\nds_read_b64 v[136:137], v23 offset:832\ns_nop 0\ns_cbranch_execz .Lhop\n"
    "ds_read_b64 v[138:139], v23 offset:840\nds_read_b64 v[140:141], v23 offset:800\nds_read_b64 v[142:143], v23 offset:808\nds_read_b64 v[144:145], v23 offset:704\n"
    "ds_read_b64 v[146:147], v23 offset:712\nds_read_b64 v[148:149], v23 offset:672\nds_read_b64 v[150:151], v23 offset:680\nds_read_b64 v[152:153], v23 offset:640\n"
    "ds_read_b64 v[154:155], v23 offset:648\nds_read_b64 v[156:157], v23 offset:608\nds_read_b64 v[158:159], v23 offset:616\nds_read_b64 v[160:161], v39 offset:512\n"
    "ds_read_b64 v[162:163], v39 offset:520\nds_read_b64 v[164:165], v39 offset:480\nds_read_b64 v[166:167], v39 offset:488\nds_read_b64 v[168:169], v39 offset:448\n"
    "ds_read_b64 v[170:171], v39 offset:456\nds_read_b64 v[172:173], v39 offset:416\nds_read_b64 v[174:175], v39 offset:424\nds_read_b64 v[176:177], v39 offset:320\n"
    "ds_read_b64 v[178:179], v39 offset:328\nds_read_b64 v[180:181], v39 offset:288\nds_read_b64 v[182:183], v39 offset:296\nds_read_b64 v[184:185], v39 offset:256\n"
    "ds_read_b64 v[186:187], v39 offset:264\nds_read_b64 v[188:189], v39 offset:224\nds_read_b64 v[190:191], v39 offset:232\n"
    ".Lln114:\ns_cmp_gt_u32 s3, 3\ns_waitcnt lgkmcnt(0)\ns_barrier\nglobal_load_dwordx4 v[240:243], v17, s[4:5] sc1\nglobal_load_dwordx4 v[208:211], v16, s[4:5] sc1\n"
    "global_load_dwordx4 v[212:215], v16, s[58:59] sc1\nglobal_load_dwordx4 v[216:219], v16, s[60:61] sc1\nglobal_load_dwordx4 v[220:223], v16, s[62:63] sc1\n"
    "s_cbranch_scc1 .Llc152\nds_read_b128 v[10:13], v59\nds_read_b32 v14, v59 offset:4236\nds_read_u16 v15, v59 offset:6350\nds_read_b64 v[64:65], v23 offset:2016\n"
    "ds_read_b64 v[66:67], v23 offset:2024\nds_read_b64 v[68:69], v23 offset:1984\nds_read_b64 v[70:71], v23 offset:1992\nds_read_b64 v[72:73], v23 offset:1952\n"
    "ds_read_b64 v[74:75], v23 offset:1960\nds_read_b64 v[76:77], v23 offset:1920\nds_read_b64 v[78:79], v23 offset:1928\nds_read_b64 v[80:81], v23 offset:1888\n"
    "ds_read_b64 v[82:83], v23 offset:1896\nds_read_b64 v[84:85], v23 offset:1856\nds_read_b64 v[86:87], v23 offset:1864\nds_read_b64 v[88:89], v23 offset:1824\n"
    "ds_read_b64 v[90:91], v23 offset:1832\nds_read_b64 v[92:93], v23 offset:1792\nds_read_b64 v[94:95], v23 offset:1800\nds_read_b64 v[96:97], v23 offset:1760\n"
    "ds_read_b64 v[98:99], v23 offset:1768\nds_read_b64 v[100:101], v23 offset:1728\nds_read_b64 v[102:103], v23 offset:1736\nds_read_b64 v[104:105], v23 offset:1696\n"
    "ds_read_b64 v[106:107], v23 offset:1704\nds_read_b64 v[108:109], v23 offset:1664\nds_read_b64 v[110:111], v23 offset:1672\nds_read_b64 v[112:113], v23 offset:1632\n"
    "ds_read_b64 v[114:115], v23 offset:1640\nds_read_b64 v[116:117], v23 offset:1600\nds_read_b64 v[118:119], v23 offset:1608\nds_read_b64 v[120:121], v23 offset:1568\n"
    "ds_read_b64 v[122:123], v23 offset:1576\nds_read_b64 v[124:125], v23 offset:1536\nds_read_b64 v[126:127], v23 offset:1544\nds_read_b64 v[128:129], v23 offset:1504\n"
    "ds_read_b64 v[130:131], v23 offset:1512\nds_read_b64 v[132:133], v23 offset:1472\nds_read_b64 v[134:135], v23 offset:1480\nds_read_b64 v[136:137], v23 offset:1440\n"
    "ds_read_b64 v[138:139], v23 offset:1448\nds_read_b64 v[140:141], v23 offset:1408\nds_read_b64 v[142:143], v23 offset:1416\nds_read_b64 v[144:145], v23 offset:1376\n"
    "ds_read_b64 v[146:147], v23 offset:1384\nds_read_b64 v[148:149], v23 offset:1344\nds_read_b64 v[150:151], v23 offset:1352\nds_read_b64 v[152:153], v23 offset:1312\n"
    "ds_read_b64 v[154:155], v23 offset:1320\nds_read_b64 v[156:157], v23 offset:1280\nds_read_b64 v[158:159], v23 offset:1288\nds_read_b64 v[160:161], v23 offset:1248\n"
    "ds_read_b64 v[162:163], v23 offset:1256\nds_read_b64 v[164:165], v23 offset:1216\nds_read_b64 v[166:167], v23 offset:1224\nds_read_b64 v[168:169], v23 offset:1184\n"
    "ds_read_b64 v[170:171], v23 offset:1192\nds_read_b64 v[172:173], v23 offset:1152\nds_read_b64 v[174:175], v23 offset:1160\nds_read_b64 v[176:177], v23 offset:1120\n"
    "ds_read_b64 v[178:179], v23 offset:1128\nds_read_b64 v[180:181], v23 offset:1088\nds_read_b64 v[182:183], v23 offset:1096\nds_read_b64 v[184:185], v23 offset:1056\n"
    "ds_read_b64 v[186:187], v23 offset:1064\nds_read_b64 v[188:189], v23 offset:1024\nds_read_b64 v[190:191], v23 offset:1032\nds_read_b64 v[192:193], v23 offset:992\n"
    "ds_read_b64 v[194:195], v23 offset:1000\nds_read_b64 v[196:197], v23 offset:960\nds_read_b64 v[198:199], v23 offset:968\nds_read_b64 v[200:201], v23 offset:928\n"
    "ds_read_b64 v[202:203], v23 offset:936\n.Llc152:\nv_mov_b64 v[236:237], 0\nv_mov_b64 v[238:239], 0\ns_cmp_lg_u32 s12, 0\ns_cbranch_scc1 .Ln153\n"
    "s_load_dword s17, s[16:17], 0\nv_mov_b32 v232, 115200\nds_read_u16 v233, v232 offset:1024\nds_read_u16 v234, v232 offset:2560\nds_read_b32 v235, v232 offset:48032\n"
    "ds_read_b32 v232, v232 offset:48040\ns_waitcnt lgkmcnt(0)\nv_fma_mix_f32 v233, v233, -v235, 0 op_sel_hi:[1,0,0]\nv_fma_mix_f32 v234, v234, -v232, 0 op_sel_hi:[1,0,0]\n"
    "s_lshl_b32 s16, s17, 16\ns_and_b32 s17, s17, 0xffff0000\nv_readfirstlane_b32 s14, v235\nv_readfirstlane_b32 s15, v232\nv_readfirstlane_b32 s28, v233\n"
    "v_readfirstlane_b32 s29, v234\ns_lshr_b32 s26, s14, 16\ns_lshr_b32 s27, s15, 16\n.Ln153:\nds_write_b128 v22, v[236:239]\ns_mov_b32 s44, 0x3f80\n"
    "s_mov_b32 s46, 0x3e800000\ns_mov_b32 s48, 0x3727c5ac\ns_mov_b32 s80, 0x10000\n.Ln155:\ns_waitcnt vmcnt(4)\ns_cbranch_execz .Lhop\nv_lshlrev_b32 v56, 16, v240\n"
    "v_lshlrev_b32 v57, 16, v241\nv_lshlrev_b32 v58, 16, v242\nv_lshlrev_b32 v59, 16, v243\nv_and_b32 v60, 0xffff0000, v240\nv_and_b32 v61, 0xffff0000, v241\n"
    "v_and_b32 v62, 0xffff0000, v242\nv_and_b32 v63, 0xffff0000, v243\nv_max3_u32 v232, v240, v241, v242\nv_max_u32 v232, v232, v243\nv_cmp_eq_u32 s[50:51], -1, v232\n"
    "s_waitcnt vmcnt(3)\nv_dot2_f32_bf16 v224, v208, s44, 0\nv_dot2_f32_bf16 v228, v208, v208, 0\nv_dot2_f32_bf16 v225, v209, s44, 0\nv_dot2_f32_bf16 v229, v209, v209, 0\n"
    "v_dot2_f32_bf16 v226, v210, s44, 0\nv_dot2_f32_bf16 v230, v210, v210, 0\nv_dot2_f32_bf16 v227, v211, s44, 0\nv_dot2_f32_bf16 v231, v211, v211, 0\ns_waitcnt vmcnt(2)\n"
    "v_dot2_f32_bf16 v224, v212, s44, v224\nv_dot2_f32_bf16 v228, v212, v212, v228\nv_dot2_f32_bf16 v225, v213, s44, v225\nv_dot2_f32_bf16 v229, v213, v213, v229\n"
    "v_dot2_f32_bf16 v226, v214, s44, v226\nv_dot2_f32_bf16 v230, v214, v214, v230\nv_dot2_f32_bf16 v227, v215, s44, v227\nv_dot2_f32_bf16 v231, v215, v215, v231\n"
    "s_waitcnt vmcnt(1)\nv_dot2_f32_bf16 v224, v216, s44, v224\nv_dot2_f32_bf16 v228, v216, v216, v228\nv_dot2_f32_bf16 v225, v217, s44, v225\n"
    "v_dot2_f32_bf16 v229, v217, v217, v229\nv_dot2_f32_bf16 v226, v218, s44, v226\nv_dot2_f32_bf16 v230, v218, v218, v230\nv_dot2_f32_bf16 v227, v219, s44, v227\n"
    "v_dot2_f32_bf16 v231, v219, v219, v231\ns_waitcnt vmcnt(0)\nv_dot2_f32_bf16 v224, v220, s44, v224\nv_dot2_f32_bf16 v228, v220, v220, v228\n"
    "v_dot2_f32_bf16 v225, v221, s44, v225\nv_dot2_f32_bf16 v229, v221, v221, v229\nv_dot2_f32_bf16 v226, v222, s44, v226\nv_dot2_f32_bf16 v230, v222, v222, v230\n"
    "v_dot2_f32_bf16 v227, v223, s44, v227\nv_dot2_f32_bf16 v231, v223, v223, v231\nv_pk_mul_f32 v[224:225], v[224:225], s[46:47] op_sel_hi:[1,0]\ns_nop 0\n"
    "v_pk_mul_f32 v[226:227], v[226:227], s[46:47] op_sel_hi:[1,0]\nv_cmp_u_f32 vcc, v228, v229\nv_cmp_u_f32 s[52:53], v230, v231\n"
    "v_pk_fma_f32 v[232:233], v[224:225], v[224:225], s[48:49] op_sel_hi:[1,1,0] neg_lo:[0,0,1] neg_hi:[0,0,1]\n"
    "v_pk_fma_f32 v[234:235], v[226:227], v[226:227], s[48:49] op_sel_hi:[1,1,0] neg_lo:[0,0,1] neg_hi:[0,0,1]\n"
    "v_pk_fma_f32 v[228:229], v[228:229], s[46:47], v[232:233] op_sel_hi:[1,0,1] neg_lo:[0,0,1] neg_hi:[0,0,1]\n"
    "v_pk_fma_f32 v[230:231], v[230:231], s[46:47], v[234:235] op_sel_hi:[1,0,1] neg_lo:[0,0,1] neg_hi:[0,0,1]\nv_max_f32 v228, s48, v228\nv_max_f32 v229, s48, v229\n"
    "v_max_f32 v230, s48, v230\nv_max_f32 v231, s48, v231\nv_rsq_f32 v228, v228\nv_rsq_f32 v229, v229\nv_rsq_f32 v230, v230\nv_rsq_f32 v231, v231\n"
    "v_pk_add_f32 v[244:245], v[56:57], v[224:225] neg_lo:[0,1] neg_hi:[0,1]\nv_pk_add_f32 v[246:247], v[58:59], v[226:227] neg_lo:[0,1] neg_hi:[0,1]\n"
    "v_pk_add_f32 v[248:249], v[60:61], v[224:225] neg_lo:[0,1] neg_hi:[0,1]\nv_pk_add_f32 v[250:251], v[62:63], v[226:227] neg_lo:[0,1] neg_hi:[0,1]\n"
    "s_or_b64 vcc, vcc, s[50:51]\nv_pk_mul_f32 v[244:245], v[244:245], v[228:229]\nv_pk_mul_f32 v[246:247], v[246:247], v[230:231]\n"
    "v_pk_mul_f32 v[248:249], v[248:249], v[228:229]\nv_pk_mul_f32 v[250:251], v[250:251], v[230:231]\ns_or_b64 vcc, vcc, s[52:53]\nv_cvt_pk_f16_f32 v232, v244, v245\n"
    "v_cvt_pk_f16_f32 v233, v246, v247\nv_cvt_pk_f16_f32 v234, v248, v249\nds_write_b64 v18, v[232:233]\nv_cvt_pk_f16_f32 v235, v250, v251\n"
    "ds_write_b64 v18, v[234:235] offset:6480\ns_cbranch_scc1 .Llw158\n.Llx159:\ns_cmp_gt_u32 s3, 3\ns_cbranch_scc1 .Lhcv156\n"
    "s_waitcnt lgkmcnt(0)\ns_barrier\ns_memrealtime s[100:101]\nds_read_b128 v[220:223], v27 offset:1392\nds_read_b128 v[224:227], v27 offset:1360\n"
    "ds_read_b128 v[228:231], v27 offset:1312\nv_fmac_f32 v56, s16, v244\nv_fmac_f32 v57, s16, v245\nv_fmac_f32 v58, s16, v246\nv_fmac_f32 v59, s16, v247\n"
    "v_fmac_f32 v60, s17, v248\nv_fmac_f32 v61, s17, v249\nv_fmac_f32 v62, s17, v250\nv_fmac_f32 v63, s17, v251\ns_waitcnt lgkmcnt(2)\n"
    "v_mfma_f32_32x32x16_f16 v[204:219], v[64:67], v[220:223], 0\nds_read_b128 v[232:235], v27 offset:1280\nds_read_b128 v[236:239], v27 offset:1232\ns_cmp_lg_u32 s13, 0\n"
    "s_cbranch_scc1 .Lz160\ns_waitcnt lgkmcnt(0)\nv_mov_b32 v40, s100\ns_mov_b64 exec, 1\nglobal_store_dword v32, v40, s[4:5] sc1\ns_mov_b64 exec, -1\ns_cmp_lg_u32 s2, 0\n"
    "s_cbranch_scc1 .Lz160\nv_mov_b32 v41, 16777216\nv_mov_b32 v42, s11\ns_mov_b64 exec, 1\nglobal_store_dword v41, v42, s[4:5] sc1\ns_mov_b64 exec, -1\n.Lz160:\n"
    "s_waitcnt lgkmcnt(3)\nv_mfma_f32_32x32x16_f16 v[204:219], v[68:71], v[224:227], v[204:219]\nds_read_b128 v[240:243], v27 offset:1200\n"
    "ds_read_b128 v[220:223], v27 offset:1152\ns_waitcnt lgkmcnt(4)\nv_mfma_f32_32x32x16_f16 v[204:219], v[72:75], v[228:231], v[204:219]\n"
    "ds_read_b128 v[224:227], v27 offset:1120\nds_read_b128 v[228:231], v27 offset:1072\ns_waitcnt lgkmcnt(5)\n"
    "v_mfma_f32_32x32x16_f16 v[204:219], v[76:79], v[232:235], v[204:219]\nds_read_b128 v[232:235], v27 offset:1040\ns_waitcnt lgkmcnt(5)\n"
    "v_mfma_f32_32x32x16_f16 v[204:219], v[80:83], v[236:239], v[204:219]\nds_read_b128 v[236:239], v27 offset:992\ns_waitcnt lgkmcnt(5)\n"
    "v_mfma_f32_32x32x16_f16 v[204:219], v[84:87], v[240:243], v[204:219]\nds_read_b128 v[240:243], v27 offset:960\ns_waitcnt lgkmcnt(5)\n"
    "v_mfma_f32_32x32x16_f16 v[204:219], v[88:91], v[220:223], v[204:219]\nds_read_b128 v[220:223], v27 offset:912\ns_waitcnt lgkmcnt(5)\n"
    "v_mfma_f32_32x32x16_f16 v[204:219], v[92:95], v[224:227], v[204:219]\nds_read_b128 v[224:227], v27 offset:880\ns_waitcnt lgkmcnt(5)\n"
    "v_mfma_f32_32x32x16_f16 v[204:219], v[96:99], v[228:231], v[204:219]\nds_read_b128 v[228:231], v27 offset:832\ns_waitcnt lgkmcnt(5)\n"
    "v_mfma_f32_32x32x16_f16 v[204:219], v[100:103], v[232:235], v[204:219]\nds_read_b128 v[232:235], v27 offset:800\ns_waitcnt lgkmcnt(5)\n"
    "v_mfma_f32_32x32x16_f16 v[204:219], v[104:107], v[236:239], v[204:219]\nds_read_b128 v[236:239], v27 offset:752\ns_waitcnt lgkmcnt(5)\n"
    "v_mfma_f32_32x32x16_f16 v[204:219], v[108:111], v[240:243], v[204:219]\nds_read_b128 v[240:243], v27 offset:720\ns_waitcnt lgkmcnt(5)\n"
    "v_mfma_f32_32x32x16_f16 v[204:219], v[112:115], v[220:223], v[204:219]\nds_read_b128 v[220:223], v27 offset:672\ns_waitcnt lgkmcnt(5)\n"
    "v_mfma_f32_32x32x16_f16 v[204:219], v[116:119], v[224:227], v[204:219]\nds_read_b128 v[224:227], v27 offset:640\ns_waitcnt lgkmcnt(5)\ns_nop 0\ns_cbranch_execz .Lhop\n"
    "v_mfma_f32_32x32x16_f16 v[204:219], v[120:123], v[228:231], v[204:219]\nds_read_b128 v[228:231], v27 offset:592\ns_waitcnt lgkmcnt(5)\n"
    "v_mfma_f32_32x32x16_f16 v[204:219], v[124:127], v[232:235], v[204:219]\nds_read_b128 v[232:235], v27 offset:560\ns_waitcnt lgkmcnt(5)\n"
    "v_mfma_f32_32x32x16_f16 v[204:219], v[128:131], v[236:239], v[204:219]\nds_read_b128 v[236:239], v27 offset:512\ns_waitcnt lgkmcnt(5)\n"
    "v_mfma_f32_32x32x16_f16 v[204:219], v[132:135], v[240:243], v[204:219]\nds_read_b128 v[240:243], v27 offset:480\ns_waitcnt lgkmcnt(5)\n"
    "v_mfma_f32_32x32x16_f16 v[204:219], v[136:139], v[220:223], v[204:219]\nds_read_b128 v[220:223], v27 offset:432\ns_waitcnt lgkmcnt(5)\n"
    "v_mfma_f32_32x32x16_f16 v[204:219], v[140:143], v[224:227], v[204:219]\nds_read_b128 v[224:227], v27 offset:400\ns_waitcnt lgkmcnt(5)\n"
    "v_mfma_f32_32x32x16_f16 v[204:219], v[144:147], v[228:231], v[204:219]\nds_read_b128 v[228:231], v27 offset:352\ns_waitcnt lgkmcnt(5)\n"
    "v_mfma_f32_32x32x16_f16 v[204:219], v[148:151], v[232:235], v[204:219]\nds_read_b128 v[232:235], v27 offset:320\ns_waitcnt lgkmcnt(5)\n"
    "v_mfma_f32_32x32x16_f16 v[204:219], v[152:155], v[236:239], v[204:219]\nds_read_b128 v[236:239], v27 offset:272\ns_waitcnt lgkmcnt(5)\n"
    "v_mfma_f32_32x32x16_f16 v[204:219], v[156:159], v[240:243], v[204:219]\nds_read_b128 v[240:243], v27 offset:240\ns_waitcnt lgkmcnt(5)\n"
    "v_mfma_f32_32x32x16_f16 v[204:219], v[160:163], v[220:223], v[204:219]\nds_read_b128 v[220:223], v27 offset:192\ns_waitcnt lgkmcnt(5)\n"
    "v_mfma_f32_32x32x16_f16 v[204:219], v[164:167], v[224:227], v[204:219]\nds_read_b128 v[224:227], v27 offset:160\ns_waitcnt lgkmcnt(5)\n"
    "v_mfma_f32_32x32x16_f16 v[204:219], v[168:171], v[228:231], v[204:219]\nds_read_b128 v[228:231], v27 offset:112\ns_waitcnt lgkmcnt(5)\n"
    "v_mfma_f32_32x32x16_f16 v[204:219], v[172:175], v[232:235], v[204:219]\nds_read_b128 v[232:235], v27 offset:80\ns_waitcnt lgkmcnt(5)\n"
    "v_mfma_f32_32x32x16_f16 v[204:219], v[176:179], v[236:239], v[204:219]\nds_read_b128 v[236:239], v27 offset:32\ns_waitcnt lgkmcnt(5)\n"
    "v_mfma_f32_32x32x16_f16 v[204:219], v[180:183], v[240:243], v[204:219]\ns_waitcnt lgkmcnt(4)\nv_mfma_f32_32x32x16_f16 v[204:219], v[184:187], v[220:223], v[204:219]\n"
    "s_waitcnt lgkmcnt(3)\nv_mfma_f32_32x32x16_f16 v[204:219], v[188:191], v[224:227], v[204:219]\ns_waitcnt lgkmcnt(2)\n"
    "v_mfma_f32_32x32x16_f16 v[204:219], v[192:195], v[228:231], v[204:219]\ns_waitcnt lgkmcnt(1)\nv_mfma_f32_32x32x16_f16 v[204:219], v[196:199], v[232:235], v[204:219]\n"
    "s_waitcnt lgkmcnt(0)\nv_mfma_f32_32x32x16_f16 v[204:219], v[200:203], v[236:239], v[204:219]\ns_nop 11\nv_cvt_pk_bf16_f32 v204, v204, v205\n"
    "v_cvt_pk_bf16_f32 v205, v206, v207\nds_write_b64 v28, v[204:205] offset:0\nv_cvt_pk_bf16_f32 v208, v208, v209\nv_cvt_pk_bf16_f32 v209, v210, v211\n"
    "ds_write_b64 v29, v[208:209] offset:0\nv_cvt_pk_bf16_f32 v212, v212, v213\nv_cvt_pk_bf16_f32 v213, v214, v215\nds_write_b64 v30, v[212:213] offset:0\n"
    "v_cvt_pk_bf16_f32 v216, v216, v217\nv_cvt_pk_bf16_f32 v217, v218, v219\nds_write_b64 v31, v[216:217] offset:0\ns_cmp_lg_u32 s12, 0\ns_cbranch_scc1 .Lz162\n"
    "v_bfe_u32 v95, v0, 5, 1\nv_and_b32 v94, 31, v0\nv_lshlrev_b32 v39, 9, v95\nv_lshl_add_u32 v39, v94, 2, v39\nv_and_b32 v93, 63, v0\ns_lshl_b32 s43, s24, 15\n"
    "s_and_b32 s42, s25, 3\ns_lshl_b32 s41, s42, 13\ns_lshl_b32 s40, s13, 11\ns_add_u32 s39, s43, s41\ns_add_u32 s39, s39, s40\ns_add_u32 s39, s39, 0xd00000\n"
    "v_lshl_add_u32 v34, v93, 2, s39\ns_lshl_b32 s38, s19, 17\ns_lshl_b32 s37, s13, 15\ns_add_u32 s82, s38, s37\ns_add_u32 s82, s82, 0x600800\ns_lshl_b32 s36, s23, 8\n"
    "s_add_u32 s35, s43, s41\ns_add_u32 s35, s35, s40\ns_add_u32 s35, s35, s36\ns_add_u32 s35, s35, 0xd00000\nv_lshl_add_u32 v35, v93, 2, s35\ns_lshr_b32 s34, s18, 4\n"
    "s_lshl_b32 s33, s34, 7\ns_and_b32 s32, s18, 15\ns_lshl_b32 s31, s32, 2\ns_add_u32 s30, s33, s31\ns_add_u32 s30, s30, 0x100e000\nv_mov_b32 v141, s30\nv_mov_b32 v142, 5\n"
    "v_bfe_u32 v95, v0, 2, 4\nv_and_b32 v94, 3, v0\ns_mov_b32 s43, 0x100e000\nv_lshl_add_u32 v143, v95, 7, s43\nv_lshl_add_u32 v143, v94, 4, v143\ns_add_u32 s82, s4, s82\n"
    "s_addc_u32 s83, s5, 0\ns_and_b32 s86, s23, 7\ns_add_u32 s86, s86, 0\ns_and_b32 s86, s86, 7\ns_lshl_b32 s44, s86, 12\ns_add_u32 s52, s82, s44\ns_addc_u32 s53, s83, 0\n"
    "s_and_b32 s86, s23, 7\ns_add_u32 s86, s86, 1\ns_and_b32 s86, s86, 7\ns_lshl_b32 s45, s86, 12\ns_add_u32 s54, s82, s45\ns_addc_u32 s55, s83, 0\ns_and_b32 s86, s23, 7\n"
    "s_add_u32 s86, s86, 2\ns_and_b32 s86, s86, 7\ns_lshl_b32 s46, s86, 12\ns_add_u32 s56, s82, s46\ns_addc_u32 s57, s83, 0\ns_and_b32 s86, s23, 7\ns_add_u32 s86, s86, 3\n"
    "s_and_b32 s86, s86, 7\ns_lshl_b32 s47, s86, 12\ns_add_u32 s58, s82, s47\ns_addc_u32 s59, s83, 0\ns_and_b32 s86, s23, 7\ns_add_u32 s86, s86, 4\ns_and_b32 s86, s86, 7\n"
    "s_lshl_b32 s48, s86, 12\ns_add_u32 s60, s82, s48\ns_addc_u32 s61, s83, 0\ns_and_b32 s86, s23, 7\ns_add_u32 s86, s86, 5\ns_and_b32 s86, s86, 7\ns_lshl_b32 s49, s86, 12\n"
    "s_add_u32 s62, s82, s49\ns_addc_u32 s63, s83, 0\ns_and_b32 s86, s23, 7\ns_add_u32 s86, s86, 6\ns_and_b32 s86, s86, 7\ns_lshl_b32 s50, s86, 12\ns_add_u32 s64, s82, s50\n"
    "s_addc_u32 s65, s83, 0\ns_and_b32 s86, s23, 7\ns_add_u32 s86, s86, 7\ns_and_b32 s86, s86, 7\ns_lshl_b32 s51, s86, 12\ns_add_u32 s66, s82, s51\ns_addc_u32 s67, s83, 0\n"
    "s_mov_b32 s81, 0x3b000000\ns_mov_b32 s84, -1\ns_mov_b32 s85, 0\nv_mov_b32 v241, 0x3f803f80\nv_mov_b64 v[120:121], 0\nv_mov_b64 v[122:123], 0\nv_mov_b64 v[124:125], 0\n"
    "v_mov_b64 v[126:127], 0\nv_mov_b64 v[128:129], 0\nv_mov_b64 v[130:131], 0\nv_mov_b64 v[132:133], 0\nv_mov_b64 v[134:135], 0\nv_mad_u32_u24 v242, s44, 3, v36\n"
    "s_mov_b32 s80, 0x10000\nds_read_b64_tr_b16 v[208:209], v242 offset:0\nds_read_b64_tr_b16 v[210:211], v242 offset:256\nds_read_b64_tr_b16 v[212:213], v242 offset:3072\n"
    "ds_read_b64_tr_b16 v[214:215], v242 offset:3328\nds_read_b64_tr_b16 v[216:217], v242 offset:6144\nds_read_b64_tr_b16 v[218:219], v242 offset:6400\n"
    "ds_read_b64_tr_b16 v[220:221], v242 offset:9216\nds_read_b64_tr_b16 v[222:223], v242 offset:9472\nds_read_b64_tr_b16 v[224:225], v242 offset:1024\n"
    "ds_read_b64_tr_b16 v[226:227], v242 offset:1280\ns_cbranch_execz .Lhop\nds_read_b64_tr_b16 v[228:229], v242 offset:4096\nds_read_b64_tr_b16 v[230:231], v242 offset:4352\n"
    "ds_read_b32 v124, v38 offset:0\nds_read_b32 v128, v38 offset:128\nds_read_b32 v132, v38 offset:256\n.Lz162:\ns_cmp_lt_u32 s3, 2\ns_waitcnt lgkmcnt(0)\ns_barrier\n"
    "s_memrealtime s[88:89]\nds_read_b64 v[44:45], v19\nds_read_b64 v[46:47], v19 offset:10240\ns_cbranch_scc1 .Le164\nds_read_b64 v[48:49], v20\n"
    "ds_read_b64 v[50:51], v20 offset:10240\ns_waitcnt lgkmcnt(0)\nv_dot2c_f32_bf16 v56, s26, v48\nv_dot2c_f32_bf16 v57, s14, v48\nv_dot2c_f32_bf16 v58, s26, v49\n"
    "v_dot2c_f32_bf16 v59, s14, v49\nv_dot2c_f32_bf16 v60, s27, v50\nv_dot2c_f32_bf16 v61, s15, v50\nv_dot2c_f32_bf16 v62, s27, v51\nv_dot2c_f32_bf16 v63, s15, v51\n"
    "s_branch .Lz161\n.Le164:\ns_cmp_lg_u32 s13, 0\ns_cbranch_scc1 .Lz161\nglobal_load_dwordx4 v[40:43], v33, s[4:5] sc1\n.Lz161:\ns_waitcnt lgkmcnt(0)\n"
    "v_dot2c_f32_bf16 v56, s26, v44\nv_dot2c_f32_bf16 v57, s14, v44\nv_dot2c_f32_bf16 v58, s26, v45\nv_dot2c_f32_bf16 v59, s14, v45\nv_dot2c_f32_bf16 v60, s27, v46\n"
    "v_dot2c_f32_bf16 v61, s15, v46\nv_dot2c_f32_bf16 v62, s27, v47\nv_dot2c_f32_bf16 v63, s15, v47\nv_cvt_pk_bf16_f32 v52, v56, v60\nv_cvt_pk_bf16_f32 v53, v57, v61\n"
    "v_cvt_pk_bf16_f32 v54, v58, v62\nv_cvt_pk_bf16_f32 v55, v59, v63\nglobal_store_dwordx4 v21, v[52:55], s[4:5] sc1\ns_cmp_lg_u32 s12, 0\ns_cbranch_scc1 .Lz163x\n"
    "s_mov_b32 exec_lo, 0xf0000000\ns_mov_b32 exec_hi, 0xf0000000\nglobal_store_dwordx4 v24, v[52:55], s[4:5] sc1\ns_mov_b64 exec, -1\ns_cmp_lg_u32 s3, 0\n"
    "s_cbranch_scc1 .Lqg166\ns_waitcnt vmcnt(2)\ns_branch .Lz163y\n.Lz163x:\ns_cmp_lg_u32 s3, 0\ns_cbranch_scc1 .Lz163\ns_waitcnt vmcnt(1)\n.Lz163y:\n"
    "s_sub_u32 s87, s100, 300\nv_subrev_u32 v40, s87, v40\nv_subrev_u32 v41, s87, v41\nv_subrev_u32 v42, s87, v42\nv_subrev_u32 v43, s87, v43\nv_max3_u32 v40, v40, v41, v42\n"
    "v_max_u32 v40, v40, v43\ns_nop 1\nv_max_u32_dpp v40, v40, v40 row_ror:1 row_mask:0xf bank_mask:0xf\ns_nop 1\n"
    "v_max_u32_dpp v40, v40, v40 row_ror:2 row_mask:0xf bank_mask:0xf\ns_nop 1\nv_max_u32_dpp v40, v40, v40 row_ror:4 row_mask:0xf bank_mask:0xf\ns_nop 1\n"
    "v_max_u32_dpp v40, v40, v40 row_ror:8 row_mask:0xf bank_mask:0xf\nv_mov_b32 v41, v40\ns_nop 1\nv_permlane16_swap_b32 v40, v41\nv_max_u32 v40, v40, v41\n"
    "v_mov_b32 v41, v40\ns_nop 1\nv_permlane32_swap_b32 v40, v41\nv_max_u32 v40, v40, v41\ns_nop 0\nv_readfirstlane_b32 s87, v40\ns_min_u32 s87, s87, 370\n"
    "s_max_u32 s87, s87, 300\ns_sub_u32 s87, s87, 300\ns_mov_b32 s100, s88\n.Lz163:\ns_cmp_eq_u32 s12, 0\ns_cbranch_scc1 .Lqg166\n"
    ".Lfin165:\ns_lshl_b32 s43, s18, 13\ns_add_u32 s42, s43, 0x1200000\nv_lshl_add_u32 v16, v0, 4, s42\nv_lshrrev_b32 v239, 3, v0\nv_and_b32 v238, 7, v0\n"
    "s_lshl_b32 s41, s25, 16\ns_lshl_b32 s40, s24, 7\ns_add_u32 s39, s41, s40\nv_lshl_add_u32 v17, v239, 10, s39\nv_lshl_add_u32 v17, v238, 4, v17\ns_lshl_b32 s38, s3, 4\n"
    "v_lshl_add_u32 v18, v0, 4, s38\nv_mul_u32_u24 v237, 0x410, v238\nv_lshl_add_u32 v22, v239, 2, v237\ns_mov_b32 s80, 0x10000\ns_cmp_lg_u32 s3, 0\ns_cbranch_scc1 .Lz170\n"
    "s_waitcnt lgkmcnt(0)\ns_add_u32 s87, s87, s100\ns_add_u32 s87, s87, 25\n.Lj171:\ns_memrealtime s[100:101]\ns_waitcnt lgkmcnt(0)\ns_sub_u32 s101, s87, s100\n"
    "s_cmp_le_i32 s101, 0\ns_cbranch_scc0 .Lj171\n.Lz170:\ns_barrier\n.Lf168:\nglobal_load_dwordx4 v[44:47], v16, s[4:5] sc1\ns_waitcnt vmcnt(0)\nds_write_b128 v18, v[44:47]\n"
    "v_max3_u32 v48, v44, v45, v46\nv_max_u32 v48, v48, v47\nv_cmp_eq_u32 vcc, -1, v48\ns_waitcnt lgkmcnt(0)\ns_cbranch_vccnz .Lf169\n.Lf169x:\ns_barrier\n"
    "ds_read2st64_b32 v[44:45], v22 offset1:1\nds_read2st64_b32 v[46:47], v22 offset0:2 offset1:3\ns_waitcnt lgkmcnt(0)\nglobal_store_dwordx4 v17, v[44:47], s[6:7] sc1\n"
    "s_endpgm\n.Lf169:\ns_sub_u32 s80, s80, 1\ns_cbranch_scc0 .Lf168\ns_branch .Lf169x\n"
    ".Lhcv156:\ns_setprio 0\ns_bitcmp1_b32 s13, 0\ns_cbranch_scc1 .Lk173\ns_cmp_lg_u32 s12, 0\ns_waitcnt lgkmcnt(0)\ns_barrier\nds_read_b64 v[192:193], v11 offset:0\n"
    "ds_read_b128 v[194:197], v10 offset:0\nds_read_b64 v[198:199], v12 offset:0\nds_read_b64 v[200:201], v11 offset:1280\nds_read_b128 v[202:205], v10 offset:1280\n"
    "ds_read_b64 v[206:207], v12 offset:1280\nds_read_b64 v[208:209], v11 offset:2560\nds_read_b128 v[210:213], v10 offset:2560\nds_read_b64 v[214:215], v12 offset:2560\n"
    "s_cbranch_scc1 .Lz176\nglobal_store_dwordx4 v37, v[252:255], s[4:5] offset:0 sc1\nglobal_store_dwordx4 v37, v[252:255], s[4:5] offset:1024 sc1\n"
    "global_store_dwordx4 v37, v[252:255], s[54:55] offset:0 sc1\nglobal_store_dwordx4 v37, v[252:255], s[54:55] offset:1024 sc1\n.Lz176:\ns_waitcnt lgkmcnt(6)\n"
    "v_alignbit_b32 v216, v194, v193, 16\nv_alignbit_b32 v217, v195, v194, 16\nv_alignbit_b32 v218, v196, v195, 16\nv_alignbit_b32 v219, v197, v196, 16\n"
    "v_alignbit_b32 v220, v198, v197, 16\nv_pk_add_f16 v224, v193, v216\nv_pk_add_f16 v225, v194, v217\nv_pk_add_f16 v226, v195, v218\nv_pk_add_f16 v227, v196, v219\n"
    "v_pk_add_f16 v228, v197, v220\nv_pk_add_f16 v236, v216, v225\nv_pk_add_f16 v237, v217, v226\nv_pk_add_f16 v238, v218, v227\nv_pk_add_f16 v239, v219, v228\n"
    "ds_write_b64 v13, v[236:237] offset:4040\nv_pk_add_f16 v232, v224, v195\nv_pk_add_f16 v233, v225, v196\nv_pk_add_f16 v234, v226, v197\nv_pk_add_f16 v235, v227, v198\n"
    "ds_write_b64 v14, v[238:239] offset:4032\nv_pk_add_f16 v232, v232, v225\nv_pk_add_f16 v233, v233, v226\nv_pk_add_f16 v234, v234, v227\nv_pk_add_f16 v235, v235, v228\n"
    "ds_write_b128 v13, v[232:235] offset:0\ns_cbranch_execz .Lhop\ns_mov_b64 exec, 1\nds_write_b32 v25, v26 offset:48\ns_mov_b64 exec, -1\ns_waitcnt lgkmcnt(6)\n"
    "v_alignbit_b32 v216, v202, v201, 16\nv_alignbit_b32 v217, v203, v202, 16\nv_alignbit_b32 v218, v204, v203, 16\nv_alignbit_b32 v219, v205, v204, 16\n"
    "v_alignbit_b32 v220, v206, v205, 16\nv_pk_add_f16 v224, v201, v216\nv_pk_add_f16 v225, v202, v217\nv_pk_add_f16 v226, v203, v218\nv_pk_add_f16 v227, v204, v219\n"
    "v_pk_add_f16 v228, v205, v220\nv_pk_add_f16 v236, v216, v225\nv_pk_add_f16 v237, v217, v226\nv_pk_add_f16 v238, v218, v227\nv_pk_add_f16 v239, v219, v228\n"
    "ds_write_b64 v13, v[236:237] offset:5192\nv_pk_add_f16 v232, v224, v203\nv_pk_add_f16 v233, v225, v204\nv_pk_add_f16 v234, v226, v205\nv_pk_add_f16 v235, v227, v206\n"
    "ds_write_b64 v14, v[238:239] offset:5184\nv_pk_add_f16 v232, v232, v225\nv_pk_add_f16 v233, v233, v226\nv_pk_add_f16 v234, v234, v227\nv_pk_add_f16 v235, v235, v228\n"
    "ds_write_b128 v13, v[232:235] offset:1152\ns_waitcnt lgkmcnt(6)\nv_alignbit_b32 v216, v210, v209, 16\nv_alignbit_b32 v217, v211, v210, 16\n"
    "v_alignbit_b32 v218, v212, v211, 16\nv_alignbit_b32 v219, v213, v212, 16\nv_alignbit_b32 v220, v214, v213, 16\nv_pk_add_f16 v224, v209, v216\n"
    "v_pk_add_f16 v225, v210, v217\nv_pk_add_f16 v226, v211, v218\nv_pk_add_f16 v227, v212, v219\nv_pk_add_f16 v228, v213, v220\nv_pk_add_f16 v236, v216, v225\n"
    "v_pk_add_f16 v237, v217, v226\nv_pk_add_f16 v238, v218, v227\nv_pk_add_f16 v239, v219, v228\nds_write_b64 v15, v[236:237] offset:6344\nv_pk_add_f16 v232, v224, v211\n"
    "v_pk_add_f16 v233, v225, v212\nv_pk_add_f16 v234, v226, v213\nv_pk_add_f16 v235, v227, v214\nds_write_b64 v35, v[238:239] offset:6336\nv_pk_add_f16 v232, v232, v225\n"
    "v_pk_add_f16 v233, v233, v226\nv_pk_add_f16 v234, v234, v227\nv_pk_add_f16 v235, v235, v228\nds_write_b128 v15, v[232:235] offset:2304\ns_setprio 0\n"
    "ds_read_b64_tr_b16 v[40:41], v27 offset:1984\nds_read_b64_tr_b16 v[42:43], v27 offset:2048\nds_read_b64_tr_b16 v[44:45], v27 offset:1728\n"
    "ds_read_b64_tr_b16 v[46:47], v27 offset:1792\nds_read_b64_tr_b16 v[48:49], v27 offset:1408\nds_read_b64_tr_b16 v[50:51], v27 offset:1472\n"
    "ds_read_b64_tr_b16 v[52:53], v27 offset:1152\nds_read_b64_tr_b16 v[54:55], v27 offset:1216\nds_read_b64_tr_b16 v[240:241], v27 offset:832\n"
    "ds_read_b64_tr_b16 v[242:243], v27 offset:896\ns_waitcnt lgkmcnt(8)\nv_mfma_f32_32x32x16_f16 v[192:207], v[40:43], v[128:131], 0\n"
    "ds_read_b64_tr_b16 v[40:41], v27 offset:576\nds_read_b64_tr_b16 v[42:43], v27 offset:640\ns_waitcnt lgkmcnt(8)\n"
    "v_mfma_f32_32x32x16_f16 v[192:207], v[44:47], v[132:135], v[192:207]\nds_read_b64_tr_b16 v[44:45], v27 offset:6016\nds_read_b64_tr_b16 v[46:47], v27 offset:6080\n"
    "v_fmac_f32 v56, s16, v244\nv_fmac_f32 v57, s16, v245\ns_waitcnt lgkmcnt(8)\nv_mfma_f32_32x32x16_f16 v[192:207], v[48:51], v[136:139], v[192:207]\n"
    "ds_read_b64_tr_b16 v[48:49], v27 offset:5760\nds_read_b64_tr_b16 v[50:51], v27 offset:5824\nv_fmac_f32 v58, s16, v246\nv_fmac_f32 v59, s16, v247\ns_waitcnt lgkmcnt(8)\n"
    "v_mfma_f32_32x32x16_f16 v[192:207], v[52:55], v[140:143], v[192:207]\nds_read_b64_tr_b16 v[52:53], v27 offset:5440\nds_read_b64_tr_b16 v[54:55], v27 offset:5504\n"
    "v_fmac_f32 v60, s17, v248\nv_fmac_f32 v61, s17, v249\ns_waitcnt lgkmcnt(8)\nv_mfma_f32_32x32x16_f16 v[192:207], v[240:243], v[160:163], v[192:207]\n"
    "ds_read_b64_tr_b16 v[240:241], v27 offset:5184\nds_read_b64_tr_b16 v[242:243], v27 offset:5248\nv_fmac_f32 v62, s17, v250\nv_fmac_f32 v63, s17, v251\n"
    "s_waitcnt lgkmcnt(8)\nv_mfma_f32_32x32x16_f16 v[192:207], v[40:43], v[164:167], v[192:207]\nds_read_b64_tr_b16 v[40:41], v27 offset:4864\n"
    "ds_read_b64_tr_b16 v[42:43], v27 offset:4928\ns_waitcnt lgkmcnt(8)\nv_mfma_f32_32x32x16_f16 v[192:207], v[44:47], v[144:147], v[192:207]\n"
    "ds_read_b64_tr_b16 v[44:45], v27 offset:4608\nds_read_b64_tr_b16 v[46:47], v27 offset:4672\ns_waitcnt lgkmcnt(8)\n"
    "v_mfma_f32_32x32x16_f16 v[192:207], v[48:51], v[148:151], v[192:207]\nds_read_b32 v244, v30\nds_read_b64 v[246:247], v31\nds_read_b32 v245, v30 offset:6480\n"
    "ds_read_b64 v[248:249], v31 offset:6480\ns_waitcnt lgkmcnt(10)\nv_mfma_f32_32x32x16_f16 v[192:207], v[52:55], v[152:155], v[192:207]\ns_waitcnt lgkmcnt(8)\n"
    "v_mfma_f32_32x32x16_f16 v[192:207], v[240:243], v[156:159], v[192:207]\ns_waitcnt lgkmcnt(6)\nv_mfma_f32_32x32x16_f16 v[192:207], v[40:43], v[176:179], v[192:207]\n"
    "s_waitcnt lgkmcnt(0)\nv_fma_mix_f32 v56, v244, s28, v56 op_sel:[1,0,0] op_sel_hi:[1,0,0]\nv_fma_mix_f32 v57, v246, s28, v57 op_sel:[0,0,0] op_sel_hi:[1,0,0]\n"
    "v_fma_mix_f32 v58, v246, s28, v58 op_sel:[1,0,0] op_sel_hi:[1,0,0]\ns_waitcnt lgkmcnt(4)\nv_mfma_f32_32x32x16_f16 v[192:207], v[44:47], v[180:183], v[192:207]\n"
    "v_fma_mix_f32 v59, v247, s28, v59 op_sel:[0,0,0] op_sel_hi:[1,0,0]\nv_fma_mix_f32 v60, v245, s29, v60 op_sel:[1,0,0] op_sel_hi:[1,0,0]\n"
    "v_fma_mix_f32 v61, v248, s29, v61 op_sel:[0,0,0] op_sel_hi:[1,0,0]\nv_fma_mix_f32 v62, v248, s29, v62 op_sel:[1,0,0] op_sel_hi:[1,0,0]\n"
    "v_fma_mix_f32 v63, v249, s29, v63 op_sel:[0,0,0] op_sel_hi:[1,0,0]\ns_nop 6\nv_cvt_pk_bf16_f32 v192, v192, v193\nv_cvt_pk_bf16_f32 v193, v194, v195\n"
    "ds_write_b64 v32, v[192:193] offset:1024\nv_cvt_pk_bf16_f32 v196, v196, v197\nv_cvt_pk_bf16_f32 v197, v198, v199\nds_write_b64 v32, v[196:197] offset:1536\n"
    "v_cvt_pk_bf16_f32 v200, v200, v201\nv_cvt_pk_bf16_f32 v201, v202, v203\nds_write_b64 v32, v[200:201] offset:2048\nv_cvt_pk_bf16_f32 v204, v204, v205\n"
    "v_cvt_pk_bf16_f32 v205, v206, v207\nds_write_b64 v32, v[204:205] offset:2560\ns_branch .Lk174\n.Lk173:\ns_cmp_lg_u32 s12, 0\ns_waitcnt lgkmcnt(0)\ns_barrier\n"
    "ds_read_b64 v[192:193], v11 offset:0\ns_cbranch_execz .Lhop\nds_read_b128 v[194:197], v10 offset:0\nds_read_b64 v[198:199], v12 offset:0\n"
    "ds_read_b64 v[200:201], v11 offset:1280\nds_read_b128 v[202:205], v10 offset:1280\nds_read_b64 v[206:207], v12 offset:1280\ns_cbranch_scc1 .Lz177\n"
    "global_store_dwordx4 v37, v[252:255], s[4:5] offset:0 sc1\nglobal_store_dwordx4 v37, v[252:255], s[4:5] offset:1024 sc1\n"
    "global_store_dwordx4 v37, v[252:255], s[54:55] offset:0 sc1\nglobal_store_dwordx4 v37, v[252:255], s[54:55] offset:1024 sc1\n.Lz177:\ns_waitcnt lgkmcnt(3)\n"
    "v_alignbit_b32 v216, v193, v192, 16\nv_alignbit_b32 v217, v194, v193, 16\nv_alignbit_b32 v218, v195, v194, 16\nv_alignbit_b32 v219, v196, v195, 16\n"
    "v_alignbit_b32 v220, v197, v196, 16\nv_alignbit_b32 v221, v198, v197, 16\nv_alignbit_b32 v222, v199, v198, 16\nv_pk_add_f16 v224, v192, v216\n"
    "v_pk_add_f16 v225, v193, v217\nv_pk_add_f16 v226, v194, v218\nv_pk_add_f16 v227, v195, v219\nv_pk_add_f16 v228, v196, v220\nv_pk_add_f16 v229, v197, v221\n"
    "v_pk_add_f16 v230, v198, v222\nv_pk_add_f16 v223, v226, v227\nv_pk_add_f16 v231, v228, v229\nv_pk_add_f16 v232, v224, v196\nv_pk_add_f16 v233, v225, v197\n"
    "v_pk_add_f16 v234, v226, v198\nv_pk_add_f16 v235, v227, v199\nv_pk_add_f16 v208, v225, v223\nv_pk_add_f16 v209, v223, v228\nv_pk_add_f16 v210, v227, v231\n"
    "v_pk_add_f16 v211, v231, v230\nv_pk_add_f16 v236, v208, v216\nv_pk_add_f16 v237, v209, v217\nv_pk_add_f16 v238, v210, v218\nv_pk_add_f16 v239, v211, v219\n"
    "ds_write_b128 v14, v[236:239] offset:0\nv_pk_add_f16 v232, v232, v208\nv_pk_add_f16 v233, v233, v209\nv_pk_add_f16 v234, v234, v210\nv_pk_add_f16 v235, v235, v211\n"
    "ds_write_b128 v13, v[232:235] offset:0\ns_waitcnt lgkmcnt(2)\nv_alignbit_b32 v216, v201, v200, 16\nv_alignbit_b32 v217, v202, v201, 16\n"
    "v_alignbit_b32 v218, v203, v202, 16\nv_alignbit_b32 v219, v204, v203, 16\nv_alignbit_b32 v220, v205, v204, 16\nv_alignbit_b32 v221, v206, v205, 16\n"
    "v_alignbit_b32 v222, v207, v206, 16\nv_pk_add_f16 v224, v200, v216\nv_pk_add_f16 v225, v201, v217\nv_pk_add_f16 v226, v202, v218\nv_pk_add_f16 v227, v203, v219\n"
    "v_pk_add_f16 v228, v204, v220\nv_pk_add_f16 v229, v205, v221\nv_pk_add_f16 v230, v206, v222\nv_pk_add_f16 v223, v226, v227\nv_pk_add_f16 v231, v228, v229\n"
    "v_pk_add_f16 v232, v224, v204\nv_pk_add_f16 v233, v225, v205\nv_pk_add_f16 v234, v226, v206\nv_pk_add_f16 v235, v227, v207\nv_pk_add_f16 v208, v225, v223\n"
    "v_pk_add_f16 v209, v223, v228\nv_pk_add_f16 v210, v227, v231\nv_pk_add_f16 v211, v231, v230\nv_pk_add_f16 v236, v208, v216\nv_pk_add_f16 v237, v209, v217\n"
    "v_pk_add_f16 v238, v210, v218\nv_pk_add_f16 v239, v211, v219\nds_write_b128 v14, v[236:239] offset:1152\nv_pk_add_f16 v232, v232, v208\nv_pk_add_f16 v233, v233, v209\n"
    "v_pk_add_f16 v234, v234, v210\nv_pk_add_f16 v235, v235, v211\nds_write_b128 v15, v[232:235] offset:1152\nv_fmac_f32 v56, s16, v244\nv_fmac_f32 v57, s16, v245\n"
    "v_fmac_f32 v58, s16, v246\nv_fmac_f32 v59, s16, v247\nv_fmac_f32 v60, s17, v248\nv_fmac_f32 v61, s17, v249\nv_fmac_f32 v62, s17, v250\nv_fmac_f32 v63, s17, v251\n"
    "ds_read_b32 v244, v30\nds_read_b64 v[246:247], v31\nds_read_b32 v245, v30 offset:6480\nds_read_b64 v[248:249], v31 offset:6480\ns_setprio 0\nds_read_b32 v250, v24\n"
    "ds_read_b64_tr_b16 v[40:41], v28 offset:512\nds_read_b64_tr_b16 v[42:43], v28 offset:640\nds_read_b64_tr_b16 v[44:45], v28 offset:0\n"
    "ds_read_b64_tr_b16 v[46:47], v28 offset:128\nds_read_b64_tr_b16 v[48:49], v29 offset:512\nds_read_b64_tr_b16 v[50:51], v29 offset:640\n"
    "ds_read_b64_tr_b16 v[52:53], v29 offset:0\nds_read_b64_tr_b16 v[54:55], v29 offset:128\nds_read_b64_tr_b16 v[240:241], v28 offset:2816\n"
    "ds_read_b64_tr_b16 v[242:243], v28 offset:2944\ns_waitcnt lgkmcnt(8)\nv_mfma_f32_32x32x16_f16 v[224:239], v[40:43], v[160:163], 0\nv_cmp_gt_i32 vcc, v26, v250\n"
    "ds_read_b64_tr_b16 v[40:41], v28 offset:2304\nds_read_b64_tr_b16 v[42:43], v28 offset:2432\ns_waitcnt lgkmcnt(8)\n"
    "v_mfma_f32_32x32x16_f16 v[224:239], v[44:47], v[164:167], v[224:239]\nds_read_b64_tr_b16 v[44:45], v29 offset:2816\nds_read_b64_tr_b16 v[46:47], v29 offset:2944\n"
    "s_waitcnt lgkmcnt(8)\nv_mfma_f32_32x32x16_f16 v[208:223], v[48:51], v[168:171], 0\nds_read_b64_tr_b16 v[48:49], v29 offset:2304\n"
    "ds_read_b64_tr_b16 v[50:51], v29 offset:2432\ns_waitcnt lgkmcnt(8)\nv_mfma_f32_32x32x16_f16 v[208:223], v[52:55], v[172:175], v[208:223]\ns_cbranch_vccnz .Lw178\n"
    ".Lr179:\nds_read_b64_tr_b16 v[52:53], v27 offset:832\nds_read_b64_tr_b16 v[54:55], v27 offset:896\ns_waitcnt lgkmcnt(8)\n"
    "v_mfma_f32_32x32x16_f16 v[224:239], v[240:243], v[176:179], v[224:239]\nds_read_b64_tr_b16 v[240:241], v27 offset:576\nds_read_b64_tr_b16 v[242:243], v27 offset:640\n"
    "v_fma_mix_f32 v56, v244, s28, v56 op_sel:[1,0,0] op_sel_hi:[1,0,0]\nv_fma_mix_f32 v57, v246, s28, v57 op_sel:[0,0,0] op_sel_hi:[1,0,0]\ns_waitcnt lgkmcnt(8)\n"
    "v_mfma_f32_32x32x16_f16 v[224:239], v[40:43], v[180:183], v[224:239]\nds_read_b64_tr_b16 v[40:41], v27 offset:256\nds_read_b64_tr_b16 v[42:43], v27 offset:320\n"
    "v_fma_mix_f32 v58, v246, s28, v58 op_sel:[1,0,0] op_sel_hi:[1,0,0]\nv_fma_mix_f32 v59, v247, s28, v59 op_sel:[0,0,0] op_sel_hi:[1,0,0]\ns_waitcnt lgkmcnt(8)\n"
    "v_mfma_f32_32x32x16_f16 v[208:223], v[44:47], v[184:187], v[208:223]\nds_read_b64_tr_b16 v[44:45], v27 offset:0\nds_read_b64_tr_b16 v[46:47], v27 offset:64\n"
    "s_cbranch_execz .Lhop\nv_fma_mix_f32 v60, v245, s29, v60 op_sel:[1,0,0] op_sel_hi:[1,0,0]\nv_fma_mix_f32 v61, v248, s29, v61 op_sel:[0,0,0] op_sel_hi:[1,0,0]\n"
    "s_waitcnt lgkmcnt(8)\nv_mfma_f32_32x32x16_f16 v[208:223], v[48:51], v[188:191], v[208:223]\nds_read_b64_tr_b16 v[48:49], v27 offset:4864\n"
    "ds_read_b64_tr_b16 v[50:51], v27 offset:4928\nv_fma_mix_f32 v62, v248, s29, v62 op_sel:[1,0,0] op_sel_hi:[1,0,0]\n"
    "v_fma_mix_f32 v63, v249, s29, v63 op_sel:[0,0,0] op_sel_hi:[1,0,0]\ns_waitcnt lgkmcnt(8)\nv_mfma_f32_32x32x16_f16 v[192:207], v[52:55], v[128:131], 0\n"
    "ds_read_b64_tr_b16 v[52:53], v27 offset:4608\nds_read_b64_tr_b16 v[54:55], v27 offset:4672\ns_waitcnt lgkmcnt(8)\n"
    "v_mfma_f32_32x32x16_f16 v[192:207], v[240:243], v[132:135], v[192:207]\nds_read_b64_tr_b16 v[240:241], v27 offset:4288\nds_read_b64_tr_b16 v[242:243], v27 offset:4352\n"
    "v_cvt_pk_bf16_f32 v224, v224, v225\nv_cvt_pk_bf16_f32 v225, v226, v227\nds_write_b64 v33, v[224:225] offset:0\ns_waitcnt lgkmcnt(9)\n"
    "v_mfma_f32_32x32x16_f16 v[192:207], v[40:43], v[136:139], v[192:207]\nds_read_b64_tr_b16 v[40:41], v27 offset:4032\nds_read_b64_tr_b16 v[42:43], v27 offset:4096\n"
    "v_cvt_pk_bf16_f32 v228, v228, v229\nv_cvt_pk_bf16_f32 v229, v230, v231\nds_write_b64 v36, v[228:229] offset:0\ns_waitcnt lgkmcnt(10)\n"
    "v_mfma_f32_32x32x16_f16 v[192:207], v[44:47], v[140:143], v[192:207]\nv_add_f32 v232, v232, v216\nv_add_f32 v233, v233, v217\nv_add_f32 v234, v234, v218\n"
    "v_add_f32 v235, v235, v219\ns_waitcnt lgkmcnt(8)\nv_mfma_f32_32x32x16_f16 v[192:207], v[48:51], v[144:147], v[192:207]\nv_add_f32 v236, v236, v220\n"
    "v_add_f32 v237, v237, v221\nv_add_f32 v238, v238, v222\nv_add_f32 v239, v239, v223\ns_waitcnt lgkmcnt(6)\n"
    "v_mfma_f32_32x32x16_f16 v[192:207], v[52:55], v[148:151], v[192:207]\nv_cvt_pk_bf16_f32 v232, v232, v233\nv_cvt_pk_bf16_f32 v233, v234, v235\n"
    "ds_write_b64 v33, v[232:233] offset:1024\ns_waitcnt lgkmcnt(5)\nv_mfma_f32_32x32x16_f16 v[192:207], v[240:243], v[152:155], v[192:207]\n"
    "v_cvt_pk_bf16_f32 v236, v236, v237\nv_cvt_pk_bf16_f32 v237, v238, v239\nds_write_b64 v36, v[236:237] offset:1024\ns_waitcnt lgkmcnt(3)\n"
    "v_mfma_f32_32x32x16_f16 v[192:207], v[40:43], v[156:159], v[192:207]\ns_nop 11\nv_cvt_pk_bf16_f32 v192, v192, v193\nv_cvt_pk_bf16_f32 v193, v194, v195\n"
    "ds_write_b64 v32, v[192:193] offset:0\nv_cvt_pk_bf16_f32 v196, v196, v197\nv_cvt_pk_bf16_f32 v197, v198, v199\nds_write_b64 v32, v[196:197] offset:512\n.Lk174:\n"
    "s_setprio 1\ns_waitcnt vmcnt(0) lgkmcnt(0)\ns_barrier\nds_read_b64 v[208:209], v19\nds_read_b64 v[210:211], v19 offset:10240\nds_read_b64 v[212:213], v20\n"
    "ds_read_b64 v[214:215], v20 offset:10240\nds_read_b64 v[216:217], v34\nds_read_b64 v[218:219], v34 offset:10240\ns_cmp_lg_u32 s12, 0\ns_waitcnt lgkmcnt(4)\n"
    "v_dot2c_f32_bf16 v56, s26, v208\nv_dot2c_f32_bf16 v57, s14, v208\nv_dot2c_f32_bf16 v58, s26, v209\nv_dot2c_f32_bf16 v59, s14, v209\nv_dot2c_f32_bf16 v60, s27, v210\n"
    "v_dot2c_f32_bf16 v61, s15, v210\nv_dot2c_f32_bf16 v62, s27, v211\nv_dot2c_f32_bf16 v63, s15, v211\ns_waitcnt lgkmcnt(2)\nv_dot2c_f32_bf16 v56, s26, v212\n"
    "v_dot2c_f32_bf16 v57, s14, v212\nv_dot2c_f32_bf16 v58, s26, v213\nv_dot2c_f32_bf16 v59, s14, v213\nv_dot2c_f32_bf16 v60, s27, v214\nv_dot2c_f32_bf16 v61, s15, v214\n"
    "v_dot2c_f32_bf16 v62, s27, v215\nv_dot2c_f32_bf16 v63, s15, v215\ns_waitcnt lgkmcnt(0)\nv_dot2c_f32_bf16 v56, s26, v216\nv_dot2c_f32_bf16 v57, s14, v216\n"
    "v_dot2c_f32_bf16 v58, s26, v217\nv_dot2c_f32_bf16 v59, s14, v217\nv_dot2c_f32_bf16 v60, s27, v218\nv_dot2c_f32_bf16 v61, s15, v218\nv_dot2c_f32_bf16 v62, s27, v219\n"
    "v_dot2c_f32_bf16 v63, s15, v219\nv_cvt_pk_bf16_f32 v52, v56, v60\nv_cvt_pk_bf16_f32 v53, v57, v61\nv_cvt_pk_bf16_f32 v54, v58, v62\nv_cvt_pk_bf16_f32 v55, v59, v63\n"
    "global_store_dwordx4 v21, v[52:55], s[4:5] sc1\ns_setprio 0\ns_cbranch_scc1 .Lfin165\ns_mov_b32 exec_lo, 0xf0000000\ns_mov_b32 exec_hi, 0xf0000000\n"
    "global_store_dwordx4 v38, v[52:55], s[4:5] sc1\ns_mov_b64 exec, -1\n"
    "s_max_u32 s43, s19, 1\ns_lshl_b32 s42, s43, 14\ns_lshl_b32 s41, s13, 12\ns_add_u32 s46, s42, s41\ns_add_u32 s46, s46, 0xd7c000\nv_and_b32 v223, 63, v0\n"
    "v_lshlrev_b32 v224, 2, v223\ns_add_u32 s48, s4, s46\ns_addc_u32 s49, s5, 0\ns_setprio 3\ns_barrier\nglobal_load_dword v192, v224, s[48:49] offset:0 sc1\n"
    "global_load_dword v193, v224, s[48:49] offset:256 sc1\nglobal_load_dword v194, v224, s[48:49] offset:512 sc1\nglobal_load_dword v195, v224, s[48:49] offset:768 sc1\n"
    "global_load_dword v196, v224, s[48:49] offset:1024 sc1\nglobal_load_dword v197, v224, s[48:49] offset:1280 sc1\nglobal_load_dword v198, v224, s[48:49] offset:1536 sc1\n"
    "global_load_dword v199, v224, s[48:49] offset:1792 sc1\nglobal_load_dword v200, v224, s[48:49] offset:2048 sc1\nglobal_load_dword v201, v224, s[48:49] offset:2304 sc1\n"
    "global_load_dword v202, v224, s[48:49] offset:2560 sc1\nglobal_load_dword v203, v224, s[48:49] offset:2816 sc1\nglobal_load_dword v204, v224, s[48:49] offset:3072 sc1\n"
    "global_load_dword v205, v224, s[48:49] offset:3328 sc1\nglobal_load_dword v206, v224, s[48:49] offset:3584 sc1\nglobal_load_dword v207, v224, s[48:49] offset:3840 sc1\n"
    "s_setprio 0\ns_mov_b32 s12, 8388608\nv_add_u32 v9, 8388608, v9\nv_bfe_u32 v223, v0, 3, 1\nv_bfe_u32 v222, v0, 4, 2\nv_bfe_u32 v221, v0, 2, 1\nv_and_b32 v220, 3, v0\n"
    "s_mul_i32 s43, s13, 0x6000\ns_add_u32 s42, s43, 0x400\nv_lshl_add_u32 v229, v223, 9, s42\nv_lshl_add_u32 v229, v222, 7, v229\nv_lshl_add_u32 v229, v221, 6, v229\n"
    "v_lshl_add_u32 v229, v220, 3, v229\nv_lshl_add_u32 v229, v223, 5, v229\nv_lshl_add_u32 v219, v223, 9, s42\nv_lshl_add_u32 v219, v222, 7, v219\n"
    "v_lshl_add_u32 v219, v221, 6, v219\nv_lshl_add_u32 v219, v220, 3, v219\nv_lshl_add_u32 v219, v223, 5, v219\nv_xor_b32 v230, 32, v219\ns_lshr_b32 s41, s13, 1\n"
    "s_lshl_b32 s40, s41, 7\ns_and_b32 s39, s13, 1\ns_lshl_b32 s38, s39, 6\ns_add_u32 s37, s40, s38\ns_add_u32 s37, s37, 0x27c80\nv_lshl_add_u32 v231, v222, 4, s37\n"
    "ds_read_b128 v[40:43], v231\nds_read_b64_tr_b16 v[128:129], v229 offset:0\nds_read_b64_tr_b16 v[130:131], v229 offset:3072\ns_nop 0\ns_cbranch_execz .Lhop\n"
    "ds_read_b64_tr_b16 v[132:133], v229 offset:6144\nds_read_b64_tr_b16 v[134:135], v229 offset:9216\nds_read_b64_tr_b16 v[136:137], v229 offset:12288\n"
    "ds_read_b64_tr_b16 v[138:139], v229 offset:15360\nds_read_b64_tr_b16 v[140:141], v229 offset:18432\nds_read_b64_tr_b16 v[142:143], v229 offset:21504\n"
    "ds_read_b64_tr_b16 v[144:145], v230 offset:0\nds_read_b64_tr_b16 v[146:147], v230 offset:3072\nds_read_b64_tr_b16 v[148:149], v230 offset:6144\n"
    "ds_read_b64_tr_b16 v[150:151], v230 offset:9216\nds_read_b64_tr_b16 v[152:153], v230 offset:12288\nds_read_b64_tr_b16 v[154:155], v230 offset:15360\n"
    "ds_read_b64_tr_b16 v[156:157], v230 offset:18432\nds_read_b64_tr_b16 v[158:159], v230 offset:21504\nds_read_b64_tr_b16 v[160:161], v229 offset:1024\n"
    "ds_read_b64_tr_b16 v[162:163], v229 offset:4096\nds_read_b64_tr_b16 v[164:165], v229 offset:7168\nds_read_b64_tr_b16 v[166:167], v229 offset:10240\n"
    "ds_read_b64_tr_b16 v[168:169], v229 offset:13312\nds_read_b64_tr_b16 v[170:171], v229 offset:16384\nds_read_b64_tr_b16 v[172:173], v229 offset:19456\n"
    "ds_read_b64_tr_b16 v[174:175], v229 offset:22528\nds_read_b64_tr_b16 v[176:177], v230 offset:1024\nds_read_b64_tr_b16 v[178:179], v230 offset:4096\n"
    "ds_read_b64_tr_b16 v[180:181], v230 offset:7168\nds_read_b64_tr_b16 v[182:183], v230 offset:10240\nds_read_b64_tr_b16 v[184:185], v230 offset:13312\n"
    "ds_read_b64_tr_b16 v[186:187], v230 offset:16384\nds_read_b64_tr_b16 v[188:189], v230 offset:19456\nds_read_b64_tr_b16 v[190:191], v230 offset:22528\n"
    "v_and_b32 v223, 63, v0\ns_lshl_b32 s43, s13, 12\ns_add_u32 s42, s43, 0x23000\nv_lshl_add_u32 v225, v223, 4, s42\ns_mov_b32 s80, 0x10000\ns_waitcnt lgkmcnt(0)\n.Lq182:\n"
    "s_setprio 3\ns_waitcnt vmcnt(0)\nv_mfma_f32_16x16x32_bf16 v[208:211], v[128:131], v[192:195], 0\nv_mfma_f32_16x16x32_bf16 v[212:215], v[144:147], v[192:195], 0\n"
    "v_mfma_f32_16x16x32_bf16 v[216:219], v[160:163], v[192:195], 0\nv_mfma_f32_16x16x32_bf16 v[220:223], v[176:179], v[192:195], 0\n"
    "v_mfma_f32_16x16x32_bf16 v[208:211], v[132:135], v[196:199], v[208:211]\nv_mfma_f32_16x16x32_bf16 v[212:215], v[148:151], v[196:199], v[212:215]\n"
    "v_mfma_f32_16x16x32_bf16 v[216:219], v[164:167], v[196:199], v[216:219]\nv_mfma_f32_16x16x32_bf16 v[220:223], v[180:183], v[196:199], v[220:223]\n"
    "v_mfma_f32_16x16x32_bf16 v[208:211], v[136:139], v[200:203], v[208:211]\nv_mfma_f32_16x16x32_bf16 v[212:215], v[152:155], v[200:203], v[212:215]\n"
    "v_mfma_f32_16x16x32_bf16 v[216:219], v[168:171], v[200:203], v[216:219]\nv_mfma_f32_16x16x32_bf16 v[220:223], v[184:187], v[200:203], v[220:223]\n"
    "v_mfma_f32_16x16x32_bf16 v[208:211], v[140:143], v[204:207], v[208:211]\nv_mfma_f32_16x16x32_bf16 v[212:215], v[156:159], v[204:207], v[212:215]\n"
    "v_mfma_f32_16x16x32_bf16 v[216:219], v[172:175], v[204:207], v[216:219]\nv_mfma_f32_16x16x32_bf16 v[220:223], v[188:191], v[204:207], v[220:223]\ns_setprio 0\n"
    "v_mov_b64 v[48:49], 0\nv_mov_b64 v[50:51], 0\ns_nop 1\nv_cmp_u_f32 vcc, v208, v208\nds_write_b128 v225, v[208:211] offset:0\nds_write_b128 v225, v[212:215] offset:1024\n"
    "ds_write_b128 v225, v[216:219] offset:2048\nds_write_b128 v225, v[220:223] offset:3072\ns_cbranch_vccnz .Lq183\n.Lq183x:\ns_lshl_b32 s43, s22, 17\n"
    "s_lshl_b32 s42, s13, 15\ns_add_u32 s44, s43, s42\ns_add_u32 s44, s44, 0xe01000\nv_and_b32 v167, 63, v0\nv_lshlrev_b32 v59, 4, v167\ns_add_u32 s68, s4, s44\n"
    "s_addc_u32 s69, s5, 0\ns_add_u32 s70, s68, 8192\ns_addc_u32 s71, s69, 0\ns_add_u32 s72, s68, 16384\ns_addc_u32 s73, s69, 0\ns_add_u32 s74, s68, 24576\n"
    "s_addc_u32 s75, s69, 0\nv_and_b32 v159, 63, v0\ns_lshl_b32 s43, s13, 10\ns_add_u32 s42, s43, 0x23000\nv_lshl_add_u32 v226, v159, 4, s42\nv_and_b32 v158, 15, v0\n"
    "s_lshl_b32 s41, s13, 7\ns_add_u32 s40, s41, 0x27d80\nv_lshl_add_u32 v227, v158, 3, s40\ns_mov_b32 s39, 0x27d80\nv_lshl_add_u32 v228, v158, 3, s39\n"
    "v_mul_u32_u24 v157, 0xd0, v158\nv_bfe_u32 v156, v0, 4, 2\ns_lshr_b32 s38, s13, 1\ns_lshl_b32 s37, s38, 6\ns_and_b32 s36, s13, 1\ns_lshl_b32 s35, s36, 5\n"
    "s_add_u32 s34, s37, s35\ns_add_u32 s34, s34, 0x18040\nv_add_u32 v240, s34, v157\nv_lshl_add_u32 v240, v156, 3, v240\nv_and_b32 v155, 3, v0\ns_mov_b32 s33, 0x27f80\n"
    "v_lshl_add_u32 v241, v155, 2, s33\ns_mov_b32 s47, 0x3b000000\nv_mov_b32 v242, 3\nv_mov_b64 v[232:233], 0\nv_mov_b64 v[234:235], 0\nv_mov_b64 v[236:237], 0\n"
    "v_mov_b64 v[238:239], 0\nv_mov_b64 v[244:245], 0\nv_mov_b64 v[246:247], 0\nv_mov_b64 v[248:249], 0\nv_mov_b64 v[250:251], 0\ns_waitcnt lgkmcnt(0)\n"
    "v_lshlrev_b32 v44, 16, v40\nv_lshlrev_b32 v45, 16, v41\nv_lshlrev_b32 v46, 16, v42\nv_lshlrev_b32 v47, 16, v43\nv_and_b32 v40, 0xffff0000, v40\n"
    "v_and_b32 v41, 0xffff0000, v41\nv_and_b32 v42, 0xffff0000, v42\nv_and_b32 v43, 0xffff0000, v43\nv_add_f32 v40, v40, v44\nv_add_f32 v41, v41, v45\n"
    "v_add_f32 v42, v42, v46\nv_add_f32 v43, v43, v47\nv_lshlrev_b32 v52, 16, v192\nv_and_b32 v53, 0xffff0000, v192\nv_add_f32 v48, v48, v52\nv_add_f32 v50, v50, v53\n"
    "v_fmac_f32 v49, v52, v52\nv_fmac_f32 v51, v53, v53\nv_lshlrev_b32 v52, 16, v193\nv_and_b32 v53, 0xffff0000, v193\nv_add_f32 v48, v48, v52\nv_add_f32 v50, v50, v53\n"
    "v_fmac_f32 v49, v52, v52\nv_fmac_f32 v51, v53, v53\nv_lshlrev_b32 v52, 16, v194\nv_and_b32 v53, 0xffff0000, v194\nv_add_f32 v48, v48, v52\nv_add_f32 v50, v50, v53\n"
    "v_fmac_f32 v49, v52, v52\nv_fmac_f32 v51, v53, v53\nv_lshlrev_b32 v52, 16, v195\nv_and_b32 v53, 0xffff0000, v195\nv_add_f32 v48, v48, v52\nv_add_f32 v50, v50, v53\n"
    "v_fmac_f32 v49, v52, v52\nv_fmac_f32 v51, v53, v53\nv_lshlrev_b32 v52, 16, v196\nv_and_b32 v53, 0xffff0000, v196\nv_add_f32 v48, v48, v52\nv_add_f32 v50, v50, v53\n"
    "v_fmac_f32 v49, v52, v52\nv_fmac_f32 v51, v53, v53\nv_lshlrev_b32 v52, 16, v197\nv_and_b32 v53, 0xffff0000, v197\nv_add_f32 v48, v48, v52\nv_add_f32 v50, v50, v53\n"
    "v_fmac_f32 v49, v52, v52\nv_fmac_f32 v51, v53, v53\nv_lshlrev_b32 v52, 16, v198\nv_and_b32 v53, 0xffff0000, v198\nv_add_f32 v48, v48, v52\nv_add_f32 v50, v50, v53\n"
    "v_fmac_f32 v49, v52, v52\nv_fmac_f32 v51, v53, v53\nv_lshlrev_b32 v52, 16, v199\nv_and_b32 v53, 0xffff0000, v199\nv_add_f32 v48, v48, v52\nv_add_f32 v50, v50, v53\n"
    "v_fmac_f32 v49, v52, v52\nv_fmac_f32 v51, v53, v53\nv_lshlrev_b32 v52, 16, v200\ns_cbranch_execz .Lhop\nv_and_b32 v53, 0xffff0000, v200\nv_add_f32 v48, v48, v52\n"
    "v_add_f32 v50, v50, v53\nv_fmac_f32 v49, v52, v52\nv_fmac_f32 v51, v53, v53\nv_lshlrev_b32 v52, 16, v201\nv_and_b32 v53, 0xffff0000, v201\nv_add_f32 v48, v48, v52\n"
    "v_add_f32 v50, v50, v53\nv_fmac_f32 v49, v52, v52\nv_fmac_f32 v51, v53, v53\nv_lshlrev_b32 v52, 16, v202\nv_and_b32 v53, 0xffff0000, v202\nv_add_f32 v48, v48, v52\n"
    "v_add_f32 v50, v50, v53\nv_fmac_f32 v49, v52, v52\nv_fmac_f32 v51, v53, v53\nv_lshlrev_b32 v52, 16, v203\nv_and_b32 v53, 0xffff0000, v203\nv_add_f32 v48, v48, v52\n"
    "v_add_f32 v50, v50, v53\nv_fmac_f32 v49, v52, v52\nv_fmac_f32 v51, v53, v53\nv_lshlrev_b32 v52, 16, v204\nv_and_b32 v53, 0xffff0000, v204\nv_add_f32 v48, v48, v52\n"
    "v_add_f32 v50, v50, v53\nv_fmac_f32 v49, v52, v52\nv_fmac_f32 v51, v53, v53\nv_lshlrev_b32 v52, 16, v205\nv_and_b32 v53, 0xffff0000, v205\nv_add_f32 v48, v48, v52\n"
    "v_add_f32 v50, v50, v53\nv_fmac_f32 v49, v52, v52\nv_fmac_f32 v51, v53, v53\nv_lshlrev_b32 v52, 16, v206\nv_and_b32 v53, 0xffff0000, v206\nv_add_f32 v48, v48, v52\n"
    "v_add_f32 v50, v50, v53\nv_fmac_f32 v49, v52, v52\nv_fmac_f32 v51, v53, v53\nv_lshlrev_b32 v52, 16, v207\nv_and_b32 v53, 0xffff0000, v207\nv_add_f32 v48, v48, v52\n"
    "v_add_f32 v50, v50, v53\nv_fmac_f32 v49, v52, v52\nv_fmac_f32 v51, v53, v53\nv_add_f32 v48, v48, v50\nv_add_f32 v49, v49, v51\ns_nop 1\nv_permlane16_swap_b32 v48, v49\n"
    "v_add_f32 v50, v48, v49\nv_add_f32 v49, v48, v49\ns_nop 1\nv_permlane32_swap_b32 v50, v49\nv_add_f32 v50, v50, v49\nv_mov_b32 v48, v50\nv_mov_b32 v49, v50\ns_nop 1\n"
    "v_permlane16_swap_b32 v48, v49\ns_mov_b64 exec, 0xffff\nds_write_b64 v227, v[48:49]\ns_mov_b64 exec, 1\nds_write_b32 v25, v242 offset:16\ns_mov_b64 exec, -1\n"
    "s_mov_b32 s86, 0x100000\n.Ls184:\nds_read_b32 v48, v241 offset:16\ns_waitcnt lgkmcnt(0)\nv_cmp_gt_i32 vcc, 3, v48\ns_sub_u32 s86, s86, 1\ns_cbranch_scc1 .Ls185\n"
    "s_cbranch_vccz .Ls185\ns_sleep 1\ns_branch .Ls184\n.Ls185:\nds_read_b64 v[48:49], v228 offset:0\nds_read_b64 v[50:51], v228 offset:128\n"
    "ds_read_b64 v[52:53], v228 offset:256\nds_read_b64 v[54:55], v228 offset:384\nds_read_b128 v[208:211], v226 offset:0\nds_read_b128 v[212:215], v226 offset:4096\n"
    "ds_read_b128 v[216:219], v226 offset:8192\nds_read_b128 v[220:223], v226 offset:12288\ns_waitcnt lgkmcnt(4)\nv_add_f32 v48, v48, v50\nv_add_f32 v49, v49, v51\n"
    "v_add_f32 v52, v52, v54\nv_add_f32 v53, v53, v55\nv_add_f32 v48, v48, v52\nv_add_f32 v49, v49, v53\nv_mul_f32 v48, s47, v48\nv_mul_f32 v50, v48, v48\n"
    "v_fma_f32 v49, v49, s47, -v50\nv_max_f32 v49, 0, v49\nv_cvt_pk_bf16_f32 v49, 0, v49\nv_add_f32 v49, 0x3727c5ac, v49\nv_cvt_pk_bf16_f32 v49, 0, v49\nv_sqrt_f32 v49, v49\n"
    "v_cvt_pk_bf16_f32 v48, 0, -v48\nv_cvt_pk_bf16_f32 v49, 0, v49\nv_rcp_f32 v49, v49\ns_waitcnt lgkmcnt(0)\nv_add_f32 v208, v208, v216\nv_add_f32 v209, v209, v217\n"
    "v_add_f32 v210, v210, v218\nv_add_f32 v211, v211, v219\nv_add_f32 v212, v212, v220\nv_add_f32 v213, v213, v221\nv_add_f32 v214, v214, v222\nv_add_f32 v215, v215, v223\n"
    "v_fmac_f32 v208, v48, v40\nv_fmac_f32 v209, v48, v41\nv_fmac_f32 v210, v48, v42\nv_fmac_f32 v211, v48, v43\nv_add_f32 v208, v208, v212\nv_add_f32 v209, v209, v213\n"
    "v_add_f32 v210, v210, v214\nv_add_f32 v211, v211, v215\nv_mul_f32 v208, v208, v49\nv_mul_f32 v209, v209, v49\nv_mul_f32 v210, v210, v49\nv_mul_f32 v211, v211, v49\n"
    "v_cvt_pk_bf16_f32 v44, v208, v209\nv_cvt_pk_bf16_f32 v45, v210, v211\nds_write_b64 v240, v[44:45]\ns_setprio 0\nv_bfe_u32 v223, v0, 5, 1\nv_and_b32 v222, 31, v0\n"
    "s_lshl_b32 s43, s21, 15\ns_lshl_b32 s42, s22, 13\ns_lshl_b32 s41, s13, 11\ns_add_u32 s40, s43, s42\ns_add_u32 s40, s40, s41\ns_add_u32 s40, s40, 0x600000\n"
    "v_lshl_add_u32 v47, v223, 8, s40\nv_lshl_add_u32 v47, v222, 2, v47\ns_mov_b32 s44, 0x00ff00ff\ns_mov_b32 s45, 0xff00ff00\ns_mov_b32 s46, s44\ns_mov_b32 s47, s44\n"
    "s_branch .Latt12\n"
    ".Lqg166:\ns_mov_b32 s12, 8388608\nv_add_u32 v9, 8388608, v9\ns_cmp_lg_u32 s3, 0\ns_cbranch_scc1 .Lz186\ns_waitcnt lgkmcnt(0)\ns_add_u32 s87, s87, s100\n"
    "s_add_u32 s87, s87, 32\n.Lj187:\ns_memrealtime s[100:101]\ns_waitcnt lgkmcnt(0)\ns_sub_u32 s101, s87, s100\ns_cmp_le_i32 s101, 0\ns_cbranch_scc0 .Lj187\n.Lz186:\n"
    "s_barrier\nglobal_load_dword v144, v39, s[52:53] offset:-2048 sc1\nglobal_load_dword v145, v39, s[52:53] offset:-1920 sc1\n"
    "global_load_dword v146, v39, s[52:53] offset:-1792 sc1\nglobal_load_dword v147, v39, s[52:53] offset:-1664 sc1\nglobal_load_dword v148, v39, s[52:53] offset:-1024 sc1\n"
    "global_load_dword v149, v39, s[52:53] offset:-896 sc1\nglobal_load_dword v150, v39, s[52:53] offset:-768 sc1\nglobal_load_dword v151, v39, s[52:53] offset:-640 sc1\n"
    "global_load_dword v152, v39, s[52:53] offset:0 sc1\nglobal_load_dword v153, v39, s[52:53] offset:128 sc1\nglobal_load_dword v154, v39, s[52:53] offset:256 sc1\n"
    "global_load_dword v155, v39, s[52:53] offset:384 sc1\nglobal_load_dword v156, v39, s[52:53] offset:1024 sc1\nglobal_load_dword v157, v39, s[52:53] offset:1152 sc1\n"
    "global_load_dword v158, v39, s[52:53] offset:1280 sc1\nglobal_load_dword v159, v39, s[52:53] offset:1408 sc1\nglobal_load_dword v160, v39, s[54:55] offset:-2048 sc1\n"
    "global_load_dword v161, v39, s[54:55] offset:-1920 sc1\nglobal_load_dword v162, v39, s[54:55] offset:-1792 sc1\nglobal_load_dword v163, v39, s[54:55] offset:-1664 sc1\n"
    "global_load_dword v164, v39, s[54:55] offset:-1024 sc1\nglobal_load_dword v165, v39, s[54:55] offset:-896 sc1\nglobal_load_dword v166, v39, s[54:55] offset:-768 sc1\n"
    "global_load_dword v167, v39, s[54:55] offset:-640 sc1\nglobal_load_dword v168, v39, s[54:55] offset:0 sc1\nglobal_load_dword v169, v39, s[54:55] offset:128 sc1\n"
    "global_load_dword v170, v39, s[54:55] offset:256 sc1\nglobal_load_dword v171, v39, s[54:55] offset:384 sc1\nglobal_load_dword v172, v39, s[54:55] offset:1024 sc1\n"
    "global_load_dword v173, v39, s[54:55] offset:1152 sc1\nglobal_load_dword v174, v39, s[54:55] offset:1280 sc1\ns_nop 0\ns_cbranch_execz .Lhop\n"
    "global_load_dword v175, v39, s[54:55] offset:1408 sc1\nglobal_load_dword v176, v39, s[56:57] offset:-2048 sc1\nglobal_load_dword v177, v39, s[56:57] offset:-1920 sc1\n"
    "global_load_dword v178, v39, s[56:57] offset:-1792 sc1\nglobal_load_dword v179, v39, s[56:57] offset:-1664 sc1\nglobal_load_dword v180, v39, s[56:57] offset:-1024 sc1\n"
    "global_load_dword v181, v39, s[56:57] offset:-896 sc1\nglobal_load_dword v182, v39, s[56:57] offset:-768 sc1\nglobal_load_dword v183, v39, s[56:57] offset:-640 sc1\n"
    "global_load_dword v184, v39, s[56:57] offset:0 sc1\nglobal_load_dword v185, v39, s[56:57] offset:128 sc1\nglobal_load_dword v186, v39, s[56:57] offset:256 sc1\n"
    "global_load_dword v187, v39, s[56:57] offset:384 sc1\nglobal_load_dword v188, v39, s[56:57] offset:1024 sc1\nglobal_load_dword v189, v39, s[56:57] offset:1152 sc1\n"
    "global_load_dword v190, v39, s[56:57] offset:1280 sc1\nglobal_load_dword v191, v39, s[56:57] offset:1408 sc1\n.Ld188:\ns_waitcnt vmcnt(44)\n"
    "v_dot2_f32_bf16 v112, v144, v241, 0\nv_dot2_f32_bf16 v113, v144, v144, 0\nv_dot2_f32_bf16 v114, v145, v241, 0\nv_dot2_f32_bf16 v115, v145, v145, 0\n"
    "v_dot2_f32_bf16 v116, v146, v241, 0\nv_dot2_f32_bf16 v117, v146, v146, 0\nv_dot2_f32_bf16 v118, v147, v241, 0\nv_dot2_f32_bf16 v119, v147, v147, 0\ns_waitcnt vmcnt(40)\n"
    "v_dot2_f32_bf16 v112, v148, v241, v112\nv_dot2_f32_bf16 v113, v148, v148, v113\nv_dot2_f32_bf16 v114, v149, v241, v114\nv_dot2_f32_bf16 v115, v149, v149, v115\n"
    "v_dot2_f32_bf16 v116, v150, v241, v116\nv_dot2_f32_bf16 v117, v150, v150, v117\nv_dot2_f32_bf16 v118, v151, v241, v118\nv_dot2_f32_bf16 v119, v151, v151, v119\n"
    "s_waitcnt vmcnt(36)\nv_dot2_f32_bf16 v112, v152, v241, v112\nv_dot2_f32_bf16 v113, v152, v152, v113\nv_dot2_f32_bf16 v114, v153, v241, v114\n"
    "v_dot2_f32_bf16 v115, v153, v153, v115\nv_dot2_f32_bf16 v116, v154, v241, v116\nv_dot2_f32_bf16 v117, v154, v154, v117\nv_dot2_f32_bf16 v118, v155, v241, v118\n"
    "v_dot2_f32_bf16 v119, v155, v155, v119\ns_waitcnt vmcnt(32)\nv_dot2_f32_bf16 v112, v156, v241, v112\nv_dot2_f32_bf16 v113, v156, v156, v113\n"
    "v_dot2_f32_bf16 v114, v157, v241, v114\nv_dot2_f32_bf16 v115, v157, v157, v115\nv_dot2_f32_bf16 v116, v158, v241, v116\nv_dot2_f32_bf16 v117, v158, v158, v117\n"
    "v_dot2_f32_bf16 v118, v159, v241, v118\nv_dot2_f32_bf16 v119, v159, v159, v119\nv_mfma_f32_32x32x16_bf16 v[64:79], v[208:211], v[144:147], 0\n"
    "ds_read_b64_tr_b16 v[208:209], v242 offset:7168\nds_read_b64_tr_b16 v[210:211], v242 offset:7424\nv_add_f32 v112, v112, v114\nv_add_f32 v113, v113, v115\n"
    "global_load_dword v192, v39, s[58:59] offset:-2048 sc1\nv_mfma_f32_32x32x16_bf16 v[64:79], v[212:215], v[148:151], v[64:79]\n"
    "ds_read_b64_tr_b16 v[212:213], v242 offset:10240\nds_read_b64_tr_b16 v[214:215], v242 offset:10496\nv_add_f32 v116, v116, v118\nv_add_f32 v117, v117, v119\n"
    "global_load_dword v193, v39, s[58:59] offset:-1920 sc1\nv_mfma_f32_32x32x16_bf16 v[64:79], v[216:219], v[152:155], v[64:79]\n"
    "ds_read_b64_tr_b16 v[216:217], v242 offset:2048\nds_read_b64_tr_b16 v[218:219], v242 offset:2304\nv_add_f32 v112, v112, v116\nv_add_f32 v113, v113, v117\n"
    "global_load_dword v194, v39, s[58:59] offset:-1792 sc1\ns_waitcnt lgkmcnt(6)\nv_mfma_f32_32x32x16_bf16 v[64:79], v[220:223], v[156:159], v[64:79]\n"
    "ds_read_b64_tr_b16 v[220:221], v242 offset:5120\nds_read_b64_tr_b16 v[222:223], v242 offset:5376\nv_cmp_u_f32 vcc, v112, v113\nv_permlane32_swap_b32 v112, v113\n"
    "v_add_f32 v112, v112, v113\nglobal_load_dword v195, v39, s[58:59] offset:-1664 sc1\nv_mfma_f32_32x32x16_bf16 v[80:95], v[224:227], v[144:147], 0\n"
    "ds_read_b64_tr_b16 v[224:225], v242 offset:8192\nds_read_b64_tr_b16 v[226:227], v242 offset:8448\nglobal_load_dword v196, v39, s[58:59] offset:-1024 sc1\n"
    "global_load_dword v197, v39, s[58:59] offset:-896 sc1\nv_mad_u32_u24 v240, s45, 3, v36\nv_mfma_f32_32x32x16_bf16 v[80:95], v[228:231], v[148:151], v[80:95]\n"
    "ds_read_b64_tr_b16 v[228:229], v242 offset:11264\nds_read_b64_tr_b16 v[230:231], v242 offset:11520\ns_cbranch_vccnz .Ld189\n.Ld190:\n"
    "global_store_dword v35, v112, s[4:5] sc1\ns_memrealtime s[100:101]\nglobal_load_dword v198, v39, s[58:59] offset:-768 sc1\ns_waitcnt lgkmcnt(6)\n"
    "v_mfma_f32_32x32x16_bf16 v[80:95], v[208:211], v[152:155], v[80:95]\nds_read_b64_tr_b16 v[208:209], v240 offset:0\nds_read_b64_tr_b16 v[210:211], v240 offset:256\n"
    "global_load_dword v199, v39, s[58:59] offset:-640 sc1\nglobal_load_dword v200, v39, s[58:59] offset:0 sc1\nglobal_load_dword v201, v39, s[58:59] offset:128 sc1\n"
    "v_mfma_f32_32x32x16_bf16 v[80:95], v[212:215], v[156:159], v[80:95]\nds_read_b64_tr_b16 v[212:213], v240 offset:3072\nds_read_b64_tr_b16 v[214:215], v240 offset:3328\n"
    "global_load_dword v202, v39, s[58:59] offset:256 sc1\nglobal_load_dword v203, v39, s[58:59] offset:384 sc1\nglobal_load_dword v204, v39, s[58:59] offset:1024 sc1\n"
    "v_mfma_f32_32x32x16_bf16 v[96:111], v[216:219], v[144:147], 0\nds_read_b64_tr_b16 v[216:217], v240 offset:6144\nds_read_b64_tr_b16 v[218:219], v240 offset:6400\n"
    "global_load_dword v205, v39, s[58:59] offset:1152 sc1\ns_waitcnt vmcnt(31)\nv_max3_u32 v244, v160, v161, v162\nv_max3_u32 v243, v163, v164, v165\ns_waitcnt lgkmcnt(6)\n"
    "v_mfma_f32_32x32x16_bf16 v[96:111], v[220:223], v[148:151], v[96:111]\nds_read_b64_tr_b16 v[220:221], v240 offset:9216\nds_read_b64_tr_b16 v[222:223], v240 offset:9472\n"
    "global_load_dword v206, v39, s[58:59] offset:1280 sc1\nv_max3_u32 v244, v244, v166, v167\nv_max3_u32 v243, v243, v168, v169\nv_max3_u32 v244, v244, v170, v171\n"
    "v_mfma_f32_32x32x16_bf16 v[96:111], v[224:227], v[152:155], v[96:111]\nds_read_b64_tr_b16 v[224:225], v240 offset:1024\nds_read_b64_tr_b16 v[226:227], v240 offset:1280\n"
    "v_max3_u32 v243, v243, v172, v173\nv_max3_u32 v244, v244, v174, v175\nv_max_u32 v243, v243, v244\nv_cmp_eq_u32 vcc, -1, v243\n"
    "v_mfma_f32_32x32x16_bf16 v[96:111], v[228:231], v[156:159], v[96:111]\nds_read_b64_tr_b16 v[228:229], v240 offset:4096\nds_read_b64_tr_b16 v[230:231], v240 offset:4352\n"
    "global_load_dword v207, v39, s[58:59] offset:1408 sc1\ns_cbranch_vccnz .Lx193\n.Lx194:\ns_waitcnt lgkmcnt(6)\n"
    "v_mfma_f32_32x32x16_bf16 v[64:79], v[208:211], v[160:163], v[64:79]\nds_read_b64_tr_b16 v[208:209], v240 offset:7168\nds_read_b64_tr_b16 v[210:211], v240 offset:7424\n"
    "global_load_dword v144, v39, s[60:61] offset:-2048 sc1\nglobal_load_dword v145, v39, s[60:61] offset:-1920 sc1\ns_nop 0\ns_cbranch_execz .Lhop\n"
    "v_mfma_f32_32x32x16_bf16 v[64:79], v[212:215], v[164:167], v[64:79]\nds_read_b64_tr_b16 v[212:213], v240 offset:10240\nds_read_b64_tr_b16 v[214:215], v240 offset:10496\n"
    "global_load_dword v146, v39, s[60:61] offset:-1792 sc1\nglobal_load_dword v147, v39, s[60:61] offset:-1664 sc1\n"
    "v_mfma_f32_32x32x16_bf16 v[64:79], v[216:219], v[168:171], v[64:79]\nds_read_b64_tr_b16 v[216:217], v240 offset:2048\nds_read_b64_tr_b16 v[218:219], v240 offset:2304\n"
    "global_load_dword v148, v39, s[60:61] offset:-1024 sc1\nglobal_load_dword v149, v39, s[60:61] offset:-896 sc1\ns_waitcnt lgkmcnt(6)\n"
    "v_mfma_f32_32x32x16_bf16 v[64:79], v[220:223], v[172:175], v[64:79]\nds_read_b64_tr_b16 v[220:221], v240 offset:5120\nds_read_b64_tr_b16 v[222:223], v240 offset:5376\n"
    "global_load_dword v150, v39, s[60:61] offset:-768 sc1\nglobal_load_dword v151, v39, s[60:61] offset:-640 sc1\n"
    "v_mfma_f32_32x32x16_bf16 v[80:95], v[224:227], v[160:163], v[80:95]\nds_read_b64_tr_b16 v[224:225], v240 offset:8192\nds_read_b64_tr_b16 v[226:227], v240 offset:8448\n"
    "global_load_dword v152, v39, s[60:61] offset:0 sc1\nglobal_load_dword v153, v39, s[60:61] offset:128 sc1\nv_mad_u32_u24 v242, s46, 3, v36\n"
    "v_mfma_f32_32x32x16_bf16 v[80:95], v[228:231], v[164:167], v[80:95]\nds_read_b64_tr_b16 v[228:229], v240 offset:11264\nds_read_b64_tr_b16 v[230:231], v240 offset:11520\n"
    "global_load_dword v154, v39, s[60:61] offset:256 sc1\nglobal_load_dword v155, v39, s[60:61] offset:384 sc1\ns_waitcnt lgkmcnt(6)\n"
    "v_mfma_f32_32x32x16_bf16 v[80:95], v[208:211], v[168:171], v[80:95]\nds_read_b64_tr_b16 v[208:209], v242 offset:0\nds_read_b64_tr_b16 v[210:211], v242 offset:256\n"
    "global_load_dword v156, v39, s[60:61] offset:1024 sc1\nglobal_load_dword v157, v39, s[60:61] offset:1152 sc1\n"
    "v_mfma_f32_32x32x16_bf16 v[80:95], v[212:215], v[172:175], v[80:95]\nds_read_b64_tr_b16 v[212:213], v242 offset:3072\nds_read_b64_tr_b16 v[214:215], v242 offset:3328\n"
    "global_load_dword v158, v39, s[60:61] offset:1280 sc1\nglobal_load_dword v159, v39, s[60:61] offset:1408 sc1\n"
    "v_mfma_f32_32x32x16_bf16 v[96:111], v[216:219], v[160:163], v[96:111]\nds_read_b64_tr_b16 v[216:217], v242 offset:6144\nds_read_b64_tr_b16 v[218:219], v242 offset:6400\n"
    "s_waitcnt vmcnt(33)\nv_max3_u32 v244, v176, v177, v178\nv_max3_u32 v243, v179, v180, v181\ns_waitcnt lgkmcnt(6)\n"
    "v_mfma_f32_32x32x16_bf16 v[96:111], v[220:223], v[164:167], v[96:111]\nds_read_b64_tr_b16 v[220:221], v242 offset:9216\nds_read_b64_tr_b16 v[222:223], v242 offset:9472\n"
    "v_max3_u32 v244, v244, v182, v183\nv_max3_u32 v243, v243, v184, v185\nv_max3_u32 v244, v244, v186, v187\n"
    "v_mfma_f32_32x32x16_bf16 v[96:111], v[224:227], v[168:171], v[96:111]\nds_read_b64_tr_b16 v[224:225], v242 offset:1024\nds_read_b64_tr_b16 v[226:227], v242 offset:1280\n"
    "v_max3_u32 v243, v243, v188, v189\nv_max3_u32 v244, v244, v190, v191\nv_max_u32 v243, v243, v244\nv_cmp_eq_u32 vcc, -1, v243\n"
    "v_mfma_f32_32x32x16_bf16 v[96:111], v[228:231], v[172:175], v[96:111]\nds_read_b64_tr_b16 v[228:229], v242 offset:4096\nds_read_b64_tr_b16 v[230:231], v242 offset:4352\n"
    "s_cbranch_vccnz .Lx195\n.Lx196:\ns_waitcnt lgkmcnt(6)\nv_mfma_f32_32x32x16_bf16 v[64:79], v[208:211], v[176:179], v[64:79]\n"
    "ds_read_b64_tr_b16 v[208:209], v242 offset:7168\nds_read_b64_tr_b16 v[210:211], v242 offset:7424\nglobal_load_dword v160, v39, s[62:63] offset:-2048 sc1\n"
    "global_load_dword v161, v39, s[62:63] offset:-1920 sc1\nv_mfma_f32_32x32x16_bf16 v[64:79], v[212:215], v[180:183], v[64:79]\n"
    "ds_read_b64_tr_b16 v[212:213], v242 offset:10240\nds_read_b64_tr_b16 v[214:215], v242 offset:10496\nglobal_load_dword v162, v39, s[62:63] offset:-1792 sc1\n"
    "global_load_dword v163, v39, s[62:63] offset:-1664 sc1\nv_mfma_f32_32x32x16_bf16 v[64:79], v[216:219], v[184:187], v[64:79]\n"
    "ds_read_b64_tr_b16 v[216:217], v242 offset:2048\nds_read_b64_tr_b16 v[218:219], v242 offset:2304\nglobal_load_dword v164, v39, s[62:63] offset:-1024 sc1\n"
    "global_load_dword v165, v39, s[62:63] offset:-896 sc1\ns_waitcnt lgkmcnt(6)\nv_mfma_f32_32x32x16_bf16 v[64:79], v[220:223], v[188:191], v[64:79]\n"
    "ds_read_b64_tr_b16 v[220:221], v242 offset:5120\nds_read_b64_tr_b16 v[222:223], v242 offset:5376\nglobal_load_dword v166, v39, s[62:63] offset:-768 sc1\n"
    "global_load_dword v167, v39, s[62:63] offset:-640 sc1\nv_mfma_f32_32x32x16_bf16 v[80:95], v[224:227], v[176:179], v[80:95]\n"
    "ds_read_b64_tr_b16 v[224:225], v242 offset:8192\nds_read_b64_tr_b16 v[226:227], v242 offset:8448\nglobal_load_dword v168, v39, s[62:63] offset:0 sc1\n"
    "global_load_dword v169, v39, s[62:63] offset:128 sc1\nv_mad_u32_u24 v240, s47, 3, v36\nv_mfma_f32_32x32x16_bf16 v[80:95], v[228:231], v[180:183], v[80:95]\n"
    "ds_read_b64_tr_b16 v[228:229], v242 offset:11264\nds_read_b64_tr_b16 v[230:231], v242 offset:11520\nglobal_load_dword v170, v39, s[62:63] offset:256 sc1\n"
    "global_load_dword v171, v39, s[62:63] offset:384 sc1\ns_waitcnt lgkmcnt(6)\nv_mfma_f32_32x32x16_bf16 v[80:95], v[208:211], v[184:187], v[80:95]\n"
    "ds_read_b64_tr_b16 v[208:209], v240 offset:0\nds_read_b64_tr_b16 v[210:211], v240 offset:256\nglobal_load_dword v172, v39, s[62:63] offset:1024 sc1\n"
    "global_load_dword v173, v39, s[62:63] offset:1152 sc1\nv_mfma_f32_32x32x16_bf16 v[80:95], v[212:215], v[188:191], v[80:95]\n"
    "ds_read_b64_tr_b16 v[212:213], v240 offset:3072\nds_read_b64_tr_b16 v[214:215], v240 offset:3328\nglobal_load_dword v174, v39, s[62:63] offset:1280 sc1\n"
    "global_load_dword v175, v39, s[62:63] offset:1408 sc1\nv_mfma_f32_32x32x16_bf16 v[96:111], v[216:219], v[176:179], v[96:111]\n"
    "ds_read_b64_tr_b16 v[216:217], v240 offset:6144\nds_read_b64_tr_b16 v[218:219], v240 offset:6400\ns_waitcnt vmcnt(32)\nv_max3_u32 v244, v192, v193, v194\n"
    "v_max3_u32 v243, v195, v196, v197\ns_waitcnt lgkmcnt(6)\nv_mfma_f32_32x32x16_bf16 v[96:111], v[220:223], v[180:183], v[96:111]\n"
    "ds_read_b64_tr_b16 v[220:221], v240 offset:9216\nds_read_b64_tr_b16 v[222:223], v240 offset:9472\nv_max3_u32 v244, v244, v198, v199\nv_max3_u32 v243, v243, v200, v201\n"
    "v_max3_u32 v244, v244, v202, v203\nv_mfma_f32_32x32x16_bf16 v[96:111], v[224:227], v[184:187], v[96:111]\nds_read_b64_tr_b16 v[224:225], v240 offset:1024\n"
    "ds_read_b64_tr_b16 v[226:227], v240 offset:1280\nv_max3_u32 v243, v243, v204, v205\nv_max3_u32 v244, v244, v206, v207\nv_max_u32 v243, v243, v244\n"
    "v_cmp_eq_u32 vcc, -1, v243\nv_mfma_f32_32x32x16_bf16 v[96:111], v[228:231], v[188:191], v[96:111]\nds_read_b64_tr_b16 v[228:229], v240 offset:4096\n"
    "ds_read_b64_tr_b16 v[230:231], v240 offset:4352\ns_cbranch_vccnz .Lx197\n.Lx198:\ns_waitcnt lgkmcnt(6)\n"
    "v_mfma_f32_32x32x16_bf16 v[64:79], v[208:211], v[192:195], v[64:79]\nds_read_b64_tr_b16 v[208:209], v240 offset:7168\nds_read_b64_tr_b16 v[210:211], v240 offset:7424\n"
    "global_load_dword v176, v39, s[64:65] offset:-2048 sc1\ns_nop 0\ns_cbranch_execz .Lhop\nglobal_load_dword v177, v39, s[64:65] offset:-1920 sc1\n"
    "v_mfma_f32_32x32x16_bf16 v[64:79], v[212:215], v[196:199], v[64:79]\nds_read_b64_tr_b16 v[212:213], v240 offset:10240\nds_read_b64_tr_b16 v[214:215], v240 offset:10496\n"
    "global_load_dword v178, v39, s[64:65] offset:-1792 sc1\nglobal_load_dword v179, v39, s[64:65] offset:-1664 sc1\n"
    "v_mfma_f32_32x32x16_bf16 v[64:79], v[216:219], v[200:203], v[64:79]\nds_read_b64_tr_b16 v[216:217], v240 offset:2048\nds_read_b64_tr_b16 v[218:219], v240 offset:2304\n"
    "global_load_dword v180, v39, s[64:65] offset:-1024 sc1\nglobal_load_dword v181, v39, s[64:65] offset:-896 sc1\ns_waitcnt lgkmcnt(6)\n"
    "v_mfma_f32_32x32x16_bf16 v[64:79], v[220:223], v[204:207], v[64:79]\nds_read_b64_tr_b16 v[220:221], v240 offset:5120\nds_read_b64_tr_b16 v[222:223], v240 offset:5376\n"
    "global_load_dword v182, v39, s[64:65] offset:-768 sc1\nglobal_load_dword v183, v39, s[64:65] offset:-640 sc1\n"
    "v_mfma_f32_32x32x16_bf16 v[80:95], v[224:227], v[192:195], v[80:95]\nds_read_b64_tr_b16 v[224:225], v240 offset:8192\nds_read_b64_tr_b16 v[226:227], v240 offset:8448\n"
    "global_load_dword v184, v39, s[64:65] offset:0 sc1\nglobal_load_dword v185, v39, s[64:65] offset:128 sc1\nv_mad_u32_u24 v242, s48, 3, v36\n"
    "v_mfma_f32_32x32x16_bf16 v[80:95], v[228:231], v[196:199], v[80:95]\nds_read_b64_tr_b16 v[228:229], v240 offset:11264\nds_read_b64_tr_b16 v[230:231], v240 offset:11520\n"
    "global_load_dword v186, v39, s[64:65] offset:256 sc1\nglobal_load_dword v187, v39, s[64:65] offset:384 sc1\ns_waitcnt lgkmcnt(6)\n"
    "v_mfma_f32_32x32x16_bf16 v[80:95], v[208:211], v[200:203], v[80:95]\nds_read_b64_tr_b16 v[208:209], v242 offset:0\nds_read_b64_tr_b16 v[210:211], v242 offset:256\n"
    "global_load_dword v188, v39, s[64:65] offset:1024 sc1\nglobal_load_dword v189, v39, s[64:65] offset:1152 sc1\n"
    "v_mfma_f32_32x32x16_bf16 v[80:95], v[212:215], v[204:207], v[80:95]\nds_read_b64_tr_b16 v[212:213], v242 offset:3072\nds_read_b64_tr_b16 v[214:215], v242 offset:3328\n"
    "global_load_dword v190, v39, s[64:65] offset:1280 sc1\nglobal_load_dword v191, v39, s[64:65] offset:1408 sc1\n"
    "v_mfma_f32_32x32x16_bf16 v[96:111], v[216:219], v[192:195], v[96:111]\nds_read_b64_tr_b16 v[216:217], v242 offset:6144\nds_read_b64_tr_b16 v[218:219], v242 offset:6400\n"
    "s_waitcnt vmcnt(32)\nv_max3_u32 v244, v144, v145, v146\nv_max3_u32 v243, v147, v148, v149\ns_waitcnt lgkmcnt(6)\n"
    "v_mfma_f32_32x32x16_bf16 v[96:111], v[220:223], v[196:199], v[96:111]\nds_read_b64_tr_b16 v[220:221], v242 offset:9216\nds_read_b64_tr_b16 v[222:223], v242 offset:9472\n"
    "v_max3_u32 v244, v244, v150, v151\nv_max3_u32 v243, v243, v152, v153\nv_max3_u32 v244, v244, v154, v155\n"
    "v_mfma_f32_32x32x16_bf16 v[96:111], v[224:227], v[200:203], v[96:111]\nds_read_b64_tr_b16 v[224:225], v242 offset:1024\nds_read_b64_tr_b16 v[226:227], v242 offset:1280\n"
    "v_max3_u32 v243, v243, v156, v157\nv_max3_u32 v244, v244, v158, v159\nv_max_u32 v243, v243, v244\nv_cmp_eq_u32 vcc, -1, v243\n"
    "v_mfma_f32_32x32x16_bf16 v[96:111], v[228:231], v[204:207], v[96:111]\nds_read_b64_tr_b16 v[228:229], v242 offset:4096\nds_read_b64_tr_b16 v[230:231], v242 offset:4352\n"
    "s_cbranch_vccnz .Lx199\n.Lx200:\ns_waitcnt lgkmcnt(6)\nv_mfma_f32_32x32x16_bf16 v[64:79], v[208:211], v[144:147], v[64:79]\n"
    "ds_read_b64_tr_b16 v[208:209], v242 offset:7168\nds_read_b64_tr_b16 v[210:211], v242 offset:7424\nglobal_load_dword v192, v39, s[66:67] offset:-2048 sc1\n"
    "global_load_dword v193, v39, s[66:67] offset:-1920 sc1\nv_mfma_f32_32x32x16_bf16 v[64:79], v[212:215], v[148:151], v[64:79]\n"
    "ds_read_b64_tr_b16 v[212:213], v242 offset:10240\nds_read_b64_tr_b16 v[214:215], v242 offset:10496\nglobal_load_dword v194, v39, s[66:67] offset:-1792 sc1\n"
    "global_load_dword v195, v39, s[66:67] offset:-1664 sc1\nv_mfma_f32_32x32x16_bf16 v[64:79], v[216:219], v[152:155], v[64:79]\n"
    "ds_read_b64_tr_b16 v[216:217], v242 offset:2048\nds_read_b64_tr_b16 v[218:219], v242 offset:2304\nglobal_load_dword v196, v39, s[66:67] offset:-1024 sc1\n"
    "global_load_dword v197, v39, s[66:67] offset:-896 sc1\ns_waitcnt lgkmcnt(6)\nv_mfma_f32_32x32x16_bf16 v[64:79], v[220:223], v[156:159], v[64:79]\n"
    "ds_read_b64_tr_b16 v[220:221], v242 offset:5120\nds_read_b64_tr_b16 v[222:223], v242 offset:5376\nglobal_load_dword v198, v39, s[66:67] offset:-768 sc1\n"
    "global_load_dword v199, v39, s[66:67] offset:-640 sc1\nv_mfma_f32_32x32x16_bf16 v[80:95], v[224:227], v[144:147], v[80:95]\n"
    "ds_read_b64_tr_b16 v[224:225], v242 offset:8192\nds_read_b64_tr_b16 v[226:227], v242 offset:8448\nglobal_load_dword v200, v39, s[66:67] offset:0 sc1\n"
    "global_load_dword v201, v39, s[66:67] offset:128 sc1\nv_mad_u32_u24 v240, s49, 3, v36\nv_mfma_f32_32x32x16_bf16 v[80:95], v[228:231], v[148:151], v[80:95]\n"
    "ds_read_b64_tr_b16 v[228:229], v242 offset:11264\nds_read_b64_tr_b16 v[230:231], v242 offset:11520\nglobal_load_dword v202, v39, s[66:67] offset:256 sc1\n"
    "global_load_dword v203, v39, s[66:67] offset:384 sc1\ns_waitcnt lgkmcnt(6)\nv_mfma_f32_32x32x16_bf16 v[80:95], v[208:211], v[152:155], v[80:95]\n"
    "ds_read_b64_tr_b16 v[208:209], v240 offset:0\nds_read_b64_tr_b16 v[210:211], v240 offset:256\nglobal_load_dword v204, v39, s[66:67] offset:1024 sc1\n"
    "global_load_dword v205, v39, s[66:67] offset:1152 sc1\nv_mfma_f32_32x32x16_bf16 v[80:95], v[212:215], v[156:159], v[80:95]\n"
    "ds_read_b64_tr_b16 v[212:213], v240 offset:3072\nds_read_b64_tr_b16 v[214:215], v240 offset:3328\nglobal_load_dword v206, v39, s[66:67] offset:1280 sc1\n"
    "global_load_dword v207, v39, s[66:67] offset:1408 sc1\nv_mfma_f32_32x32x16_bf16 v[96:111], v[216:219], v[144:147], v[96:111]\n"
    "ds_read_b64_tr_b16 v[216:217], v240 offset:6144\nds_read_b64_tr_b16 v[218:219], v240 offset:6400\ns_waitcnt vmcnt(32)\nv_max3_u32 v244, v160, v161, v162\n"
    "v_max3_u32 v243, v163, v164, v165\ns_waitcnt lgkmcnt(6)\nv_mfma_f32_32x32x16_bf16 v[96:111], v[220:223], v[148:151], v[96:111]\n"
    "ds_read_b64_tr_b16 v[220:221], v240 offset:9216\nds_read_b64_tr_b16 v[222:223], v240 offset:9472\nv_max3_u32 v244, v244, v166, v167\nv_max3_u32 v243, v243, v168, v169\n"
    "v_max3_u32 v244, v244, v170, v171\nv_mfma_f32_32x32x16_bf16 v[96:111], v[224:227], v[152:155], v[96:111]\nds_read_b64_tr_b16 v[224:225], v240 offset:1024\n"
    "ds_read_b64_tr_b16 v[226:227], v240 offset:1280\nv_max3_u32 v243, v243, v172, v173\nv_max3_u32 v244, v244, v174, v175\nv_max_u32 v243, v243, v244\n"
    "v_cmp_eq_u32 vcc, -1, v243\nv_mfma_f32_32x32x16_bf16 v[96:111], v[228:231], v[156:159], v[96:111]\nds_read_b64_tr_b16 v[228:229], v240 offset:4096\n"
    "ds_read_b64_tr_b16 v[230:231], v240 offset:4352\ns_cbranch_vccnz .Lx201\n.Lx202:\nglobal_load_dword v112, v34, s[4:5] offset:0 sc1\n"
    "global_load_dword v113, v34, s[4:5] offset:256 sc1\ns_waitcnt lgkmcnt(6)\nv_mfma_f32_32x32x16_bf16 v[64:79], v[208:211], v[160:163], v[64:79]\ns_nop 0\n"
    "s_cbranch_execz .Lhop\nds_read_b64_tr_b16 v[208:209], v240 offset:7168\nds_read_b64_tr_b16 v[210:211], v240 offset:7424\n"
    "global_load_dword v114, v34, s[4:5] offset:512 sc1\nglobal_load_dword v115, v34, s[4:5] offset:768 sc1\n"
    "v_mfma_f32_32x32x16_bf16 v[64:79], v[212:215], v[164:167], v[64:79]\nds_read_b64_tr_b16 v[212:213], v240 offset:10240\nds_read_b64_tr_b16 v[214:215], v240 offset:10496\n"
    "global_load_dword v116, v34, s[4:5] offset:1024 sc1\nglobal_load_dword v117, v34, s[4:5] offset:1280 sc1\n"
    "v_mfma_f32_32x32x16_bf16 v[64:79], v[216:219], v[168:171], v[64:79]\nds_read_b64_tr_b16 v[216:217], v240 offset:2048\nds_read_b64_tr_b16 v[218:219], v240 offset:2304\n"
    "v_mad_u32_u24 v242, s50, 3, v36\nglobal_load_dword v118, v34, s[4:5] offset:1536 sc1\nglobal_load_dword v119, v34, s[4:5] offset:1792 sc1\ns_waitcnt lgkmcnt(6)\n"
    "v_mfma_f32_32x32x16_bf16 v[64:79], v[220:223], v[172:175], v[64:79]\nds_read_b64_tr_b16 v[220:221], v240 offset:5120\nds_read_b64_tr_b16 v[222:223], v240 offset:5376\n"
    "s_waitcnt vmcnt(24)\nv_max3_u32 v244, v176, v177, v178\nv_max3_u32 v243, v179, v180, v181\nv_mfma_f32_32x32x16_bf16 v[80:95], v[224:227], v[160:163], v[80:95]\n"
    "ds_read_b64_tr_b16 v[224:225], v240 offset:8192\nds_read_b64_tr_b16 v[226:227], v240 offset:8448\nv_max3_u32 v244, v244, v182, v183\nv_max3_u32 v243, v243, v184, v185\n"
    "v_max3_u32 v244, v244, v186, v187\nv_mfma_f32_32x32x16_bf16 v[80:95], v[228:231], v[164:167], v[80:95]\nds_read_b64_tr_b16 v[228:229], v240 offset:11264\n"
    "ds_read_b64_tr_b16 v[230:231], v240 offset:11520\nv_max3_u32 v243, v243, v188, v189\nv_max3_u32 v244, v244, v190, v191\nv_max_u32 v243, v243, v244\n"
    "v_cmp_eq_u32 vcc, -1, v243\ns_waitcnt lgkmcnt(6)\nv_mfma_f32_32x32x16_bf16 v[80:95], v[208:211], v[168:171], v[80:95]\nds_read_b64_tr_b16 v[208:209], v242 offset:0\n"
    "ds_read_b64_tr_b16 v[210:211], v242 offset:256\ns_cbranch_vccnz .Lx203\n.Lx204:\nv_mfma_f32_32x32x16_bf16 v[80:95], v[212:215], v[172:175], v[80:95]\n"
    "ds_read_b64_tr_b16 v[212:213], v242 offset:3072\nds_read_b64_tr_b16 v[214:215], v242 offset:3328\nv_mad_u32_u24 v240, s51, 3, v36\n"
    "v_mfma_f32_32x32x16_bf16 v[96:111], v[216:219], v[160:163], v[96:111]\nds_read_b64_tr_b16 v[216:217], v242 offset:6144\nds_read_b64_tr_b16 v[218:219], v242 offset:6400\n"
    "s_waitcnt vmcnt(8)\nv_max3_u32 v244, v192, v193, v194\nv_max3_u32 v243, v195, v196, v197\ns_waitcnt lgkmcnt(6)\n"
    "v_mfma_f32_32x32x16_bf16 v[96:111], v[220:223], v[164:167], v[96:111]\nds_read_b64_tr_b16 v[220:221], v242 offset:9216\nds_read_b64_tr_b16 v[222:223], v242 offset:9472\n"
    "v_max3_u32 v244, v244, v198, v199\nv_max3_u32 v243, v243, v200, v201\nv_max3_u32 v244, v244, v202, v203\n"
    "v_mfma_f32_32x32x16_bf16 v[96:111], v[224:227], v[168:171], v[96:111]\nds_read_b64_tr_b16 v[224:225], v240 offset:0\nds_read_b64_tr_b16 v[226:227], v240 offset:256\n"
    "v_max3_u32 v243, v243, v204, v205\nv_max3_u32 v244, v244, v206, v207\nv_max_u32 v243, v243, v244\nv_cmp_eq_u32 vcc, -1, v243\n"
    "v_mfma_f32_32x32x16_bf16 v[96:111], v[228:231], v[172:175], v[96:111]\nds_read_b64_tr_b16 v[228:229], v240 offset:3072\nds_read_b64_tr_b16 v[230:231], v240 offset:3328\n"
    "s_cbranch_vccnz .Lx205\n.Lx206:\ns_waitcnt lgkmcnt(6)\nv_mfma_f32_32x32x16_bf16 v[64:79], v[208:211], v[176:179], v[64:79]\n"
    "ds_read_b64_tr_b16 v[208:209], v240 offset:6144\nds_read_b64_tr_b16 v[210:211], v240 offset:6400\nv_mfma_f32_32x32x16_bf16 v[64:79], v[212:215], v[180:183], v[64:79]\n"
    "ds_read_b64_tr_b16 v[212:213], v240 offset:9216\nds_read_b64_tr_b16 v[214:215], v240 offset:9472\ns_waitcnt vmcnt(0)\nv_add_f32 v138, v112, v113\n"
    "v_add_f32 v139, v114, v115\nv_add_f32 v244, v116, v117\nv_mfma_f32_32x32x16_bf16 v[64:79], v[216:219], v[184:187], v[64:79]\n"
    "ds_read_b64_tr_b16 v[216:217], v242 offset:1024\nds_read_b64_tr_b16 v[218:219], v242 offset:1280\nv_add_f32 v243, v118, v119\nv_add_f32 v138, v138, v139\n"
    "v_add_f32 v244, v244, v243\nv_add_f32 v139, v138, v244\ns_waitcnt lgkmcnt(6)\nv_mfma_f32_32x32x16_bf16 v[64:79], v[220:223], v[188:191], v[64:79]\n"
    "ds_read_b64_tr_b16 v[220:221], v242 offset:4096\nds_read_b64_tr_b16 v[222:223], v242 offset:4352\nv_add_f32 v138, v138, v244\nv_cmp_u_f32 vcc, v138, v138\n"
    "v_mfma_f32_32x32x16_bf16 v[64:79], v[224:227], v[192:195], v[64:79]\nds_read_b64_tr_b16 v[224:225], v242 offset:7168\nds_read_b64_tr_b16 v[226:227], v242 offset:7424\n"
    "s_cbranch_vccnz .Lp191\n.Lp192:\nv_permlane32_swap_b32 v138, v139\nv_mul_f32 v138, s81, v138\nv_mul_f32 v112, v138, v138\n"
    "v_mfma_f32_32x32x16_bf16 v[64:79], v[228:231], v[196:199], v[64:79]\nds_read_b64_tr_b16 v[228:229], v242 offset:10240\nds_read_b64_tr_b16 v[230:231], v242 offset:10496\n"
    "s_memrealtime s[100:101]\ns_memtime s[78:79]\nv_cvt_pk_bf16_f32 v138, -v138, -v138\nv_fma_f32 v139, v139, s81, -v112\nv_cndmask_b32 v120, 0, v138, s[84:85]\n"
    "s_waitcnt lgkmcnt(6)\nv_mfma_f32_32x32x16_bf16 v[64:79], v[208:211], v[200:203], v[64:79]\nds_read_b64_tr_b16 v[208:209], v240 offset:1024\n"
    "ds_read_b64_tr_b16 v[210:211], v240 offset:1280\nv_max_f32 v139, 0, v139\nv_cvt_pk_bf16_f32 v139, 0, v139\nv_add_f32 v139, 0x3727c5ac, v139\n"
    "v_mfma_f32_32x32x16_bf16 v[64:79], v[212:215], v[204:207], v[64:79]\nds_read_b64_tr_b16 v[212:213], v240 offset:4096\nds_read_b64_tr_b16 v[214:215], v240 offset:4352\n"
    "v_mfma_f32_32x32x16_bf16 v[64:79], v[124:127], v[120:123], v[64:79]\nv_cvt_pk_bf16_f32 v139, 0, v139\nv_sqrt_f32 v139, v139\nv_mov_b64 v[232:233], 0\n"
    "v_mov_b64 v[234:235], 0\nv_cvt_pk_bf16_f32 v139, 0, v139\nv_rcp_f32 v136, v139\nv_mfma_f32_32x32x16_bf16 v[80:95], v[128:131], v[120:123], v[80:95]\n"
    "v_mov_b32 v137, v136\nv_mov_b64 v[236:237], 0\nv_mov_b64 v[238:239], 0\nv_mov_b64 v[244:245], 0\nv_mov_b64 v[246:247], 0\n"
    "v_mfma_f32_32x32x16_bf16 v[80:95], v[216:219], v[176:179], v[80:95]\nds_read_b64_tr_b16 v[216:217], v240 offset:7168\nds_read_b64_tr_b16 v[218:219], v240 offset:7424\n"
    "v_mul_f32 v64, v64, v136\nv_mul_f32 v65, v65, v136\nv_mul_f32 v66, v66, v136\ns_waitcnt lgkmcnt(6)\nv_mfma_f32_32x32x16_bf16 v[80:95], v[220:223], v[180:183], v[80:95]\n"
    "ds_read_b64_tr_b16 v[220:221], v240 offset:10240\nds_read_b64_tr_b16 v[222:223], v240 offset:10496\nv_mul_f32 v67, v67, v136\nv_cvt_pk_bf16_f32 v64, v64, v65\n"
    "v_cvt_pk_bf16_f32 v65, v66, v67\nv_mfma_f32_32x32x16_bf16 v[80:95], v[224:227], v[184:187], v[80:95]\nds_read_b64_tr_b16 v[224:225], v242 offset:2048\n"
    "ds_read_b64_tr_b16 v[226:227], v242 offset:2304\nds_write_b64 v37, v[64:65] offset:0\nv_mul_f32 v68, v68, v136\nv_mul_f32 v69, v69, v136\n"
    "v_mfma_f32_32x32x16_bf16 v[80:95], v[228:231], v[188:191], v[80:95]\ns_cbranch_execz .Lhop\nds_read_b64_tr_b16 v[228:229], v242 offset:5120\n"
    "ds_read_b64_tr_b16 v[230:231], v242 offset:5376\nv_mul_f32 v70, v70, v136\nv_mul_f32 v71, v71, v136\nv_cvt_pk_bf16_f32 v68, v68, v69\ns_waitcnt lgkmcnt(7)\n"
    "v_mfma_f32_32x32x16_bf16 v[80:95], v[208:211], v[192:195], v[80:95]\nds_read_b64_tr_b16 v[208:209], v242 offset:8192\nds_read_b64_tr_b16 v[210:211], v242 offset:8448\n"
    "v_cvt_pk_bf16_f32 v69, v70, v71\nds_write_b64 v37, v[68:69] offset:16\nv_mul_f32 v72, v72, v136\nv_mfma_f32_32x32x16_bf16 v[80:95], v[212:215], v[196:199], v[80:95]\n"
    "ds_read_b64_tr_b16 v[212:213], v242 offset:11264\nds_read_b64_tr_b16 v[214:215], v242 offset:11520\nv_mul_f32 v73, v73, v136\nv_mul_f32 v74, v74, v136\n"
    "v_mul_f32 v75, v75, v136\nv_mfma_f32_32x32x16_bf16 v[80:95], v[216:219], v[200:203], v[80:95]\nds_read_b64_tr_b16 v[216:217], v240 offset:2048\n"
    "ds_read_b64_tr_b16 v[218:219], v240 offset:2304\nv_cvt_pk_bf16_f32 v72, v72, v73\nv_cvt_pk_bf16_f32 v73, v74, v75\nds_write_b64 v37, v[72:73] offset:32\n"
    "s_waitcnt lgkmcnt(8)\nv_mfma_f32_32x32x16_bf16 v[80:95], v[220:223], v[204:207], v[80:95]\nds_read_b64_tr_b16 v[220:221], v240 offset:5120\n"
    "ds_read_b64_tr_b16 v[222:223], v240 offset:5376\nv_mul_f32 v76, v76, v136\nv_mul_f32 v77, v77, v136\nv_mul_f32 v78, v78, v136\n"
    "v_mfma_f32_32x32x16_bf16 v[96:111], v[224:227], v[176:179], v[96:111]\nds_read_b64_tr_b16 v[224:225], v240 offset:8192\nds_read_b64_tr_b16 v[226:227], v240 offset:8448\n"
    "v_mul_f32 v79, v79, v136\nv_cvt_pk_bf16_f32 v76, v76, v77\nv_cvt_pk_bf16_f32 v77, v78, v79\nv_mfma_f32_32x32x16_bf16 v[96:111], v[132:135], v[120:123], v[96:111]\n"
    "ds_write_b64 v37, v[76:77] offset:48\ns_cmp_lg_u32 s13, 1\ns_cbranch_scc1 .Lz207\nv_mov_b32 v140, s100\ns_mov_b64 exec, 1\nglobal_store_dword v141, v140, s[4:5] sc1\n"
    "s_mov_b64 exec, -1\n.Lz207:\nv_mfma_f32_32x32x16_bf16 v[96:111], v[228:231], v[180:183], v[96:111]\nds_read_b64_tr_b16 v[228:229], v240 offset:11264\n"
    "ds_read_b64_tr_b16 v[230:231], v240 offset:11520\nv_mul_f32 v80, v80, v136\nv_mul_f32 v81, v81, v136\nv_mul_f32 v82, v82, v136\nv_mul_f32 v83, v83, v136\n"
    "v_cvt_pk_bf16_f32 v80, v80, v81\ns_waitcnt lgkmcnt(8)\nv_mfma_f32_32x32x16_bf16 v[96:111], v[208:211], v[184:187], v[96:111]\nv_cvt_pk_bf16_f32 v81, v82, v83\n"
    "ds_write_b64 v37, v[80:81] offset:64\nv_mul_f32 v84, v84, v136\nv_mul_f32 v85, v85, v136\nv_mul_f32 v86, v86, v136\n"
    "v_mfma_f32_32x32x16_bf16 v[96:111], v[212:215], v[188:191], v[96:111]\nv_mul_f32 v87, v87, v136\nv_cvt_pk_bf16_f32 v84, v84, v85\nv_cvt_pk_bf16_f32 v85, v86, v87\n"
    "ds_write_b64 v37, v[84:85] offset:80\nv_mul_f32 v88, v88, v136\nv_mfma_f32_32x32x16_bf16 v[96:111], v[216:219], v[192:195], v[96:111]\nv_mul_f32 v89, v89, v136\n"
    "v_mul_f32 v90, v90, v136\nv_mul_f32 v91, v91, v136\nv_cvt_pk_bf16_f32 v88, v88, v89\nv_cvt_pk_bf16_f32 v89, v90, v91\ns_waitcnt lgkmcnt(2)\n"
    "v_mfma_f32_32x32x16_bf16 v[96:111], v[220:223], v[196:199], v[96:111]\nds_write_b64 v37, v[88:89] offset:96\nv_mul_f32 v92, v92, v136\nv_mul_f32 v93, v93, v136\n"
    "v_mul_f32 v94, v94, v136\nv_mul_f32 v95, v95, v136\nv_mfma_f32_32x32x16_bf16 v[96:111], v[224:227], v[200:203], v[96:111]\nv_cvt_pk_bf16_f32 v92, v92, v93\n"
    "v_cvt_pk_bf16_f32 v93, v94, v95\nds_write_b64 v37, v[92:93] offset:112\nv_mfma_f32_32x32x16_bf16 v[96:111], v[228:231], v[204:207], v[96:111]\nv_mov_b64 v[248:249], 0\n"
    "v_mov_b64 v[250:251], 0\ns_mov_b32 s44, 0x00ff00ff\ns_mov_b32 s45, 0xff00ff00\ns_mov_b32 s46, s44\ns_mov_b32 s47, s44\ns_nop 5\n"
    "v_pk_mul_f32 v[96:97], v[96:97], v[136:137]\nv_pk_mul_f32 v[98:99], v[98:99], v[136:137]\nv_cvt_pk_bf16_f32 v96, v96, v97\nv_cvt_pk_bf16_f32 v97, v98, v99\n"
    "ds_write_b64 v37, v[96:97] offset:128\nv_pk_mul_f32 v[100:101], v[100:101], v[136:137]\nv_pk_mul_f32 v[102:103], v[102:103], v[136:137]\n"
    "v_cvt_pk_bf16_f32 v100, v100, v101\nv_cvt_pk_bf16_f32 v101, v102, v103\nds_write_b64 v37, v[100:101] offset:144\nv_pk_mul_f32 v[104:105], v[104:105], v[136:137]\n"
    "v_pk_mul_f32 v[106:107], v[106:107], v[136:137]\nv_cvt_pk_bf16_f32 v104, v104, v105\nv_cvt_pk_bf16_f32 v105, v106, v107\nds_write_b64 v37, v[104:105] offset:160\n"
    "v_pk_mul_f32 v[108:109], v[108:109], v[136:137]\nv_pk_mul_f32 v[110:111], v[110:111], v[136:137]\nv_cvt_pk_bf16_f32 v108, v108, v109\nv_cvt_pk_bf16_f32 v109, v110, v111\n"
    "ds_write_b64 v37, v[108:109] offset:176\ns_branch .Latt12\n"
    ".Ll73x:\ns_add_u32 s59, s58, 0\nv_mov_b32 v70, s59\ns_mov_b64 exec, 1\ns_waitcnt vmcnt(8)\nds_max_i32 v56, v70\ns_mov_b64 exec, -1\ns_setprio 0\ns_waitcnt vmcnt(0)\n"
    "s_branch .Ll73y\n.La102:\ns_waitcnt vmcnt(0)\ns_branch .La104\n.Lw123:\ns_sub_u32 s80, s80, 1\ns_cbranch_scc1 .Lw123x\ns_mov_b64 exec, vcc\ns_add_u32 m0, s51, 0\n"
    "s_nop 2\nglobal_load_lds_dwordx4 v192, s[4:5] sc1\ns_mov_b64 exec, -1\ns_waitcnt vmcnt(0)\ns_branch .Lr122\n.Lw125:\ns_sub_u32 s80, s80, 1\ns_cbranch_scc1 .Lw125x\n"
    "s_mov_b64 exec, vcc\ns_add_u32 m0, s51, 4096\ns_nop 2\nglobal_load_lds_dwordx4 v193, s[4:5] sc1\ns_mov_b64 exec, -1\ns_waitcnt vmcnt(0)\ns_branch .Lr124\n.Lw127:\n"
    "s_sub_u32 s80, s80, 1\ns_cbranch_scc1 .Lw127x\ns_mov_b64 exec, vcc\ns_add_u32 m0, s51, 8192\ns_nop 2\nglobal_load_lds_dwordx4 v194, s[4:5] sc1\ns_add_u32 m0, s51, 12288\n"
    "s_nop 2\nglobal_load_lds_dwordx4 v195, s[4:5] sc1\ns_mov_b64 exec, -1\ns_waitcnt vmcnt(0)\ns_branch .Lr126\n.Lw129:\ns_sub_u32 s80, s80, 1\ns_cbranch_scc1 .Lw129x\n"
    "s_mov_b64 exec, vcc\ns_add_u32 m0, s51, 16384\ns_nop 2\nglobal_load_lds_dwordx4 v196, s[4:5] sc1\ns_add_u32 m0, s51, 20480\ns_nop 2\n"
    "global_load_lds_dwordx4 v197, s[4:5] sc1\ns_mov_b64 exec, -1\ns_waitcnt vmcnt(0)\ns_branch .Lr128\n.Lw131:\ns_sub_u32 s80, s80, 1\ns_cbranch_scc1 .Lw131x\n"
    "s_mov_b64 exec, vcc\ns_add_u32 m0, s51, 24576\ns_nop 2\ns_nop 0\ns_cbranch_execz .Lhop\nglobal_load_lds_dwordx4 v198, s[4:5] sc1\ns_add_u32 m0, s51, 28672\ns_nop 2\n"
    "global_load_lds_dwordx4 v199, s[4:5] sc1\ns_mov_b64 exec, -1\ns_waitcnt vmcnt(0)\ns_branch .Lr130\n.Lh140:\ns_cmp_lg_u32 s13, 0\ns_cbranch_scc1 .Lh143\n"
    "s_mov_b32 s80, 0x10000\nv_mov_b32 v241, 18\n.Lh142:\nglobal_load_dwordx4 v[192:195], v57, s[4:5] sc1\ns_waitcnt vmcnt(0)\nv_xor_b32 v192, s11, v192\n"
    "v_xor_b32 v193, s11, v193\nv_xor_b32 v194, s11, v194\nv_xor_b32 v195, s11, v195\nv_or3_b32 v242, v192, v193, v194\nv_or_b32 v242, v242, v195\nv_cmp_ne_u32 vcc, 0, v242\n"
    "s_cbranch_vccz .Lh144\ns_sub_u32 s80, s80, 1\ns_cbranch_scc1 .Lh144\ns_sleep 4\ns_branch .Lh142\n.Lh144:\ns_mov_b64 exec, 1\nds_write_b32 v56, v241 offset:4\n"
    "s_mov_b64 exec, -1\n.Lh143:\ns_mov_b32 s86, 0x100000\n.Ls145:\nds_read_b32 v242, v56 offset:4\ns_waitcnt lgkmcnt(0)\nv_cmp_gt_i32 vcc, 18, v242\ns_sub_u32 s86, s86, 1\n"
    "s_cbranch_scc1 .Ls146\ns_cbranch_vccz .Ls146\ns_sleep 1\ns_branch .Ls145\n.Ls146:\nglobal_load_dwordx4 v[64:67], v59, s[68:69] offset:-4096 sc1\n"
    "global_load_dwordx4 v[68:71], v59, s[68:69] offset:-3072 sc1\nglobal_load_dwordx4 v[72:75], v59, s[68:69] offset:-2048 sc1\n"
    "global_load_dwordx4 v[76:79], v59, s[68:69] offset:-1024 sc1\nglobal_load_dwordx4 v[80:83], v59, s[68:69] offset:0 sc1\n"
    "global_load_dwordx4 v[84:87], v59, s[68:69] offset:1024 sc1\nglobal_load_dwordx4 v[88:91], v59, s[68:69] offset:2048 sc1\n"
    "global_load_dwordx4 v[92:95], v59, s[68:69] offset:3072 sc1\nglobal_load_dwordx4 v[96:99], v59, s[70:71] offset:-4096 sc1\n"
    "global_load_dwordx4 v[100:103], v59, s[70:71] offset:-3072 sc1\nglobal_load_dwordx4 v[104:107], v59, s[70:71] offset:-2048 sc1\n"
    "global_load_dwordx4 v[108:111], v59, s[70:71] offset:-1024 sc1\nglobal_load_dwordx4 v[112:115], v59, s[70:71] offset:0 sc1\n"
    "global_load_dwordx4 v[116:119], v59, s[70:71] offset:1024 sc1\nglobal_load_dwordx4 v[120:123], v59, s[70:71] offset:2048 sc1\n"
    "global_load_dwordx4 v[124:127], v59, s[70:71] offset:3072 sc1\ns_branch .Lh141\n.Lw147:\ns_sub_u32 s80, s80, 1\ns_cbranch_scc1 .Lhwo113\n"
    "global_load_dword v240, v47, s[4:5] offset:0 sc1\nglobal_load_dword v241, v47, s[4:5] offset:128 sc1\nglobal_load_dword v242, v47, s[4:5] offset:512 sc1\n"
    "global_load_dword v243, v47, s[4:5] offset:640 sc1\nglobal_load_dword v244, v47, s[4:5] offset:1024 sc1\nglobal_load_dword v245, v47, s[4:5] offset:1152 sc1\n"
    "global_load_dword v246, v47, s[4:5] offset:1536 sc1\nglobal_load_dword v247, v47, s[4:5] offset:1664 sc1\ns_waitcnt vmcnt(0)\nv_max3_u32 v232, v240, v241, v242\n"
    "v_max3_u32 v233, v243, v244, v245\nv_max3_u32 v232, v232, v246, v247\nv_max_u32 v232, v232, v233\nv_cmp_eq_u32 vcc, -1, v232\ns_cbranch_vccnz .Lw147\ns_branch .Lhwo113\n"
    ".Llw158:\nv_max3_u32 v232, v208, v209, v210\nv_max3_u32 v232, v232, v211, v212\nv_max3_u32 v232, v232, v213, v214\nv_max3_u32 v232, v232, v215, v216\n"
    "v_max3_u32 v232, v232, v217, v218\nv_max3_u32 v232, v232, v219, v220\nv_max3_u32 v232, v232, v221, v222\nv_max_u32 v232, v232, v223\nv_cmp_eq_u32 vcc, -1, v232\n"
    "s_or_b64 vcc, vcc, s[50:51]\ns_cbranch_scc0 .Llx159\ns_sub_u32 s80, s80, 1\ns_cbranch_scc1 .Llx159\ns_mov_b64 exec, vcc\nglobal_load_dwordx4 v[240:243], v17, s[4:5] sc1\n"
    "global_load_dwordx4 v[208:211], v16, s[4:5] sc1\nglobal_load_dwordx4 v[212:215], v16, s[58:59] sc1\nglobal_load_dwordx4 v[216:219], v16, s[60:61] sc1\n"
    "global_load_dwordx4 v[220:223], v16, s[62:63] sc1\ns_mov_b64 exec, -1\ns_branch .Ln155\n.Lw178:\ns_mov_b32 s86, 0x100000\n.Ls180:\nds_read_b32 v250, v24 offset:0\n"
    "s_waitcnt lgkmcnt(0)\nv_cmp_gt_i32 vcc, v26, v250\ns_sub_u32 s86, s86, 1\ns_cbranch_scc1 .Ls181\ns_cbranch_vccz .Ls181\ns_sleep 1\ns_branch .Ls180\n.Ls181:\n"
    "s_branch .Lr179\n.Lq183:\nv_max3_u32 v52, v192, v193, v194\nv_max3_u32 v52, v52, v195, v196\nv_max3_u32 v52, v52, v197, v198\nv_max3_u32 v52, v52, v199, v200\n"
    "v_max3_u32 v52, v52, v201, v202\nv_max3_u32 v52, v52, v203, v204\nv_max3_u32 v52, v52, v205, v206\nv_max_u32 v52, v52, v207\nv_cmp_eq_u32 vcc, -1, v52\n"
    "s_cbranch_vccz .Lq183x\ns_sub_u32 s80, s80, 1\ns_cbranch_scc1 .Lq183x\ns_setprio 3\ns_waitcnt lgkmcnt(0)\nglobal_load_dword v192, v224, s[48:49] offset:0 sc1\n"
    "global_load_dword v193, v224, s[48:49] offset:256 sc1\nglobal_load_dword v194, v224, s[48:49] offset:512 sc1\nglobal_load_dword v195, v224, s[48:49] offset:768 sc1\n"
    "global_load_dword v196, v224, s[48:49] offset:1024 sc1\nglobal_load_dword v197, v224, s[48:49] offset:1280 sc1\nglobal_load_dword v198, v224, s[48:49] offset:1536 sc1\n"
    "global_load_dword v199, v224, s[48:49] offset:1792 sc1\nglobal_load_dword v200, v224, s[48:49] offset:2048 sc1\nglobal_load_dword v201, v224, s[48:49] offset:2304 sc1\n"
    "global_load_dword v202, v224, s[48:49] offset:2560 sc1\nglobal_load_dword v203, v224, s[48:49] offset:2816 sc1\nglobal_load_dword v204, v224, s[48:49] offset:3072 sc1\n"
    "global_load_dword v205, v224, s[48:49] offset:3328 sc1\nglobal_load_dword v206, v224, s[48:49] offset:3584 sc1\nglobal_load_dword v207, v224, s[48:49] offset:3840 sc1\n"
    "s_branch .Lq182\n.Ld189:\nv_max3_u32 v244, v144, v145, v146\nv_max3_u32 v243, v147, v148, v149\nv_max3_u32 v244, v244, v150, v151\nv_max3_u32 v243, v243, v152, v153\n"
    "v_max3_u32 v244, v244, v154, v155\nv_max3_u32 v243, v243, v156, v157\nv_max3_u32 v244, v244, v158, v159\nv_max_u32 v243, v243, v244\nv_cmp_eq_u32 vcc, -1, v243\n"
    "s_cbranch_vccz .Ld190\ns_sub_u32 s80, s80, 1\ns_cbranch_scc1 .Ld190\ns_sleep 2\nglobal_load_dword v144, v39, s[52:53] offset:-2048 sc1\n"
    "global_load_dword v145, v39, s[52:53] offset:-1920 sc1\nglobal_load_dword v146, v39, s[52:53] offset:-1792 sc1\nglobal_load_dword v147, v39, s[52:53] offset:-1664 sc1\n"
    "global_load_dword v148, v39, s[52:53] offset:-1024 sc1\nglobal_load_dword v149, v39, s[52:53] offset:-896 sc1\nglobal_load_dword v150, v39, s[52:53] offset:-768 sc1\n"
    "global_load_dword v151, v39, s[52:53] offset:-640 sc1\nglobal_load_dword v152, v39, s[52:53] offset:0 sc1\nglobal_load_dword v153, v39, s[52:53] offset:128 sc1\n"
    "global_load_dword v154, v39, s[52:53] offset:256 sc1\nglobal_load_dword v155, v39, s[52:53] offset:384 sc1\nglobal_load_dword v156, v39, s[52:53] offset:1024 sc1\n"
    "global_load_dword v157, v39, s[52:53] offset:1152 sc1\ns_cbranch_execz .Lhop\nglobal_load_dword v158, v39, s[52:53] offset:1280 sc1\n"
    "global_load_dword v159, v39, s[52:53] offset:1408 sc1\nds_read_b64_tr_b16 v[208:209], v242 offset:0\nds_read_b64_tr_b16 v[210:211], v242 offset:256\n"
    "ds_read_b64_tr_b16 v[212:213], v242 offset:3072\nds_read_b64_tr_b16 v[214:215], v242 offset:3328\nds_read_b64_tr_b16 v[216:217], v242 offset:6144\n"
    "ds_read_b64_tr_b16 v[218:219], v242 offset:6400\nds_read_b64_tr_b16 v[220:221], v242 offset:9216\nds_read_b64_tr_b16 v[222:223], v242 offset:9472\n"
    "ds_read_b64_tr_b16 v[224:225], v242 offset:1024\nds_read_b64_tr_b16 v[226:227], v242 offset:1280\nds_read_b64_tr_b16 v[228:229], v242 offset:4096\n"
    "ds_read_b64_tr_b16 v[230:231], v242 offset:4352\ns_waitcnt vmcnt(0) lgkmcnt(0)\ns_branch .Ld188\n.Lp191:\nv_max3_u32 v243, v112, v113, v114\n"
    "v_max3_u32 v243, v243, v115, v116\nv_max3_u32 v243, v243, v117, v118\nv_max_u32 v243, v243, v119\nv_cmp_eq_u32 vcc, -1, v243\ns_cbranch_vccz .Lp192\n"
    "s_sub_u32 s80, s80, 1\ns_cbranch_scc1 .Lp192\nglobal_load_dword v112, v34, s[4:5] offset:0 sc1\nglobal_load_dword v113, v34, s[4:5] offset:256 sc1\n"
    "global_load_dword v114, v34, s[4:5] offset:512 sc1\nglobal_load_dword v115, v34, s[4:5] offset:768 sc1\nglobal_load_dword v116, v34, s[4:5] offset:1024 sc1\n"
    "global_load_dword v117, v34, s[4:5] offset:1280 sc1\nglobal_load_dword v118, v34, s[4:5] offset:1536 sc1\nglobal_load_dword v119, v34, s[4:5] offset:1792 sc1\n"
    "s_waitcnt vmcnt(0)\nv_add_f32 v138, v112, v113\nv_add_f32 v139, v114, v115\nv_add_f32 v244, v116, v117\nv_add_f32 v243, v118, v119\nv_add_f32 v138, v138, v139\n"
    "v_add_f32 v244, v244, v243\nv_add_f32 v139, v138, v244\nv_add_f32 v138, v138, v244\nv_cmp_u_f32 vcc, v138, v138\ns_nop 0\ns_branch .Lp191\n.Lx193:\n"
    "s_sub_u32 s80, s80, 1\ns_cbranch_scc1 .Lx194\ns_sleep 2\nglobal_load_dword v160, v39, s[54:55] offset:-2048 sc1\nglobal_load_dword v161, v39, s[54:55] offset:-1920 sc1\n"
    "global_load_dword v162, v39, s[54:55] offset:-1792 sc1\nglobal_load_dword v163, v39, s[54:55] offset:-1664 sc1\nglobal_load_dword v164, v39, s[54:55] offset:-1024 sc1\n"
    "global_load_dword v165, v39, s[54:55] offset:-896 sc1\nglobal_load_dword v166, v39, s[54:55] offset:-768 sc1\nglobal_load_dword v167, v39, s[54:55] offset:-640 sc1\n"
    "global_load_dword v168, v39, s[54:55] offset:0 sc1\nglobal_load_dword v169, v39, s[54:55] offset:128 sc1\nglobal_load_dword v170, v39, s[54:55] offset:256 sc1\n"
    "global_load_dword v171, v39, s[54:55] offset:384 sc1\nglobal_load_dword v172, v39, s[54:55] offset:1024 sc1\nglobal_load_dword v173, v39, s[54:55] offset:1152 sc1\n"
    "global_load_dword v174, v39, s[54:55] offset:1280 sc1\nglobal_load_dword v175, v39, s[54:55] offset:1408 sc1\ns_waitcnt vmcnt(0)\nv_max3_u32 v244, v160, v161, v162\n"
    "v_max3_u32 v243, v163, v164, v165\nv_max3_u32 v244, v244, v166, v167\nv_max3_u32 v243, v243, v168, v169\nv_max3_u32 v244, v244, v170, v171\n"
    "v_max3_u32 v243, v243, v172, v173\nv_max3_u32 v244, v244, v174, v175\nv_max_u32 v243, v243, v244\nv_cmp_eq_u32 vcc, -1, v243\ns_cbranch_vccnz .Lx193\ns_branch .Lx194\n"
    ".Lx195:\ns_sub_u32 s80, s80, 1\ns_cbranch_scc1 .Lx196\ns_sleep 2\nglobal_load_dword v176, v39, s[56:57] offset:-2048 sc1\n"
    "global_load_dword v177, v39, s[56:57] offset:-1920 sc1\nglobal_load_dword v178, v39, s[56:57] offset:-1792 sc1\nglobal_load_dword v179, v39, s[56:57] offset:-1664 sc1\n"
    "global_load_dword v180, v39, s[56:57] offset:-1024 sc1\nglobal_load_dword v181, v39, s[56:57] offset:-896 sc1\nglobal_load_dword v182, v39, s[56:57] offset:-768 sc1\n"
    "global_load_dword v183, v39, s[56:57] offset:-640 sc1\nglobal_load_dword v184, v39, s[56:57] offset:0 sc1\nglobal_load_dword v185, v39, s[56:57] offset:128 sc1\n"
    "global_load_dword v186, v39, s[56:57] offset:256 sc1\nglobal_load_dword v187, v39, s[56:57] offset:384 sc1\nglobal_load_dword v188, v39, s[56:57] offset:1024 sc1\n"
    "global_load_dword v189, v39, s[56:57] offset:1152 sc1\nglobal_load_dword v190, v39, s[56:57] offset:1280 sc1\nglobal_load_dword v191, v39, s[56:57] offset:1408 sc1\n"
    "s_waitcnt vmcnt(0)\nv_max3_u32 v244, v176, v177, v178\nv_max3_u32 v243, v179, v180, v181\nv_max3_u32 v244, v244, v182, v183\nv_max3_u32 v243, v243, v184, v185\n"
    "v_max3_u32 v244, v244, v186, v187\nv_max3_u32 v243, v243, v188, v189\nv_max3_u32 v244, v244, v190, v191\nv_max_u32 v243, v243, v244\nv_cmp_eq_u32 vcc, -1, v243\n"
    "s_cbranch_vccnz .Lx195\ns_branch .Lx196\n.Lx197:\ns_sub_u32 s80, s80, 1\ns_cbranch_scc1 .Lx198\ns_sleep 2\nglobal_load_dword v192, v39, s[58:59] offset:-2048 sc1\n"
    "global_load_dword v193, v39, s[58:59] offset:-1920 sc1\nglobal_load_dword v194, v39, s[58:59] offset:-1792 sc1\nglobal_load_dword v195, v39, s[58:59] offset:-1664 sc1\n"
    "global_load_dword v196, v39, s[58:59] offset:-1024 sc1\nglobal_load_dword v197, v39, s[58:59] offset:-896 sc1\nglobal_load_dword v198, v39, s[58:59] offset:-768 sc1\n"
    "global_load_dword v199, v39, s[58:59] offset:-640 sc1\nglobal_load_dword v200, v39, s[58:59] offset:0 sc1\nglobal_load_dword v201, v39, s[58:59] offset:128 sc1\n"
    "global_load_dword v202, v39, s[58:59] offset:256 sc1\nglobal_load_dword v203, v39, s[58:59] offset:384 sc1\nglobal_load_dword v204, v39, s[58:59] offset:1024 sc1\n"
    "global_load_dword v205, v39, s[58:59] offset:1152 sc1\nglobal_load_dword v206, v39, s[58:59] offset:1280 sc1\nglobal_load_dword v207, v39, s[58:59] offset:1408 sc1\n"
    "s_waitcnt vmcnt(0)\nv_max3_u32 v244, v192, v193, v194\nv_max3_u32 v243, v195, v196, v197\nv_max3_u32 v244, v244, v198, v199\nv_max3_u32 v243, v243, v200, v201\n"
    "v_max3_u32 v244, v244, v202, v203\nv_max3_u32 v243, v243, v204, v205\nv_max3_u32 v244, v244, v206, v207\nv_max_u32 v243, v243, v244\nv_cmp_eq_u32 vcc, -1, v243\n"
    "s_cbranch_vccnz .Lx197\ns_branch .Lx198\n.Lx199:\ns_sub_u32 s80, s80, 1\ns_cbranch_scc1 .Lx200\ns_sleep 2\nglobal_load_dword v144, v39, s[60:61] offset:-2048 sc1\n"
    "global_load_dword v145, v39, s[60:61] offset:-1920 sc1\nglobal_load_dword v146, v39, s[60:61] offset:-1792 sc1\nglobal_load_dword v147, v39, s[60:61] offset:-1664 sc1\n"
    "global_load_dword v148, v39, s[60:61] offset:-1024 sc1\nglobal_load_dword v149, v39, s[60:61] offset:-896 sc1\nglobal_load_dword v150, v39, s[60:61] offset:-768 sc1\n"
    "global_load_dword v151, v39, s[60:61] offset:-640 sc1\nglobal_load_dword v152, v39, s[60:61] offset:0 sc1\nglobal_load_dword v153, v39, s[60:61] offset:128 sc1\ns_nop 0\n"
    "s_cbranch_execz .Lhop\nglobal_load_dword v154, v39, s[60:61] offset:256 sc1\nglobal_load_dword v155, v39, s[60:61] offset:384 sc1\n"
    "global_load_dword v156, v39, s[60:61] offset:1024 sc1\nglobal_load_dword v157, v39, s[60:61] offset:1152 sc1\nglobal_load_dword v158, v39, s[60:61] offset:1280 sc1\n"
    "global_load_dword v159, v39, s[60:61] offset:1408 sc1\ns_waitcnt vmcnt(0)\nv_max3_u32 v244, v144, v145, v146\nv_max3_u32 v243, v147, v148, v149\n"
    "v_max3_u32 v244, v244, v150, v151\nv_max3_u32 v243, v243, v152, v153\nv_max3_u32 v244, v244, v154, v155\nv_max3_u32 v243, v243, v156, v157\n"
    "v_max3_u32 v244, v244, v158, v159\nv_max_u32 v243, v243, v244\nv_cmp_eq_u32 vcc, -1, v243\ns_cbranch_vccnz .Lx199\ns_branch .Lx200\n.Lx201:\ns_sub_u32 s80, s80, 1\n"
    "s_cbranch_scc1 .Lx202\ns_sleep 2\nglobal_load_dword v160, v39, s[62:63] offset:-2048 sc1\nglobal_load_dword v161, v39, s[62:63] offset:-1920 sc1\n"
    "global_load_dword v162, v39, s[62:63] offset:-1792 sc1\nglobal_load_dword v163, v39, s[62:63] offset:-1664 sc1\nglobal_load_dword v164, v39, s[62:63] offset:-1024 sc1\n"
    "global_load_dword v165, v39, s[62:63] offset:-896 sc1\nglobal_load_dword v166, v39, s[62:63] offset:-768 sc1\nglobal_load_dword v167, v39, s[62:63] offset:-640 sc1\n"
    "global_load_dword v168, v39, s[62:63] offset:0 sc1\nglobal_load_dword v169, v39, s[62:63] offset:128 sc1\nglobal_load_dword v170, v39, s[62:63] offset:256 sc1\n"
    "global_load_dword v171, v39, s[62:63] offset:384 sc1\nglobal_load_dword v172, v39, s[62:63] offset:1024 sc1\nglobal_load_dword v173, v39, s[62:63] offset:1152 sc1\n"
    "global_load_dword v174, v39, s[62:63] offset:1280 sc1\nglobal_load_dword v175, v39, s[62:63] offset:1408 sc1\ns_waitcnt vmcnt(0)\nv_max3_u32 v244, v160, v161, v162\n"
    "v_max3_u32 v243, v163, v164, v165\nv_max3_u32 v244, v244, v166, v167\nv_max3_u32 v243, v243, v168, v169\nv_max3_u32 v244, v244, v170, v171\n"
    "v_max3_u32 v243, v243, v172, v173\nv_max3_u32 v244, v244, v174, v175\nv_max_u32 v243, v243, v244\nv_cmp_eq_u32 vcc, -1, v243\ns_cbranch_vccnz .Lx201\ns_branch .Lx202\n"
    ".Lx203:\ns_sub_u32 s80, s80, 1\ns_cbranch_scc1 .Lx204\ns_sleep 2\nglobal_load_dword v176, v39, s[64:65] offset:-2048 sc1\n"
    "global_load_dword v177, v39, s[64:65] offset:-1920 sc1\nglobal_load_dword v178, v39, s[64:65] offset:-1792 sc1\nglobal_load_dword v179, v39, s[64:65] offset:-1664 sc1\n"
    "global_load_dword v180, v39, s[64:65] offset:-1024 sc1\nglobal_load_dword v181, v39, s[64:65] offset:-896 sc1\nglobal_load_dword v182, v39, s[64:65] offset:-768 sc1\n"
    "global_load_dword v183, v39, s[64:65] offset:-640 sc1\nglobal_load_dword v184, v39, s[64:65] offset:0 sc1\nglobal_load_dword v185, v39, s[64:65] offset:128 sc1\n"
    "global_load_dword v186, v39, s[64:65] offset:256 sc1\nglobal_load_dword v187, v39, s[64:65] offset:384 sc1\nglobal_load_dword v188, v39, s[64:65] offset:1024 sc1\n"
    "global_load_dword v189, v39, s[64:65] offset:1152 sc1\nglobal_load_dword v190, v39, s[64:65] offset:1280 sc1\nglobal_load_dword v191, v39, s[64:65] offset:1408 sc1\n"
    "s_waitcnt vmcnt(0)\nv_max3_u32 v244, v176, v177, v178\nv_max3_u32 v243, v179, v180, v181\nv_max3_u32 v244, v244, v182, v183\nv_max3_u32 v243, v243, v184, v185\n"
    "v_max3_u32 v244, v244, v186, v187\nv_max3_u32 v243, v243, v188, v189\nv_max3_u32 v244, v244, v190, v191\nv_max_u32 v243, v243, v244\nv_cmp_eq_u32 vcc, -1, v243\n"
    "s_cbranch_vccnz .Lx203\ns_branch .Lx204\n.Lx205:\ns_sub_u32 s80, s80, 1\ns_cbranch_scc1 .Lx206\ns_sleep 2\nglobal_load_dword v192, v39, s[66:67] offset:-2048 sc1\n"
    "global_load_dword v193, v39, s[66:67] offset:-1920 sc1\nglobal_load_dword v194, v39, s[66:67] offset:-1792 sc1\nglobal_load_dword v195, v39, s[66:67] offset:-1664 sc1\n"
    "global_load_dword v196, v39, s[66:67] offset:-1024 sc1\nglobal_load_dword v197, v39, s[66:67] offset:-896 sc1\nglobal_load_dword v198, v39, s[66:67] offset:-768 sc1\n"
    "global_load_dword v199, v39, s[66:67] offset:-640 sc1\nglobal_load_dword v200, v39, s[66:67] offset:0 sc1\nglobal_load_dword v201, v39, s[66:67] offset:128 sc1\n"
    "global_load_dword v202, v39, s[66:67] offset:256 sc1\nglobal_load_dword v203, v39, s[66:67] offset:384 sc1\nglobal_load_dword v204, v39, s[66:67] offset:1024 sc1\n"
    "global_load_dword v205, v39, s[66:67] offset:1152 sc1\nglobal_load_dword v206, v39, s[66:67] offset:1280 sc1\nglobal_load_dword v207, v39, s[66:67] offset:1408 sc1\n"
    "s_waitcnt vmcnt(0)\nv_max3_u32 v244, v192, v193, v194\nv_max3_u32 v243, v195, v196, v197\nv_max3_u32 v244, v244, v198, v199\nv_max3_u32 v243, v243, v200, v201\n"
    "v_max3_u32 v244, v244, v202, v203\nv_max3_u32 v243, v243, v204, v205\nv_max3_u32 v244, v244, v206, v207\nv_max_u32 v243, v243, v244\nv_cmp_eq_u32 vcc, -1, v243\n"
    "s_cbranch_vccnz .Lx205\ns_branch .Lx206\n.fill 62, 4, 0xBF800000\ns_cbranch_execz .Lhop\n.fill 255, 4, 0xBF800000\ns_cbranch_execz .Lhop\n.Lk_all_end:\n"
    :: "{s[0:1]}"(__builtin_amdgcn_kernarg_segment_ptr()), "{s2}"(blockIdx.x), "{v0}"(threadIdx.x), "{s3}"((uint32_t)(uintptr_t)(__attribute__((address_space(3))) char*)smem)
    : "v255", "s101", "vcc", "scc", "memory");
}

extern "C" void kernel_launch(void* const* d_in, const int* in_sizes, int n_in,
                              void* d_out, int out_size, void* d_ws, size_t ws_size,
                              hipStream_t stream) {
  Args a;
  a.x = (const u16*)d_in[0]; a.w = (const u16*)d_in[1]; a.wo = (const u16*)d_in[2];
  a.A_re = (const u16*)d_in[4]; a.A_im = (const u16*)d_in[5]; a.B_re = (const u16*)d_in[6]; a.B_im = (const u16*)d_in[7];
  a.C_re = (const u16*)d_in[8]; a.C_im = (const u16*)d_in[9]; a.log_dt = (const u16*)d_in[10]; a.Dskip = (const u16*)d_in[11];
  a.out = (u16*)d_out; a.ws = (char*)d_ws;
  k_all<<<256, 512, 0, stream>>>(a);
}
```
